```python
import torch

def _softmax(x, axis=-1):
    return torch.softmax(x, dim=axis)
def _gelu(x):
    return torch.nn.functional.gelu(x, approximate="tanh")
def _silu(x):
    return torch.nn.functional.silu(x)
def _sigmoid(x):
    return torch.sigmoid(x)
def _layernorm(x, axis=-1, eps=1e-5):
    m = x.mean(dim=axis, keepdim=True)
    v = x.var(dim=axis, keepdim=True, unbiased=False)
    return (x - m) / torch.sqrt(v + eps)
def _scatter_add(tgt, idx, src, axis=0):
    return tgt.clone().index_add_(axis, idx, src)
def _segment_sum(src, seg, n):
    out = torch.zeros((n,) + src.shape[1:], dtype=src.dtype)
    return out.index_add_(0, seg, src)
def _pad(x, pw):
    flat = tuple(v for pair in reversed(pw) for v in pair)
    return torch.nn.functional.pad(x, flat)

kernel_name = 'standalone__sequential_behavior_din__recsys'

def setup_inputs():

    _w = (lambda a: (lambda t: t.to(torch.bfloat16) if t.is_floating_point() else t)(torch.as_tensor(a)))
    B, L, D = 16384, 200, 64
    H = D
    return {
        "target": _w((lambda *s: torch.randn(*s, dtype=torch.bfloat16) * 1.6)(B, D)),
        "hist": _w((lambda *s: torch.randn(*s, dtype=torch.bfloat16) * 1.6)(B, L, D)),
        "mask": _w((lambda lo, hi, *s: torch.randint(lo, hi, s))(0, 2, B, L).to(float)),
        "w1": _w((lambda *s: torch.randn(*s, dtype=torch.bfloat16) * 1.6)(4 * D, H) * (4 * D) ** -0.5),
        "b1": _w((lambda *s: torch.randn(*s, dtype=torch.bfloat16) * 1.6)(H) * 0.1),
        "w2": _w((lambda *s: torch.randn(*s, dtype=torch.bfloat16) * 0.05)(H, 1) * H ** -0.5),
        "b2": _w((lambda *s: torch.randn(*s, dtype=torch.bfloat16) * 0.05)(1) * 0.1),
    }

def reference(target, hist, mask, w1, b1, w2, b2):
    L = hist.shape[1]
    tgt = (lambda x, n, axis: torch.repeat_interleave(x, n, dim=axis))(target[:, None, :], L, axis=1)
    feat = torch.concatenate([hist, tgt, hist - tgt, hist * tgt], axis=-1)
    h = _sigmoid(feat @ w1 + b1)
    scores = (h @ w2 + b2)[..., 0] * mask
    return torch.einsum('bl,bld->bd', scores, hist)
```

```cpp
#include <hip/hip_runtime.h>
#include <stdint.h>

__device__ __forceinline__ float bf2f(unsigned short u) { return __uint_as_float(((uint32_t)u) << 16); }

__global__ __launch_bounds__(512, 2) void din_kernel(
    const unsigned short* hist, uint32_t hn, uint32_t hf, const unsigned short* mask, uint32_t mn, uint32_t mf,
    const unsigned short* w1, uint32_t wn, uint32_t wf, unsigned short* out, uint32_t on, uint32_t of,
    const unsigned short* target, uint32_t tn, uint32_t tf, const unsigned short* b1, const unsigned short* w2, const unsigned short* b2,
    uint32_t row_bytes, uint32_t round_bytes, uint32_t row_lanes, uint32_t round_out, uint32_t paced, uint32_t usual_units) {
  __shared__ __attribute__((aligned(1024))) char smem[154624];
  asm volatile(
    ".p2align 3\n"
    "s_load_dwordx16 s[40:55], s[0:1], 0x0\n"
    "s_load_dwordx16 s[4:19], s[0:1], 0x40\n"
    "s_memrealtime s[76:77]\n"
    "v_readfirstlane_b32 s3, v0\n"
    "v_and_b32 v1, 63, v0\n"
    "v_and_b32 v2, 48, v0\n"
    "v_lshlrev_b32 v251, 4, v1\n"
    "s_lshr_b32 s20, s3, 6\n"
    "s_lshl_b32 s78, s2, 3\n"
    "s_and_b32 s21, s20, 3\n"
    "s_lshr_b32 s75, s3, 8\n"
    "s_cselect_b64 vcc, -1, 0\n"
    "s_cbranch_scc0 .Ldin_p0\n"
    "s_setprio 1\n"
    ".Ldin_p0:\n"
    "s_cmp_lt_u32 s2, 32\n"
    "s_cbranch_scc0 .Ldin_ncp\n"
    "s_getpc_b64 s[34:35]\n"
    "v_lshlrev_b32 v5, 7, v1\n"
    "s_mul_i32 s23, s20, 2560\n"
    "s_add_u32 s34, s34, s23\n"
    "s_addc_u32 s35, s35, 0\n"
    "s_mov_b64 exec, 0xfffff\n"
    "global_load_dword v253, v5, s[34:35] offset:1024\n"
    "s_mov_b64 exec, -1\n"
    ".Ldin_ncp:\n"
    "v_lshlrev_b32 v6, 2, v0\n"
    "v_and_b32 v7, 32, v0\n"
    "v_and_b32 v6, 64, v6\n"
    "v_xor_b32 v5, v251, v2\n"
    "v_bitop3_b32 v6, v6, v7, v251 bitop3:0x96\n"
    "v_and_b32 v3, 0x1f0, v251\n"
    "v_xor_b32 v7, 32, v6\n"
    "v_mov_b64 v[40:41], 0\n"
    "v_mov_b64 v[42:43], 0\n"
    "v_mov_b64 v[44:45], 0\n"
    "v_mov_b64 v[46:47], 0\n"
    "v_bfe_u32 v50, v251, 3, 5\n"
    "s_lshl_b32 s23, s21, 5\n"
    "v_lshrrev_b32_e64 v51, 1, v2\n"
    "s_lshl_b32 s24, s20, 12\n"
    "v_or_b32 v51, s23, v51\n"
    "s_add_u32 m0, s24, 54144\n"
    "s_add_u32 s22, s78, s20\n"
    "s_lshl_b32 s25, s20, 10\n"
    "s_lshl_b32 s26, s2, 10\n"
    "s_waitcnt lgkmcnt(0)\n"
    "s_mul_i32 s67, s22, s14\n"
    "s_cbranch_vccz .Ldin_pm\n"
    "global_load_short_d16_hi v40, v50, s[10:11] offset:0\n"
    "global_load_short_d16_hi v41, v50, s[10:11] offset:32\n"
    "global_load_short_d16_hi v42, v50, s[10:11] offset:64\n"
    "global_load_short_d16_hi v43, v50, s[10:11] offset:96\n"
    "global_load_short_d16_hi v44, v45, s[12:13]\n"
    "global_load_dwordx2 v[46:47], v51, s[10:11]\n"
    "global_load_dwordx2 v[48:49], v51, s[8:9]\n"
    "s_branch .Ldin_p1\n"
    ".Ldin_pm:\n"
    "s_mov_b32 exec_lo, s16\n"
    "s_mov_b32 exec_hi, 0\n"
    "s_lshl2_add_u32 s28, s14, s67\n"
    "buffer_load_dwordx4 v[40:43], v3, s[44:47], s67 offen nt\n"
    "s_add_u32 s29, s67, s15\n"
    "buffer_load_dwordx4 v[44:47], v3, s[44:47], s28 offen nt\n"
    "s_mov_b32 exec_hi, s16\n"
    "s_mov_b32 exec_lo, 0\n"
    "s_add_u32 s30, s28, s15\n"
    "buffer_load_dwordx4 v[40:43], v3, s[44:47], s29 offen nt\n"
    "buffer_load_dwordx4 v[44:47], v3, s[44:47], s30 offen nt\n"
    "s_mov_b64 exec, -1\n"
    ".Ldin_p1:\n"
    "s_mul_i32 s23, s20, s17\n"
    "buffer_load_dwordx4 v6, s[48:51], s24 offen offset:0 lds\n"
    "buffer_load_dwordx4 v7, s[48:51], s24 offen offset:1024 lds\n"
    "buffer_load_dwordx4 v6, s[48:51], s24 offen offset:2048 lds\n"
    "buffer_load_dwordx4 v7, s[48:51], s24 offen offset:3072 lds\n"
    "s_add_u32 m0, s25, 24576\n"
    "s_add_u32 s23, s23, s26\n"
    "buffer_load_dwordx4 v5, s[4:7], s23 offen nt lds\n"
    ".Ldin_p2:\n"
    "s_lshl_b32 s67, s67, 6\n"
    "s_lshl_b32 s68, s20, 13\n"
    "s_lshl_b32 s23, s20, 8\n"
    "s_add_u32 s68, s68, 89088\n"
    "s_add_u32 s23, s23, 86912\n"
    "v_and_b32 v32, 0x70, v5\n"
    "v_lshrrev_b32 v4, 1, v3\n"
    "v_or_b32 v237, 0x400, v32\n"
    "v_xor_b32 v239, 64, v32\n"
    "v_and_b32 v32, 0x70, v4\n"
    "v_lshlrev_b32 v33, 6, v50\n"
    "v_bitop3_b32 v36, v32, v2, v33 bitop3:0x96\n"
    "v_and_b32 v32, 8, v0\n"
    "v_add_u32 v234, s68, v36\n"
    "v_mul_u32_u24 v33, 0x88, v32\n"
    "v_xor_b32 v235, 64, v234\n"
    "v_lshrrev_b32 v245, 1, v32\n"
    "v_lshlrev_b32 v32, 1, v2\n"
    "v_and_b32 v34, 0x18, v4\n"
    "v_bitop3_b32 v33, v33, v32, v34 bitop3:0x96\n"
    "v_or3_b32 v245, v245, v32, s23\n"
    "v_and_b32 v32, 0x40, v251\n"
    "v_lshl_or_b32 v246, v34, 2, s23\n"
    "v_lshl_or_b32 v33, v32, 1, v33\n"
    "v_and_b32 v32, 24, v50\n"
    "v_add_u32 v240, s68, v33\n"
    "v_or_b32 v246, v246, v32\n"
    "v_xor_b32 v241, 0x110, v240\n"
    "v_xor_b32 v242, 0x220, v240\n"
    "v_xor_b32 v243, 0x330, v240\n"
    "v_and_b32 v32, 12, v50\n"
    "v_and_b32 v33, 2, v50\n"
    "v_lshl_or_b32 v32, v32, 1, v33\n"
    "v_bfe_u32 v244, v0, 3, 3\n"
    "v_or_b32 v245, v245, v32\n"
    "s_mov_b32 s24, 49664\n"
    "v_lshl_add_u32 v244, v244, 1, s24\n"
    "v_or_b32 v250, 0x6000, v2\n"
    "s_mov_b64 s[26:27], 0xffff\n"
    "v_mov_b64 v[248:249], 0\n"
    "s_mov_b32 exec_lo, 0x00f0000f\n"
    "s_mov_b32 exec_hi, 0xf0000f00\n"
    "v_lshlrev_b64 v[248:249], v251, s[26:27]\n"
    "s_mov_b64 exec, -1\n"
    "v_add_u32 v13, 1, v4\n"
    "v_mov_b64 v[14:15], -1\n"
    "v_lshl_add_u32 v8, v13, 16, v4\n"
    "v_add_u32 v9, 0x20002, v8\n"
    "v_add_u32 v10, 0x40004, v8\n"
    "v_add_u32 v11, 0x60006, v8\n"
    "v_mov_b32 v17, v237\n"
    "v_mov_b32 v19, v239\n"
    "v_mov_b32 v21, v237\n"
    "v_mov_b32 v23, v239\n"
    "v_mov_b32 v25, v237\n"
    "v_mov_b32 v27, v239\n"
    "v_mov_b32 v29, v237\n"
    "v_mov_b32 v31, v239\n"
    "v_mov_b32 v32, -1\n"
    "v_mov_b32 v33, 0\n"
    "s_mov_b64 exec, 0xff00\n"
    "v_mov_b32 v32, 0x8c000000\n"
    "s_mov_b64 exec, 0xfffc\n"
    "ds_write_b64 v246, v[32:33] offset:128\n"
    "s_mov_b64 exec, 0xfff\n"
    "ds_write_b64 v4, v[14:15] offset:49664\n"
    "ds_write_b32 v33, v33 offset:49416\n"
    "s_mov_b64 exec, -1\n"
    "s_mov_b32 s28, 0xc080c080\n"
    "s_mov_b32 s29, 0xffff0001\n"
    "s_mov_b32 s30, 0x01000100\n"
    "s_mov_b32 s31, 0x00010001\n"
    "s_add_u32 s40, s40, 0xfffffc00\n"
    "s_addc_u32 s41, s41, 0x7fffff\n"
    "s_lshl_b32 s56, s14, 6\n"
    "s_lshl_b32 s57, s15, 6\n"
    "s_lshr_b32 s79, s17, 7\n"
    "s_mul_i32 s58, s78, s56\n"
    "s_mov_b32 s59, 1\n"
    "s_add_u32 s58, s58, s57\n"
    "s_mul_i32 s62, s20, 2304\n"
    "s_mov_b32 s70, 0x47000000\n"
    "s_mov_b32 s71, 0x47000000\n"
    "s_add_u32 s62, s62, 18016\n"
    "s_and_b32 s77, s18, 52\n"
    "s_and_b32 s73, s18, 15\n"
    "s_mov_b32 s72, s77\n"
    "s_lshr_b32 s74, s6, 7\n"
    "s_cbranch_vccnz .Ldin_py\n"
    "v_lshrrev_b32 v32, 5, v1\n"
    "s_mul_i32 s24, s20, 272\n"
    "v_mul_u32_u24 v12, 2176, v32\n"
    "v_and_b32 v60, 32, v0\n"
    "v_add3_u32 v12, v12, s24, 15\n"
    "v_add_u32 v60, s23, v60\n"
    "v_add_u32 v13, v13, v12\n"
    "v_mov_b32 v57, 0\n"
    "v_mov_b32_e64 v59, 0\n"
    "ds_write_b64 v13, v[14:15] offset:49760\n"
    "ds_write_b64 v13, v[14:15] offset:50848\n"
    "s_mul_i32 s23, s20, 0x80110\n"
    "s_lshr_b32 s24, s20, 1\n"
    "s_lshl_b32 s24, s24, 16\n"
    "s_add_u32 s23, s23, s24\n"
    "s_sub_u32 s24, s74, s22\n"
    "s_sub_u32 s25, s24, s79\n"
    "s_add_u32 s26, s23, 0x70\n"
    "s_cmp_gt_i32 s24, 0\n"
    "s_cselect_b32 s26, s26, 0x80000000\n"
    "v_writelane_b32 v56, s26, 31\n"
    "s_add_u32 s26, s23, 0x4008f0\n"
    "s_cmp_gt_i32 s25, 0\n"
    "s_cselect_b32 s26, s26, 0x80000000\n"
    "v_writelane_b32 v56, s26, 63\n"
    "s_add_u32 s26, s23, 0x2204b0\n"
    "s_cmp_gt_i32 s24, 4\n"
    "s_cselect_b32 s26, s26, 0x80000000\n"
    "v_writelane_b32 v58, s26, 31\n"
    "s_add_u32 s26, s23, 0x620d30\n"
    "s_cmp_gt_i32 s25, 4\n"
    "s_cselect_b32 s26, s26, 0x80000000\n"
    "v_writelane_b32 v58, s26, 63\n"
    "s_cmp_lg_u32 s21, 0\n"
    "s_cbranch_scc1 .Ldin_tw0\n"
    "s_call_b64 s[34:35], .Ldin_stub_A0_poll\n"
    "s_call_b64 s[34:35], .Ldin_stub_L2\n"
    "s_branch .Ldin_twe\n"
    ".Ldin_tw0:\n"
    "s_cmp_lg_u32 s21, 1\n"
    "s_cbranch_scc1 .Ldin_tw1\n"
    "s_call_b64 s[34:35], .Ldin_stub_L1\n"
    "s_call_b64 s[34:35], .Ldin_stub_L0\n"
    "s_branch .Ldin_twe\n"
    ".Ldin_tw1:\n"
    "s_cmp_lg_u32 s21, 2\n"
    "s_cbranch_scc1 .Ldin_tw2\n"
    "s_call_b64 s[34:35], .Ldin_stub_L3\n"
    "s_call_b64 s[34:35], .Ldin_stub_E0\n"
    "s_branch .Ldin_twe\n"
    ".Ldin_tw2:\n"
    "s_call_b64 s[34:35], .Ldin_stub_E1\n"
    "s_call_b64 s[34:35], .Ldin_stub_E1\n"
    ".Ldin_twe:\n"
    "s_and_b32 s23, s14, 14\n"
    "s_waitcnt vmcnt(5)\n"
    "s_cbranch_scc1 .Ldin_trimo\n"
    ".Ldin_trimo_r:\n"
    "v_pk_min_u16 v48, v40, 1 op_sel_hi:[1,0]\n"
    "v_pk_min_u16 v49, v41, 1 op_sel_hi:[1,0]\n"
    "v_pk_min_u16 v50, v42, 1 op_sel_hi:[1,0]\n"
    "v_pk_min_u16 v51, v43, 1 op_sel_hi:[1,0]\n"
    "v_add3_u32 v64, v48, v49, v50\n"
    "v_add_u32_e64 v64, v64, v51\n"
    "v_dot2_u32_u16 v62, v64, s31, 0\n"
    "v_pk_min_u16 v52, v44, 1 op_sel_hi:[1,0]\n"
    "v_pk_min_u16 v53, v45, 1 op_sel_hi:[1,0]\n"
    "v_pk_min_u16 v54, v46, 1 op_sel_hi:[1,0]\n"
    "v_pk_min_u16 v55, v47, 1 op_sel_hi:[1,0]\n"
    "v_add3_u32 v64, v52, v53, v54\n"
    "v_add_u32_e64 v64, v64, v55\n"
    "v_dot2_u32_u16 v62, v64, s30, v62\n"
    "v_pk_mad_u16 v40, v48, s28, v40\n"
    "v_pk_mad_u16 v41, v49, s28, v41\n"
    "v_pk_mad_u16 v42, v50, s28, v42\n"
    "v_add_u32_dpp v63, v62, v62 row_shr:1 row_mask:0xf bank_mask:0xf bound_ctrl:1\n"
    "v_sub_u32 v64, 0x10101, v62\n"
    "v_pk_mad_u16 v43, v51, s28, v43\n"
    "v_add_u32_dpp v63, v63, v63 row_shr:2 row_mask:0xf bank_mask:0xf bound_ctrl:1\n"
    "v_or3_b32 v65, v40, v41, 0\n"
    "v_or3_b32 v65, v42, v43, v65\n"
    "v_add_u32_dpp v63, v63, v63 row_shr:4 row_mask:0xf bank_mask:0xf bound_ctrl:1\n"
    "v_pk_mad_u16 v44, v52, s28, v44\n"
    "v_pk_mad_u16 v45, v53, s28, v45\n"
    "v_add_u32_dpp v63, v63, v63 row_shr:8 row_mask:0xf bank_mask:0xf bound_ctrl:1\n"
    "v_pk_mad_u16 v46, v54, s28, v46\n"
    "v_pk_mad_u16 v47, v55, s28, v47\n"
    "v_add_u32_dpp v63, v63, v63 row_bcast:15 row_mask:0xa bank_mask:0xf\n"
    "v_or3_b32 v65, v44, v45, v65\n"
    "v_or3_b32 v65, v46, v47, v65\n"
    "v_add_u32_e64 v64, v64, v63\n"
    "s_mov_b32 s26, 0xc040c00\n"
    "v_perm_b32 v66, v63, v64, s26\n"
    "v_mad_u32_u16 v68, v48, v66, v12\n"
    "v_mad_u32_u16 v69, v48, v66, v12 op_sel:[1,1,0,0]\n"
    "v_pk_mad_u16 v66, v48, s29, v66\n"
    "ds_write_b8 v68, v8 offset:49760\n"
    "ds_write_b8_d16_hi v69, v8 offset:49760\n"
    "v_mad_u32_u16 v70, v49, v66, v12\n"
    "v_mad_u32_u16 v71, v49, v66, v12 op_sel:[1,1,0,0]\n"
    "v_pk_mad_u16 v66, v49, s29, v66\n"
    "ds_write_b8 v70, v9 offset:49760\n"
    "ds_write_b8_d16_hi v71, v9 offset:49760\n"
    "v_mad_u32_u16 v68, v50, v66, v12\n"
    "v_mad_u32_u16 v69, v50, v66, v12 op_sel:[1,1,0,0]\n"
    "v_pk_mad_u16 v66, v50, s29, v66\n"
    "ds_write_b8 v68, v10 offset:49760\n"
    "ds_write_b8_d16_hi v69, v10 offset:49760\n"
    "v_mad_u32_u16 v70, v51, v66, v12\n"
    "v_mad_u32_u16 v71, v51, v66, v12 op_sel:[1,1,0,0]\n"
    "ds_write_b8 v70, v11 offset:49760\n"
    "ds_write_b8_d16_hi v71, v11 offset:49760\n"
    "s_waitcnt vmcnt(0)\n"
    "s_barrier\n"
    "s_mul_i32 s23, s20, 272\n"
    "v_add_u32_e64 v66, s23, v244\n"
    "ds_read_u8 v16, v66 offset:112\n"
    "ds_read_u8 v18, v66 offset:113\n"
    "ds_read_u8 v20, v66 offset:128\n"
    "ds_read_u8 v22, v66 offset:129\n"
    "ds_read_u8 v24, v66 offset:144\n"
    "ds_read_u8 v26, v66 offset:145\n"
    "ds_read_u8 v28, v66 offset:160\n"
    "ds_read_u8 v30, v66 offset:161\n"
    "v_cmp_ne_u32 s[26:27], 0, v65\n"
    "s_mov_b32 s24, 0x0c010c00\n"
    "v_perm_b32 v62, v63, v63, s24\n"
    "s_cmp_lg_u64 s[26:27], 0\n"
    "v_pk_add_u16 v62, v62, 15 op_sel_hi:[1,0]\n"
    "s_cselect_b32 s24, 0x200020, 0\n"
    "v_pk_lshrrev_b16 v62, 4, v62 op_sel_hi:[0,1]\n"
    "v_pk_max_u16 v62, v62, 6 op_sel_hi:[1,0]\n"
    "v_or_b32 v62, s24, v62\n"
    "v_lshl_or_b32 v56, v62, 25, v56\n"
    "v_lshrrev_b32 v62, 16, v62\n"
    "s_add_u32 m0, s68, 0\n"
    "s_waitcnt lgkmcnt(6)\n"
    "buffer_load_dwordx4 v[16:17], s[40:43], s67 idxen offen offset:0 nt lds\n"
    "buffer_load_dwordx4 v[18:19], s[40:43], s67 idxen offen offset:1024 nt lds\n"
    "s_add_u32 m0, s68, 2048\n"
    "s_waitcnt lgkmcnt(4)\n"
    "buffer_load_dwordx4 v[20:21], s[40:43], s67 idxen offen offset:0 nt lds\n"
    "buffer_load_dwordx4 v[22:23], s[40:43], s67 idxen offen offset:1024 nt lds\n"
    "s_add_u32 m0, s68, 4096\n"
    "s_waitcnt lgkmcnt(2)\n"
    "buffer_load_dwordx4 v[24:25], s[40:43], s67 idxen offen offset:0 nt lds\n"
    "buffer_load_dwordx4 v[26:27], s[40:43], s67 idxen offen offset:1024 nt lds\n"
    "s_add_u32 m0, s68, 6144\n"
    "s_waitcnt lgkmcnt(0)\n"
    "buffer_load_dwordx4 v[28:29], s[40:43], s67 idxen offen offset:0 nt lds\n"
    "buffer_load_dwordx4 v[30:31], s[40:43], s67 idxen offen offset:1024 nt lds\n"
    "v_lshl_or_b32 v58, v62, 25, v58\n"
    "v_readlane_b32 s60, v56, 31\n"
    "s_mov_b32 s26, 0xc050c01\n"
    "v_perm_b32 v66, v63, v64, s26\n"
    "v_mad_u32_u16 v68, v52, v66, v12\n"
    "v_mad_u32_u16 v69, v52, v66, v12 op_sel:[1,1,0,0]\n"
    "v_pk_mad_u16 v66, v52, s29, v66\n"
    "ds_write_b8 v68, v8 offset:50848\n"
    "ds_write_b8_d16_hi v69, v8 offset:50848\n"
    "v_mad_u32_u16 v70, v53, v66, v12\n"
    "v_mad_u32_u16 v71, v53, v66, v12 op_sel:[1,1,0,0]\n"
    "v_pk_mad_u16 v66, v53, s29, v66\n"
    "ds_write_b8 v70, v9 offset:50848\n"
    "ds_write_b8_d16_hi v71, v9 offset:50848\n"
    "v_mad_u32_u16 v68, v54, v66, v12\n"
    "v_mad_u32_u16 v69, v54, v66, v12 op_sel:[1,1,0,0]\n"
    "v_pk_mad_u16 v66, v54, s29, v66\n"
    "ds_write_b8 v68, v10 offset:50848\n"
    "ds_write_b8_d16_hi v69, v10 offset:50848\n"
    "v_mad_u32_u16 v70, v55, v66, v12\n"
    "v_mad_u32_u16 v71, v55, v66, v12 op_sel:[1,1,0,0]\n"
    "ds_write_b8 v70, v11 offset:50848\n"
    "ds_write_b8_d16_hi v71, v11 offset:50848\n"
    "s_brev_b32 exec_lo, 1\n"
    "s_brev_b32 exec_hi, 1\n"
    "ds_write_b64 v60, v[56:57] offset:128\n"
    "ds_write_b64 v60, v[58:59] offset:1152\n"
    "s_mov_b64 exec, -1\n"
    "s_waitcnt lgkmcnt(0)\n"
    "s_barrier\n"
    ".Ldin_go:\n"
    "s_or_b32 s45, s45, 0x20000\n"
    "s_mov_b32 s46, s42\n"
    "s_cmp_lt_i32 s60, 0\n"
    "s_cbranch_scc1 .Ldin_idle\n"
    "s_lshr_b32 s94, s60, 12\n"
    "s_and_b32 s94, s94, 0x1ff0\n"
    "v_xor_b32 v252, s94, v250\n"
    "ds_read_b128 v[216:219], v252 offset:0\n"
    "ds_read_b128 v[220:223], v252 offset:64\n"
    "ds_read_b128 v[96:99], v251 offset:16384\n"
    "ds_read_b128 v[32:35], v251 offset:0\n"
    "ds_read_b128 v[36:39], v251 offset:1024\n"
    "ds_read_b128 v[100:103], v251 offset:17408\n"
    "ds_read_b128 v[40:43], v251 offset:2048\n"
    "ds_read_b128 v[44:47], v251 offset:3072\n"
    "ds_read_b128 v[104:107], v251 offset:18432\n"
    "ds_read_b128 v[48:51], v251 offset:4096\n"
    "ds_read_b128 v[52:55], v251 offset:5120\n"
    "ds_read_b128 v[108:111], v251 offset:19456\n"
    "ds_read_b128 v[56:59], v251 offset:6144\n"
    "ds_read_b128 v[60:63], v251 offset:7168\n"
    "s_and_b32 s66, s60, 0xffff\n"
    "s_bfe_u32 s95, s60, 0x50019\n"
    "s_mov_b32 s81, s66\n"
    "s_lshr_b32 s63, s67, 6\n"
    "s_lshl4_add_u32 s80, s95, s66\n"
    "v_add_u32 v230, s66, v244\n"
    "s_add_u32 s66, s66, 80\n"
    "s_add_u32 m0, s68, 0\n"
    "s_cmp_lg_u32 s75, 0\n"
    "s_cselect_b64 vcc, -1, 0\n"
    "s_waitcnt lgkmcnt(12)\n"
    "v_and_b32 v216, v248, v216\n"
    "v_and_b32 v217, v249, v217\n"
    "v_and_b32 v218, v248, v218\n"
    "v_and_b32 v219, v249, v219\n"
    "s_waitcnt lgkmcnt(9)\n"
    "ds_read_b128 v[112:115], v251 offset:20480\n"
    "ds_read_b128 v[64:67], v251 offset:8192\n"
    "ds_read_b128 v[68:71], v251 offset:9216\n"
    "v_mfma_f32_16x16x16_bf16 v[176:179], v[216:217], v[96:97], v[32:35]\n"
    "v_and_b32 v220, v248, v220\n"
    "v_and_b32 v221, v249, v221\n"
    "v_and_b32 v222, v248, v222\n"
    "v_mfma_f32_16x16x16_bf16 v[180:183], v[218:219], v[98:99], v[36:39]\n"
    "v_and_b32 v223, v249, v223\n"
    "s_waitcnt lgkmcnt(9)\n"
    "ds_read_b128 v[116:119], v251 offset:21504\n"
    "ds_read_b128 v[72:75], v251 offset:10240\n"
    "ds_read_b128 v[76:79], v251 offset:11264\n"
    "v_mfma_f32_16x16x16_bf16 v[184:187], v[220:221], v[100:101], v[40:43]\n"
    "v_cvt_pk_bf16_f32 v0, v176, v177\n"
    "v_cvt_pk_bf16_f32 v1, v178, v179\n"
    "v_cvt_pk_bf16_f32 v2, v180, v181\n"
    "v_mfma_f32_16x16x16_bf16 v[188:191], v[222:223], v[102:103], v[44:47]\n"
    "v_cvt_pk_bf16_f32 v3, v182, v183\n"
    "s_waitcnt lgkmcnt(9)\n"
    "s_nop 0\n"
    "ds_read_b128 v[120:123], v251 offset:22528\n"
    "ds_read_b128 v[80:83], v251 offset:12288\n"
    "ds_read_b128 v[84:87], v251 offset:13312\n"
    "v_mfma_f32_16x16x16_bf16 v[176:179], v[216:217], v[104:105], v[48:51]\n"
    "v_cvt_pk_bf16_f32 v4, v184, v185\n"
    "v_cvt_pk_bf16_f32 v5, v186, v187\n"
    "v_cvt_pk_bf16_f32 v6, v188, v189\n"
    "v_mfma_f32_16x16x16_bf16 v[180:183], v[218:219], v[106:107], v[52:55]\n"
    "v_cvt_pk_bf16_f32 v7, v190, v191\n"
    "s_waitcnt lgkmcnt(9)\n"
    "s_nop 0\n"
    "ds_read_b128 v[124:127], v251 offset:23552\n"
    "ds_read_b128 v[88:91], v251 offset:14336\n"
    "ds_read_b128 v[92:95], v251 offset:15360\n"
    "v_mfma_f32_16x16x16_bf16 v[184:187], v[220:221], v[108:109], v[56:59]\n"
    "v_cvt_pk_bf16_f32 v8, v176, v177\n"
    "v_cvt_pk_bf16_f32 v9, v178, v179\n"
    "v_cvt_pk_bf16_f32 v10, v180, v181\n"
    "v_mfma_f32_16x16x16_bf16 v[188:191], v[222:223], v[110:111], v[60:63]\n"
    "v_cvt_pk_bf16_f32 v11, v182, v183\n"
    "s_waitcnt lgkmcnt(9)\n"
    "s_nop 0\n"
    "ds_read_b128 v[144:147], v252 offset:8192\n"
    "ds_read_b128 v[148:151], v252 offset:8256\n"
    "ds_read_b128 v[152:155], v252 offset:16384\n"
    "ds_read_b128 v[156:159], v252 offset:16448\n"
    "ds_read_u8 v236, v230 offset:64\n"
    "ds_read_u8 v238, v230 offset:65\n"
    "v_mfma_f32_16x16x16_bf16 v[176:179], v[216:217], v[112:113], v[64:67]\n"
    "v_cvt_pk_bf16_f32 v12, v184, v185\n"
    "v_cvt_pk_bf16_f32 v13, v186, v187\n"
    "v_cvt_pk_bf16_f32 v14, v188, v189\n"
    "v_mfma_f32_16x16x16_bf16 v[180:183], v[218:219], v[114:115], v[68:71]\n"
    "v_cvt_pk_bf16_f32 v15, v190, v191\n"
    "s_waitcnt lgkmcnt(12)\n"
    "s_waitcnt vmcnt(4)\n"
    "ds_read_b128 v[192:195], v234 offset:0\n"
    "ds_read_b128 v[196:199], v235 offset:0\n"
    "ds_read_b64_tr_b16 v[200:201], v240 offset:0\n"
    "ds_read_b64_tr_b16 v[202:203], v241 offset:0\n"
    "ds_read_b64_tr_b16 v[204:205], v242 offset:0\n"
    "ds_read_b64_tr_b16 v[206:207], v243 offset:0\n"
    "v_mfma_f32_16x16x16_bf16 v[184:187], v[220:221], v[116:117], v[72:75]\n"
    "v_cvt_pk_bf16_f32 v16, v176, v177\n"
    "v_cvt_pk_bf16_f32 v17, v178, v179\n"
    "v_cvt_pk_bf16_f32 v18, v180, v181\n"
    "v_mfma_f32_16x16x16_bf16 v[188:191], v[222:223], v[118:119], v[76:79]\n"
    "v_cvt_pk_bf16_f32 v19, v182, v183\n"
    "s_waitcnt lgkmcnt(15)\n"
    "s_nop 0\n"
    "ds_read_b128 v[128:131], v250 offset:24576\n"
    "ds_read_b128 v[132:135], v250 offset:24640\n"
    "ds_read_b128 v[136:139], v250 offset:24704\n"
    "ds_read_b128 v[140:143], v250 offset:24768\n"
    "ds_read_b64 v[232:233], v250 offset:24832\n"
    "v_mfma_f32_16x16x16_bf16 v[176:179], v[216:217], v[120:121], v[80:83]\n"
    "v_cvt_pk_bf16_f32 v20, v184, v185\n"
    "v_cvt_pk_bf16_f32 v21, v186, v187\n"
    "v_cvt_pk_bf16_f32 v22, v188, v189\n"
    "v_mfma_f32_16x16x16_bf16 v[180:183], v[218:219], v[122:123], v[84:87]\n"
    "v_cvt_pk_bf16_f32 v23, v190, v191\n"
    "s_waitcnt lgkmcnt(15)\n"
    "v_mfma_f32_16x16x16_bf16 v[184:187], v[220:221], v[124:125], v[88:91]\n"
    "v_mfma_f32_16x16x16_bf16 v[188:191], v[222:223], v[126:127], v[92:95]\n"
    "v_cvt_pk_bf16_f32 v24, v176, v177\n"
    "v_cvt_pk_bf16_f32 v25, v178, v179\n"
    "s_waitcnt lgkmcnt(5)\n"
    "buffer_load_dwordx4 v[236:237], s[40:43], s67 idxen offen offset:0 nt lds\n"
    "buffer_load_dwordx4 v[238:239], s[40:43], s67 idxen offen offset:1024 nt lds\n"
    "v_mfma_f32_16x16x32_bf16 v[160:163], v[0:3], v[192:195], v[144:147]\n"
    "v_cvt_pk_bf16_f32 v26, v180, v181\n"
    "v_cvt_pk_bf16_f32 v27, v182, v183\n"
    "v_mfma_f32_16x16x32_bf16 v[160:163], v[4:7], v[196:199], v[160:163]\n"
    "v_cvt_pk_bf16_f32 v28, v184, v185\n"
    "v_cvt_pk_bf16_f32 v29, v186, v187\n"
    "v_mfma_f32_16x16x32_bf16 v[164:167], v[8:11], v[192:195], v[148:151]\n"
    "v_cvt_pk_bf16_f32 v30, v188, v189\n"
    "v_cvt_pk_bf16_f32 v31, v190, v191\n"
    "v_mfma_f32_16x16x32_bf16 v[164:167], v[12:15], v[196:199], v[164:167]\n"
    "v_mfma_f32_16x16x32_bf16 v[168:171], v[16:19], v[192:195], v[152:155]\n"
    "v_mfma_f32_16x16x32_bf16 v[168:171], v[20:23], v[196:199], v[168:171]\n"
    "v_mfma_f32_16x16x32_bf16 v[172:175], v[24:27], v[192:195], v[156:159]\n"
    "v_mfma_f32_16x16x32_bf16 v[172:175], v[28:31], v[196:199], v[172:175]\n"
    "ds_read_b128 v[192:195], v234 offset:2048\n"
    "ds_read_b128 v[196:199], v235 offset:2048\n"
    "s_waitcnt lgkmcnt(0)\n"
    "v_mov_b32 v247, v232\n"
    "v_readfirstlane_b32 s69, v233\n"
    "s_bitcmp1_b32 s60, 30\n"
    "s_nop 0\n"
    "s_cbranch_scc1 .Ldin_A0s\n"
    ".Ldin_A0:\n"
    "s_cbranch_vccz .Ldin_A0_np\n"
    "s_setprio 1\n"
    ".Ldin_A0_np:\n"
    "v_mfma_f32_16x16x32_bf16 v[176:179], v[0:3], v[192:195], v[144:147]\n"
    "v_exp_f32 v160, v160 clamp\n"
    "v_mfma_f32_16x16x32_bf16 v[176:179], v[4:7], v[196:199], v[176:179]\n"
    "v_exp_f32 v161, v161 clamp\n"
    "v_mfma_f32_16x16x32_bf16 v[180:183], v[8:11], v[192:195], v[148:151]\n"
    "v_exp_f32 v162, v162 clamp\n"
    "v_mfma_f32_16x16x32_bf16 v[180:183], v[12:15], v[196:199], v[180:183]\n"
    "v_exp_f32 v163, v163 clamp\n"
    "v_mfma_f32_16x16x32_bf16 v[184:187], v[16:19], v[192:195], v[152:155]\n"
    "v_exp_f32 v164, v164 clamp\n"
    "v_mfma_f32_16x16x32_bf16 v[184:187], v[20:23], v[196:199], v[184:187]\n"
    "v_exp_f32 v165, v165 clamp\n"
    "v_mfma_f32_16x16x32_bf16 v[188:191], v[24:27], v[192:195], v[156:159]\n"
    "v_exp_f32 v166, v166 clamp\n"
    "v_mfma_f32_16x16x32_bf16 v[188:191], v[28:31], v[196:199], v[188:191]\n"
    "v_exp_f32 v167, v167 clamp\n"
    "v_exp_f32 v168, v168 clamp\n"
    "s_mov_b32 m0, s62\n"
    "ds_read_b64 v[216:217], v246 offset:128\n"
    "v_exp_f32 v169, v169 clamp\n"
    "v_add_u32_e64 v251, s66, v244\n"
    "s_waitcnt lgkmcnt(1)\n"
    "v_mfma_f32_4x4x4_16b_bf16 v[224:227], v[228:229], v[208:209], v[224:227] cbsz:4 abid:0\n"
    "v_exp_f32 v170, v170 clamp\n"
    "v_exp_f32 v171, v171 clamp\n"
    "v_mfma_f32_4x4x4_16b_bf16 v[224:227], v[228:229], v[210:211], v[224:227] cbsz:4 abid:1\n"
    "v_exp_f32 v172, v172 clamp\n"
    "v_exp_f32 v173, v173 clamp\n"
    "v_mfma_f32_4x4x4_16b_bf16 v[224:227], v[228:229], v[212:213], v[224:227] cbsz:4 abid:2\n"
    "v_exp_f32 v174, v174 clamp\n"
    "v_exp_f32 v175, v175 clamp\n"
    "v_mfma_f32_4x4x4_16b_bf16 v[224:227], v[228:229], v[214:215], v[224:227] cbsz:4 abid:3\n"
    "s_waitcnt vmcnt(4)\n"
    "s_waitcnt lgkmcnt(0)\n"
    "v_readlane_b32 s60, v216, s59\n"
    "v_readlane_b32 s61, v217, s59\n"
    "ds_read_b64_tr_b16 v[208:209], v240 offset:2048\n"
    "ds_read_b64_tr_b16 v[210:211], v241 offset:2048\n"
    "ds_read_b64_tr_b16 v[212:213], v242 offset:2048\n"
    "ds_read_b64_tr_b16 v[214:215], v243 offset:2048\n"
    "ds_read_u8 v236, v251 offset:0\n"
    "ds_read_u8 v238, v251 offset:1\n"
    "ds_read_b128 v[192:195], v234 offset:4096\n"
    "ds_read_b128 v[196:199], v235 offset:4096\n"
    "s_cmp_eq_u32 s60, -1\n"
    "s_cbranch_scc1 .Ldin_A0_poll\n"
    ".Ldin_A0_pr:\n"
    "s_lshr_b32 s94, s60, 12\n"
    "s_and_b32 s94, s94, 0x1ff0\n"
    "v_xor_b32 v252, s94, v250\n"
    "v_pk_add_f32 v[230:231], v[224:225], v[226:227]\n"
    "v_add_f32 v230, v230, v231\n"
    "ds_write_addtid_b32 v230 offset:49152\n"
    "ds_read_b128 v[216:219], v252 offset:0\n"
    "ds_read_b128 v[220:223], v252 offset:64\n"
    "v_mov_b64 v[224:225], 0\n"
    "v_mov_b64_e64 v[226:227], 0\n"
    "v_pk_fma_f32 v[160:161], v[160:161], s[70:71], v[128:129]\n"
    "v_pk_fma_f32 v[162:163], v[162:163], s[70:71], v[130:131]\n"
    "v_pk_add_f32 v[230:231], v[160:161], v[162:163]\n"
    "v_pk_mul_f32 v[232:233], v[160:161], v[162:163]\n"
    "v_pk_fma_f32 v[164:165], v[164:165], s[70:71], v[132:133]\n"
    "v_pk_fma_f32 v[230:231], v[230:231], v[164:165], v[232:233]\n"
    "v_pk_mul_f32 v[232:233], v[232:233], v[164:165]\n"
    "v_pk_fma_f32 v[166:167], v[166:167], s[70:71], v[134:135]\n"
    "v_pk_fma_f32 v[230:231], v[230:231], v[166:167], v[232:233]\n"
    "v_pk_mul_f32 v[232:233], v[232:233], v[166:167]\n"
    "v_pk_fma_f32 v[168:169], v[168:169], s[70:71], v[136:137]\n"
    "v_pk_fma_f32 v[230:231], v[230:231], v[168:169], v[232:233]\n"
    "v_pk_mul_f32 v[232:233], v[232:233], v[168:169]\n"
    "v_pk_fma_f32 v[170:171], v[170:171], s[70:71], v[138:139]\n"
    "v_pk_fma_f32 v[230:231], v[230:231], v[170:171], v[232:233]\n"
    "v_pk_mul_f32 v[232:233], v[232:233], v[170:171]\n"
    "v_pk_fma_f32 v[172:173], v[172:173], s[70:71], v[140:141]\n"
    "v_pk_fma_f32 v[230:231], v[230:231], v[172:173], v[232:233]\n"
    "v_pk_mul_f32 v[232:233], v[232:233], v[172:173]\n"
    "v_pk_fma_f32 v[174:175], v[174:175], s[70:71], v[142:143]\n"
    "v_pk_fma_f32 v[230:231], v[230:231], v[174:175], v[232:233]\n"
    "v_pk_mul_f32 v[232:233], v[232:233], v[174:175]\n"
    "s_add_u32 m0, s68, 2048\n"
    "s_and_b32 s64, s60, 0xffff\n"
    "s_bfe_u32 s94, s60, 0x30013\n"
    "s_bfe_u32 s95, s60, 0x50019\n"
    "s_mul_i32 s94, s94, s56\n"
    "s_lshl4_add_u32 s83, s95, s64\n"
    "s_add_u32 s65, s58, s94\n"
    "s_add_u32 s82, s64, 64\n"
    "s_add_u32 s58, s58, s57\n"
    "s_add_u32 s59, s59, 1\n"
    "s_sub_u32 s95, s61, s76\n"
    "s_lshr_b32 s94, s61, 16\n"
    "s_sext_i32_i16 s95, s95\n"
    "s_mul_i32 s95, s95, s94\n"
    "s_ashr_i32 s95, s95, 16\n"
    "s_max_i32 s95, s95, -8\n"
    "s_min_i32 s95, s95, 4\n"
    "s_add_u32 s72, s77, s95\n"
    "s_cmp_lt_i32 s60, 0\n"
    "s_cselect_b32 s72, 0, s72\n"
    "v_rcp_f32 v232, v232\n"
    "v_rcp_f32 v233, v233\n"
    "v_fma_f32 v230, v230, v232, v247\n"
    "v_fmac_f32 v230, v231, v233\n"
    "v_cvt_pk_bf16_f32 v230, v230, v230\n"
    "s_add_u32 s66, s66, 16\n"
    "s_waitcnt lgkmcnt(0)\n"
    "ds_write_b16 v245, v230\n"
    "ds_read_b64 v[228:229], v246\n"
    "buffer_load_dwordx4 v[236:237], s[40:43], s67 idxen offen offset:0 nt lds\n"
    "buffer_load_dwordx4 v[238:239], s[40:43], s67 idxen offen offset:1024 nt lds\n"
    ".Ldin_G1:\n"
    "s_cbranch_vccz .Ldin_G1_np\n"
    "s_setprio 1\n"
    ".Ldin_G1_np:\n"
    "v_mfma_f32_16x16x32_bf16 v[160:163], v[0:3], v[192:195], v[144:147]\n"
    "v_exp_f32 v176, v176 clamp\n"
    "v_mfma_f32_16x16x32_bf16 v[160:163], v[4:7], v[196:199], v[160:163]\n"
    "v_exp_f32 v177, v177 clamp\n"
    "v_mfma_f32_16x16x32_bf16 v[164:167], v[8:11], v[192:195], v[148:151]\n"
    "v_exp_f32 v178, v178 clamp\n"
    "v_mfma_f32_16x16x32_bf16 v[164:167], v[12:15], v[196:199], v[164:167]\n"
    "v_exp_f32 v179, v179 clamp\n"
    "v_mfma_f32_16x16x32_bf16 v[168:171], v[16:19], v[192:195], v[152:155]\n"
    "v_exp_f32 v180, v180 clamp\n"
    "v_mfma_f32_16x16x32_bf16 v[168:171], v[20:23], v[196:199], v[168:171]\n"
    "v_exp_f32 v181, v181 clamp\n"
    "v_mfma_f32_16x16x32_bf16 v[172:175], v[24:27], v[192:195], v[156:159]\n"
    "v_exp_f32 v182, v182 clamp\n"
    "v_mfma_f32_16x16x32_bf16 v[172:175], v[28:31], v[196:199], v[172:175]\n"
    "v_exp_f32 v183, v183 clamp\n"
    "v_exp_f32 v184, v184 clamp\n"
    "s_cmp_eq_u32 s66, s80\n"
    "s_cselect_b64 s[66:67], s[64:65], s[66:67]\n"
    "s_add_u32 m0, s68, 4096\n"
    "v_exp_f32 v185, v185 clamp\n"
    "v_add_u32 v251, s66, v244\n"
    "s_waitcnt lgkmcnt(0)\n"
    "v_mfma_f32_4x4x4_16b_bf16 v[224:227], v[228:229], v[200:201], v[224:227] cbsz:4 abid:0\n"
    "v_exp_f32 v186, v186 clamp\n"
    "v_exp_f32 v187, v187 clamp\n"
    "v_mfma_f32_4x4x4_16b_bf16 v[224:227], v[228:229], v[202:203], v[224:227] cbsz:4 abid:1\n"
    "v_exp_f32 v188, v188 clamp\n"
    "v_exp_f32 v189, v189 clamp\n"
    "v_mfma_f32_4x4x4_16b_bf16 v[224:227], v[228:229], v[204:205], v[224:227] cbsz:4 abid:2\n"
    "v_exp_f32 v190, v190 clamp\n"
    "v_exp_f32 v191, v191 clamp\n"
    "v_mfma_f32_4x4x4_16b_bf16 v[224:227], v[228:229], v[206:207], v[224:227] cbsz:4 abid:3\n"
    "s_waitcnt vmcnt(4)\n"
    "s_memrealtime s[34:35]\n"
    "ds_read_b64_tr_b16 v[200:201], v240 offset:4096\n"
    "ds_read_b64_tr_b16 v[202:203], v241 offset:4096\n"
    "ds_read_b64_tr_b16 v[204:205], v242 offset:4096\n"
    "ds_read_b64_tr_b16 v[206:207], v243 offset:4096\n"
    "ds_read_u8 v236, v251 offset:0\n"
    "ds_read_u8 v238, v251 offset:1\n"
    "ds_read_b128 v[192:195], v234 offset:6144\n"
    "ds_read_b128 v[196:199], v235 offset:6144\n"
    "v_pk_fma_f32 v[176:177], v[176:177], s[70:71], v[128:129]\n"
    "v_pk_fma_f32 v[178:179], v[178:179], s[70:71], v[130:131]\n"
    "v_pk_add_f32 v[230:231], v[176:177], v[178:179]\n"
    "v_pk_mul_f32 v[232:233], v[176:177], v[178:179]\n"
    "v_pk_fma_f32 v[180:181], v[180:181], s[70:71], v[132:133]\n"
    "v_pk_fma_f32 v[230:231], v[230:231], v[180:181], v[232:233]\n"
    "v_pk_mul_f32 v[232:233], v[232:233], v[180:181]\n"
    "v_pk_fma_f32 v[182:183], v[182:183], s[70:71], v[134:135]\n"
    "v_pk_fma_f32 v[230:231], v[230:231], v[182:183], v[232:233]\n"
    "v_pk_mul_f32 v[232:233], v[232:233], v[182:183]\n"
    "v_pk_fma_f32 v[184:185], v[184:185], s[70:71], v[136:137]\n"
    "v_pk_fma_f32 v[230:231], v[230:231], v[184:185], v[232:233]\n"
    "v_pk_mul_f32 v[232:233], v[232:233], v[184:185]\n"
    "v_pk_fma_f32 v[186:187], v[186:187], s[70:71], v[138:139]\n"
    "v_pk_fma_f32 v[230:231], v[230:231], v[186:187], v[232:233]\n"
    "v_pk_mul_f32 v[232:233], v[232:233], v[186:187]\n"
    "v_pk_fma_f32 v[188:189], v[188:189], s[70:71], v[140:141]\n"
    "v_pk_fma_f32 v[230:231], v[230:231], v[188:189], v[232:233]\n"
    "v_pk_mul_f32 v[232:233], v[232:233], v[188:189]\n"
    "v_pk_fma_f32 v[190:191], v[190:191], s[70:71], v[142:143]\n"
    "v_pk_fma_f32 v[230:231], v[230:231], v[190:191], v[232:233]\n"
    "v_pk_mul_f32 v[232:233], v[232:233], v[190:191]\n"
    "v_rcp_f32 v232, v232\n"
    "v_rcp_f32 v233, v233\n"
    "v_fma_f32 v230, v230, v232, v247\n"
    "v_fmac_f32 v230, v231, v233\n"
    "v_cvt_pk_bf16_f32 v230, v230, v230\n"
    "s_add_u32 s66, s66, 16\n"
    "s_waitcnt lgkmcnt(0)\n"
    "ds_write_b16 v245, v230\n"
    "ds_read_b64 v[228:229], v246\n"
    "s_cmp_lt_u32 s34, s76\n"
    "s_cbranch_scc1 .Ldin_G1_wait\n"
    ".Ldin_G1_go:\n"
    "s_add_u32 s76, s76, s72\n"
    "buffer_load_dwordx4 v[236:237], s[40:43], s67 idxen offen offset:0 nt lds\n"
    "buffer_load_dwordx4 v[238:239], s[40:43], s67 idxen offen offset:1024 nt lds\n"
    "s_cmp_eq_u32 s66, s82\n"
    "s_cbranch_scc1 .Ldin_L2\n"
    ".Ldin_G2:\n"
    "v_mfma_f32_16x16x32_bf16 v[176:179], v[0:3], v[192:195], v[144:147]\n"
    "v_exp_f32 v160, v160 clamp\n"
    "v_mfma_f32_16x16x32_bf16 v[176:179], v[4:7], v[196:199], v[176:179]\n"
    "v_exp_f32 v161, v161 clamp\n"
    "v_mfma_f32_16x16x32_bf16 v[180:183], v[8:11], v[192:195], v[148:151]\n"
    "v_exp_f32 v162, v162 clamp\n"
    "v_mfma_f32_16x16x32_bf16 v[180:183], v[12:15], v[196:199], v[180:183]\n"
    "v_exp_f32 v163, v163 clamp\n"
    "v_mfma_f32_16x16x32_bf16 v[184:187], v[16:19], v[192:195], v[152:155]\n"
    "v_exp_f32 v164, v164 clamp\n"
    "v_mfma_f32_16x16x32_bf16 v[184:187], v[20:23], v[196:199], v[184:187]\n"
    "v_exp_f32 v165, v165 clamp\n"
    "v_mfma_f32_16x16x32_bf16 v[188:191], v[24:27], v[192:195], v[156:159]\n"
    "v_exp_f32 v166, v166 clamp\n"
    "v_mfma_f32_16x16x32_bf16 v[188:191], v[28:31], v[196:199], v[188:191]\n"
    "v_exp_f32 v167, v167 clamp\n"
    "v_exp_f32 v168, v168 clamp\n"
    "s_cmp_eq_u32 s66, s80\n"
    "s_cselect_b64 s[66:67], s[64:65], s[66:67]\n"
    "s_add_u32 m0, s68, 6144\n"
    "v_exp_f32 v169, v169 clamp\n"
    "v_add_u32_e64 v251, s66, v244\n"
    "s_waitcnt lgkmcnt(0)\n"
    "v_mfma_f32_4x4x4_16b_bf16 v[224:227], v[228:229], v[208:209], v[224:227] cbsz:4 abid:0\n"
    "v_exp_f32 v170, v170 clamp\n"
    "v_exp_f32 v171, v171 clamp\n"
    "v_mfma_f32_4x4x4_16b_bf16 v[224:227], v[228:229], v[210:211], v[224:227] cbsz:4 abid:1\n"
    "v_exp_f32 v172, v172 clamp\n"
    "v_exp_f32 v173, v173 clamp\n"
    "v_mfma_f32_4x4x4_16b_bf16 v[224:227], v[228:229], v[212:213], v[224:227] cbsz:4 abid:2\n"
    "v_exp_f32 v174, v174 clamp\n"
    "v_exp_f32 v175, v175 clamp\n"
    "v_mfma_f32_4x4x4_16b_bf16 v[224:227], v[228:229], v[214:215], v[224:227] cbsz:4 abid:3\n"
    "s_waitcnt vmcnt(4)\n"
    "s_memrealtime s[34:35]\n"
    "ds_read_b64_tr_b16 v[208:209], v240 offset:6144\n"
    "ds_read_b64_tr_b16 v[210:211], v241 offset:6144\n"
    "ds_read_b64_tr_b16 v[212:213], v242 offset:6144\n"
    "ds_read_b64_tr_b16 v[214:215], v243 offset:6144\n"
    "ds_read_u8 v236, v251 offset:0\n"
    "ds_read_u8 v238, v251 offset:1\n"
    "ds_read_b128 v[192:195], v234 offset:0\n"
    "ds_read_b128 v[196:199], v235 offset:0\n"
    "v_pk_fma_f32 v[160:161], v[160:161], s[70:71], v[128:129]\n"
    "v_pk_fma_f32 v[162:163], v[162:163], s[70:71], v[130:131]\n"
    "v_pk_add_f32 v[230:231], v[160:161], v[162:163]\n"
    "v_pk_mul_f32 v[232:233], v[160:161], v[162:163]\n"
    "v_pk_fma_f32 v[164:165], v[164:165], s[70:71], v[132:133]\n"
    "v_pk_fma_f32 v[230:231], v[230:231], v[164:165], v[232:233]\n"
    "v_pk_mul_f32 v[232:233], v[232:233], v[164:165]\n"
    "v_pk_fma_f32 v[166:167], v[166:167], s[70:71], v[134:135]\n"
    "v_pk_fma_f32 v[230:231], v[230:231], v[166:167], v[232:233]\n"
    "v_pk_mul_f32 v[232:233], v[232:233], v[166:167]\n"
    "v_pk_fma_f32 v[168:169], v[168:169], s[70:71], v[136:137]\n"
    "v_pk_fma_f32 v[230:231], v[230:231], v[168:169], v[232:233]\n"
    "v_pk_mul_f32 v[232:233], v[232:233], v[168:169]\n"
    "v_pk_fma_f32 v[170:171], v[170:171], s[70:71], v[138:139]\n"
    "v_pk_fma_f32 v[230:231], v[230:231], v[170:171], v[232:233]\n"
    "v_pk_mul_f32 v[232:233], v[232:233], v[170:171]\n"
    "v_pk_fma_f32 v[172:173], v[172:173], s[70:71], v[140:141]\n"
    "v_pk_fma_f32 v[230:231], v[230:231], v[172:173], v[232:233]\n"
    "v_pk_mul_f32 v[232:233], v[232:233], v[172:173]\n"
    "v_pk_fma_f32 v[174:175], v[174:175], s[70:71], v[142:143]\n"
    "v_pk_fma_f32 v[230:231], v[230:231], v[174:175], v[232:233]\n"
    "v_pk_mul_f32 v[232:233], v[232:233], v[174:175]\n"
    "v_rcp_f32 v232, v232\n"
    "v_rcp_f32 v233, v233\n"
    "v_fma_f32 v230, v230, v232, v247\n"
    "v_fmac_f32 v230, v231, v233\n"
    "v_cvt_pk_bf16_f32 v230, v230, v230\n"
    "s_add_u32 s66, s66, 16\n"
    "s_waitcnt lgkmcnt(0)\n"
    "ds_write_b16 v245, v230\n"
    "ds_read_b64 v[228:229], v246\n"
    "s_cmp_lt_u32 s34, s76\n"
    "s_cbranch_scc1 .Ldin_G2_wait\n"
    ".Ldin_G2_go:\n"
    "s_sub_u32 s94, s34, 30\n"
    "s_max_u32 s76, s76, s94\n"
    "s_add_u32 s76, s76, s72\n"
    "buffer_load_dwordx4 v[236:237], s[40:43], s67 idxen offen offset:0 nt lds\n"
    "buffer_load_dwordx4 v[238:239], s[40:43], s67 idxen offen offset:1024 nt lds\n"
    "s_cmp_eq_u32 s66, s82\n"
    "s_cbranch_scc1 .Ldin_L3\n"
    ".Ldin_G3:\n"
    "v_mfma_f32_16x16x32_bf16 v[160:163], v[0:3], v[192:195], v[144:147]\n"
    "v_exp_f32 v176, v176 clamp\n"
    "v_mfma_f32_16x16x32_bf16 v[160:163], v[4:7], v[196:199], v[160:163]\n"
    "v_exp_f32 v177, v177 clamp\n"
    "v_mfma_f32_16x16x32_bf16 v[164:167], v[8:11], v[192:195], v[148:151]\n"
    "v_exp_f32 v178, v178 clamp\n"
    "v_mfma_f32_16x16x32_bf16 v[164:167], v[12:15], v[196:199], v[164:167]\n"
    "v_exp_f32 v179, v179 clamp\n"
    "v_mfma_f32_16x16x32_bf16 v[168:171], v[16:19], v[192:195], v[152:155]\n"
    "v_exp_f32 v180, v180 clamp\n"
    "v_mfma_f32_16x16x32_bf16 v[168:171], v[20:23], v[196:199], v[168:171]\n"
    "v_exp_f32 v181, v181 clamp\n"
    "v_mfma_f32_16x16x32_bf16 v[172:175], v[24:27], v[192:195], v[156:159]\n"
    "v_exp_f32 v182, v182 clamp\n"
    "v_mfma_f32_16x16x32_bf16 v[172:175], v[28:31], v[196:199], v[172:175]\n"
    "v_exp_f32 v183, v183 clamp\n"
    "v_exp_f32 v184, v184 clamp\n"
    "s_cmp_eq_u32 s66, s80\n"
    "s_cselect_b64 s[66:67], s[64:65], s[66:67]\n"
    "s_add_u32 m0, s68, 0\n"
    "v_exp_f32 v185, v185 clamp\n"
    "v_add_u32 v251, s66, v244\n"
    "s_waitcnt lgkmcnt(0)\n"
    "v_mfma_f32_4x4x4_16b_bf16 v[224:227], v[228:229], v[200:201], v[224:227] cbsz:4 abid:0\n"
    "v_exp_f32 v186, v186 clamp\n"
    "v_exp_f32 v187, v187 clamp\n"
    "v_mfma_f32_4x4x4_16b_bf16 v[224:227], v[228:229], v[202:203], v[224:227] cbsz:4 abid:1\n"
    "v_exp_f32 v188, v188 clamp\n"
    "v_exp_f32 v189, v189 clamp\n"
    "v_mfma_f32_4x4x4_16b_bf16 v[224:227], v[228:229], v[204:205], v[224:227] cbsz:4 abid:2\n"
    "v_exp_f32 v190, v190 clamp\n"
    "v_exp_f32 v191, v191 clamp\n"
    "v_mfma_f32_4x4x4_16b_bf16 v[224:227], v[228:229], v[206:207], v[224:227] cbsz:4 abid:3\n"
    "s_waitcnt vmcnt(4)\n"
    "s_memrealtime s[34:35]\n"
    "ds_read_b64_tr_b16 v[200:201], v240 offset:0\n"
    "ds_read_b64_tr_b16 v[202:203], v241 offset:0\n"
    "ds_read_b64_tr_b16 v[204:205], v242 offset:0\n"
    "ds_read_b64_tr_b16 v[206:207], v243 offset:0\n"
    "ds_read_u8 v236, v251 offset:0\n"
    "ds_read_u8 v238, v251 offset:1\n"
    "ds_read_b128 v[192:195], v234 offset:2048\n"
    "ds_read_b128 v[196:199], v235 offset:2048\n"
    "v_pk_fma_f32 v[176:177], v[176:177], s[70:71], v[128:129]\n"
    "v_pk_fma_f32 v[178:179], v[178:179], s[70:71], v[130:131]\n"
    "v_pk_add_f32 v[230:231], v[176:177], v[178:179]\n"
    "v_pk_mul_f32 v[232:233], v[176:177], v[178:179]\n"
    "v_pk_fma_f32 v[180:181], v[180:181], s[70:71], v[132:133]\n"
    "v_pk_fma_f32 v[230:231], v[230:231], v[180:181], v[232:233]\n"
    "v_pk_mul_f32 v[232:233], v[232:233], v[180:181]\n"
    "v_pk_fma_f32 v[182:183], v[182:183], s[70:71], v[134:135]\n"
    "v_pk_fma_f32 v[230:231], v[230:231], v[182:183], v[232:233]\n"
    "v_pk_mul_f32 v[232:233], v[232:233], v[182:183]\n"
    "v_pk_fma_f32 v[184:185], v[184:185], s[70:71], v[136:137]\n"
    "v_pk_fma_f32 v[230:231], v[230:231], v[184:185], v[232:233]\n"
    "v_pk_mul_f32 v[232:233], v[232:233], v[184:185]\n"
    "v_pk_fma_f32 v[186:187], v[186:187], s[70:71], v[138:139]\n"
    "v_pk_fma_f32 v[230:231], v[230:231], v[186:187], v[232:233]\n"
    "v_pk_mul_f32 v[232:233], v[232:233], v[186:187]\n"
    "v_pk_fma_f32 v[188:189], v[188:189], s[70:71], v[140:141]\n"
    "v_pk_fma_f32 v[230:231], v[230:231], v[188:189], v[232:233]\n"
    "v_pk_mul_f32 v[232:233], v[232:233], v[188:189]\n"
    "v_pk_fma_f32 v[190:191], v[190:191], s[70:71], v[142:143]\n"
    "v_pk_fma_f32 v[230:231], v[230:231], v[190:191], v[232:233]\n"
    "v_pk_mul_f32 v[232:233], v[232:233], v[190:191]\n"
    "v_rcp_f32 v232, v232\n"
    "v_rcp_f32 v233, v233\n"
    "v_fma_f32 v230, v230, v232, v247\n"
    "v_fmac_f32 v230, v231, v233\n"
    "v_cvt_pk_bf16_f32 v230, v230, v230\n"
    "s_add_u32 s66, s66, 16\n"
    "s_waitcnt lgkmcnt(0)\n"
    "ds_write_b16 v245, v230\n"
    "ds_read_b64 v[228:229], v246\n"
    "s_cmp_lt_u32 s34, s76\n"
    "s_cbranch_scc1 .Ldin_G3_wait\n"
    ".Ldin_G3_go:\n"
    "s_add_u32 s76, s76, s72\n"
    "buffer_load_dwordx4 v[236:237], s[40:43], s67 idxen offen offset:0 nt lds\n"
    "buffer_load_dwordx4 v[238:239], s[40:43], s67 idxen offen offset:1024 nt lds\n"
    "s_cmp_eq_u32 s66, s82\n"
    "s_cbranch_scc1 .Ldin_L0\n"
    ".Ldin_G0:\n"
    "s_setprio 0\n"
    "v_mfma_f32_16x16x32_bf16 v[176:179], v[0:3], v[192:195], v[144:147]\n"
    "v_exp_f32 v160, v160 clamp\n"
    "v_mfma_f32_16x16x32_bf16 v[176:179], v[4:7], v[196:199], v[176:179]\n"
    "v_exp_f32 v161, v161 clamp\n"
    "v_mfma_f32_16x16x32_bf16 v[180:183], v[8:11], v[192:195], v[148:151]\n"
    "v_exp_f32 v162, v162 clamp\n"
    "v_mfma_f32_16x16x32_bf16 v[180:183], v[12:15], v[196:199], v[180:183]\n"
    "v_exp_f32 v163, v163 clamp\n"
    "v_mfma_f32_16x16x32_bf16 v[184:187], v[16:19], v[192:195], v[152:155]\n"
    "v_exp_f32 v164, v164 clamp\n"
    "v_mfma_f32_16x16x32_bf16 v[184:187], v[20:23], v[196:199], v[184:187]\n"
    "v_exp_f32 v165, v165 clamp\n"
    "v_mfma_f32_16x16x32_bf16 v[188:191], v[24:27], v[192:195], v[156:159]\n"
    "v_exp_f32 v166, v166 clamp\n"
    "v_mfma_f32_16x16x32_bf16 v[188:191], v[28:31], v[196:199], v[188:191]\n"
    "v_exp_f32 v167, v167 clamp\n"
    "v_exp_f32 v168, v168 clamp\n"
    "s_cmp_eq_u32 s66, s80\n"
    "s_cselect_b64 s[66:67], s[64:65], s[66:67]\n"
    "s_add_u32 m0, s68, 2048\n"
    "v_exp_f32 v169, v169 clamp\n"
    "v_add_u32 v251, s66, v244\n"
    "s_waitcnt lgkmcnt(0)\n"
    "v_mfma_f32_4x4x4_16b_bf16 v[224:227], v[228:229], v[208:209], v[224:227] cbsz:4 abid:0\n"
    "v_exp_f32 v170, v170 clamp\n"
    "v_exp_f32 v171, v171 clamp\n"
    "v_mfma_f32_4x4x4_16b_bf16 v[224:227], v[228:229], v[210:211], v[224:227] cbsz:4 abid:1\n"
    "v_exp_f32 v172, v172 clamp\n"
    "v_exp_f32 v173, v173 clamp\n"
    "v_mfma_f32_4x4x4_16b_bf16 v[224:227], v[228:229], v[212:213], v[224:227] cbsz:4 abid:2\n"
    "v_exp_f32 v174, v174 clamp\n"
    "v_exp_f32 v175, v175 clamp\n"
    "v_mfma_f32_4x4x4_16b_bf16 v[224:227], v[228:229], v[214:215], v[224:227] cbsz:4 abid:3\n"
    "s_waitcnt vmcnt(4)\n"
    "s_memrealtime s[34:35]\n"
    "ds_read_b64_tr_b16 v[208:209], v240 offset:2048\n"
    "ds_read_b64_tr_b16 v[210:211], v241 offset:2048\n"
    "ds_read_b64_tr_b16 v[212:213], v242 offset:2048\n"
    "ds_read_b64_tr_b16 v[214:215], v243 offset:2048\n"
    "ds_read_u8 v236, v251 offset:0\n"
    "ds_read_u8 v238, v251 offset:1\n"
    "ds_read_b128 v[192:195], v234 offset:4096\n"
    "ds_read_b128 v[196:199], v235 offset:4096\n"
    "v_pk_fma_f32 v[160:161], v[160:161], s[70:71], v[128:129]\n"
    "v_pk_fma_f32 v[162:163], v[162:163], s[70:71], v[130:131]\n"
    "v_pk_add_f32 v[230:231], v[160:161], v[162:163]\n"
    "v_pk_mul_f32 v[232:233], v[160:161], v[162:163]\n"
    "v_pk_fma_f32 v[164:165], v[164:165], s[70:71], v[132:133]\n"
    "v_pk_fma_f32 v[230:231], v[230:231], v[164:165], v[232:233]\n"
    "v_pk_mul_f32 v[232:233], v[232:233], v[164:165]\n"
    "v_pk_fma_f32 v[166:167], v[166:167], s[70:71], v[134:135]\n"
    "v_pk_fma_f32 v[230:231], v[230:231], v[166:167], v[232:233]\n"
    "v_pk_mul_f32 v[232:233], v[232:233], v[166:167]\n"
    "v_pk_fma_f32 v[168:169], v[168:169], s[70:71], v[136:137]\n"
    "v_pk_fma_f32 v[230:231], v[230:231], v[168:169], v[232:233]\n"
    "v_pk_mul_f32 v[232:233], v[232:233], v[168:169]\n"
    "v_pk_fma_f32 v[170:171], v[170:171], s[70:71], v[138:139]\n"
    "v_pk_fma_f32 v[230:231], v[230:231], v[170:171], v[232:233]\n"
    "v_pk_mul_f32 v[232:233], v[232:233], v[170:171]\n"
    "v_pk_fma_f32 v[172:173], v[172:173], s[70:71], v[140:141]\n"
    "v_pk_fma_f32 v[230:231], v[230:231], v[172:173], v[232:233]\n"
    "v_pk_mul_f32 v[232:233], v[232:233], v[172:173]\n"
    "v_pk_fma_f32 v[174:175], v[174:175], s[70:71], v[142:143]\n"
    "v_pk_fma_f32 v[230:231], v[230:231], v[174:175], v[232:233]\n"
    "v_pk_mul_f32 v[232:233], v[232:233], v[174:175]\n"
    "v_rcp_f32 v232, v232\n"
    "v_rcp_f32 v233, v233\n"
    "v_fma_f32 v230, v230, v232, v247\n"
    "v_fmac_f32 v230, v231, v233\n"
    "v_cvt_pk_bf16_f32 v230, v230, v230\n"
    "s_add_u32 s66, s66, 16\n"
    "s_waitcnt lgkmcnt(0)\n"
    "ds_write_b16 v245, v230\n"
    "ds_read_b64 v[228:229], v246\n"
    "s_cmp_lt_u32 s34, s76\n"
    "s_cbranch_scc1 .Ldin_G0_wait\n"
    ".Ldin_G0_go:\n"
    "s_sub_u32 s94, s34, 30\n"
    "s_max_u32 s76, s76, s94\n"
    "s_add_u32 s76, s76, s72\n"
    "buffer_load_dwordx4 v[236:237], s[40:43], s67 idxen offen offset:0 nt lds\n"
    "buffer_load_dwordx4 v[238:239], s[40:43], s67 idxen offen offset:1024 nt lds\n"
    "s_cmp_eq_u32 s66, s82\n"
    "s_cbranch_scc1 .Ldin_L1\n"
    "s_branch .Ldin_G1\n"
    ".Ldin_stub_A0_poll:\n"
    "s_setpc_b64 s[34:35]\n"
    ".Ldin_A0_poll:\n"
    "s_sleep 1\n"
    "ds_read_b64 v[216:217], v246 offset:128\n"
    "s_waitcnt lgkmcnt(0)\n"
    "v_readlane_b32 s60, v216, s59\n"
    "v_readlane_b32 s61, v217, s59\n"
    "s_cmp_eq_u32 s60, -1\n"
    "s_cbranch_scc1 .Ldin_A0_poll\n"
    "s_branch .Ldin_A0_pr\n"
    ".Ldin_G1_slp:\n"
    "s_sleep 1\n"
    ".Ldin_G1_wait:\n"
    "s_memrealtime s[34:35]\n"
    "s_waitcnt lgkmcnt(0)\n"
    "s_sub_u32 s94, s34, s76\n"
    "s_cmp_lt_i32 s94, 0\n"
    "s_cbranch_scc1 .Ldin_G1_slp\n"
    "s_branch .Ldin_G1_go\n"
    ".Ldin_G2_slp:\n"
    "s_sleep 1\n"
    ".Ldin_G2_wait:\n"
    "s_memrealtime s[34:35]\n"
    "s_waitcnt lgkmcnt(0)\n"
    "s_sub_u32 s94, s34, s76\n"
    "s_cmp_lt_i32 s94, 0\n"
    "s_cbranch_scc1 .Ldin_G2_slp\n"
    "s_branch .Ldin_G2_go\n"
    ".Ldin_G3_slp:\n"
    "s_sleep 1\n"
    ".Ldin_G3_wait:\n"
    "s_memrealtime s[34:35]\n"
    "s_waitcnt lgkmcnt(0)\n"
    "s_sub_u32 s94, s34, s76\n"
    "s_cmp_lt_i32 s94, 0\n"
    "s_cbranch_scc1 .Ldin_G3_slp\n"
    "s_branch .Ldin_G3_go\n"
    ".Ldin_G0_slp:\n"
    "s_sleep 1\n"
    ".Ldin_G0_wait:\n"
    "s_memrealtime s[34:35]\n"
    "s_waitcnt lgkmcnt(0)\n"
    "s_sub_u32 s94, s34, s76\n"
    "s_cmp_lt_i32 s94, 0\n"
    "s_cbranch_scc1 .Ldin_G0_slp\n"
    "s_branch .Ldin_G0_go\n"
    ".Ldin_stub_L2:\n"
    "s_setpc_b64 s[34:35]\n"
    ".p2align 5\n"
    ".Ldin_L2:\n"
    "s_setprio 0\n"
    "s_cmp_lt_i32 s60, 0\n"
    "s_cbranch_scc1 .Ldin_E0\n"
    "ds_read_b128 v[144:147], v252 offset:8192\n"
    "ds_read_b128 v[148:151], v252 offset:8256\n"
    "ds_read_b128 v[152:155], v252 offset:16384\n"
    "ds_read_b128 v[156:159], v252 offset:16448\n"
    "s_add_u32 s76, s76, s73\n"
    "s_add_u32 m0, s68, 6144\n"
    "v_and_b32 v216, v248, v216\n"
    "v_and_b32 v217, v249, v217\n"
    "v_and_b32 v218, v248, v218\n"
    "v_and_b32 v219, v249, v219\n"
    "v_mfma_f32_16x16x16_bf16 v[176:179], v[216:217], v[96:97], v[32:35]\n"
    "v_and_b32 v220, v248, v220\n"
    "v_and_b32 v221, v249, v221\n"
    "v_and_b32 v222, v248, v222\n"
    "v_mfma_f32_16x16x16_bf16 v[180:183], v[218:219], v[98:99], v[36:39]\n"
    "v_exp_f32 v160, v160 clamp\n"
    "v_and_b32 v223, v249, v223\n"
    "v_mfma_f32_16x16x16_bf16 v[184:187], v[220:221], v[100:101], v[40:43]\n"
    "v_add_u32 v251, s66, v244\n"
    "v_cvt_pk_bf16_f32 v0, v176, v177\n"
    "v_cvt_pk_bf16_f32 v1, v178, v179\n"
    "s_waitcnt lgkmcnt(4)\n"
    "v_mfma_f32_16x16x16_bf16 v[188:191], v[222:223], v[102:103], v[44:47]\n"
    "v_exp_f32 v161, v161 clamp\n"
    "v_cvt_pk_bf16_f32 v2, v180, v181\n"
    "v_mfma_f32_4x4x4_16b_bf16 v[224:227], v[228:229], v[208:209], v[224:227] cbsz:4 abid:0\n"
    "v_cvt_pk_bf16_f32 v3, v182, v183\n"
    "v_mfma_f32_16x16x16_bf16 v[176:179], v[216:217], v[104:105], v[48:51]\n"
    "v_exp_f32 v162, v162 clamp\n"
    "v_cvt_pk_bf16_f32 v4, v184, v185\n"
    "v_mfma_f32_16x16x16_bf16 v[180:183], v[218:219], v[106:107], v[52:55]\n"
    "v_cvt_pk_bf16_f32 v5, v186, v187\n"
    "v_cvt_pk_bf16_f32 v6, v188, v189\n"
    "v_cvt_pk_bf16_f32 v7, v190, v191\n"
    "v_mfma_f32_16x16x16_bf16 v[184:187], v[220:221], v[108:109], v[56:59]\n"
    "v_exp_f32 v163, v163 clamp\n"
    "v_cvt_pk_bf16_f32 v8, v176, v177\n"
    "v_mfma_f32_4x4x4_16b_bf16 v[224:227], v[228:229], v[210:211], v[224:227] cbsz:4 abid:1\n"
    "v_cvt_pk_bf16_f32 v9, v178, v179\n"
    "v_mfma_f32_16x16x16_bf16 v[188:191], v[222:223], v[110:111], v[60:63]\n"
    "v_exp_f32 v164, v164 clamp\n"
    "v_cvt_pk_bf16_f32 v10, v180, v181\n"
    "v_mfma_f32_16x16x16_bf16 v[176:179], v[216:217], v[112:113], v[64:67]\n"
    "v_cvt_pk_bf16_f32 v11, v182, v183\n"
    "v_cvt_pk_bf16_f32 v12, v184, v185\n"
    "v_cvt_pk_bf16_f32 v13, v186, v187\n"
    "v_mfma_f32_16x16x16_bf16 v[180:183], v[218:219], v[114:115], v[68:71]\n"
    "v_exp_f32 v165, v165 clamp\n"
    "v_cvt_pk_bf16_f32 v14, v188, v189\n"
    "v_mfma_f32_4x4x4_16b_bf16 v[224:227], v[228:229], v[212:213], v[224:227] cbsz:4 abid:2\n"
    "v_cvt_pk_bf16_f32 v15, v190, v191\n"
    "v_mfma_f32_16x16x16_bf16 v[184:187], v[220:221], v[116:117], v[72:75]\n"
    "v_exp_f32 v166, v166 clamp\n"
    "v_cvt_pk_bf16_f32 v16, v176, v177\n"
    "v_mfma_f32_16x16x16_bf16 v[188:191], v[222:223], v[118:119], v[76:79]\n"
    "v_cvt_pk_bf16_f32 v17, v178, v179\n"
    "v_cvt_pk_bf16_f32 v18, v180, v181\n"
    "v_cvt_pk_bf16_f32 v19, v182, v183\n"
    "v_mfma_f32_16x16x16_bf16 v[176:179], v[216:217], v[120:121], v[80:83]\n"
    "v_exp_f32 v167, v167 clamp\n"
    "v_cvt_pk_bf16_f32 v20, v184, v185\n"
    "v_mfma_f32_4x4x4_16b_bf16 v[224:227], v[228:229], v[214:215], v[224:227] cbsz:4 abid:3\n"
    "v_cvt_pk_bf16_f32 v21, v186, v187\n"
    "v_mfma_f32_16x16x16_bf16 v[180:183], v[218:219], v[122:123], v[84:87]\n"
    "v_exp_f32 v168, v168 clamp\n"
    "v_cvt_pk_bf16_f32 v22, v188, v189\n"
    "v_mfma_f32_16x16x16_bf16 v[184:187], v[220:221], v[124:125], v[88:91]\n"
    "v_cvt_pk_bf16_f32 v23, v190, v191\n"
    "v_cvt_pk_bf16_f32 v24, v176, v177\n"
    "v_cvt_pk_bf16_f32 v25, v178, v179\n"
    "v_mfma_f32_16x16x16_bf16 v[188:191], v[222:223], v[126:127], v[92:95]\n"
    "v_exp_f32 v169, v169 clamp\n"
    "v_cvt_pk_bf16_f32 v26, v180, v181\n"
    "s_waitcnt lgkmcnt(0)\n"
    "v_mfma_f32_16x16x32_bf16 v[176:179], v[0:3], v[192:195], v[144:147]\n"
    "v_cvt_pk_bf16_f32 v27, v182, v183\n"
    "v_cvt_pk_bf16_f32 v28, v184, v185\n"
    "v_mfma_f32_16x16x32_bf16 v[176:179], v[4:7], v[196:199], v[176:179]\n"
    "v_cvt_pk_bf16_f32 v29, v186, v187\n"
    "v_cvt_pk_bf16_f32 v30, v188, v189\n"
    "v_mfma_f32_16x16x32_bf16 v[180:183], v[8:11], v[192:195], v[148:151]\n"
    "v_exp_f32 v170, v170 clamp\n"
    "v_mfma_f32_16x16x32_bf16 v[180:183], v[12:15], v[196:199], v[180:183]\n"
    "v_exp_f32 v171, v171 clamp\n"
    "v_mfma_f32_16x16x32_bf16 v[184:187], v[16:19], v[192:195], v[152:155]\n"
    "v_exp_f32 v172, v172 clamp\n"
    "v_mfma_f32_16x16x32_bf16 v[184:187], v[20:23], v[196:199], v[184:187]\n"
    "v_exp_f32 v173, v173 clamp\n"
    "v_cvt_pk_bf16_f32 v31, v190, v191\n"
    "v_mfma_f32_16x16x32_bf16 v[188:191], v[24:27], v[192:195], v[156:159]\n"
    "v_exp_f32 v174, v174 clamp\n"
    "v_mfma_f32_16x16x32_bf16 v[188:191], v[28:31], v[196:199], v[188:191]\n"
    "v_exp_f32 v175, v175 clamp\n"
    "s_waitcnt vmcnt(4)\n"
    "ds_read_b64_tr_b16 v[208:209], v240 offset:6144\n"
    "ds_read_b64_tr_b16 v[210:211], v241 offset:6144\n"
    "ds_read_b64_tr_b16 v[212:213], v242 offset:6144\n"
    "ds_read_b64_tr_b16 v[214:215], v243 offset:6144\n"
    "ds_read_u8 v236, v251 offset:0\n"
    "ds_read_u8 v238, v251 offset:1\n"
    "ds_read_b128 v[192:195], v234 offset:0\n"
    "ds_read_b128 v[196:199], v235 offset:0\n"
    "v_pk_fma_f32 v[160:161], v[160:161], s[70:71], v[128:129]\n"
    "v_pk_fma_f32 v[162:163], v[162:163], s[70:71], v[130:131]\n"
    "v_pk_add_f32 v[230:231], v[160:161], v[162:163]\n"
    "v_pk_mul_f32 v[232:233], v[160:161], v[162:163]\n"
    "v_pk_fma_f32 v[164:165], v[164:165], s[70:71], v[132:133]\n"
    "v_pk_fma_f32 v[230:231], v[230:231], v[164:165], v[232:233]\n"
    "v_pk_mul_f32 v[232:233], v[232:233], v[164:165]\n"
    "v_pk_fma_f32 v[166:167], v[166:167], s[70:71], v[134:135]\n"
    "v_pk_fma_f32 v[230:231], v[230:231], v[166:167], v[232:233]\n"
    "v_pk_mul_f32 v[232:233], v[232:233], v[166:167]\n"
    "v_pk_fma_f32 v[168:169], v[168:169], s[70:71], v[136:137]\n"
    "v_pk_fma_f32 v[230:231], v[230:231], v[168:169], v[232:233]\n"
    "v_pk_mul_f32 v[232:233], v[232:233], v[168:169]\n"
    "v_pk_fma_f32 v[170:171], v[170:171], s[70:71], v[138:139]\n"
    "v_pk_fma_f32 v[230:231], v[230:231], v[170:171], v[232:233]\n"
    "v_pk_mul_f32 v[232:233], v[232:233], v[170:171]\n"
    "v_pk_fma_f32 v[172:173], v[172:173], s[70:71], v[140:141]\n"
    "v_pk_fma_f32 v[230:231], v[230:231], v[172:173], v[232:233]\n"
    "v_pk_mul_f32 v[232:233], v[232:233], v[172:173]\n"
    "v_pk_fma_f32 v[174:175], v[174:175], s[70:71], v[142:143]\n"
    "v_pk_fma_f32 v[230:231], v[230:231], v[174:175], v[232:233]\n"
    "v_pk_mul_f32 v[232:233], v[232:233], v[174:175]\n"
    "v_rcp_f32 v232, v232\n"
    "v_rcp_f32 v233, v233\n"
    "v_fma_f32 v230, v230, v232, v247\n"
    "v_fmac_f32 v230, v231, v233\n"
    "v_cvt_pk_bf16_f32 v230, v230, v230\n"
    "s_add_u32 s66, s66, 16\n"
    "s_waitcnt lgkmcnt(0)\n"
    "ds_write_b16 v245, v230\n"
    "ds_read_b64 v[228:229], v246\n"
    "s_lshl1_add_u32 s76, s72, s76\n"
    "buffer_load_dwordx4 v[236:237], s[40:43], s67 idxen offen offset:0 nt lds\n"
    "buffer_load_dwordx4 v[238:239], s[40:43], s67 idxen offen offset:1024 nt lds\n"
    "s_mov_b32 s81, s64\n"
    "s_lshr_b32 s63, s65, 6\n"
    "s_mov_b32 s80, s83\n"
    "s_add_u32 s62, s62, 256\n"
    "s_bitcmp1_b32 s60, 30\n"
    "s_cbranch_scc1 .Ldin_A3s\n"
    ".Ldin_A3:\n"
    "s_cbranch_vccz .Ldin_A3_np\n"
    "s_setprio 1\n"
    ".Ldin_A3_np:\n"
    "v_mfma_f32_16x16x32_bf16 v[160:163], v[0:3], v[192:195], v[144:147]\n"
    "v_exp_f32 v176, v176 clamp\n"
    "v_mfma_f32_16x16x32_bf16 v[160:163], v[4:7], v[196:199], v[160:163]\n"
    "v_exp_f32 v177, v177 clamp\n"
    "v_mfma_f32_16x16x32_bf16 v[164:167], v[8:11], v[192:195], v[148:151]\n"
    "v_exp_f32 v178, v178 clamp\n"
    "v_mfma_f32_16x16x32_bf16 v[164:167], v[12:15], v[196:199], v[164:167]\n"
    "v_exp_f32 v179, v179 clamp\n"
    "v_mfma_f32_16x16x32_bf16 v[168:171], v[16:19], v[192:195], v[152:155]\n"
    "v_exp_f32 v180, v180 clamp\n"
    "v_mfma_f32_16x16x32_bf16 v[168:171], v[20:23], v[196:199], v[168:171]\n"
    "v_exp_f32 v181, v181 clamp\n"
    "v_mfma_f32_16x16x32_bf16 v[172:175], v[24:27], v[192:195], v[156:159]\n"
    "v_exp_f32 v182, v182 clamp\n"
    "v_mfma_f32_16x16x32_bf16 v[172:175], v[28:31], v[196:199], v[172:175]\n"
    "v_exp_f32 v183, v183 clamp\n"
    "v_exp_f32 v184, v184 clamp\n"
    "s_mov_b32 m0, s62\n"
    "ds_read_b64 v[216:217], v246 offset:128\n"
    "v_exp_f32 v185, v185 clamp\n"
    "v_add_u32 v251, s66, v244\n"
    "s_waitcnt lgkmcnt(1)\n"
    "v_mfma_f32_4x4x4_16b_bf16 v[224:227], v[228:229], v[200:201], v[224:227] cbsz:4 abid:0\n"
    "v_exp_f32 v186, v186 clamp\n"
    "v_exp_f32 v187, v187 clamp\n"
    "v_mfma_f32_4x4x4_16b_bf16 v[224:227], v[228:229], v[202:203], v[224:227] cbsz:4 abid:1\n"
    "v_exp_f32 v188, v188 clamp\n"
    "v_exp_f32 v189, v189 clamp\n"
    "v_mfma_f32_4x4x4_16b_bf16 v[224:227], v[228:229], v[204:205], v[224:227] cbsz:4 abid:2\n"
    "v_exp_f32 v190, v190 clamp\n"
    "v_exp_f32 v191, v191 clamp\n"
    "v_mfma_f32_4x4x4_16b_bf16 v[224:227], v[228:229], v[206:207], v[224:227] cbsz:4 abid:3\n"
    "s_waitcnt vmcnt(4)\n"
    "s_waitcnt lgkmcnt(0)\n"
    "v_readlane_b32 s60, v216, s59\n"
    "v_readlane_b32 s61, v217, s59\n"
    "ds_read_b64_tr_b16 v[200:201], v240 offset:0\n"
    "ds_read_b64_tr_b16 v[202:203], v241 offset:0\n"
    "ds_read_b64_tr_b16 v[204:205], v242 offset:0\n"
    "ds_read_b64_tr_b16 v[206:207], v243 offset:0\n"
    "ds_read_u8 v236, v251 offset:0\n"
    "ds_read_u8 v238, v251 offset:1\n"
    "ds_read_b128 v[192:195], v234 offset:2048\n"
    "ds_read_b128 v[196:199], v235 offset:2048\n"
    "s_cmp_eq_u32 s60, -1\n"
    "s_cbranch_scc1 .Ldin_A3_poll\n"
    ".Ldin_A3_pr:\n"
    "s_lshr_b32 s94, s60, 12\n"
    "s_and_b32 s94, s94, 0x1ff0\n"
    "v_xor_b32 v252, s94, v250\n"
    "v_pk_add_f32 v[230:231], v[224:225], v[226:227]\n"
    "v_add_f32 v230, v230, v231\n"
    "ds_write_addtid_b32 v230 offset:49152\n"
    "ds_read_b128 v[216:219], v252 offset:0\n"
    "ds_read_b128 v[220:223], v252 offset:64\n"
    "v_mov_b64 v[224:225], 0\n"
    "v_mov_b64_e64 v[226:227], 0\n"
    "v_pk_fma_f32 v[176:177], v[176:177], s[70:71], v[128:129]\n"
    "v_pk_fma_f32 v[178:179], v[178:179], s[70:71], v[130:131]\n"
    "v_pk_add_f32 v[230:231], v[176:177], v[178:179]\n"
    "v_pk_mul_f32 v[232:233], v[176:177], v[178:179]\n"
    "v_pk_fma_f32 v[180:181], v[180:181], s[70:71], v[132:133]\n"
    "v_pk_fma_f32 v[230:231], v[230:231], v[180:181], v[232:233]\n"
    "v_pk_mul_f32 v[232:233], v[232:233], v[180:181]\n"
    "v_pk_fma_f32 v[182:183], v[182:183], s[70:71], v[134:135]\n"
    "v_pk_fma_f32 v[230:231], v[230:231], v[182:183], v[232:233]\n"
    "v_pk_mul_f32 v[232:233], v[232:233], v[182:183]\n"
    "v_pk_fma_f32 v[184:185], v[184:185], s[70:71], v[136:137]\n"
    "v_pk_fma_f32 v[230:231], v[230:231], v[184:185], v[232:233]\n"
    "v_pk_mul_f32 v[232:233], v[232:233], v[184:185]\n"
    "v_pk_fma_f32 v[186:187], v[186:187], s[70:71], v[138:139]\n"
    "v_pk_fma_f32 v[230:231], v[230:231], v[186:187], v[232:233]\n"
    "v_pk_mul_f32 v[232:233], v[232:233], v[186:187]\n"
    "v_pk_fma_f32 v[188:189], v[188:189], s[70:71], v[140:141]\n"
    "v_pk_fma_f32 v[230:231], v[230:231], v[188:189], v[232:233]\n"
    "v_pk_mul_f32 v[232:233], v[232:233], v[188:189]\n"
    "v_pk_fma_f32 v[190:191], v[190:191], s[70:71], v[142:143]\n"
    "v_pk_fma_f32 v[230:231], v[230:231], v[190:191], v[232:233]\n"
    "v_pk_mul_f32 v[232:233], v[232:233], v[190:191]\n"
    "s_add_u32 m0, s68, 0\n"
    "s_and_b32 s64, s60, 0xffff\n"
    "s_bfe_u32 s94, s60, 0x30013\n"
    "s_bfe_u32 s95, s60, 0x50019\n"
    "s_mul_i32 s94, s94, s56\n"
    "s_lshl4_add_u32 s83, s95, s64\n"
    "s_add_u32 s65, s58, s94\n"
    "s_add_u32 s82, s64, 64\n"
    "s_add_u32 s58, s58, s57\n"
    "s_add_u32 s59, s59, 1\n"
    "s_sub_u32 s95, s61, s76\n"
    "s_lshr_b32 s94, s61, 16\n"
    "s_sext_i32_i16 s95, s95\n"
    "s_mul_i32 s95, s95, s94\n"
    "s_ashr_i32 s95, s95, 16\n"
    "s_max_i32 s95, s95, -8\n"
    "s_min_i32 s95, s95, 4\n"
    "s_add_u32 s72, s77, s95\n"
    "s_cmp_lt_i32 s60, 0\n"
    "s_cselect_b32 s72, 0, s72\n"
    "v_rcp_f32 v232, v232\n"
    "v_rcp_f32 v233, v233\n"
    "v_fma_f32 v230, v230, v232, v247\n"
    "v_fmac_f32 v230, v231, v233\n"
    "v_cvt_pk_bf16_f32 v230, v230, v230\n"
    "s_add_u32 s66, s66, 16\n"
    "s_waitcnt lgkmcnt(0)\n"
    "ds_write_b16 v245, v230\n"
    "ds_read_b64 v[228:229], v246\n"
    "buffer_load_dwordx4 v[236:237], s[40:43], s67 idxen offen offset:0 nt lds\n"
    "buffer_load_dwordx4 v[238:239], s[40:43], s67 idxen offen offset:1024 nt lds\n"
    "s_branch .Ldin_G0\n"
    ".Ldin_A3_poll:\n"
    "s_sleep 1\n"
    "ds_read_b64 v[216:217], v246 offset:128\n"
    "s_waitcnt lgkmcnt(0)\n"
    "v_readlane_b32 s60, v216, s59\n"
    "v_readlane_b32 s61, v217, s59\n"
    "s_cmp_eq_u32 s60, -1\n"
    "s_cbranch_scc1 .Ldin_A3_poll\n"
    "s_branch .Ldin_A3_pr\n"
    ".Ldin_stub_L1:\n"
    "s_setpc_b64 s[34:35]\n"
    ".p2align 5\n"
    ".Ldin_L1:\n"
    "s_setprio 0\n"
    "s_cmp_lt_i32 s60, 0\n"
    "s_cbranch_scc1 .Ldin_E1\n"
    "ds_read_b128 v[144:147], v252 offset:8192\n"
    "ds_read_b128 v[148:151], v252 offset:8256\n"
    "ds_read_b128 v[152:155], v252 offset:16384\n"
    "ds_read_b128 v[156:159], v252 offset:16448\n"
    "s_add_u32 s76, s76, s73\n"
    "s_add_u32 m0, s68, 4096\n"
    "v_and_b32 v216, v248, v216\n"
    "v_and_b32 v217, v249, v217\n"
    "v_and_b32 v218, v248, v218\n"
    "v_and_b32 v219, v249, v219\n"
    "v_mfma_f32_16x16x16_bf16 v[160:163], v[216:217], v[96:97], v[32:35]\n"
    "v_and_b32 v220, v248, v220\n"
    "v_and_b32 v221, v249, v221\n"
    "v_and_b32 v222, v248, v222\n"
    "v_mfma_f32_16x16x16_bf16 v[164:167], v[218:219], v[98:99], v[36:39]\n"
    "v_exp_f32 v176, v176 clamp\n"
    "v_and_b32 v223, v249, v223\n"
    "v_mfma_f32_16x16x16_bf16 v[168:171], v[220:221], v[100:101], v[40:43]\n"
    "v_add_u32 v251, s66, v244\n"
    "v_cvt_pk_bf16_f32 v0, v160, v161\n"
    "v_cvt_pk_bf16_f32 v1, v162, v163\n"
    "s_waitcnt lgkmcnt(4)\n"
    "v_mfma_f32_16x16x16_bf16 v[172:175], v[222:223], v[102:103], v[44:47]\n"
    "v_exp_f32 v177, v177 clamp\n"
    "v_cvt_pk_bf16_f32 v2, v164, v165\n"
    "v_mfma_f32_4x4x4_16b_bf16 v[224:227], v[228:229], v[200:201], v[224:227] cbsz:4 abid:0\n"
    "v_cvt_pk_bf16_f32 v3, v166, v167\n"
    "v_mfma_f32_16x16x16_bf16 v[160:163], v[216:217], v[104:105], v[48:51]\n"
    "v_exp_f32 v178, v178 clamp\n"
    "v_cvt_pk_bf16_f32 v4, v168, v169\n"
    "v_mfma_f32_16x16x16_bf16 v[164:167], v[218:219], v[106:107], v[52:55]\n"
    "v_cvt_pk_bf16_f32 v5, v170, v171\n"
    "v_cvt_pk_bf16_f32 v6, v172, v173\n"
    "v_cvt_pk_bf16_f32 v7, v174, v175\n"
    "v_mfma_f32_16x16x16_bf16 v[168:171], v[220:221], v[108:109], v[56:59]\n"
    "v_exp_f32 v179, v179 clamp\n"
    "v_cvt_pk_bf16_f32 v8, v160, v161\n"
    "v_mfma_f32_4x4x4_16b_bf16 v[224:227], v[228:229], v[202:203], v[224:227] cbsz:4 abid:1\n"
    "v_cvt_pk_bf16_f32 v9, v162, v163\n"
    "v_mfma_f32_16x16x16_bf16 v[172:175], v[222:223], v[110:111], v[60:63]\n"
    "v_exp_f32 v180, v180 clamp\n"
    "v_cvt_pk_bf16_f32 v10, v164, v165\n"
    "v_mfma_f32_16x16x16_bf16 v[160:163], v[216:217], v[112:113], v[64:67]\n"
    "v_cvt_pk_bf16_f32 v11, v166, v167\n"
    "v_cvt_pk_bf16_f32 v12, v168, v169\n"
    "v_cvt_pk_bf16_f32 v13, v170, v171\n"
    "v_mfma_f32_16x16x16_bf16 v[164:167], v[218:219], v[114:115], v[68:71]\n"
    "v_exp_f32 v181, v181 clamp\n"
    "v_cvt_pk_bf16_f32 v14, v172, v173\n"
    "v_mfma_f32_4x4x4_16b_bf16 v[224:227], v[228:229], v[204:205], v[224:227] cbsz:4 abid:2\n"
    "v_cvt_pk_bf16_f32 v15, v174, v175\n"
    "v_mfma_f32_16x16x16_bf16 v[168:171], v[220:221], v[116:117], v[72:75]\n"
    "v_exp_f32 v182, v182 clamp\n"
    "v_cvt_pk_bf16_f32 v16, v160, v161\n"
    "v_mfma_f32_16x16x16_bf16 v[172:175], v[222:223], v[118:119], v[76:79]\n"
    "v_cvt_pk_bf16_f32 v17, v162, v163\n"
    "v_cvt_pk_bf16_f32 v18, v164, v165\n"
    "v_cvt_pk_bf16_f32 v19, v166, v167\n"
    "v_mfma_f32_16x16x16_bf16 v[160:163], v[216:217], v[120:121], v[80:83]\n"
    "v_exp_f32 v183, v183 clamp\n"
    "v_cvt_pk_bf16_f32 v20, v168, v169\n"
    "v_mfma_f32_4x4x4_16b_bf16 v[224:227], v[228:229], v[206:207], v[224:227] cbsz:4 abid:3\n"
    "v_cvt_pk_bf16_f32 v21, v170, v171\n"
    "v_mfma_f32_16x16x16_bf16 v[164:167], v[218:219], v[122:123], v[84:87]\n"
    "v_exp_f32 v184, v184 clamp\n"
    "v_cvt_pk_bf16_f32 v22, v172, v173\n"
    "v_mfma_f32_16x16x16_bf16 v[168:171], v[220:221], v[124:125], v[88:91]\n"
    "v_cvt_pk_bf16_f32 v23, v174, v175\n"
    "v_cvt_pk_bf16_f32 v24, v160, v161\n"
    "v_cvt_pk_bf16_f32 v25, v162, v163\n"
    "v_mfma_f32_16x16x16_bf16 v[172:175], v[222:223], v[126:127], v[92:95]\n"
    "v_exp_f32 v185, v185 clamp\n"
    "v_cvt_pk_bf16_f32 v26, v164, v165\n"
    "s_waitcnt lgkmcnt(0)\n"
    "v_mfma_f32_16x16x32_bf16 v[160:163], v[0:3], v[192:195], v[144:147]\n"
    "v_cvt_pk_bf16_f32 v27, v166, v167\n"
    "v_cvt_pk_bf16_f32 v28, v168, v169\n"
    "v_mfma_f32_16x16x32_bf16 v[160:163], v[4:7], v[196:199], v[160:163]\n"
    "v_cvt_pk_bf16_f32 v29, v170, v171\n"
    "v_cvt_pk_bf16_f32 v30, v172, v173\n"
    "v_mfma_f32_16x16x32_bf16 v[164:167], v[8:11], v[192:195], v[148:151]\n"
    "v_exp_f32 v186, v186 clamp\n"
    "v_mfma_f32_16x16x32_bf16 v[164:167], v[12:15], v[196:199], v[164:167]\n"
    "v_exp_f32 v187, v187 clamp\n"
    "v_mfma_f32_16x16x32_bf16 v[168:171], v[16:19], v[192:195], v[152:155]\n"
    "v_exp_f32 v188, v188 clamp\n"
    "v_mfma_f32_16x16x32_bf16 v[168:171], v[20:23], v[196:199], v[168:171]\n"
    "v_exp_f32 v189, v189 clamp\n"
    "v_cvt_pk_bf16_f32 v31, v174, v175\n"
    "v_mfma_f32_16x16x32_bf16 v[172:175], v[24:27], v[192:195], v[156:159]\n"
    "v_exp_f32 v190, v190 clamp\n"
    "v_mfma_f32_16x16x32_bf16 v[172:175], v[28:31], v[196:199], v[172:175]\n"
    "v_exp_f32 v191, v191 clamp\n"
    "s_waitcnt vmcnt(4)\n"
    "ds_read_b64_tr_b16 v[200:201], v240 offset:4096\n"
    "ds_read_b64_tr_b16 v[202:203], v241 offset:4096\n"
    "ds_read_b64_tr_b16 v[204:205], v242 offset:4096\n"
    "ds_read_b64_tr_b16 v[206:207], v243 offset:4096\n"
    "ds_read_u8 v236, v251 offset:0\n"
    "ds_read_u8 v238, v251 offset:1\n"
    "ds_read_b128 v[192:195], v234 offset:6144\n"
    "ds_read_b128 v[196:199], v235 offset:6144\n"
    "v_pk_fma_f32 v[176:177], v[176:177], s[70:71], v[128:129]\n"
    "v_pk_fma_f32 v[178:179], v[178:179], s[70:71], v[130:131]\n"
    "v_pk_add_f32 v[230:231], v[176:177], v[178:179]\n"
    "v_pk_mul_f32 v[232:233], v[176:177], v[178:179]\n"
    "v_pk_fma_f32 v[180:181], v[180:181], s[70:71], v[132:133]\n"
    "v_pk_fma_f32 v[230:231], v[230:231], v[180:181], v[232:233]\n"
    "v_pk_mul_f32 v[232:233], v[232:233], v[180:181]\n"
    "v_pk_fma_f32 v[182:183], v[182:183], s[70:71], v[134:135]\n"
    "v_pk_fma_f32 v[230:231], v[230:231], v[182:183], v[232:233]\n"
    "v_pk_mul_f32 v[232:233], v[232:233], v[182:183]\n"
    "v_pk_fma_f32 v[184:185], v[184:185], s[70:71], v[136:137]\n"
    "v_pk_fma_f32 v[230:231], v[230:231], v[184:185], v[232:233]\n"
    "v_pk_mul_f32 v[232:233], v[232:233], v[184:185]\n"
    "v_pk_fma_f32 v[186:187], v[186:187], s[70:71], v[138:139]\n"
    "v_pk_fma_f32 v[230:231], v[230:231], v[186:187], v[232:233]\n"
    "v_pk_mul_f32 v[232:233], v[232:233], v[186:187]\n"
    "v_pk_fma_f32 v[188:189], v[188:189], s[70:71], v[140:141]\n"
    "v_pk_fma_f32 v[230:231], v[230:231], v[188:189], v[232:233]\n"
    "v_pk_mul_f32 v[232:233], v[232:233], v[188:189]\n"
    "v_pk_fma_f32 v[190:191], v[190:191], s[70:71], v[142:143]\n"
    "v_pk_fma_f32 v[230:231], v[230:231], v[190:191], v[232:233]\n"
    "v_pk_mul_f32 v[232:233], v[232:233], v[190:191]\n"
    "v_rcp_f32 v232, v232\n"
    "v_rcp_f32 v233, v233\n"
    "v_fma_f32 v230, v230, v232, v247\n"
    "v_fmac_f32 v230, v231, v233\n"
    "v_cvt_pk_bf16_f32 v230, v230, v230\n"
    "s_add_u32 s66, s66, 16\n"
    "s_waitcnt lgkmcnt(0)\n"
    "ds_write_b16 v245, v230\n"
    "ds_read_b64 v[228:229], v246\n"
    "s_lshl1_add_u32 s76, s72, s76\n"
    "buffer_load_dwordx4 v[236:237], s[40:43], s67 idxen offen offset:0 nt lds\n"
    "buffer_load_dwordx4 v[238:239], s[40:43], s67 idxen offen offset:1024 nt lds\n"
    "s_mov_b32 s81, s64\n"
    "s_lshr_b32 s63, s65, 6\n"
    "s_mov_b32 s80, s83\n"
    "s_add_u32 s62, s62, 256\n"
    "s_bitcmp1_b32 s60, 30\n"
    "s_cbranch_scc1 .Ldin_A2s\n"
    ".Ldin_A2:\n"
    "s_cbranch_vccz .Ldin_A2_np\n"
    "s_setprio 1\n"
    ".Ldin_A2_np:\n"
    "v_mfma_f32_16x16x32_bf16 v[176:179], v[0:3], v[192:195], v[144:147]\n"
    "v_exp_f32 v160, v160 clamp\n"
    "v_mfma_f32_16x16x32_bf16 v[176:179], v[4:7], v[196:199], v[176:179]\n"
    "v_exp_f32 v161, v161 clamp\n"
    "v_mfma_f32_16x16x32_bf16 v[180:183], v[8:11], v[192:195], v[148:151]\n"
    "v_exp_f32 v162, v162 clamp\n"
    "v_mfma_f32_16x16x32_bf16 v[180:183], v[12:15], v[196:199], v[180:183]\n"
    "v_exp_f32 v163, v163 clamp\n"
    "v_mfma_f32_16x16x32_bf16 v[184:187], v[16:19], v[192:195], v[152:155]\n"
    "v_exp_f32 v164, v164 clamp\n"
    "v_mfma_f32_16x16x32_bf16 v[184:187], v[20:23], v[196:199], v[184:187]\n"
    "v_exp_f32 v165, v165 clamp\n"
    "v_mfma_f32_16x16x32_bf16 v[188:191], v[24:27], v[192:195], v[156:159]\n"
    "v_exp_f32 v166, v166 clamp\n"
    "v_mfma_f32_16x16x32_bf16 v[188:191], v[28:31], v[196:199], v[188:191]\n"
    "v_exp_f32 v167, v167 clamp\n"
    "v_exp_f32 v168, v168 clamp\n"
    "s_mov_b32 m0, s62\n"
    "ds_read_b64 v[216:217], v246 offset:128\n"
    "v_exp_f32 v169, v169 clamp\n"
    "v_add_u32 v251, s66, v244\n"
    "s_waitcnt lgkmcnt(1)\n"
    "v_mfma_f32_4x4x4_16b_bf16 v[224:227], v[228:229], v[208:209], v[224:227] cbsz:4 abid:0\n"
    "v_exp_f32 v170, v170 clamp\n"
    "v_exp_f32 v171, v171 clamp\n"
    "v_mfma_f32_4x4x4_16b_bf16 v[224:227], v[228:229], v[210:211], v[224:227] cbsz:4 abid:1\n"
    "v_exp_f32 v172, v172 clamp\n"
    "v_exp_f32 v173, v173 clamp\n"
    "v_mfma_f32_4x4x4_16b_bf16 v[224:227], v[228:229], v[212:213], v[224:227] cbsz:4 abid:2\n"
    "v_exp_f32 v174, v174 clamp\n"
    "v_exp_f32 v175, v175 clamp\n"
    "v_mfma_f32_4x4x4_16b_bf16 v[224:227], v[228:229], v[214:215], v[224:227] cbsz:4 abid:3\n"
    "s_waitcnt vmcnt(4)\n"
    "s_waitcnt lgkmcnt(0)\n"
    "v_readlane_b32 s60, v216, s59\n"
    "v_readlane_b32 s61, v217, s59\n"
    "ds_read_b64_tr_b16 v[208:209], v240 offset:6144\n"
    "ds_read_b64_tr_b16 v[210:211], v241 offset:6144\n"
    "ds_read_b64_tr_b16 v[212:213], v242 offset:6144\n"
    "ds_read_b64_tr_b16 v[214:215], v243 offset:6144\n"
    "ds_read_u8 v236, v251 offset:0\n"
    "ds_read_u8 v238, v251 offset:1\n"
    "ds_read_b128 v[192:195], v234 offset:0\n"
    "ds_read_b128 v[196:199], v235 offset:0\n"
    "s_cmp_eq_u32 s60, -1\n"
    "s_cbranch_scc1 .Ldin_A2_poll\n"
    ".Ldin_A2_pr:\n"
    "s_lshr_b32 s94, s60, 12\n"
    "s_and_b32 s94, s94, 0x1ff0\n"
    "v_xor_b32 v252, s94, v250\n"
    "v_pk_add_f32 v[230:231], v[224:225], v[226:227]\n"
    "v_add_f32 v230, v230, v231\n"
    "ds_write_addtid_b32 v230 offset:49152\n"
    "ds_read_b128 v[216:219], v252 offset:0\n"
    "ds_read_b128 v[220:223], v252 offset:64\n"
    "v_mov_b64 v[224:225], 0\n"
    "v_mov_b64_e64 v[226:227], 0\n"
    "v_pk_fma_f32 v[160:161], v[160:161], s[70:71], v[128:129]\n"
    "v_pk_fma_f32 v[162:163], v[162:163], s[70:71], v[130:131]\n"
    "v_pk_add_f32 v[230:231], v[160:161], v[162:163]\n"
    "v_pk_mul_f32 v[232:233], v[160:161], v[162:163]\n"
    "v_pk_fma_f32 v[164:165], v[164:165], s[70:71], v[132:133]\n"
    "v_pk_fma_f32 v[230:231], v[230:231], v[164:165], v[232:233]\n"
    "v_pk_mul_f32 v[232:233], v[232:233], v[164:165]\n"
    "v_pk_fma_f32 v[166:167], v[166:167], s[70:71], v[134:135]\n"
    "v_pk_fma_f32 v[230:231], v[230:231], v[166:167], v[232:233]\n"
    "v_pk_mul_f32 v[232:233], v[232:233], v[166:167]\n"
    "v_pk_fma_f32 v[168:169], v[168:169], s[70:71], v[136:137]\n"
    "v_pk_fma_f32 v[230:231], v[230:231], v[168:169], v[232:233]\n"
    "v_pk_mul_f32 v[232:233], v[232:233], v[168:169]\n"
    "v_pk_fma_f32 v[170:171], v[170:171], s[70:71], v[138:139]\n"
    "v_pk_fma_f32 v[230:231], v[230:231], v[170:171], v[232:233]\n"
    "v_pk_mul_f32 v[232:233], v[232:233], v[170:171]\n"
    "v_pk_fma_f32 v[172:173], v[172:173], s[70:71], v[140:141]\n"
    "v_pk_fma_f32 v[230:231], v[230:231], v[172:173], v[232:233]\n"
    "v_pk_mul_f32 v[232:233], v[232:233], v[172:173]\n"
    "v_pk_fma_f32 v[174:175], v[174:175], s[70:71], v[142:143]\n"
    "v_pk_fma_f32 v[230:231], v[230:231], v[174:175], v[232:233]\n"
    "v_pk_mul_f32 v[232:233], v[232:233], v[174:175]\n"
    "s_add_u32 m0, s68, 6144\n"
    "s_and_b32 s64, s60, 0xffff\n"
    "s_bfe_u32 s94, s60, 0x30013\n"
    "s_bfe_u32 s95, s60, 0x50019\n"
    "s_mul_i32 s94, s94, s56\n"
    "s_lshl4_add_u32 s83, s95, s64\n"
    "s_add_u32 s65, s58, s94\n"
    "s_add_u32 s82, s64, 64\n"
    "s_add_u32 s58, s58, s57\n"
    "s_add_u32 s59, s59, 1\n"
    "s_sub_u32 s95, s61, s76\n"
    "s_lshr_b32 s94, s61, 16\n"
    "s_sext_i32_i16 s95, s95\n"
    "s_mul_i32 s95, s95, s94\n"
    "s_ashr_i32 s95, s95, 16\n"
    "s_max_i32 s95, s95, -8\n"
    "s_min_i32 s95, s95, 4\n"
    "s_add_u32 s72, s77, s95\n"
    "s_cmp_lt_i32 s60, 0\n"
    "s_cselect_b32 s72, 0, s72\n"
    "v_rcp_f32 v232, v232\n"
    "v_rcp_f32 v233, v233\n"
    "v_fma_f32 v230, v230, v232, v247\n"
    "v_fmac_f32 v230, v231, v233\n"
    "v_cvt_pk_bf16_f32 v230, v230, v230\n"
    "s_add_u32 s66, s66, 16\n"
    "s_waitcnt lgkmcnt(0)\n"
    "ds_write_b16 v245, v230\n"
    "ds_read_b64 v[228:229], v246\n"
    "buffer_load_dwordx4 v[236:237], s[40:43], s67 idxen offen offset:0 nt lds\n"
    "buffer_load_dwordx4 v[238:239], s[40:43], s67 idxen offen offset:1024 nt lds\n"
    "s_branch .Ldin_G3\n"
    ".Ldin_A2_poll:\n"
    "s_sleep 1\n"
    "ds_read_b64 v[216:217], v246 offset:128\n"
    "s_waitcnt lgkmcnt(0)\n"
    "v_readlane_b32 s60, v216, s59\n"
    "v_readlane_b32 s61, v217, s59\n"
    "s_cmp_eq_u32 s60, -1\n"
    "s_cbranch_scc1 .Ldin_A2_poll\n"
    "s_branch .Ldin_A2_pr\n"
    ".Ldin_stub_L0:\n"
    "s_setpc_b64 s[34:35]\n"
    ".p2align 5\n"
    ".Ldin_L0:\n"
    "s_setprio 0\n"
    "s_cmp_lt_i32 s60, 0\n"
    "s_cbranch_scc1 .Ldin_E0\n"
    "ds_read_b128 v[144:147], v252 offset:8192\n"
    "ds_read_b128 v[148:151], v252 offset:8256\n"
    "ds_read_b128 v[152:155], v252 offset:16384\n"
    "ds_read_b128 v[156:159], v252 offset:16448\n"
    "s_add_u32 s76, s76, s73\n"
    "s_add_u32 m0, s68, 2048\n"
    "v_and_b32 v216, v248, v216\n"
    "v_and_b32 v217, v249, v217\n"
    "v_and_b32 v218, v248, v218\n"
    "v_and_b32 v219, v249, v219\n"
    "v_mfma_f32_16x16x16_bf16 v[176:179], v[216:217], v[96:97], v[32:35]\n"
    "v_and_b32 v220, v248, v220\n"
    "v_and_b32 v221, v249, v221\n"
    "v_and_b32 v222, v248, v222\n"
    "v_mfma_f32_16x16x16_bf16 v[180:183], v[218:219], v[98:99], v[36:39]\n"
    "v_exp_f32 v160, v160 clamp\n"
    "v_and_b32 v223, v249, v223\n"
    "v_mfma_f32_16x16x16_bf16 v[184:187], v[220:221], v[100:101], v[40:43]\n"
    "v_add_u32 v251, s66, v244\n"
    "v_cvt_pk_bf16_f32 v0, v176, v177\n"
    "v_cvt_pk_bf16_f32 v1, v178, v179\n"
    "s_waitcnt lgkmcnt(4)\n"
    "v_mfma_f32_16x16x16_bf16 v[188:191], v[222:223], v[102:103], v[44:47]\n"
    "v_exp_f32 v161, v161 clamp\n"
    "v_cvt_pk_bf16_f32 v2, v180, v181\n"
    "v_mfma_f32_4x4x4_16b_bf16 v[224:227], v[228:229], v[208:209], v[224:227] cbsz:4 abid:0\n"
    "v_cvt_pk_bf16_f32 v3, v182, v183\n"
    "v_mfma_f32_16x16x16_bf16 v[176:179], v[216:217], v[104:105], v[48:51]\n"
    "v_exp_f32 v162, v162 clamp\n"
    "v_cvt_pk_bf16_f32 v4, v184, v185\n"
    "v_mfma_f32_16x16x16_bf16 v[180:183], v[218:219], v[106:107], v[52:55]\n"
    "v_cvt_pk_bf16_f32 v5, v186, v187\n"
    "v_cvt_pk_bf16_f32 v6, v188, v189\n"
    "v_cvt_pk_bf16_f32 v7, v190, v191\n"
    "v_mfma_f32_16x16x16_bf16 v[184:187], v[220:221], v[108:109], v[56:59]\n"
    "v_exp_f32 v163, v163 clamp\n"
    "v_cvt_pk_bf16_f32 v8, v176, v177\n"
    "v_mfma_f32_4x4x4_16b_bf16 v[224:227], v[228:229], v[210:211], v[224:227] cbsz:4 abid:1\n"
    "v_cvt_pk_bf16_f32 v9, v178, v179\n"
    "v_mfma_f32_16x16x16_bf16 v[188:191], v[222:223], v[110:111], v[60:63]\n"
    "v_exp_f32 v164, v164 clamp\n"
    "v_cvt_pk_bf16_f32 v10, v180, v181\n"
    "v_mfma_f32_16x16x16_bf16 v[176:179], v[216:217], v[112:113], v[64:67]\n"
    "v_cvt_pk_bf16_f32 v11, v182, v183\n"
    "v_cvt_pk_bf16_f32 v12, v184, v185\n"
    "v_cvt_pk_bf16_f32 v13, v186, v187\n"
    "v_mfma_f32_16x16x16_bf16 v[180:183], v[218:219], v[114:115], v[68:71]\n"
    "v_exp_f32 v165, v165 clamp\n"
    "v_cvt_pk_bf16_f32 v14, v188, v189\n"
    "v_mfma_f32_4x4x4_16b_bf16 v[224:227], v[228:229], v[212:213], v[224:227] cbsz:4 abid:2\n"
    "v_cvt_pk_bf16_f32 v15, v190, v191\n"
    "v_mfma_f32_16x16x16_bf16 v[184:187], v[220:221], v[116:117], v[72:75]\n"
    "v_exp_f32 v166, v166 clamp\n"
    "v_cvt_pk_bf16_f32 v16, v176, v177\n"
    "v_mfma_f32_16x16x16_bf16 v[188:191], v[222:223], v[118:119], v[76:79]\n"
    "v_cvt_pk_bf16_f32 v17, v178, v179\n"
    "v_cvt_pk_bf16_f32 v18, v180, v181\n"
    "v_cvt_pk_bf16_f32 v19, v182, v183\n"
    "v_mfma_f32_16x16x16_bf16 v[176:179], v[216:217], v[120:121], v[80:83]\n"
    "v_exp_f32 v167, v167 clamp\n"
    "v_cvt_pk_bf16_f32 v20, v184, v185\n"
    "v_mfma_f32_4x4x4_16b_bf16 v[224:227], v[228:229], v[214:215], v[224:227] cbsz:4 abid:3\n"
    "v_cvt_pk_bf16_f32 v21, v186, v187\n"
    "v_mfma_f32_16x16x16_bf16 v[180:183], v[218:219], v[122:123], v[84:87]\n"
    "v_exp_f32 v168, v168 clamp\n"
    "v_cvt_pk_bf16_f32 v22, v188, v189\n"
    "v_mfma_f32_16x16x16_bf16 v[184:187], v[220:221], v[124:125], v[88:91]\n"
    "v_cvt_pk_bf16_f32 v23, v190, v191\n"
    "v_cvt_pk_bf16_f32 v24, v176, v177\n"
    "v_cvt_pk_bf16_f32 v25, v178, v179\n"
    "v_mfma_f32_16x16x16_bf16 v[188:191], v[222:223], v[126:127], v[92:95]\n"
    "v_exp_f32 v169, v169 clamp\n"
    "v_cvt_pk_bf16_f32 v26, v180, v181\n"
    "s_waitcnt lgkmcnt(0)\n"
    "v_mfma_f32_16x16x32_bf16 v[176:179], v[0:3], v[192:195], v[144:147]\n"
    "v_cvt_pk_bf16_f32 v27, v182, v183\n"
    "v_cvt_pk_bf16_f32 v28, v184, v185\n"
    "v_mfma_f32_16x16x32_bf16 v[176:179], v[4:7], v[196:199], v[176:179]\n"
    "v_cvt_pk_bf16_f32 v29, v186, v187\n"
    "v_cvt_pk_bf16_f32 v30, v188, v189\n"
    "v_mfma_f32_16x16x32_bf16 v[180:183], v[8:11], v[192:195], v[148:151]\n"
    "v_exp_f32 v170, v170 clamp\n"
    "v_mfma_f32_16x16x32_bf16 v[180:183], v[12:15], v[196:199], v[180:183]\n"
    "v_exp_f32 v171, v171 clamp\n"
    "v_mfma_f32_16x16x32_bf16 v[184:187], v[16:19], v[192:195], v[152:155]\n"
    "v_exp_f32 v172, v172 clamp\n"
    "v_mfma_f32_16x16x32_bf16 v[184:187], v[20:23], v[196:199], v[184:187]\n"
    "v_exp_f32 v173, v173 clamp\n"
    "v_cvt_pk_bf16_f32 v31, v190, v191\n"
    "v_mfma_f32_16x16x32_bf16 v[188:191], v[24:27], v[192:195], v[156:159]\n"
    "v_exp_f32 v174, v174 clamp\n"
    "v_mfma_f32_16x16x32_bf16 v[188:191], v[28:31], v[196:199], v[188:191]\n"
    "v_exp_f32 v175, v175 clamp\n"
    "s_waitcnt vmcnt(4)\n"
    "ds_read_b64_tr_b16 v[208:209], v240 offset:2048\n"
    "ds_read_b64_tr_b16 v[210:211], v241 offset:2048\n"
    "ds_read_b64_tr_b16 v[212:213], v242 offset:2048\n"
    "ds_read_b64_tr_b16 v[214:215], v243 offset:2048\n"
    "ds_read_u8 v236, v251 offset:0\n"
    "ds_read_u8 v238, v251 offset:1\n"
    "ds_read_b128 v[192:195], v234 offset:4096\n"
    "ds_read_b128 v[196:199], v235 offset:4096\n"
    "v_pk_fma_f32 v[160:161], v[160:161], s[70:71], v[128:129]\n"
    "v_pk_fma_f32 v[162:163], v[162:163], s[70:71], v[130:131]\n"
    "v_pk_add_f32 v[230:231], v[160:161], v[162:163]\n"
    "v_pk_mul_f32 v[232:233], v[160:161], v[162:163]\n"
    "v_pk_fma_f32 v[164:165], v[164:165], s[70:71], v[132:133]\n"
    "v_pk_fma_f32 v[230:231], v[230:231], v[164:165], v[232:233]\n"
    "v_pk_mul_f32 v[232:233], v[232:233], v[164:165]\n"
    "v_pk_fma_f32 v[166:167], v[166:167], s[70:71], v[134:135]\n"
    "v_pk_fma_f32 v[230:231], v[230:231], v[166:167], v[232:233]\n"
    "v_pk_mul_f32 v[232:233], v[232:233], v[166:167]\n"
    "v_pk_fma_f32 v[168:169], v[168:169], s[70:71], v[136:137]\n"
    "v_pk_fma_f32 v[230:231], v[230:231], v[168:169], v[232:233]\n"
    "v_pk_mul_f32 v[232:233], v[232:233], v[168:169]\n"
    "v_pk_fma_f32 v[170:171], v[170:171], s[70:71], v[138:139]\n"
    "v_pk_fma_f32 v[230:231], v[230:231], v[170:171], v[232:233]\n"
    "v_pk_mul_f32 v[232:233], v[232:233], v[170:171]\n"
    "v_pk_fma_f32 v[172:173], v[172:173], s[70:71], v[140:141]\n"
    "v_pk_fma_f32 v[230:231], v[230:231], v[172:173], v[232:233]\n"
    "v_pk_mul_f32 v[232:233], v[232:233], v[172:173]\n"
    "v_pk_fma_f32 v[174:175], v[174:175], s[70:71], v[142:143]\n"
    "v_pk_fma_f32 v[230:231], v[230:231], v[174:175], v[232:233]\n"
    "v_pk_mul_f32 v[232:233], v[232:233], v[174:175]\n"
    "v_rcp_f32 v232, v232\n"
    "v_rcp_f32 v233, v233\n"
    "v_fma_f32 v230, v230, v232, v247\n"
    "v_fmac_f32 v230, v231, v233\n"
    "v_cvt_pk_bf16_f32 v230, v230, v230\n"
    "s_add_u32 s66, s66, 16\n"
    "s_waitcnt lgkmcnt(0)\n"
    "ds_write_b16 v245, v230\n"
    "ds_read_b64 v[228:229], v246\n"
    "s_lshl1_add_u32 s76, s72, s76\n"
    "buffer_load_dwordx4 v[236:237], s[40:43], s67 idxen offen offset:0 nt lds\n"
    "buffer_load_dwordx4 v[238:239], s[40:43], s67 idxen offen offset:1024 nt lds\n"
    "s_mov_b32 s81, s64\n"
    "s_lshr_b32 s63, s65, 6\n"
    "s_mov_b32 s80, s83\n"
    "s_add_u32 s62, s62, 256\n"
    "s_bitcmp1_b32 s60, 30\n"
    "s_cbranch_scc1 .Ldin_A1s\n"
    ".Ldin_A1:\n"
    "s_cbranch_vccz .Ldin_A1_np\n"
    "s_setprio 1\n"
    ".Ldin_A1_np:\n"
    "v_mfma_f32_16x16x32_bf16 v[160:163], v[0:3], v[192:195], v[144:147]\n"
    "v_exp_f32 v176, v176 clamp\n"
    "v_mfma_f32_16x16x32_bf16 v[160:163], v[4:7], v[196:199], v[160:163]\n"
    "v_exp_f32 v177, v177 clamp\n"
    "v_mfma_f32_16x16x32_bf16 v[164:167], v[8:11], v[192:195], v[148:151]\n"
    "v_exp_f32 v178, v178 clamp\n"
    "v_mfma_f32_16x16x32_bf16 v[164:167], v[12:15], v[196:199], v[164:167]\n"
    "v_exp_f32 v179, v179 clamp\n"
    "v_mfma_f32_16x16x32_bf16 v[168:171], v[16:19], v[192:195], v[152:155]\n"
    "v_exp_f32 v180, v180 clamp\n"
    "v_mfma_f32_16x16x32_bf16 v[168:171], v[20:23], v[196:199], v[168:171]\n"
    "v_exp_f32 v181, v181 clamp\n"
    "v_mfma_f32_16x16x32_bf16 v[172:175], v[24:27], v[192:195], v[156:159]\n"
    "v_exp_f32 v182, v182 clamp\n"
    "v_mfma_f32_16x16x32_bf16 v[172:175], v[28:31], v[196:199], v[172:175]\n"
    "v_exp_f32 v183, v183 clamp\n"
    "v_exp_f32 v184, v184 clamp\n"
    "s_mov_b32 m0, s62\n"
    "ds_read_b64 v[216:217], v246 offset:128\n"
    "v_exp_f32 v185, v185 clamp\n"
    "v_add_u32 v251, s66, v244\n"
    "s_waitcnt lgkmcnt(1)\n"
    "v_mfma_f32_4x4x4_16b_bf16 v[224:227], v[228:229], v[200:201], v[224:227] cbsz:4 abid:0\n"
    "v_exp_f32 v186, v186 clamp\n"
    "v_exp_f32 v187, v187 clamp\n"
    "v_mfma_f32_4x4x4_16b_bf16 v[224:227], v[228:229], v[202:203], v[224:227] cbsz:4 abid:1\n"
    "v_exp_f32 v188, v188 clamp\n"
    "v_exp_f32 v189, v189 clamp\n"
    "v_mfma_f32_4x4x4_16b_bf16 v[224:227], v[228:229], v[204:205], v[224:227] cbsz:4 abid:2\n"
    "v_exp_f32 v190, v190 clamp\n"
    "v_exp_f32 v191, v191 clamp\n"
    "v_mfma_f32_4x4x4_16b_bf16 v[224:227], v[228:229], v[206:207], v[224:227] cbsz:4 abid:3\n"
    "s_waitcnt vmcnt(4)\n"
    "s_waitcnt lgkmcnt(0)\n"
    "v_readlane_b32 s60, v216, s59\n"
    "v_readlane_b32 s61, v217, s59\n"
    "ds_read_b64_tr_b16 v[200:201], v240 offset:4096\n"
    "ds_read_b64_tr_b16 v[202:203], v241 offset:4096\n"
    "ds_read_b64_tr_b16 v[204:205], v242 offset:4096\n"
    "ds_read_b64_tr_b16 v[206:207], v243 offset:4096\n"
    "ds_read_u8 v236, v251 offset:0\n"
    "ds_read_u8 v238, v251 offset:1\n"
    "ds_read_b128 v[192:195], v234 offset:6144\n"
    "ds_read_b128 v[196:199], v235 offset:6144\n"
    "s_cmp_eq_u32 s60, -1\n"
    "s_cbranch_scc1 .Ldin_A1_poll\n"
    ".Ldin_A1_pr:\n"
    "s_lshr_b32 s94, s60, 12\n"
    "s_and_b32 s94, s94, 0x1ff0\n"
    "v_xor_b32 v252, s94, v250\n"
    "v_pk_add_f32 v[230:231], v[224:225], v[226:227]\n"
    "v_add_f32 v230, v230, v231\n"
    "ds_write_addtid_b32 v230 offset:49152\n"
    "ds_read_b128 v[216:219], v252 offset:0\n"
    "ds_read_b128 v[220:223], v252 offset:64\n"
    "v_mov_b64 v[224:225], 0\n"
    "v_mov_b64_e64 v[226:227], 0\n"
    "v_pk_fma_f32 v[176:177], v[176:177], s[70:71], v[128:129]\n"
    "v_pk_fma_f32 v[178:179], v[178:179], s[70:71], v[130:131]\n"
    "v_pk_add_f32 v[230:231], v[176:177], v[178:179]\n"
    "v_pk_mul_f32 v[232:233], v[176:177], v[178:179]\n"
    "v_pk_fma_f32 v[180:181], v[180:181], s[70:71], v[132:133]\n"
    "v_pk_fma_f32 v[230:231], v[230:231], v[180:181], v[232:233]\n"
    "v_pk_mul_f32 v[232:233], v[232:233], v[180:181]\n"
    "v_pk_fma_f32 v[182:183], v[182:183], s[70:71], v[134:135]\n"
    "v_pk_fma_f32 v[230:231], v[230:231], v[182:183], v[232:233]\n"
    "v_pk_mul_f32 v[232:233], v[232:233], v[182:183]\n"
    "v_pk_fma_f32 v[184:185], v[184:185], s[70:71], v[136:137]\n"
    "v_pk_fma_f32 v[230:231], v[230:231], v[184:185], v[232:233]\n"
    "v_pk_mul_f32 v[232:233], v[232:233], v[184:185]\n"
    "v_pk_fma_f32 v[186:187], v[186:187], s[70:71], v[138:139]\n"
    "v_pk_fma_f32 v[230:231], v[230:231], v[186:187], v[232:233]\n"
    "v_pk_mul_f32 v[232:233], v[232:233], v[186:187]\n"
    "v_pk_fma_f32 v[188:189], v[188:189], s[70:71], v[140:141]\n"
    "v_pk_fma_f32 v[230:231], v[230:231], v[188:189], v[232:233]\n"
    "v_pk_mul_f32 v[232:233], v[232:233], v[188:189]\n"
    "v_pk_fma_f32 v[190:191], v[190:191], s[70:71], v[142:143]\n"
    "v_pk_fma_f32 v[230:231], v[230:231], v[190:191], v[232:233]\n"
    "v_pk_mul_f32 v[232:233], v[232:233], v[190:191]\n"
    "s_add_u32 m0, s68, 4096\n"
    "s_and_b32 s64, s60, 0xffff\n"
    "s_bfe_u32 s94, s60, 0x30013\n"
    "s_bfe_u32 s95, s60, 0x50019\n"
    "s_mul_i32 s94, s94, s56\n"
    "s_lshl4_add_u32 s83, s95, s64\n"
    "s_add_u32 s65, s58, s94\n"
    "s_add_u32 s82, s64, 64\n"
    "s_add_u32 s58, s58, s57\n"
    "s_add_u32 s59, s59, 1\n"
    "s_sub_u32 s95, s61, s76\n"
    "s_lshr_b32 s94, s61, 16\n"
    "s_sext_i32_i16 s95, s95\n"
    "s_mul_i32 s95, s95, s94\n"
    "s_ashr_i32 s95, s95, 16\n"
    "s_max_i32 s95, s95, -8\n"
    "s_min_i32 s95, s95, 4\n"
    "s_add_u32 s72, s77, s95\n"
    "s_cmp_lt_i32 s60, 0\n"
    "s_cselect_b32 s72, 0, s72\n"
    "v_rcp_f32 v232, v232\n"
    "v_rcp_f32 v233, v233\n"
    "v_fma_f32 v230, v230, v232, v247\n"
    "v_fmac_f32 v230, v231, v233\n"
    "v_cvt_pk_bf16_f32 v230, v230, v230\n"
    "s_add_u32 s66, s66, 16\n"
    "s_waitcnt lgkmcnt(0)\n"
    "ds_write_b16 v245, v230\n"
    "ds_read_b64 v[228:229], v246\n"
    "buffer_load_dwordx4 v[236:237], s[40:43], s67 idxen offen offset:0 nt lds\n"
    "buffer_load_dwordx4 v[238:239], s[40:43], s67 idxen offen offset:1024 nt lds\n"
    "s_branch .Ldin_G2\n"
    ".Ldin_A1_poll:\n"
    "s_sleep 1\n"
    "ds_read_b64 v[216:217], v246 offset:128\n"
    "s_waitcnt lgkmcnt(0)\n"
    "v_readlane_b32 s60, v216, s59\n"
    "v_readlane_b32 s61, v217, s59\n"
    "s_cmp_eq_u32 s60, -1\n"
    "s_cbranch_scc1 .Ldin_A1_poll\n"
    "s_branch .Ldin_A1_pr\n"
    ".Ldin_stub_L3:\n"
    "s_setpc_b64 s[34:35]\n"
    ".p2align 5\n"
    ".Ldin_L3:\n"
    "s_setprio 0\n"
    "s_cmp_lt_i32 s60, 0\n"
    "s_cbranch_scc1 .Ldin_E1\n"
    "ds_read_b128 v[144:147], v252 offset:8192\n"
    "ds_read_b128 v[148:151], v252 offset:8256\n"
    "ds_read_b128 v[152:155], v252 offset:16384\n"
    "ds_read_b128 v[156:159], v252 offset:16448\n"
    "s_add_u32 s76, s76, s73\n"
    "s_add_u32 m0, s68, 0\n"
    "v_and_b32 v216, v248, v216\n"
    "v_and_b32 v217, v249, v217\n"
    "v_and_b32 v218, v248, v218\n"
    "v_and_b32 v219, v249, v219\n"
    "v_mfma_f32_16x16x16_bf16 v[160:163], v[216:217], v[96:97], v[32:35]\n"
    "v_and_b32 v220, v248, v220\n"
    "v_and_b32 v221, v249, v221\n"
    "v_and_b32 v222, v248, v222\n"
    "v_mfma_f32_16x16x16_bf16 v[164:167], v[218:219], v[98:99], v[36:39]\n"
    "v_exp_f32 v176, v176 clamp\n"
    "v_and_b32 v223, v249, v223\n"
    "v_mfma_f32_16x16x16_bf16 v[168:171], v[220:221], v[100:101], v[40:43]\n"
    "v_add_u32_e64 v251, s66, v244\n"
    "v_cvt_pk_bf16_f32 v0, v160, v161\n"
    "v_cvt_pk_bf16_f32 v1, v162, v163\n"
    "s_waitcnt lgkmcnt(4)\n"
    "v_mfma_f32_16x16x16_bf16 v[172:175], v[222:223], v[102:103], v[44:47]\n"
    "v_exp_f32 v177, v177 clamp\n"
    "v_cvt_pk_bf16_f32 v2, v164, v165\n"
    "v_mfma_f32_4x4x4_16b_bf16 v[224:227], v[228:229], v[200:201], v[224:227] cbsz:4 abid:0\n"
    "v_cvt_pk_bf16_f32 v3, v166, v167\n"
    "v_mfma_f32_16x16x16_bf16 v[160:163], v[216:217], v[104:105], v[48:51]\n"
    "v_exp_f32 v178, v178 clamp\n"
    "v_cvt_pk_bf16_f32 v4, v168, v169\n"
    "v_mfma_f32_16x16x16_bf16 v[164:167], v[218:219], v[106:107], v[52:55]\n"
    "v_cvt_pk_bf16_f32 v5, v170, v171\n"
    "v_cvt_pk_bf16_f32 v6, v172, v173\n"
    "v_cvt_pk_bf16_f32 v7, v174, v175\n"
    "v_mfma_f32_16x16x16_bf16 v[168:171], v[220:221], v[108:109], v[56:59]\n"
    "v_exp_f32 v179, v179 clamp\n"
    "v_cvt_pk_bf16_f32 v8, v160, v161\n"
    "v_mfma_f32_4x4x4_16b_bf16 v[224:227], v[228:229], v[202:203], v[224:227] cbsz:4 abid:1\n"
    "v_cvt_pk_bf16_f32 v9, v162, v163\n"
    "v_mfma_f32_16x16x16_bf16 v[172:175], v[222:223], v[110:111], v[60:63]\n"
    "v_exp_f32 v180, v180 clamp\n"
    "v_cvt_pk_bf16_f32 v10, v164, v165\n"
    "v_mfma_f32_16x16x16_bf16 v[160:163], v[216:217], v[112:113], v[64:67]\n"
    "v_cvt_pk_bf16_f32 v11, v166, v167\n"
    "v_cvt_pk_bf16_f32 v12, v168, v169\n"
    "v_cvt_pk_bf16_f32 v13, v170, v171\n"
    "v_mfma_f32_16x16x16_bf16 v[164:167], v[218:219], v[114:115], v[68:71]\n"
    "v_exp_f32 v181, v181 clamp\n"
    "v_cvt_pk_bf16_f32 v14, v172, v173\n"
    "v_mfma_f32_4x4x4_16b_bf16 v[224:227], v[228:229], v[204:205], v[224:227] cbsz:4 abid:2\n"
    "v_cvt_pk_bf16_f32 v15, v174, v175\n"
    "v_mfma_f32_16x16x16_bf16 v[168:171], v[220:221], v[116:117], v[72:75]\n"
    "v_exp_f32 v182, v182 clamp\n"
    "v_cvt_pk_bf16_f32 v16, v160, v161\n"
    "v_mfma_f32_16x16x16_bf16 v[172:175], v[222:223], v[118:119], v[76:79]\n"
    "v_cvt_pk_bf16_f32 v17, v162, v163\n"
    "v_cvt_pk_bf16_f32 v18, v164, v165\n"
    "v_cvt_pk_bf16_f32 v19, v166, v167\n"
    "v_mfma_f32_16x16x16_bf16 v[160:163], v[216:217], v[120:121], v[80:83]\n"
    "v_exp_f32 v183, v183 clamp\n"
    "v_cvt_pk_bf16_f32 v20, v168, v169\n"
    "v_mfma_f32_4x4x4_16b_bf16 v[224:227], v[228:229], v[206:207], v[224:227] cbsz:4 abid:3\n"
    "v_cvt_pk_bf16_f32 v21, v170, v171\n"
    "v_mfma_f32_16x16x16_bf16 v[164:167], v[218:219], v[122:123], v[84:87]\n"
    "v_exp_f32 v184, v184 clamp\n"
    "v_cvt_pk_bf16_f32 v22, v172, v173\n"
    "v_mfma_f32_16x16x16_bf16 v[168:171], v[220:221], v[124:125], v[88:91]\n"
    "v_cvt_pk_bf16_f32 v23, v174, v175\n"
    "v_cvt_pk_bf16_f32 v24, v160, v161\n"
    "v_cvt_pk_bf16_f32 v25, v162, v163\n"
    "v_mfma_f32_16x16x16_bf16 v[172:175], v[222:223], v[126:127], v[92:95]\n"
    "v_exp_f32 v185, v185 clamp\n"
    "v_cvt_pk_bf16_f32 v26, v164, v165\n"
    "s_waitcnt lgkmcnt(0)\n"
    "v_mfma_f32_16x16x32_bf16 v[160:163], v[0:3], v[192:195], v[144:147]\n"
    "v_cvt_pk_bf16_f32 v27, v166, v167\n"
    "v_cvt_pk_bf16_f32 v28, v168, v169\n"
    "v_mfma_f32_16x16x32_bf16 v[160:163], v[4:7], v[196:199], v[160:163]\n"
    "v_cvt_pk_bf16_f32 v29, v170, v171\n"
    "v_cvt_pk_bf16_f32 v30, v172, v173\n"
    "v_mfma_f32_16x16x32_bf16 v[164:167], v[8:11], v[192:195], v[148:151]\n"
    "v_exp_f32 v186, v186 clamp\n"
    "v_mfma_f32_16x16x32_bf16 v[164:167], v[12:15], v[196:199], v[164:167]\n"
    "v_exp_f32 v187, v187 clamp\n"
    "v_mfma_f32_16x16x32_bf16 v[168:171], v[16:19], v[192:195], v[152:155]\n"
    "v_exp_f32 v188, v188 clamp\n"
    "v_mfma_f32_16x16x32_bf16 v[168:171], v[20:23], v[196:199], v[168:171]\n"
    "v_exp_f32 v189, v189 clamp\n"
    "v_cvt_pk_bf16_f32 v31, v174, v175\n"
    "v_mfma_f32_16x16x32_bf16 v[172:175], v[24:27], v[192:195], v[156:159]\n"
    "v_exp_f32 v190, v190 clamp\n"
    "v_mfma_f32_16x16x32_bf16 v[172:175], v[28:31], v[196:199], v[172:175]\n"
    "v_exp_f32 v191, v191 clamp\n"
    "s_waitcnt vmcnt(4)\n"
    "ds_read_b64_tr_b16 v[200:201], v240 offset:0\n"
    "ds_read_b64_tr_b16 v[202:203], v241 offset:0\n"
    "ds_read_b64_tr_b16 v[204:205], v242 offset:0\n"
    "ds_read_b64_tr_b16 v[206:207], v243 offset:0\n"
    "ds_read_u8 v236, v251 offset:0\n"
    "ds_read_u8 v238, v251 offset:1\n"
    "ds_read_b128 v[192:195], v234 offset:2048\n"
    "ds_read_b128 v[196:199], v235 offset:2048\n"
    "v_pk_fma_f32 v[176:177], v[176:177], s[70:71], v[128:129]\n"
    "v_pk_fma_f32 v[178:179], v[178:179], s[70:71], v[130:131]\n"
    "v_pk_add_f32 v[230:231], v[176:177], v[178:179]\n"
    "v_pk_mul_f32 v[232:233], v[176:177], v[178:179]\n"
    "v_pk_fma_f32 v[180:181], v[180:181], s[70:71], v[132:133]\n"
    "v_pk_fma_f32 v[230:231], v[230:231], v[180:181], v[232:233]\n"
    "v_pk_mul_f32 v[232:233], v[232:233], v[180:181]\n"
    "v_pk_fma_f32 v[182:183], v[182:183], s[70:71], v[134:135]\n"
    "v_pk_fma_f32 v[230:231], v[230:231], v[182:183], v[232:233]\n"
    "v_pk_mul_f32 v[232:233], v[232:233], v[182:183]\n"
    "v_pk_fma_f32 v[184:185], v[184:185], s[70:71], v[136:137]\n"
    "v_pk_fma_f32 v[230:231], v[230:231], v[184:185], v[232:233]\n"
    "v_pk_mul_f32 v[232:233], v[232:233], v[184:185]\n"
    "v_pk_fma_f32 v[186:187], v[186:187], s[70:71], v[138:139]\n"
    "v_pk_fma_f32 v[230:231], v[230:231], v[186:187], v[232:233]\n"
    "v_pk_mul_f32 v[232:233], v[232:233], v[186:187]\n"
    "v_pk_fma_f32 v[188:189], v[188:189], s[70:71], v[140:141]\n"
    "v_pk_fma_f32 v[230:231], v[230:231], v[188:189], v[232:233]\n"
    "v_pk_mul_f32 v[232:233], v[232:233], v[188:189]\n"
    "v_pk_fma_f32 v[190:191], v[190:191], s[70:71], v[142:143]\n"
    "v_pk_fma_f32 v[230:231], v[230:231], v[190:191], v[232:233]\n"
    "v_pk_mul_f32 v[232:233], v[232:233], v[190:191]\n"
    "v_rcp_f32 v232, v232\n"
    "v_rcp_f32 v233, v233\n"
    "v_fma_f32 v230, v230, v232, v247\n"
    "v_fmac_f32 v230, v231, v233\n"
    "v_cvt_pk_bf16_f32 v230, v230, v230\n"
    "s_add_u32 s66, s66, 16\n"
    "s_waitcnt lgkmcnt(0)\n"
    "ds_write_b16 v245, v230\n"
    "ds_read_b64 v[228:229], v246\n"
    "s_lshl1_add_u32 s76, s72, s76\n"
    "buffer_load_dwordx4 v[236:237], s[40:43], s67 idxen offen offset:0 nt lds\n"
    "buffer_load_dwordx4 v[238:239], s[40:43], s67 idxen offen offset:1024 nt lds\n"
    "s_mov_b32 s81, s64\n"
    "s_lshr_b32 s63, s65, 6\n"
    "s_mov_b32 s80, s83\n"
    "s_add_u32 s62, s62, 256\n"
    "s_bitcmp1_b32 s60, 30\n"
    "s_cbranch_scc1 .Ldin_A0s\n"
    "s_branch .Ldin_A0\n"
    ".Ldin_stub_E0:\n"
    "s_setpc_b64 s[34:35]\n"
    ".p2align 5\n"
    ".Ldin_E0:\n"
    "v_exp_f32 v160, v160 clamp\n"
    "v_exp_f32 v161, v161 clamp\n"
    "v_exp_f32 v162, v162 clamp\n"
    "v_exp_f32 v163, v163 clamp\n"
    "v_exp_f32 v164, v164 clamp\n"
    "v_exp_f32 v165, v165 clamp\n"
    "v_exp_f32 v166, v166 clamp\n"
    "v_exp_f32 v167, v167 clamp\n"
    "s_add_u32 m0, s62, 256\n"
    "s_waitcnt lgkmcnt(0)\n"
    "v_mfma_f32_4x4x4_16b_bf16 v[224:227], v[228:229], v[208:209], v[224:227] cbsz:4 abid:0\n"
    "v_exp_f32 v168, v168 clamp\n"
    "v_exp_f32 v169, v169 clamp\n"
    "v_mfma_f32_4x4x4_16b_bf16 v[224:227], v[228:229], v[210:211], v[224:227] cbsz:4 abid:1\n"
    "v_exp_f32 v170, v170 clamp\n"
    "v_exp_f32 v171, v171 clamp\n"
    "v_mfma_f32_4x4x4_16b_bf16 v[224:227], v[228:229], v[212:213], v[224:227] cbsz:4 abid:2\n"
    "v_exp_f32 v172, v172 clamp\n"
    "v_exp_f32 v173, v173 clamp\n"
    "v_mfma_f32_4x4x4_16b_bf16 v[224:227], v[228:229], v[214:215], v[224:227] cbsz:4 abid:3\n"
    "v_exp_f32 v174, v174 clamp\n"
    "v_exp_f32 v175, v175 clamp\n"
    "s_nop 0\n"
    "v_pk_fma_f32 v[160:161], v[160:161], s[70:71], v[128:129]\n"
    "v_pk_fma_f32 v[162:163], v[162:163], s[70:71], v[130:131]\n"
    "v_pk_add_f32 v[230:231], v[160:161], v[162:163]\n"
    "v_pk_mul_f32 v[232:233], v[160:161], v[162:163]\n"
    "v_pk_fma_f32 v[164:165], v[164:165], s[70:71], v[132:133]\n"
    "v_pk_fma_f32 v[230:231], v[230:231], v[164:165], v[232:233]\n"
    "v_pk_mul_f32 v[232:233], v[232:233], v[164:165]\n"
    "v_pk_fma_f32 v[166:167], v[166:167], s[70:71], v[134:135]\n"
    "v_pk_fma_f32 v[230:231], v[230:231], v[166:167], v[232:233]\n"
    "v_pk_mul_f32 v[232:233], v[232:233], v[166:167]\n"
    "v_pk_fma_f32 v[168:169], v[168:169], s[70:71], v[136:137]\n"
    "v_pk_fma_f32 v[230:231], v[230:231], v[168:169], v[232:233]\n"
    "v_pk_mul_f32 v[232:233], v[232:233], v[168:169]\n"
    "v_pk_fma_f32 v[170:171], v[170:171], s[70:71], v[138:139]\n"
    "v_pk_fma_f32 v[230:231], v[230:231], v[170:171], v[232:233]\n"
    "v_pk_mul_f32 v[232:233], v[232:233], v[170:171]\n"
    "v_pk_fma_f32 v[172:173], v[172:173], s[70:71], v[140:141]\n"
    "v_pk_fma_f32 v[230:231], v[230:231], v[172:173], v[232:233]\n"
    "v_pk_mul_f32 v[232:233], v[232:233], v[172:173]\n"
    "v_pk_fma_f32 v[174:175], v[174:175], s[70:71], v[142:143]\n"
    "v_pk_fma_f32 v[230:231], v[230:231], v[174:175], v[232:233]\n"
    "v_pk_mul_f32 v[232:233], v[232:233], v[174:175]\n"
    "v_rcp_f32 v232, v232\n"
    "v_rcp_f32 v233, v233\n"
    "v_fma_f32 v230, v230, v232, v247\n"
    "v_fmac_f32_e64 v230, v231, v233\n"
    "v_cvt_pk_bf16_f32 v230, v230, v230\n"
    "ds_write_b16 v245, v230\n"
    "ds_read_b64 v[228:229], v246\n"
    ".Ldin_drain0:\n"
    "v_mbcnt_lo_u32_b32 v0, -1, 0\n"
    "v_mbcnt_hi_u32_b32 v0, -1, v0\n"
    "v_lshrrev_b32 v1, 3, v0\n"
    "v_lshlrev_b32 v2, 2, v1\n"
    "ds_read_b32 v3, v246 offset:128\n"
    "s_lshl_b32 s94, s59, 8\n"
    "s_sub_u32 s94, s62, s94\n"
    "s_waitcnt lgkmcnt(0)\n"
    "ds_bpermute_b32 v3, v2, v3\n"
    "v_mfma_f32_4x4x4_16b_bf16 v[224:227], v[228:229], v[200:201], v[224:227] cbsz:4 abid:0\n"
    "v_lshlrev_b32 v2, 5, v0\n"
    "v_mul_lo_u32 v1, v1, s79\n"
    "v_mfma_f32_4x4x4_16b_bf16 v[224:227], v[228:229], v[202:203], v[224:227] cbsz:4 abid:1\n"
    "v_add_u32 v2, s94, v2\n"
    "v_and_b32 v0, 7, v0\n"
    "v_mfma_f32_4x4x4_16b_bf16 v[224:227], v[228:229], v[204:205], v[224:227] cbsz:4 abid:2\n"
    "v_add_u32 v1, s78, v1\n"
    "v_lshlrev_b32 v0, 4, v0\n"
    "v_mfma_f32_4x4x4_16b_bf16 v[224:227], v[228:229], v[206:207], v[224:227] cbsz:4 abid:3\n"
    "s_waitcnt lgkmcnt(0)\n"
    "v_bfe_u32 v4, v3, 19, 3\n"
    "v_cmp_le_i32 vcc, 0, v3\n"
    "v_add_u32 v1, v1, v4\n"
    "s_mov_b32 s70, s69\n"
    "s_mov_b32 s71, s69\n"
    "v_pk_add_f32 v[232:233], v[224:225], v[226:227]\n"
    "v_add_f32_e64 v232, v232, v233\n"
    "ds_write_addtid_b32 v232 offset:49152\n"
    "ds_read_b128 v[8:11], v2 offset:49920\n"
    "ds_read_b128 v[12:15], v2 offset:49936\n"
    "v_lshl_add_u32 v1, v1, 7, v0\n"
    "v_bfrev_b32 v4, 1\n"
    "v_cndmask_b32 v1, v4, v1, vcc\n"
    "s_waitcnt vmcnt(0) lgkmcnt(0)\n"
    "v_pk_mul_f32 v[8:9], v[8:9], s[70:71]\n"
    "v_pk_mul_f32 v[10:11], v[10:11], s[70:71]\n"
    "v_pk_mul_f32 v[12:13], v[12:13], s[70:71]\n"
    "v_pk_mul_f32 v[14:15], v[14:15], s[70:71]\n"
    "v_cvt_pk_bf16_f32 v16, v8, v9\n"
    "v_cvt_pk_bf16_f32 v17, v10, v11\n"
    "v_cvt_pk_bf16_f32 v18, v12, v13\n"
    "v_cvt_pk_bf16_f32 v19, v14, v15\n"
    "buffer_store_dwordx4 v[16:19], v1, s[52:55], 0 offen sc1\n"
    "s_endpgm\n"
    ".Ldin_stub_E1:\n"
    "s_setpc_b64 s[34:35]\n"
    ".p2align 5\n"
    ".Ldin_E1:\n"
    "v_exp_f32 v176, v176 clamp\n"
    "v_exp_f32 v177, v177 clamp\n"
    "v_exp_f32 v178, v178 clamp\n"
    "v_exp_f32 v179, v179 clamp\n"
    "v_exp_f32 v180, v180 clamp\n"
    "v_exp_f32 v181, v181 clamp\n"
    "v_exp_f32 v182, v182 clamp\n"
    "v_exp_f32 v183, v183 clamp\n"
    "s_add_u32 m0, s62, 256\n"
    "s_waitcnt lgkmcnt(0)\n"
    "v_mfma_f32_4x4x4_16b_bf16 v[224:227], v[228:229], v[200:201], v[224:227] cbsz:4 abid:0\n"
    "v_exp_f32 v184, v184 clamp\n"
    "v_exp_f32 v185, v185 clamp\n"
    "v_mfma_f32_4x4x4_16b_bf16 v[224:227], v[228:229], v[202:203], v[224:227] cbsz:4 abid:1\n"
    "v_exp_f32 v186, v186 clamp\n"
    "v_exp_f32 v187, v187 clamp\n"
    "v_mfma_f32_4x4x4_16b_bf16 v[224:227], v[228:229], v[204:205], v[224:227] cbsz:4 abid:2\n"
    "v_exp_f32 v188, v188 clamp\n"
    "v_exp_f32 v189, v189 clamp\n"
    "v_mfma_f32_4x4x4_16b_bf16 v[224:227], v[228:229], v[206:207], v[224:227] cbsz:4 abid:3\n"
    "v_exp_f32 v190, v190 clamp\n"
    "v_exp_f32 v191, v191 clamp\n"
    "s_nop 0\n"
    "v_pk_fma_f32 v[176:177], v[176:177], s[70:71], v[128:129]\n"
    "v_pk_fma_f32 v[178:179], v[178:179], s[70:71], v[130:131]\n"
    "v_pk_add_f32 v[230:231], v[176:177], v[178:179]\n"
    "v_pk_mul_f32 v[232:233], v[176:177], v[178:179]\n"
    "v_pk_fma_f32 v[180:181], v[180:181], s[70:71], v[132:133]\n"
    "v_pk_fma_f32 v[230:231], v[230:231], v[180:181], v[232:233]\n"
    "v_pk_mul_f32 v[232:233], v[232:233], v[180:181]\n"
    "v_pk_fma_f32 v[182:183], v[182:183], s[70:71], v[134:135]\n"
    "v_pk_fma_f32 v[230:231], v[230:231], v[182:183], v[232:233]\n"
    "v_pk_mul_f32 v[232:233], v[232:233], v[182:183]\n"
    "v_pk_fma_f32 v[184:185], v[184:185], s[70:71], v[136:137]\n"
    "v_pk_fma_f32 v[230:231], v[230:231], v[184:185], v[232:233]\n"
    "v_pk_mul_f32 v[232:233], v[232:233], v[184:185]\n"
    "v_pk_fma_f32 v[186:187], v[186:187], s[70:71], v[138:139]\n"
    "v_pk_fma_f32 v[230:231], v[230:231], v[186:187], v[232:233]\n"
    "v_pk_mul_f32 v[232:233], v[232:233], v[186:187]\n"
    "v_pk_fma_f32 v[188:189], v[188:189], s[70:71], v[140:141]\n"
    "v_pk_fma_f32 v[230:231], v[230:231], v[188:189], v[232:233]\n"
    "v_pk_mul_f32 v[232:233], v[232:233], v[188:189]\n"
    "v_pk_fma_f32 v[190:191], v[190:191], s[70:71], v[142:143]\n"
    "v_pk_fma_f32 v[230:231], v[230:231], v[190:191], v[232:233]\n"
    "v_pk_mul_f32 v[232:233], v[232:233], v[190:191]\n"
    "v_rcp_f32 v232, v232\n"
    "v_rcp_f32 v233, v233\n"
    "v_fma_f32 v230, v230, v232, v247\n"
    "v_fmac_f32_e64 v230, v231, v233\n"
    "v_cvt_pk_bf16_f32 v230, v230, v230\n"
    "ds_write_b16 v245, v230\n"
    "ds_read_b64 v[228:229], v246\n"
    ".Ldin_drain1:\n"
    "v_mbcnt_lo_u32_b32 v0, -1, 0\n"
    "v_mbcnt_hi_u32_b32 v0, -1, v0\n"
    "v_lshrrev_b32 v1, 3, v0\n"
    "v_lshlrev_b32 v2, 2, v1\n"
    "ds_read_b32 v3, v246 offset:128\n"
    "s_lshl_b32 s94, s59, 8\n"
    "s_sub_u32 s94, s62, s94\n"
    "s_waitcnt lgkmcnt(0)\n"
    "ds_bpermute_b32 v3, v2, v3\n"
    "v_mfma_f32_4x4x4_16b_bf16 v[224:227], v[228:229], v[208:209], v[224:227] cbsz:4 abid:0\n"
    "v_lshlrev_b32 v2, 5, v0\n"
    "v_mul_lo_u32 v1, v1, s79\n"
    "v_mfma_f32_4x4x4_16b_bf16 v[224:227], v[228:229], v[210:211], v[224:227] cbsz:4 abid:1\n"
    "v_add_u32 v2, s94, v2\n"
    "v_and_b32 v0, 7, v0\n"
    "v_mfma_f32_4x4x4_16b_bf16 v[224:227], v[228:229], v[212:213], v[224:227] cbsz:4 abid:2\n"
    "v_add_u32 v1, s78, v1\n"
    "v_lshlrev_b32 v0, 4, v0\n"
    "v_mfma_f32_4x4x4_16b_bf16 v[224:227], v[228:229], v[214:215], v[224:227] cbsz:4 abid:3\n"
    "s_waitcnt lgkmcnt(0)\n"
    "v_bfe_u32 v4, v3, 19, 3\n"
    "v_cmp_le_i32 vcc, 0, v3\n"
    "v_add_u32 v1, v1, v4\n"
    "s_mov_b32 s70, s69\n"
    "s_mov_b32 s71, s69\n"
    "v_pk_add_f32 v[232:233], v[224:225], v[226:227]\n"
    "v_add_f32_e64 v232, v232, v233\n"
    "ds_write_addtid_b32 v232 offset:49152\n"
    "ds_read_b128 v[8:11], v2 offset:49920\n"
    "ds_read_b128 v[12:15], v2 offset:49936\n"
    "v_lshl_add_u32 v1, v1, 7, v0\n"
    "v_bfrev_b32 v4, 1\n"
    "v_cndmask_b32 v1, v4, v1, vcc\n"
    "s_waitcnt vmcnt(0) lgkmcnt(0)\n"
    "v_pk_mul_f32 v[8:9], v[8:9], s[70:71]\n"
    "v_pk_mul_f32 v[10:11], v[10:11], s[70:71]\n"
    "v_pk_mul_f32 v[12:13], v[12:13], s[70:71]\n"
    "v_pk_mul_f32 v[14:15], v[14:15], s[70:71]\n"
    "v_cvt_pk_bf16_f32 v16, v8, v9\n"
    "v_cvt_pk_bf16_f32 v17, v10, v11\n"
    "v_cvt_pk_bf16_f32 v18, v12, v13\n"
    "v_cvt_pk_bf16_f32 v19, v14, v15\n"
    "buffer_store_dwordx4 v[16:19], v1, s[52:55], 0 offen sc1\n"
    "s_endpgm\n"
    ".p2align 5\n"
    ".Ldin_py:\n"
    "v_and_b32 v32, 0x58, v4\n"
    "v_lshlrev_b32 v33, 1, v2\n"
    "v_and_b32 v34, 0x3c0, v251\n"
    "v_bitop3_b32 v35, v33, v0, 32 bitop3:0x28\n"
    "v_lshl_or_b32 v34, v34, 1, v32\n"
    "s_lshl_b32 s23, s21, 11\n"
    "s_add_u32 s23, s23, 54144\n"
    "v_add3_u32 v52, v34, v35, s23\n"
    "v_and_or_b32 v32, v33, 32, v32\n"
    "v_xor_b32 v53, 32, v52\n"
    "v_xor_b32 v54, 64, v52\n"
    "v_xor_b32 v55, 96, v52\n"
    "v_and_b32 v34, 0xc0, v251\n"
    "v_lshl_or_b32 v32, v2, 6, v32\n"
    "s_lshl_b32 s23, s21, 5\n"
    "v_lshl_or_b32 v32, v34, 1, v32\n"
    "v_and_b32 v58, 0x7bf, v36\n"
    "v_xor_b32 v32, s23, v32\n"
    "s_mul_i32 s23, s21, 0x1040\n"
    "v_add_u32 v56, 54144, v32\n"
    "s_and_b32 s23, s23, 0x2040\n"
    "v_xor_b32 v57, 32, v56\n"
    "v_add_u32 v59, s23, v58\n"
    "v_and_b32 v32, 0xf0, v251\n"
    "v_and_b32 v33, 32, v0\n"
    "v_and_b32 v34, 16, v0\n"
    "v_lshl_or_b32 v32, v33, 3, v32\n"
    "s_lshl_b32 s24, s21, 9\n"
    "s_and_b32 s23, s24, 0x400\n"
    "v_lshl_or_b32 v60, v34, 6, v32\n"
    "s_add_u32 s23, s23, s24\n"
    "v_lshrrev_b32 v34, 1, v34\n"
    "v_or_b32 v60, s23, v60\n"
    "v_or3_b32 v61, v32, v34, s24\n"
    "v_and_b32 v62, 0x3f803f80, v248\n"
    "v_and_b32 v63, 0x3f803f80, v249\n"
    "v_lshrrev_b32 v32, 5, v1\n"
    "s_mul_i32 s23, s21, 544\n"
    "v_mul_u32_u24 v12, 272, v32\n"
    "v_mad_u32_u24 v64, v32, s14, v3\n"
    "v_add3_u32 v12, v12, s23, 15\n"
    "s_not_b32 exec_lo, s16\n"
    "s_not_b32 exec_hi, s16\n"
    "v_bfrev_b32 v64, 1\n"
    "s_mov_b64 exec, -1\n"
    "v_add_u32 v13, v13, v12\n"
    "s_lshl1_add_u32 s23, s21, s78\n"
    "s_mul_i32 s23, s23, s14\n"
    "s_lshl1_add_u32 s22, s15, s23\n"
    "s_mov_b32 s26, 0xbfb8aa3b\n"
    "v_bfrev_b32 v35, 1\n"
    "s_waitcnt vmcnt(5)\n"
    "v_mul_f32 v32, 0x35800000, v44\n"
    "v_max3_f32 v33, |v40|, |v41|, |v42|\n"
    "v_max3_f32 v33, v33, |v43|, |v32|\n"
    "v_lshlrev_b32 v76, 16, v46\n"
    "v_and_b32 v77, 0xffff0000, v46\n"
    "v_max_f32_dpp v33, v33, v33 quad_perm:[1,0,3,2] row_mask:0xf bank_mask:0xf bound_ctrl:1\n"
    "v_lshlrev_b32_e64 v78, 16, v47\n"
    "v_and_b32 v79, 0xffff0000, v47\n"
    "v_max_f32_dpp v33, v33, v33 quad_perm:[2,3,0,1] row_mask:0xf bank_mask:0xf bound_ctrl:1\n"
    "v_bitop3_b32 v68, v76, s26, v35 bitop3:0x6c\n"
    "v_bitop3_b32 v69, v77, s26, v35 bitop3:0x6c\n"
    "v_max_f32_dpp v33, v33, v33 row_half_mirror row_mask:0xf bank_mask:0xf bound_ctrl:1\n"
    "v_bitop3_b32 v70, v78, s26, v35 bitop3:0x6c\n"
    "v_bitop3_b32 v71, v79, s26, v35 bitop3:0x6c\n"
    "v_max_f32_dpp v33, v33, v33 row_mirror row_mask:0xf bank_mask:0xf bound_ctrl:1\n"
    "v_rcp_f32_e64 v76, |v76|\n"
    "v_rcp_f32_e64 v77, |v77|\n"
    "v_rcp_f32_e64 v78, |v78|\n"
    "v_rcp_f32_e64 v79, |v79|\n"
    "v_lshrrev_b32 v33, 23, v33\n"
    "s_movk_i32 s23, 253\n"
    "v_med3_u32 v33, v33, 15, s23\n"
    "v_add_lshl_u32 v33, v33, -14, 23\n"
    "s_lshl_b32 s23, s21, 6\n"
    "v_pk_mul_f32 v[76:77], v[76:77], v[32:33] op_sel:[0,1] op_sel_hi:[1,1]\n"
    "v_pk_mul_f32 v[78:79], v[78:79], v[32:33] op_sel:[0,1] op_sel_hi:[1,1]\n"
    "v_or_b32 v34, s23, v2\n"
    "v_min_f32 v76, 0x46800000, v76\n"
    "v_min_f32 v77, 0x46800000, v77\n"
    "v_min_f32 v78, 0x46800000, v78\n"
    "v_min_f32 v79, 0x46800000, v79\n"
    "ds_write_b128 v34, v[76:79] offset:49152\n"
    "v_log_f32 v72, v76\n"
    "v_log_f32 v73, v77\n"
    "v_log_f32 v74, v78\n"
    "v_log_f32 v75, v79\n"
    "v_bitop3_b32 v80, v40, s26, v35 bitop3:0x6c\n"
    "v_bitop3_b32 v81, v41, s26, v35 bitop3:0x6c\n"
    "v_bitop3_b32 v82, v42, s26, v35 bitop3:0x6c\n"
    "v_bitop3_b32 v83, v43, s26, v35 bitop3:0x6c\n"
    "s_cmp_eq_u32 s21, 0\n"
    "v_add_f32 v72, 0xc1700000, v72\n"
    "v_add_f32 v73, 0xc1700000, v73\n"
    "v_add_f32 v74, 0xc1700000, v74\n"
    "v_add_f32 v75, 0xc1700000, v75\n"
    "v_lshlrev_b32 v84, 16, v48\n"
    "v_and_b32 v85, 0xffff0000, v48\n"
    "v_lshlrev_b32 v86, 16, v49\n"
    "v_and_b32 v87, 0xffff0000, v49\n"
    "s_cbranch_scc0 .Ldin_cg_r\n"
    "v_min_f32 v40, 0, v40\n"
    "v_min_f32 v41, 0, v41\n"
    "v_min_f32 v42, 0, v42\n"
    "v_min_f32 v43, 0, v43\n"
    "v_pk_add_f32 v[40:41], v[40:41], v[42:43]\n"
    "v_sub_u32 v34, 0x7f000000, v33\n"
    "v_add_f32 v32, v40, v41\n"
    "v_and_b32 v42, 12, v0\n"
    "v_lshlrev_b32 v42, 2, v42\n"
    "v_add_f32_dpp v32, v32, v32 quad_perm:[1,0,3,2] row_mask:0xf bank_mask:0xf bound_ctrl:1\n"
    "s_nop 1\n"
    "v_add_f32_dpp v32, v32, v32 quad_perm:[2,3,0,1] row_mask:0xf bank_mask:0xf bound_ctrl:1\n"
    "s_mov_b64 exec, 15\n"
    "v_add_f32 v32, v32, v44\n"
    "s_mov_b64 exec, 0xffff\n"
    "v_mul_f32_e64 v32, v32, v34\n"
    "ds_write_b64 v42, v[32:33] offset:49408\n"
    "s_mov_b64 exec, -1\n"
    ".Ldin_cg_r:\n"
    "s_waitcnt vmcnt(0) lgkmcnt(0)\n"
    "buffer_load_dwordx4 v[96:99], v64, s[44:47], s22 offen nt\n"
    "s_add_u32 s22, s22, s15\n"
    "buffer_load_dwordx4 v[100:103], v64, s[44:47], s22 offen nt\n"
    "s_add_u32 s22, s22, s15\n"
    "buffer_load_dwordx4 v[104:107], v64, s[44:47], s22 offen nt\n"
    "s_add_u32 s22, s22, s15\n"
    "buffer_load_dwordx4 v[108:111], v64, s[44:47], s22 offen nt\n"
    "s_add_u32 s22, s22, s15\n"
    "buffer_load_dwordx4 v[112:115], v64, s[44:47], s22 offen nt\n"
    "s_add_u32 s22, s22, s15\n"
    "buffer_load_dwordx4 v[116:119], v64, s[44:47], s22 offen nt\n"
    "s_barrier\n"
    "ds_read_b64_tr_b16 v[120:121], v52 offset:0\n"
    "ds_read_b64_tr_b16 v[122:123], v52 offset:16384\n"
    "ds_read_b64_tr_b16 v[124:125], v52 offset:24576\n"
    "ds_read_b64_tr_b16 v[126:127], v53 offset:0\n"
    "ds_read_b64_tr_b16 v[128:129], v53 offset:16384\n"
    "ds_read_b64_tr_b16 v[130:131], v53 offset:24576\n"
    "ds_read_b64_tr_b16 v[132:133], v54 offset:0\n"
    "ds_read_b64_tr_b16 v[134:135], v54 offset:16384\n"
    "ds_read_b64_tr_b16 v[136:137], v54 offset:24576\n"
    "ds_read_b64_tr_b16 v[138:139], v55 offset:0\n"
    "ds_read_b64_tr_b16 v[140:141], v55 offset:16384\n"
    "ds_read_b64_tr_b16 v[142:143], v55 offset:24576\n"
    "s_waitcnt lgkmcnt(9)\n"
    "v_mfma_f32_16x16x16_bf16 v[192:195], v[62:63], v[120:121], 0\n"
    "v_mfma_f32_16x16x16_bf16 v[192:195], v[62:63], v[122:123], v[192:195]\n"
    "v_mfma_f32_16x16x16_bf16 v[196:199], v[62:63], v[124:125], 0\n"
    "ds_read_b64_tr_b16 v[144:145], v56 offset:8192\n"
    "ds_read_b64_tr_b16 v[146:147], v57 offset:8704\n"
    "ds_read_b64_tr_b16 v[148:149], v56 offset:12288\n"
    "ds_read_b64_tr_b16 v[150:151], v57 offset:12800\n"
    "s_waitcnt lgkmcnt(10)\n"
    "v_mfma_f32_16x16x16_bf16 v[200:203], v[62:63], v[126:127], 0\n"
    "v_mfma_f32_16x16x16_bf16 v[200:203], v[62:63], v[128:129], v[200:203]\n"
    "v_mfma_f32_16x16x16_bf16 v[204:207], v[62:63], v[130:131], 0\n"
    "ds_read_b128 v[160:163], v58 offset:24576\n"
    "ds_read_b128 v[164:167], v58 offset:24640\n"
    "s_waitcnt lgkmcnt(9)\n"
    "v_mfma_f32_16x16x16_bf16 v[208:211], v[62:63], v[132:133], 0\n"
    "v_mfma_f32_16x16x16_bf16 v[208:211], v[62:63], v[134:135], v[208:211]\n"
    "v_mfma_f32_16x16x16_bf16 v[212:215], v[62:63], v[136:137], 0\n"
    "ds_read_b64_tr_b16 v[152:153], v56 offset:16384\n"
    "ds_read_b64_tr_b16 v[154:155], v57 offset:16896\n"
    "ds_read_b64_tr_b16 v[156:157], v56 offset:20480\n"
    "ds_read_b64_tr_b16 v[158:159], v57 offset:20992\n"
    "s_waitcnt lgkmcnt(10)\n"
    "v_mfma_f32_16x16x16_bf16 v[216:219], v[62:63], v[138:139], 0\n"
    "v_mfma_f32_16x16x16_bf16 v[216:219], v[62:63], v[140:141], v[216:219]\n"
    "v_mfma_f32_16x16x16_bf16 v[220:223], v[62:63], v[142:143], 0\n"
    "s_waitcnt lgkmcnt(4)\n"
    "v_mfma_f32_16x16x32_bf16 v[224:227], v[144:147], v[160:163], v[84:87]\n"
    "v_mfma_f32_16x16x32_bf16 v[224:227], v[148:151], v[164:167], v[224:227]\n"
    "s_waitcnt lgkmcnt(0)\n"
    "v_mfma_f32_16x16x32_bf16 v[228:231], v[152:155], v[160:163], 0\n"
    "v_mfma_f32_16x16x32_bf16 v[228:231], v[156:159], v[164:167], v[228:231]\n"
    "v_pk_mul_f32 v[192:193], v[192:193], v[80:81] op_sel_hi:[1,0]\n"
    "v_pk_mul_f32 v[194:195], v[194:195], v[80:81] op_sel_hi:[1,0]\n"
    "ds_write_b128 v60, v[192:195] offset:0\n"
    "v_pk_mul_f32 v[196:197], v[196:197], v[80:81] op_sel_hi:[1,0]\n"
    "v_pk_mul_f32 v[198:199], v[198:199], v[80:81] op_sel_hi:[1,0]\n"
    "v_cvt_pk_bf16_f32 v196, v196, v197\n"
    "v_cvt_pk_bf16_f32 v197, v198, v199\n"
    "ds_write_b64 v61, v[196:197] offset:16384\n"
    "v_pk_mul_f32 v[200:201], v[200:201], v[80:81] op_sel:[0,1] op_sel_hi:[1,1]\n"
    "v_pk_mul_f32 v[202:203], v[202:203], v[80:81] op_sel:[0,1] op_sel_hi:[1,1]\n"
    "ds_write_b128 v60, v[200:203] offset:4096\n"
    "v_pk_mul_f32 v[204:205], v[204:205], v[80:81] op_sel:[0,1] op_sel_hi:[1,1]\n"
    "v_pk_mul_f32 v[206:207], v[206:207], v[80:81] op_sel:[0,1] op_sel_hi:[1,1]\n"
    "v_cvt_pk_bf16_f32 v204, v204, v205\n"
    "v_cvt_pk_bf16_f32 v205, v206, v207\n"
    "ds_write_b64 v61, v[204:205] offset:18432\n"
    "v_pk_mul_f32 v[208:209], v[208:209], v[82:83] op_sel_hi:[1,0]\n"
    "v_pk_mul_f32 v[210:211], v[210:211], v[82:83] op_sel_hi:[1,0]\n"
    "ds_write_b128 v60, v[208:211] offset:8192\n"
    "v_pk_mul_f32 v[212:213], v[212:213], v[82:83] op_sel_hi:[1,0]\n"
    "v_pk_mul_f32 v[214:215], v[214:215], v[82:83] op_sel_hi:[1,0]\n"
    "v_cvt_pk_bf16_f32 v212, v212, v213\n"
    "v_cvt_pk_bf16_f32 v213, v214, v215\n"
    "ds_write_b64 v61, v[212:213] offset:20480\n"
    "v_pk_mul_f32 v[216:217], v[216:217], v[82:83] op_sel:[0,1] op_sel_hi:[1,1]\n"
    "v_pk_mul_f32 v[218:219], v[218:219], v[82:83] op_sel:[0,1] op_sel_hi:[1,1]\n"
    "ds_write_b128 v60, v[216:219] offset:12288\n"
    "v_pk_mul_f32 v[220:221], v[220:221], v[82:83] op_sel:[0,1] op_sel_hi:[1,1]\n"
    "v_pk_mul_f32 v[222:223], v[222:223], v[82:83] op_sel:[0,1] op_sel_hi:[1,1]\n"
    "v_cvt_pk_bf16_f32 v220, v220, v221\n"
    "v_cvt_pk_bf16_f32 v221, v222, v223\n"
    "ds_write_b64 v61, v[220:221] offset:22528\n"
    "v_pk_add_f32 v[224:225], v[224:225], v[228:229] neg_lo:[0,1] neg_hi:[0,1]\n"
    "v_pk_add_f32 v[226:227], v[226:227], v[230:231] neg_lo:[0,1] neg_hi:[0,1]\n"
    "v_pk_fma_f32 v[224:225], v[224:225], v[68:69], v[72:73]\n"
    "v_pk_fma_f32 v[226:227], v[226:227], v[70:71], v[74:75]\n"
    "ds_write_b128 v59, v[224:227] offset:32768\n"
    "s_waitcnt lgkmcnt(0)\n"
    "s_barrier\n"
    "s_setprio 0\n"
    "s_mul_i32 s23, s20, 272\n"
    "v_add_u32 v32, s23, v244\n"
    "ds_read_u8 v16, v32 offset:112\n"
    "ds_read_u8 v18, v32 offset:113\n"
    "ds_read_u8 v20, v32 offset:128\n"
    "ds_read_u8 v22, v32 offset:129\n"
    "ds_read_u8 v24, v32 offset:144\n"
    "ds_read_u8 v26, v32 offset:145\n"
    "ds_read_u8 v28, v32 offset:160\n"
    "ds_read_u8 v30, v32 offset:161\n"
    "ds_read_b32 v33, v246 offset:128\n"
    "ds_read_b128 v[168:171], v58 offset:26624\n"
    "ds_read_b128 v[172:175], v58 offset:26688\n"
    "ds_read_b128 v[176:179], v58 offset:28672\n"
    "ds_read_b128 v[180:183], v58 offset:28736\n"
    "ds_read_b128 v[184:187], v58 offset:30720\n"
    "ds_read_b128 v[188:191], v58 offset:30784\n"
    "s_add_u32 m0, s68, 0\n"
    "s_waitcnt lgkmcnt(13)\n"
    "buffer_load_dwordx4 v[16:17], s[40:43], s67 idxen offen offset:0 nt lds\n"
    "buffer_load_dwordx4 v[18:19], s[40:43], s67 idxen offen offset:1024 nt lds\n"
    "s_add_u32 m0, s68, 2048\n"
    "s_waitcnt lgkmcnt(11)\n"
    "buffer_load_dwordx4 v[20:21], s[40:43], s67 idxen offen offset:0 nt lds\n"
    "buffer_load_dwordx4 v[22:23], s[40:43], s67 idxen offen offset:1024 nt lds\n"
    "s_add_u32 m0, s68, 4096\n"
    "s_waitcnt lgkmcnt(9)\n"
    "buffer_load_dwordx4 v[24:25], s[40:43], s67 idxen offen offset:0 nt lds\n"
    "buffer_load_dwordx4 v[26:27], s[40:43], s67 idxen offen offset:1024 nt lds\n"
    "s_add_u32 m0, s68, 6144\n"
    "s_waitcnt lgkmcnt(7)\n"
    "s_nop 0\n"
    "buffer_load_dwordx4 v[28:29], s[40:43], s67 idxen offen offset:0 nt lds\n"
    "buffer_load_dwordx4 v[30:31], s[40:43], s67 idxen offen offset:1024 nt lds\n"
    "ds_write_b64 v13, v[14:15] offset:54112\n"
    "ds_write_b64 v13, v[14:15] offset:56288\n"
    "ds_write_b64 v13, v[14:15] offset:58464\n"
    "ds_write_b64 v13, v[14:15] offset:60640\n"
    "ds_write_b64 v13, v[14:15] offset:62816\n"
    "ds_write_b64 v13, v[14:15] offset:64992\n"
    "s_waitcnt lgkmcnt(6)\n"
    "v_readfirstlane_b32 s60, v33\n"
    "v_mfma_f32_16x16x32_bf16 v[120:123], v[144:147], v[168:171], v[84:87]\n"
    "v_mfma_f32_16x16x32_bf16 v[120:123], v[148:151], v[172:175], v[120:123]\n"
    "v_mfma_f32_16x16x32_bf16 v[124:127], v[152:155], v[168:171], 0\n"
    "v_mfma_f32_16x16x32_bf16 v[124:127], v[156:159], v[172:175], v[124:127]\n"
    "v_mfma_f32_16x16x32_bf16 v[128:131], v[144:147], v[176:179], v[84:87]\n"
    "v_mfma_f32_16x16x32_bf16 v[128:131], v[148:151], v[180:183], v[128:131]\n"
    "v_mfma_f32_16x16x32_bf16 v[132:135], v[152:155], v[176:179], 0\n"
    "v_mfma_f32_16x16x32_bf16 v[132:135], v[156:159], v[180:183], v[132:135]\n"
    "v_mfma_f32_16x16x32_bf16 v[136:139], v[144:147], v[184:187], v[84:87]\n"
    "v_mfma_f32_16x16x32_bf16 v[136:139], v[148:151], v[188:191], v[136:139]\n"
    "v_mfma_f32_16x16x32_bf16 v[140:143], v[152:155], v[184:187], 0\n"
    "v_mfma_f32_16x16x32_bf16 v[140:143], v[156:159], v[188:191], v[140:143]\n"
    "s_and_b32 s23, s14, 14\n"
    "s_waitcnt vmcnt(8)\n"
    "s_cbranch_scc1 .Ldin_trimy\n"
    ".Ldin_trimy_r:\n"
    "v_pk_min_u16 v192, v96, 1 op_sel_hi:[1,0]\n"
    "v_pk_min_u16 v193, v97, 1 op_sel_hi:[1,0]\n"
    "v_pk_min_u16 v194, v98, 1 op_sel_hi:[1,0]\n"
    "v_pk_min_u16 v195, v99, 1 op_sel_hi:[1,0]\n"
    "v_add3_u32 v44, v192, v193, v194\n"
    "v_add_u32 v44, v44, v195\n"
    "v_dot2_u32_u16 v40, v44, s31, 0\n"
    "v_pk_min_u16 v196, v100, 1 op_sel_hi:[1,0]\n"
    "v_pk_min_u16 v197, v101, 1 op_sel_hi:[1,0]\n"
    "v_pk_min_u16 v198, v102, 1 op_sel_hi:[1,0]\n"
    "v_pk_min_u16 v199, v103, 1 op_sel_hi:[1,0]\n"
    "v_add3_u32 v44, v196, v197, v198\n"
    "v_add_u32_e64 v44, v44, v199\n"
    "v_dot2_u32_u16 v40, v44, s30, v40\n"
    "v_pk_min_u16 v200, v104, 1 op_sel_hi:[1,0]\n"
    "v_pk_min_u16 v201, v105, 1 op_sel_hi:[1,0]\n"
    "v_pk_min_u16 v202, v106, 1 op_sel_hi:[1,0]\n"
    "v_pk_min_u16 v203, v107, 1 op_sel_hi:[1,0]\n"
    "v_add3_u32 v42, v200, v201, v202\n"
    "v_add_u32 v42, v42, v203\n"
    "v_lshl_add_u32 v42, v42, 16, v42\n"
    "v_and_b32 v42, 0xffff0000, v42\n"
    "v_or_b32 v40, v40, v42\n"
    "v_pk_min_u16 v204, v108, 1 op_sel_hi:[1,0]\n"
    "v_pk_min_u16 v205, v109, 1 op_sel_hi:[1,0]\n"
    "v_pk_min_u16 v206, v110, 1 op_sel_hi:[1,0]\n"
    "v_pk_min_u16 v207, v111, 1 op_sel_hi:[1,0]\n"
    "v_add3_u32 v45, v204, v205, v206\n"
    "v_add_u32_e64 v45, v45, v207\n"
    "v_dot2_u32_u16 v41, v45, s31, 0\n"
    "v_pk_min_u16 v208, v112, 1 op_sel_hi:[1,0]\n"
    "v_pk_min_u16 v209, v113, 1 op_sel_hi:[1,0]\n"
    "v_pk_min_u16 v210, v114, 1 op_sel_hi:[1,0]\n"
    "v_pk_min_u16 v211, v115, 1 op_sel_hi:[1,0]\n"
    "v_add3_u32 v45, v208, v209, v210\n"
    "v_add_u32_e64 v45, v45, v211\n"
    "v_dot2_u32_u16 v41, v45, s30, v41\n"
    "v_pk_min_u16 v212, v116, 1 op_sel_hi:[1,0]\n"
    "v_pk_min_u16 v213, v117, 1 op_sel_hi:[1,0]\n"
    "v_pk_min_u16 v214, v118, 1 op_sel_hi:[1,0]\n"
    "v_pk_min_u16 v215, v119, 1 op_sel_hi:[1,0]\n"
    "v_add3_u32 v43, v212, v213, v214\n"
    "v_add_u32 v43, v43, v215\n"
    "v_lshl_add_u32 v43, v43, 16, v43\n"
    "v_and_b32 v43, 0xffff0000, v43\n"
    "v_or_b32 v41, v41, v43\n"
    "v_pk_add_f32 v[120:121], v[120:121], v[124:125] neg_lo:[0,1] neg_hi:[0,1]\n"
    "v_pk_add_f32 v[122:123], v[122:123], v[126:127] neg_lo:[0,1] neg_hi:[0,1]\n"
    "v_pk_fma_f32 v[120:121], v[120:121], v[68:69], v[72:73]\n"
    "v_pk_fma_f32 v[122:123], v[122:123], v[70:71], v[74:75]\n"
    "ds_write_b128 v59, v[120:123] offset:34816\n"
    "v_pk_add_f32 v[128:129], v[128:129], v[132:133] neg_lo:[0,1] neg_hi:[0,1]\n"
    "v_pk_add_f32 v[130:131], v[130:131], v[134:135] neg_lo:[0,1] neg_hi:[0,1]\n"
    "v_pk_fma_f32 v[128:129], v[128:129], v[68:69], v[72:73]\n"
    "v_pk_fma_f32 v[130:131], v[130:131], v[70:71], v[74:75]\n"
    "ds_write_b128 v59, v[128:131] offset:36864\n"
    "v_pk_add_f32 v[136:137], v[136:137], v[140:141] neg_lo:[0,1] neg_hi:[0,1]\n"
    "v_pk_add_f32 v[138:139], v[138:139], v[142:143] neg_lo:[0,1] neg_hi:[0,1]\n"
    "v_pk_fma_f32 v[136:137], v[136:137], v[68:69], v[72:73]\n"
    "v_pk_fma_f32 v[138:139], v[138:139], v[70:71], v[74:75]\n"
    "ds_write_b128 v59, v[136:139] offset:38912\n"
    "v_sub_u32 v44, 0x10101, v40\n"
    "v_sub_u32 v45, 0x10101, v41\n"
    "v_add_u32_dpp v42, v40, v40 row_shr:1 row_mask:0xf bank_mask:0xf bound_ctrl:1\n"
    "v_add_u32_dpp v43, v41, v41 row_shr:1 row_mask:0xf bank_mask:0xf bound_ctrl:1\n"
    "v_pk_mad_u16 v96, v192, s28, v96\n"
    "v_add_u32_dpp v42, v42, v42 row_shr:2 row_mask:0xf bank_mask:0xf bound_ctrl:1\n"
    "v_add_u32_dpp v43, v43, v43 row_shr:2 row_mask:0xf bank_mask:0xf bound_ctrl:1\n"
    "v_pk_mad_u16 v97, v193, s28, v97\n"
    "v_add_u32_dpp v42, v42, v42 row_shr:4 row_mask:0xf bank_mask:0xf bound_ctrl:1\n"
    "v_add_u32_dpp v43, v43, v43 row_shr:4 row_mask:0xf bank_mask:0xf bound_ctrl:1\n"
    "v_pk_mad_u16 v98, v194, s28, v98\n"
    "v_add_u32_dpp v42, v42, v42 row_shr:8 row_mask:0xf bank_mask:0xf bound_ctrl:1\n"
    "v_add_u32_dpp v43, v43, v43 row_shr:8 row_mask:0xf bank_mask:0xf bound_ctrl:1\n"
    "v_pk_mad_u16 v99, v195, s28, v99\n"
    "v_add_u32_dpp v42, v42, v42 row_bcast:15 row_mask:0xa bank_mask:0xf\n"
    "v_add_u32_dpp v43, v43, v43 row_bcast:15 row_mask:0xa bank_mask:0xf\n"
    "v_or3_b32 v46, v96, v97, 0\n"
    "v_add_u32 v44, v44, v42\n"
    "v_add_u32 v45, v45, v43\n"
    "v_lshrrev_b32 v32, 3, v1\n"
    "v_and_b32_e64 v32, 4, v32\n"
    "v_lshl_add_u32 v32, s21, 3, v32\n"
    "s_brev_b32 exec_lo, 1\n"
    "s_brev_b32 exec_hi, 1\n"
    "ds_write_b32 v32, v42 offset:49472\n"
    "ds_write_b32 v32, v43 offset:49504\n"
    "s_mov_b64 exec, -1\n"
    "s_mov_b32 s26, 0xc040c00\n"
    "v_perm_b32 v47, v42, v44, s26\n"
    "v_mad_u32_u16 v48, v192, v47, v12\n"
    "v_mad_u32_u16 v49, v192, v47, v12 op_sel:[1,1,0,0]\n"
    "v_pk_mad_u16 v47, v192, s29, v47\n"
    "ds_write_b8 v48, v8 offset:54112\n"
    "ds_write_b8_d16_hi v49, v8 offset:54112\n"
    "v_mad_u32_u16 v50, v193, v47, v12\n"
    "v_mad_u32_u16 v51, v193, v47, v12 op_sel:[1,1,0,0]\n"
    "v_pk_mad_u16 v47, v193, s29, v47\n"
    "ds_write_b8 v50, v9 offset:54112\n"
    "ds_write_b8_d16_hi v51, v9 offset:54112\n"
    "v_mad_u32_u16 v48, v194, v47, v12\n"
    "v_mad_u32_u16 v49, v194, v47, v12 op_sel:[1,1,0,0]\n"
    "v_pk_mad_u16 v47, v194, s29, v47\n"
    "ds_write_b8 v48, v10 offset:54112\n"
    "ds_write_b8_d16_hi v49, v10 offset:54112\n"
    "v_mad_u32_u16 v50, v195, v47, v12\n"
    "v_mad_u32_u16 v51, v195, v47, v12 op_sel:[1,1,0,0]\n"
    "ds_write_b8 v50, v11 offset:54112\n"
    "ds_write_b8_d16_hi v51, v11 offset:54112\n"
    "v_or3_b32 v46, v98, v99, v46\n"
    "v_pk_mad_u16 v100, v196, s28, v100\n"
    "v_pk_mad_u16 v101, v197, s28, v101\n"
    "v_pk_mad_u16 v102, v198, s28, v102\n"
    "v_pk_mad_u16 v103, v199, s28, v103\n"
    "v_or3_b32 v46, v100, v101, v46\n"
    "s_mov_b32 s26, 0xc050c01\n"
    "v_perm_b32 v47, v42, v44, s26\n"
    "v_mad_u32_u16 v48, v196, v47, v12\n"
    "v_mad_u32_u16 v49, v196, v47, v12 op_sel:[1,1,0,0]\n"
    "v_pk_mad_u16 v47, v196, s29, v47\n"
    "ds_write_b8 v48, v8 offset:56288\n"
    "ds_write_b8_d16_hi v49, v8 offset:56288\n"
    "v_mad_u32_u16 v50, v197, v47, v12\n"
    "v_mad_u32_u16 v51, v197, v47, v12 op_sel:[1,1,0,0]\n"
    "v_pk_mad_u16 v47, v197, s29, v47\n"
    "ds_write_b8 v50, v9 offset:56288\n"
    "ds_write_b8_d16_hi v51, v9 offset:56288\n"
    "v_mad_u32_u16 v48, v198, v47, v12\n"
    "v_mad_u32_u16 v49, v198, v47, v12 op_sel:[1,1,0,0]\n"
    "v_pk_mad_u16 v47, v198, s29, v47\n"
    "ds_write_b8 v48, v10 offset:56288\n"
    "ds_write_b8_d16_hi v49, v10 offset:56288\n"
    "v_mad_u32_u16 v50, v199, v47, v12\n"
    "v_mad_u32_u16 v51, v199, v47, v12 op_sel:[1,1,0,0]\n"
    "ds_write_b8 v50, v11 offset:56288\n"
    "ds_write_b8_d16_hi v51, v11 offset:56288\n"
    "v_or3_b32 v46, v102, v103, v46\n"
    "v_pk_mad_u16 v104, v200, s28, v104\n"
    "v_pk_mad_u16 v105, v201, s28, v105\n"
    "v_pk_mad_u16 v106, v202, s28, v106\n"
    "v_pk_mad_u16 v107, v203, s28, v107\n"
    "v_or3_b32 v46, v104, v105, v46\n"
    "s_mov_b32 s26, 0xc060c02\n"
    "v_perm_b32 v47, v42, v44, s26\n"
    "v_mad_u32_u16 v48, v200, v47, v12\n"
    "v_mad_u32_u16 v49, v200, v47, v12 op_sel:[1,1,0,0]\n"
    "v_pk_mad_u16 v47, v200, s29, v47\n"
    "ds_write_b8 v48, v8 offset:58464\n"
    "ds_write_b8_d16_hi v49, v8 offset:58464\n"
    "v_mad_u32_u16 v50, v201, v47, v12\n"
    "v_mad_u32_u16 v51, v201, v47, v12 op_sel:[1,1,0,0]\n"
    "v_pk_mad_u16 v47, v201, s29, v47\n"
    "ds_write_b8 v50, v9 offset:58464\n"
    "ds_write_b8_d16_hi v51, v9 offset:58464\n"
    "v_mad_u32_u16 v48, v202, v47, v12\n"
    "v_mad_u32_u16 v49, v202, v47, v12 op_sel:[1,1,0,0]\n"
    "v_pk_mad_u16 v47, v202, s29, v47\n"
    "ds_write_b8 v48, v10 offset:58464\n"
    "ds_write_b8_d16_hi v49, v10 offset:58464\n"
    "v_mad_u32_u16 v50, v203, v47, v12\n"
    "v_mad_u32_u16 v51, v203, v47, v12 op_sel:[1,1,0,0]\n"
    "ds_write_b8 v50, v11 offset:58464\n"
    "ds_write_b8_d16_hi v51, v11 offset:58464\n"
    "v_or3_b32 v46, v106, v107, v46\n"
    "v_pk_mad_u16 v108, v204, s28, v108\n"
    "v_pk_mad_u16 v109, v205, s28, v109\n"
    "v_pk_mad_u16 v110, v206, s28, v110\n"
    "v_pk_mad_u16 v111, v207, s28, v111\n"
    "v_or3_b32 v46, v108, v109, v46\n"
    "s_mov_b32 s26, 0xc040c00\n"
    "v_perm_b32 v47, v43, v45, s26\n"
    "v_mad_u32_u16 v48, v204, v47, v12\n"
    "v_mad_u32_u16 v49, v204, v47, v12 op_sel:[1,1,0,0]\n"
    "v_pk_mad_u16 v47, v204, s29, v47\n"
    "ds_write_b8 v48, v8 offset:60640\n"
    "ds_write_b8_d16_hi v49, v8 offset:60640\n"
    "v_mad_u32_u16 v50, v205, v47, v12\n"
    "v_mad_u32_u16 v51, v205, v47, v12 op_sel:[1,1,0,0]\n"
    "v_pk_mad_u16 v47, v205, s29, v47\n"
    "ds_write_b8 v50, v9 offset:60640\n"
    "ds_write_b8_d16_hi v51, v9 offset:60640\n"
    "v_mad_u32_u16 v48, v206, v47, v12\n"
    "v_mad_u32_u16 v49, v206, v47, v12 op_sel:[1,1,0,0]\n"
    "v_pk_mad_u16 v47, v206, s29, v47\n"
    "ds_write_b8 v48, v10 offset:60640\n"
    "ds_write_b8_d16_hi v49, v10 offset:60640\n"
    "v_mad_u32_u16 v50, v207, v47, v12\n"
    "v_mad_u32_u16 v51, v207, v47, v12 op_sel:[1,1,0,0]\n"
    "ds_write_b8 v50, v11 offset:60640\n"
    "ds_write_b8_d16_hi v51, v11 offset:60640\n"
    "v_or3_b32 v46, v110, v111, v46\n"
    "v_pk_mad_u16 v112, v208, s28, v112\n"
    "v_pk_mad_u16 v113, v209, s28, v113\n"
    "v_pk_mad_u16 v114, v210, s28, v114\n"
    "v_pk_mad_u16 v115, v211, s28, v115\n"
    "v_or3_b32 v46, v112, v113, v46\n"
    "s_mov_b32 s26, 0xc050c01\n"
    "v_perm_b32 v47, v43, v45, s26\n"
    "v_mad_u32_u16 v48, v208, v47, v12\n"
    "v_mad_u32_u16 v49, v208, v47, v12 op_sel:[1,1,0,0]\n"
    "v_pk_mad_u16 v47, v208, s29, v47\n"
    "ds_write_b8 v48, v8 offset:62816\n"
    "ds_write_b8_d16_hi v49, v8 offset:62816\n"
    "v_mad_u32_u16 v50, v209, v47, v12\n"
    "v_mad_u32_u16 v51, v209, v47, v12 op_sel:[1,1,0,0]\n"
    "v_pk_mad_u16 v47, v209, s29, v47\n"
    "ds_write_b8 v50, v9 offset:62816\n"
    "ds_write_b8_d16_hi v51, v9 offset:62816\n"
    "v_mad_u32_u16 v48, v210, v47, v12\n"
    "v_mad_u32_u16 v49, v210, v47, v12 op_sel:[1,1,0,0]\n"
    "v_pk_mad_u16 v47, v210, s29, v47\n"
    "ds_write_b8 v48, v10 offset:62816\n"
    "ds_write_b8_d16_hi v49, v10 offset:62816\n"
    "v_mad_u32_u16 v50, v211, v47, v12\n"
    "v_mad_u32_u16 v51, v211, v47, v12 op_sel:[1,1,0,0]\n"
    "ds_write_b8 v50, v11 offset:62816\n"
    "ds_write_b8_d16_hi v51, v11 offset:62816\n"
    "v_or3_b32 v46, v114, v115, v46\n"
    "v_pk_mad_u16 v116, v212, s28, v116\n"
    "v_pk_mad_u16 v117, v213, s28, v117\n"
    "v_pk_mad_u16 v118, v214, s28, v118\n"
    "v_pk_mad_u16 v119, v215, s28, v119\n"
    "v_or3_b32 v46, v116, v117, v46\n"
    "s_mov_b32 s26, 0xc060c02\n"
    "v_perm_b32 v47, v43, v45, s26\n"
    "v_mad_u32_u16 v48, v212, v47, v12\n"
    "v_mad_u32_u16 v49, v212, v47, v12 op_sel:[1,1,0,0]\n"
    "v_pk_mad_u16 v47, v212, s29, v47\n"
    "ds_write_b8 v48, v8 offset:64992\n"
    "ds_write_b8_d16_hi v49, v8 offset:64992\n"
    "v_mad_u32_u16 v50, v213, v47, v12\n"
    "v_mad_u32_u16 v51, v213, v47, v12 op_sel:[1,1,0,0]\n"
    "v_pk_mad_u16 v47, v213, s29, v47\n"
    "ds_write_b8 v50, v9 offset:64992\n"
    "ds_write_b8_d16_hi v51, v9 offset:64992\n"
    "v_mad_u32_u16 v48, v214, v47, v12\n"
    "v_mad_u32_u16 v49, v214, v47, v12 op_sel:[1,1,0,0]\n"
    "v_pk_mad_u16 v47, v214, s29, v47\n"
    "ds_write_b8 v48, v10 offset:64992\n"
    "ds_write_b8_d16_hi v49, v10 offset:64992\n"
    "v_mad_u32_u16 v50, v215, v47, v12\n"
    "v_mad_u32_u16 v51, v215, v47, v12 op_sel:[1,1,0,0]\n"
    "ds_write_b8 v50, v11 offset:64992\n"
    "ds_write_b8_d16_hi v51, v11 offset:64992\n"
    "v_or3_b32 v46, v118, v119, v46\n"
    "v_cmp_ne_u32 vcc, 0, v46\n"
    "v_mov_b32 v32, 0\n"
    "s_cmp_lg_u64 vcc, 0\n"
    "s_cselect_b32 s23, 0x101, 1\n"
    "s_mov_b64 exec, 1\n"
    "v_mov_b32 v33, s23\n"
    "ds_add_rtn_u32 v33, v32, v33 offset:49416\n"
    "s_mov_b64 exec, -1\n"
    "s_waitcnt lgkmcnt(0)\n"
    "v_readfirstlane_b32 s24, v33\n"
    "s_add_u32 s24, s24, s23\n"
    "s_bitcmp1_b32 s24, 2\n"
    "s_cbranch_scc0 .Ldin_go\n"
    "s_setprio 1\n"
    "v_lshrrev_b32 v40, 3, v1\n"
    "v_and_b32 v41, 7, v0\n"
    "v_lshl_add_u32 v60, v41, 2, v40\n"
    "s_mov_b32 s23, 86912\n"
    "s_mov_b32 exec_lo, 0xff000000\n"
    "v_add_u32 v60, 29, v60\n"
    "s_mov_b32 exec_lo, -1\n"
    "v_lshl_add_u32 v62, v40, 8, s23\n"
    "v_lshl_add_u32 v55, v41, 8, s23\n"
    "ds_read_u8 v44, v60 offset:49472\n"
    "ds_read2_b32 v[46:47], v62 offset0:32 offset1:40\n"
    "ds_read2_b32 v[48:49], v55 offset0:32 offset1:40\n"
    "s_cmp_gt_u32 s24, 255\n"
    "s_cselect_b32 s24, 2.0, 0\n"
    "s_add_u32 s24, s24, 0x801170\n"
    "v_mul_u32_u24 v52, 0x80110, v1\n"
    "v_bfe_u32 v60, v0, 1, 2\n"
    "v_mul_lo_u32 v62, v40, s79\n"
    "v_lshl_add_u32 v52, v60, 16, v52\n"
    "s_lshl1_add_u32 s23, s79, s78\n"
    "v_add_u32 v52, s24, v52\n"
    "v_add3_u32 v62, v62, v41, s23\n"
    "v_and_b32 v42, 56, v0\n"
    "v_cmp_gt_u32 vcc, s74, v62\n"
    "v_lshlrev_b32 v43, 3, v41\n"
    "s_waitcnt lgkmcnt(2)\n"
    "v_add_u32 v44, 15, v44\n"
    "v_bfrev_b32 v60, 1\n"
    "v_lshrrev_b32 v44, 4, v44\n"
    "v_cndmask_b32 v52, v60, v52, vcc\n"
    "v_max_u32 v44, 6, v44\n"
    "v_lshl_or_b32 v52, v44, 25, v52\n"
    "v_add_u32_e64 v44, -5, v44\n"
    "v_cndmask_b32 v44, 0, v44, vcc\n"
    "v_lshl_or_b32 v53, v44, 3, v41\n"
    "s_waitcnt lgkmcnt(0)\n"
    "v_bfe_u32 v46, v46, 25, 5\n"
    "v_bfe_u32 v47, v47, 25, 5\n"
    "v_mov_b32_dpp v54, v53 row_half_mirror row_mask:0xf bank_mask:0xf bound_ctrl:1\n"
    "v_sub_u32_dpp v64, v53, v53 quad_perm:[1,0,3,2] row_mask:0xf bank_mask:0xf bound_ctrl:1\n"
    "v_sub_u32_dpp v65, v53, v53 quad_perm:[2,3,0,1] row_mask:0xf bank_mask:0xf bound_ctrl:1\n"
    "v_sub_u32_dpp v66, v53, v53 quad_perm:[3,2,1,0] row_mask:0xf bank_mask:0xf bound_ctrl:1\n"
    "v_sub_u32 v67, v54, v53\n"
    "v_sub_u32_dpp v68, v54, v53 quad_perm:[1,0,3,2] row_mask:0xf bank_mask:0xf bound_ctrl:1\n"
    "v_sub_u32_dpp v69, v54, v53 quad_perm:[2,3,0,1] row_mask:0xf bank_mask:0xf bound_ctrl:1\n"
    "v_sub_u32_dpp v70, v54, v53 quad_perm:[3,2,1,0] row_mask:0xf bank_mask:0xf bound_ctrl:1\n"
    "v_lshrrev_b32 v56, 31, v64\n"
    "v_alignbit_b32 v56, v56, v65, 31\n"
    "v_alignbit_b32 v56, v56, v66, 31\n"
    "v_alignbit_b32 v56, v56, v67, 31\n"
    "v_alignbit_b32 v56, v56, v68, 31\n"
    "v_alignbit_b32 v56, v56, v69, 31\n"
    "v_alignbit_b32 v56, v56, v70, 31\n"
    "v_bcnt_u32_b32 v56, v56, -7\n"
    "v_mul_i32_i24 v56, -4, v56\n"
    "v_lshlrev_b32 v44, v56, v44\n"
    "v_lshl_add_u32 v56, v42, 2, v56\n"
    "ds_permute_b32 v52, v56, v52\n"
    "v_or_b32_dpp v44, v44, v44 quad_perm:[1,0,3,2] row_mask:0xf bank_mask:0xf bound_ctrl:1\n"
    "v_and_b32_e64 v50, 4, v40\n"
    "v_mad_u32_u24 v50, v50, 6, v40\n"
    "v_or_b32_dpp v44, v44, v44 quad_perm:[2,3,0,1] row_mask:0xf bank_mask:0xf bound_ctrl:1\n"
    "v_lshl_add_u32 v50, v46, 3, v50\n"
    "v_bfe_u32 v48, v48, 25, 5\n"
    "v_or_b32_dpp v44, v44, v44 row_half_mirror row_mask:0xf bank_mask:0xf bound_ctrl:1\n"
    "v_bfe_u32 v49, v49, 25, 5\n"
    "v_and_b32_e64 v51, 4, v41\n"
    "v_mad_u32_u24 v51, v51, 6, v41\n"
    "v_lshl_add_u32 v51, v48, 3, v51\n"
    "v_readlane_b32 s4, v44, 0\n"
    "v_readlane_b32 s5, v44, 8\n"
    "v_readlane_b32 s6, v44, 16\n"
    "v_readlane_b32 s7, v44, 24\n"
    "v_readlane_b32 s8, v44, 32\n"
    "v_readlane_b32 s9, v44, 40\n"
    "v_cmp_eq_u32 vcc, s20, v40\n"
    "s_nop 1\n"
    "v_cndmask_b32 v60, 0, 8, vcc\n"
    "v_add_u32 v50, v50, v60\n"
    "v_cmp_eq_u32 vcc, s20, v41\n"
    "s_nop 1\n"
    "v_cndmask_b32 v60, 0, 8, vcc\n"
    "v_add_u32 v51, v51, v60\n"
    "v_add_u32 v57, 40, v51\n"
    "v_lshl_add_u32 v50, v47, 3, v50\n"
    "v_lshl_add_u32 v51, v49, 3, v51\n"
    "v_cmp_lt_u32 vcc, v51, v50\n"
    "s_lshl_b64 exec, 0xff, 8\n"
    "v_mov_b32 v57, v51\n"
    "s_mov_b64 exec, -1\n"
    "v_lshrrev_b64 v[62:63], v43, vcc\n"
    "v_lshrrev_b64 v[60:61], v42, vcc\n"
    "v_and_b32 v62, 0xff, v62\n"
    "v_and_b32 v60, 0xff, v60\n"
    "v_bcnt_u32_b32 v62, v62, 0\n"
    "v_bcnt_u32_b32 v60, v60, 0\n"
    "v_lshlrev_b32 v62, 2, v62\n"
    "v_lshlrev_b32 v60, 2, v60\n"
    "v_bfe_u32 v63, s4, v62, 4\n"
    "v_bfe_u32 v61, s4, v60, 4\n"
    "s_lshl_b64 exec, 0xff, 0\n"
    "v_lshl_add_u32 v58, v42, 2, v62\n"
    "s_mov_b64 exec, -1\n"
    "v_lshl_add_u32 v51, v63, 3, v51\n"
    "v_lshl_add_u32 v50, v61, 3, v50\n"
    "v_cmp_lt_u32 vcc, v51, v50\n"
    "s_lshl_b64 exec, 0xff, 16\n"
    "v_mov_b32 v57, v51\n"
    "s_mov_b64 exec, -1\n"
    "v_lshrrev_b64 v[62:63], v43, vcc\n"
    "v_lshrrev_b64 v[60:61], v42, vcc\n"
    "v_and_b32 v62, 0xff, v62\n"
    "v_and_b32 v60, 0xff, v60\n"
    "v_bcnt_u32_b32 v62, v62, 0\n"
    "v_bcnt_u32_b32 v60, v60, 0\n"
    "v_lshlrev_b32 v62, 2, v62\n"
    "v_lshlrev_b32 v60, 2, v60\n"
    "v_bfe_u32 v63, s5, v62, 4\n"
    "v_bfe_u32 v61, s5, v60, 4\n"
    "s_lshl_b64 exec, 0xff, 8\n"
    "v_lshl_add_u32 v58, v42, 2, v62\n"
    "s_mov_b64 exec, -1\n"
    "v_lshl_add_u32 v51, v63, 3, v51\n"
    "v_lshl_add_u32 v50, v61, 3, v50\n"
    "v_cmp_lt_u32 vcc, v51, v50\n"
    "s_lshl_b64 exec, 0xff, 24\n"
    "v_mov_b32 v57, v51\n"
    "s_mov_b64 exec, -1\n"
    "v_lshrrev_b64 v[62:63], v43, vcc\n"
    "v_lshrrev_b64 v[60:61], v42, vcc\n"
    "v_and_b32 v62, 0xff, v62\n"
    "v_and_b32 v60, 0xff, v60\n"
    "v_bcnt_u32_b32 v62, v62, 0\n"
    "v_bcnt_u32_b32 v60, v60, 0\n"
    "v_lshlrev_b32 v62, 2, v62\n"
    "v_lshlrev_b32 v60, 2, v60\n"
    "v_bfe_u32 v63, s6, v62, 4\n"
    "v_bfe_u32 v61, s6, v60, 4\n"
    "s_lshl_b64 exec, 0xff, 16\n"
    "v_lshl_add_u32 v58, v42, 2, v62\n"
    "s_mov_b64 exec, -1\n"
    "v_lshl_add_u32 v51, v63, 3, v51\n"
    "v_lshl_add_u32 v50, v61, 3, v50\n"
    "v_cmp_lt_u32 vcc, v51, v50\n"
    "s_lshl_b64 exec, 0xff, 32\n"
    "v_mov_b32 v57, v51\n"
    "s_mov_b64 exec, -1\n"
    "v_lshrrev_b64 v[62:63], v43, vcc\n"
    "v_lshrrev_b64 v[60:61], v42, vcc\n"
    "v_and_b32 v62, 0xff, v62\n"
    "v_and_b32 v60, 0xff, v60\n"
    "v_bcnt_u32_b32 v62, v62, 0\n"
    "v_bcnt_u32_b32 v60, v60, 0\n"
    "v_lshlrev_b32 v62, 2, v62\n"
    "v_lshlrev_b32 v60, 2, v60\n"
    "v_bfe_u32 v63, s7, v62, 4\n"
    "v_bfe_u32 v61, s7, v60, 4\n"
    "s_lshl_b64 exec, 0xff, 24\n"
    "v_lshl_add_u32 v58, v42, 2, v62\n"
    "s_mov_b64 exec, -1\n"
    "v_lshl_add_u32 v51, v63, 3, v51\n"
    "v_lshl_add_u32 v50, v61, 3, v50\n"
    "v_cmp_lt_u32 vcc, v51, v50\n"
    "s_lshl_b64 exec, 0xff, 40\n"
    "v_mov_b32 v57, v51\n"
    "s_mov_b64 exec, -1\n"
    "v_lshrrev_b64 v[62:63], v43, vcc\n"
    "v_lshrrev_b64 v[60:61], v42, vcc\n"
    "v_and_b32 v62, 0xff, v62\n"
    "v_and_b32 v60, 0xff, v60\n"
    "v_bcnt_u32_b32 v62, v62, 0\n"
    "v_bcnt_u32_b32 v60, v60, 0\n"
    "v_lshlrev_b32 v62, 2, v62\n"
    "v_lshlrev_b32 v60, 2, v60\n"
    "v_bfe_u32 v63, s8, v62, 4\n"
    "v_bfe_u32 v61, s8, v60, 4\n"
    "s_lshl_b64 exec, 0xff, 32\n"
    "v_lshl_add_u32 v58, v42, 2, v62\n"
    "s_mov_b64 exec, -1\n"
    "v_lshl_add_u32 v51, v63, 3, v51\n"
    "v_lshl_add_u32 v50, v61, 3, v50\n"
    "v_cmp_lt_u32 vcc, v51, v50\n"
    "s_nop 1\n"
    "v_lshrrev_b64 v[62:63], v43, vcc\n"
    "v_and_b32 v62, 0xff, v62\n"
    "v_bcnt_u32_b32 v62, v62, 0\n"
    "v_lshlrev_b32 v62, 2, v62\n"
    "v_bfe_u32 v63, s9, v62, 4\n"
    "s_lshl_b64 exec, 0xff, 40\n"
    "v_lshl_add_u32 v58, v42, 2, v62\n"
    "s_mov_b64 exec, -1\n"
    "s_waitcnt lgkmcnt(0)\n"
    "ds_bpermute_b32 v52, v58, v52\n"
    "v_lshl_add_u32 v60, v63, 3, v51\n"
    "v_sub_u32 v57, v51, v57\n"
    "v_sub_u32 v50, 6, v40\n"
    "v_add_u32_dpp v60, v60, v60 quad_perm:[1,0,3,2] row_mask:0xf bank_mask:0xf bound_ctrl:1\n"
    "v_mad_u32_u24 v57, v50, 40, v57\n"
    "v_mul_u32_u24 v50, 15, v50\n"
    "v_add_u32_dpp v60, v60, v60 quad_perm:[2,3,0,1] row_mask:0xf bank_mask:0xf bound_ctrl:1\n"
    "v_cvt_f32_u32 v54, v57\n"
    "v_mul_u32_u24 v57, 103, v57\n"
    "v_add_u32_dpp v60, v60, v60 row_half_mirror row_mask:0xf bank_mask:0xf bound_ctrl:1\n"
    "v_rcp_f32 v54, v54\n"
    "v_mad_u32_u24 v50, v63, 30, v50\n"
    "v_lshrrev_b32 v60, 3, v60\n"
    "v_lshrrev_b32 v57, 4, v57\n"
    "v_add_u32 v60, 224, v60\n"
    "v_mul_f32 v54, 0x49000000, v54\n"
    "v_lshrrev_b32 v56, 5, v60\n"
    "v_cmp_lt_u32 vcc, 0, v63\n"
    "v_cvt_u32_f32 v54, v54\n"
    "v_sub_u32 v44, v60, v56\n"
    "v_add_u32 v60, v60, v56\n"
    "v_cndmask_b32 v54, 0, v54, vcc\n"
    "v_med3_u32 v60, s19, v44, v60\n"
    "v_and_b32 v54, s18, v54\n"
    "v_mul_u32_u24 v60, 394, v60\n"
    "s_add_u32 s23, s76, 410\n"
    "v_add_u32 v50, v50, v57\n"
    "v_lshrrev_b32 v60, 6, v60\n"
    "v_add_u32 v56, 16, v42\n"
    "v_sub_u32 v60, v60, v50\n"
    "v_and_b32 v44, 24, v56\n"
    "v_add_u32 v60, s23, v60\n"
    "v_lshl_add_u32 v55, v44, 2, v55\n"
    "v_and_b32 v60, 0xffff, v60\n"
    "v_lshrrev_b32 v56, 5, v56\n"
    "v_lshl_or_b32 v53, v54, 16, v60\n"
    "v_lshl_add_u32 v55, v56, 3, v55\n"
    "s_mov_b32 exec_hi, 0xffff\n"
    "s_waitcnt lgkmcnt(0)\n"
    "ds_write_b64 v55, v[52:53] offset:128\n"
    "s_mov_b32 exec_hi, -1\n"
    "s_branch .Ldin_go\n"
    ".Ldin_trimo:\n"
    "v_add_u32 v66, 4, v3\n"
    "v_cmp_gt_u32 vcc, s14, v66\n"
    "s_nop 1\n"
    "v_cndmask_b32 v41, 0, v41, vcc\n"
    "v_cndmask_b32 v45, 0, v45, vcc\n"
    "v_add_u32 v66, 8, v3\n"
    "v_cmp_gt_u32 vcc, s14, v66\n"
    "s_nop 1\n"
    "v_cndmask_b32 v42, 0, v42, vcc\n"
    "v_cndmask_b32 v46, 0, v46, vcc\n"
    "v_add_u32 v66, 12, v3\n"
    "v_cmp_gt_u32 vcc, s14, v66\n"
    "s_nop 1\n"
    "v_cndmask_b32 v43, 0, v43, vcc\n"
    "v_cndmask_b32 v47, 0, v47, vcc\n"
    "s_branch .Ldin_trimo_r\n"
    ".Ldin_trimy:\n"
    "v_add_u32 v32, 4, v3\n"
    "v_cmp_gt_u32 vcc, s14, v32\n"
    "s_nop 1\n"
    "v_cndmask_b32 v97, 0, v97, vcc\n"
    "v_cndmask_b32 v101, 0, v101, vcc\n"
    "v_cndmask_b32 v105, 0, v105, vcc\n"
    "v_cndmask_b32 v109, 0, v109, vcc\n"
    "v_cndmask_b32 v113, 0, v113, vcc\n"
    "v_cndmask_b32 v117, 0, v117, vcc\n"
    "v_add_u32 v32, 8, v3\n"
    "v_cmp_gt_u32 vcc, s14, v32\n"
    "s_nop 1\n"
    "v_cndmask_b32 v98, 0, v98, vcc\n"
    "v_cndmask_b32 v102, 0, v102, vcc\n"
    "v_cndmask_b32 v106, 0, v106, vcc\n"
    "v_cndmask_b32 v110, 0, v110, vcc\n"
    "v_cndmask_b32 v114, 0, v114, vcc\n"
    "v_cndmask_b32 v118, 0, v118, vcc\n"
    "v_add_u32 v32, 12, v3\n"
    "v_cmp_gt_u32 vcc, s14, v32\n"
    "s_nop 1\n"
    "v_cndmask_b32 v99, 0, v99, vcc\n"
    "v_cndmask_b32 v103, 0, v103, vcc\n"
    "v_cndmask_b32 v107, 0, v107, vcc\n"
    "v_cndmask_b32 v111, 0, v111, vcc\n"
    "v_cndmask_b32 v115, 0, v115, vcc\n"
    "v_cndmask_b32 v119, 0, v119, vcc\n"
    "s_branch .Ldin_trimy_r\n"
    ".Ldin_A0s:\n"
    "s_cbranch_vccz .Ldin_A0s_np\n"
    "s_setprio 1\n"
    ".Ldin_A0s_np:\n"
    "v_mfma_f32_16x16x32_bf16 v[176:179], v[0:3], v[192:195], v[144:147]\n"
    "v_exp_f32 v160, v160 clamp\n"
    "v_mfma_f32_16x16x32_bf16 v[176:179], v[4:7], v[196:199], v[176:179]\n"
    "v_exp_f32 v161, v161 clamp\n"
    "v_mfma_f32_16x16x32_bf16 v[180:183], v[8:11], v[192:195], v[148:151]\n"
    "v_exp_f32 v162, v162 clamp\n"
    "v_mfma_f32_16x16x32_bf16 v[180:183], v[12:15], v[196:199], v[180:183]\n"
    "v_exp_f32 v163, v163 clamp\n"
    "v_mfma_f32_16x16x32_bf16 v[184:187], v[16:19], v[192:195], v[152:155]\n"
    "v_exp_f32 v164, v164 clamp\n"
    "v_mfma_f32_16x16x32_bf16 v[184:187], v[20:23], v[196:199], v[184:187]\n"
    "v_exp_f32 v165, v165 clamp\n"
    "v_mfma_f32_16x16x32_bf16 v[188:191], v[24:27], v[192:195], v[156:159]\n"
    "v_exp_f32 v166, v166 clamp\n"
    "v_mfma_f32_16x16x32_bf16 v[188:191], v[28:31], v[196:199], v[188:191]\n"
    "v_exp_f32 v167, v167 clamp\n"
    "v_exp_f32 v168, v168 clamp\n"
    "s_mov_b32 m0, s62\n"
    "ds_read_b64 v[216:217], v246 offset:128\n"
    "v_exp_f32 v169, v169 clamp\n"
    "v_add_u32_e64 v251, s66, v244\n"
    "s_waitcnt lgkmcnt(1)\n"
    "v_mfma_f32_4x4x4_16b_bf16 v[224:227], v[228:229], v[208:209], v[224:227] cbsz:4 abid:0\n"
    "v_exp_f32 v170, v170 clamp\n"
    "v_exp_f32 v171, v171 clamp\n"
    "v_mfma_f32_4x4x4_16b_bf16 v[224:227], v[228:229], v[210:211], v[224:227] cbsz:4 abid:1\n"
    "v_exp_f32 v172, v172 clamp\n"
    "v_exp_f32 v173, v173 clamp\n"
    "v_mfma_f32_4x4x4_16b_bf16 v[224:227], v[228:229], v[212:213], v[224:227] cbsz:4 abid:2\n"
    "v_exp_f32 v174, v174 clamp\n"
    "v_exp_f32 v175, v175 clamp\n"
    "v_mfma_f32_4x4x4_16b_bf16 v[224:227], v[228:229], v[214:215], v[224:227] cbsz:4 abid:3\n"
    "s_waitcnt vmcnt(4)\n"
    "s_waitcnt lgkmcnt(0)\n"
    "v_readlane_b32 s60, v216, s59\n"
    "v_readlane_b32 s61, v217, s59\n"
    "ds_read_b64_tr_b16 v[208:209], v240 offset:2048\n"
    "ds_read_b64_tr_b16 v[210:211], v241 offset:2048\n"
    "ds_read_b64_tr_b16 v[212:213], v242 offset:2048\n"
    "ds_read_b64_tr_b16 v[214:215], v243 offset:2048\n"
    "ds_read_u8 v236, v251 offset:0\n"
    "ds_read_u8 v238, v251 offset:1\n"
    "ds_read_b128 v[192:195], v234 offset:4096\n"
    "ds_read_b128 v[196:199], v235 offset:4096\n"
    "s_cmp_eq_u32 s60, -1\n"
    "s_cbranch_scc1 .Ldin_A0s_poll\n"
    ".Ldin_A0s_pr:\n"
    "s_lshr_b32 s94, s60, 12\n"
    "s_and_b32 s94, s94, 0x1ff0\n"
    "v_xor_b32 v252, s94, v250\n"
    "v_pk_add_f32 v[230:231], v[224:225], v[226:227]\n"
    "v_add_f32 v230, v230, v231\n"
    "ds_write_addtid_b32 v230 offset:49152\n"
    "ds_read_b128 v[216:219], v252 offset:0\n"
    "ds_read_b128 v[220:223], v252 offset:64\n"
    "v_mov_b64 v[224:225], 0\n"
    "v_mov_b64_e64 v[226:227], 0\n"
    "v_pk_fma_f32 v[160:161], v[160:161], s[70:71], v[128:129]\n"
    "v_pk_fma_f32 v[162:163], v[162:163], s[70:71], v[130:131]\n"
    "v_pk_add_f32 v[230:231], v[160:161], v[162:163]\n"
    "v_pk_mul_f32 v[232:233], v[160:161], v[162:163]\n"
    "v_pk_fma_f32 v[164:165], v[164:165], s[70:71], v[132:133]\n"
    "v_pk_fma_f32 v[230:231], v[230:231], v[164:165], v[232:233]\n"
    "v_pk_mul_f32 v[232:233], v[232:233], v[164:165]\n"
    "v_pk_fma_f32 v[166:167], v[166:167], s[70:71], v[134:135]\n"
    "v_pk_fma_f32 v[230:231], v[230:231], v[166:167], v[232:233]\n"
    "v_pk_mul_f32 v[232:233], v[232:233], v[166:167]\n"
    "v_pk_fma_f32 v[168:169], v[168:169], s[70:71], v[136:137]\n"
    "v_pk_fma_f32 v[230:231], v[230:231], v[168:169], v[232:233]\n"
    "v_pk_mul_f32 v[232:233], v[232:233], v[168:169]\n"
    "v_pk_fma_f32 v[170:171], v[170:171], s[70:71], v[138:139]\n"
    "v_pk_fma_f32 v[230:231], v[230:231], v[170:171], v[232:233]\n"
    "v_pk_mul_f32 v[232:233], v[232:233], v[170:171]\n"
    "v_pk_fma_f32 v[172:173], v[172:173], s[70:71], v[140:141]\n"
    "v_pk_fma_f32 v[230:231], v[230:231], v[172:173], v[232:233]\n"
    "v_pk_mul_f32 v[232:233], v[232:233], v[172:173]\n"
    "v_pk_fma_f32 v[174:175], v[174:175], s[70:71], v[142:143]\n"
    "v_pk_fma_f32 v[230:231], v[230:231], v[174:175], v[232:233]\n"
    "v_pk_mul_f32 v[232:233], v[232:233], v[174:175]\n"
    "s_add_u32 m0, s68, 2048\n"
    "s_and_b32 s64, s60, 0xffff\n"
    "s_bfe_u32 s94, s60, 0x30013\n"
    "s_bfe_u32 s95, s60, 0x50019\n"
    "s_mul_i32 s94, s94, s56\n"
    "s_lshl4_add_u32 s83, s95, s64\n"
    "s_add_u32 s65, s58, s94\n"
    "s_add_u32 s82, s64, 64\n"
    "s_add_u32 s58, s58, s57\n"
    "s_add_u32 s59, s59, 1\n"
    "s_sub_u32 s95, s61, s76\n"
    "s_lshr_b32 s94, s61, 16\n"
    "s_sext_i32_i16 s95, s95\n"
    "s_mul_i32 s95, s95, s94\n"
    "s_ashr_i32 s95, s95, 16\n"
    "s_max_i32 s95, s95, -8\n"
    "s_min_i32 s95, s95, 4\n"
    "s_add_u32 s72, s77, s95\n"
    "s_cmp_lt_i32 s60, 0\n"
    "s_cselect_b32 s72, 0, s72\n"
    "v_rcp_f32 v232, v232\n"
    "v_rcp_f32 v233, v233\n"
    "v_fma_f32 v230, v230, v232, v247\n"
    "v_fmac_f32 v230, v231, v233\n"
    "v_mbcnt_lo_u32_b32 v232, -1, 0\n"
    "v_mbcnt_hi_u32_b32 v232, -1, v232\n"
    "v_bfe_u32 v233, v232, 3, 1\n"
    "v_and_b32 v232, 7, v232\n"
    "v_lshl_add_u32 v232, v232, 1, v233\n"
    "v_add_u32 v232, s81, v232\n"
    "ds_read_u8 v232, v232 offset:49664\n"
    "s_add_u32 s81, s81, 16\n"
    "s_waitcnt lgkmcnt(0)\n"
    "buffer_load_ushort v232, v232, s[44:47], s63 idxen\n"
    "s_waitcnt vmcnt(0)\n"
    "v_lshlrev_b32 v232, 16, v232\n"
    "v_mul_f32 v230, v230, v232\n"
    "v_cvt_pk_bf16_f32 v230, v230, v230\n"
    "s_add_u32 s66, s66, 16\n"
    "s_waitcnt lgkmcnt(0)\n"
    "ds_write_b16 v245, v230\n"
    "ds_read_b64 v[228:229], v246\n"
    "buffer_load_dwordx4 v[236:237], s[40:43], s67 idxen offen offset:0 nt lds\n"
    "buffer_load_dwordx4 v[238:239], s[40:43], s67 idxen offen offset:1024 nt lds\n"
    ".Ldin_G1s:\n"
    "s_cbranch_vccz .Ldin_G1s_np\n"
    "s_setprio 1\n"
    ".Ldin_G1s_np:\n"
    "v_mfma_f32_16x16x32_bf16 v[160:163], v[0:3], v[192:195], v[144:147]\n"
    "v_exp_f32 v176, v176 clamp\n"
    "v_mfma_f32_16x16x32_bf16 v[160:163], v[4:7], v[196:199], v[160:163]\n"
    "v_exp_f32 v177, v177 clamp\n"
    "v_mfma_f32_16x16x32_bf16 v[164:167], v[8:11], v[192:195], v[148:151]\n"
    "v_exp_f32 v178, v178 clamp\n"
    "v_mfma_f32_16x16x32_bf16 v[164:167], v[12:15], v[196:199], v[164:167]\n"
    "v_exp_f32 v179, v179 clamp\n"
    "v_mfma_f32_16x16x32_bf16 v[168:171], v[16:19], v[192:195], v[152:155]\n"
    "v_exp_f32 v180, v180 clamp\n"
    "v_mfma_f32_16x16x32_bf16 v[168:171], v[20:23], v[196:199], v[168:171]\n"
    "v_exp_f32 v181, v181 clamp\n"
    "v_mfma_f32_16x16x32_bf16 v[172:175], v[24:27], v[192:195], v[156:159]\n"
    "v_exp_f32 v182, v182 clamp\n"
    "v_mfma_f32_16x16x32_bf16 v[172:175], v[28:31], v[196:199], v[172:175]\n"
    "v_exp_f32 v183, v183 clamp\n"
    "v_exp_f32 v184, v184 clamp\n"
    "s_cmp_eq_u32 s66, s80\n"
    "s_cselect_b64 s[66:67], s[64:65], s[66:67]\n"
    "s_add_u32 m0, s68, 4096\n"
    "v_exp_f32 v185, v185 clamp\n"
    "v_add_u32_e64 v251, s66, v244\n"
    "s_waitcnt lgkmcnt(0)\n"
    "v_mfma_f32_4x4x4_16b_bf16 v[224:227], v[228:229], v[200:201], v[224:227] cbsz:4 abid:0\n"
    "v_exp_f32 v186, v186 clamp\n"
    "v_exp_f32 v187, v187 clamp\n"
    "v_mfma_f32_4x4x4_16b_bf16 v[224:227], v[228:229], v[202:203], v[224:227] cbsz:4 abid:1\n"
    "v_exp_f32 v188, v188 clamp\n"
    "v_exp_f32 v189, v189 clamp\n"
    "v_mfma_f32_4x4x4_16b_bf16 v[224:227], v[228:229], v[204:205], v[224:227] cbsz:4 abid:2\n"
    "v_exp_f32 v190, v190 clamp\n"
    "v_exp_f32 v191, v191 clamp\n"
    "v_mfma_f32_4x4x4_16b_bf16 v[224:227], v[228:229], v[206:207], v[224:227] cbsz:4 abid:3\n"
    "s_waitcnt vmcnt(4)\n"
    "s_memrealtime s[34:35]\n"
    "ds_read_b64_tr_b16 v[200:201], v240 offset:4096\n"
    "ds_read_b64_tr_b16 v[202:203], v241 offset:4096\n"
    "ds_read_b64_tr_b16 v[204:205], v242 offset:4096\n"
    "ds_read_b64_tr_b16 v[206:207], v243 offset:4096\n"
    "ds_read_u8 v236, v251 offset:0\n"
    "ds_read_u8 v238, v251 offset:1\n"
    "ds_read_b128 v[192:195], v234 offset:6144\n"
    "ds_read_b128 v[196:199], v235 offset:6144\n"
    "v_pk_fma_f32 v[176:177], v[176:177], s[70:71], v[128:129]\n"
    "v_pk_fma_f32 v[178:179], v[178:179], s[70:71], v[130:131]\n"
    "v_pk_add_f32 v[230:231], v[176:177], v[178:179]\n"
    "v_pk_mul_f32 v[232:233], v[176:177], v[178:179]\n"
    "v_pk_fma_f32 v[180:181], v[180:181], s[70:71], v[132:133]\n"
    "v_pk_fma_f32 v[230:231], v[230:231], v[180:181], v[232:233]\n"
    "v_pk_mul_f32 v[232:233], v[232:233], v[180:181]\n"
    "v_pk_fma_f32 v[182:183], v[182:183], s[70:71], v[134:135]\n"
    "v_pk_fma_f32 v[230:231], v[230:231], v[182:183], v[232:233]\n"
    "v_pk_mul_f32 v[232:233], v[232:233], v[182:183]\n"
    "v_pk_fma_f32 v[184:185], v[184:185], s[70:71], v[136:137]\n"
    "v_pk_fma_f32 v[230:231], v[230:231], v[184:185], v[232:233]\n"
    "v_pk_mul_f32 v[232:233], v[232:233], v[184:185]\n"
    "v_pk_fma_f32 v[186:187], v[186:187], s[70:71], v[138:139]\n"
    "v_pk_fma_f32 v[230:231], v[230:231], v[186:187], v[232:233]\n"
    "v_pk_mul_f32 v[232:233], v[232:233], v[186:187]\n"
    "v_pk_fma_f32 v[188:189], v[188:189], s[70:71], v[140:141]\n"
    "v_pk_fma_f32 v[230:231], v[230:231], v[188:189], v[232:233]\n"
    "v_pk_mul_f32 v[232:233], v[232:233], v[188:189]\n"
    "v_pk_fma_f32 v[190:191], v[190:191], s[70:71], v[142:143]\n"
    "v_pk_fma_f32 v[230:231], v[230:231], v[190:191], v[232:233]\n"
    "v_pk_mul_f32 v[232:233], v[232:233], v[190:191]\n"
    "v_rcp_f32 v232, v232\n"
    "v_rcp_f32 v233, v233\n"
    "v_fma_f32 v230, v230, v232, v247\n"
    "v_fmac_f32 v230, v231, v233\n"
    "v_mbcnt_lo_u32_b32 v232, -1, 0\n"
    "v_mbcnt_hi_u32_b32 v232, -1, v232\n"
    "v_bfe_u32 v233, v232, 3, 1\n"
    "v_and_b32 v232, 7, v232\n"
    "v_lshl_add_u32 v232, v232, 1, v233\n"
    "v_add_u32 v232, s81, v232\n"
    "ds_read_u8 v232, v232 offset:49664\n"
    "s_add_u32 s81, s81, 16\n"
    "s_waitcnt lgkmcnt(0)\n"
    "buffer_load_ushort v232, v232, s[44:47], s63 idxen\n"
    "s_waitcnt vmcnt(0)\n"
    "v_lshlrev_b32 v232, 16, v232\n"
    "v_mul_f32 v230, v230, v232\n"
    "v_cvt_pk_bf16_f32 v230, v230, v230\n"
    "s_add_u32 s66, s66, 16\n"
    "s_waitcnt lgkmcnt(0)\n"
    "ds_write_b16 v245, v230\n"
    "ds_read_b64 v[228:229], v246\n"
    "s_cmp_lt_u32 s34, s76\n"
    "s_cbranch_scc1 .Ldin_G1s_wait\n"
    ".Ldin_G1s_go:\n"
    "s_add_u32 s76, s76, s72\n"
    "buffer_load_dwordx4 v[236:237], s[40:43], s67 idxen offen offset:0 nt lds\n"
    "buffer_load_dwordx4 v[238:239], s[40:43], s67 idxen offen offset:1024 nt lds\n"
    "s_cmp_eq_u32 s66, s82\n"
    "s_cbranch_scc1 .Ldin_L2s\n"
    ".Ldin_G2s:\n"
    "v_mfma_f32_16x16x32_bf16 v[176:179], v[0:3], v[192:195], v[144:147]\n"
    "v_exp_f32 v160, v160 clamp\n"
    "v_mfma_f32_16x16x32_bf16 v[176:179], v[4:7], v[196:199], v[176:179]\n"
    "v_exp_f32 v161, v161 clamp\n"
    "v_mfma_f32_16x16x32_bf16 v[180:183], v[8:11], v[192:195], v[148:151]\n"
    "v_exp_f32 v162, v162 clamp\n"
    "v_mfma_f32_16x16x32_bf16 v[180:183], v[12:15], v[196:199], v[180:183]\n"
    "v_exp_f32 v163, v163 clamp\n"
    "v_mfma_f32_16x16x32_bf16 v[184:187], v[16:19], v[192:195], v[152:155]\n"
    "v_exp_f32 v164, v164 clamp\n"
    "v_mfma_f32_16x16x32_bf16 v[184:187], v[20:23], v[196:199], v[184:187]\n"
    "v_exp_f32 v165, v165 clamp\n"
    "v_mfma_f32_16x16x32_bf16 v[188:191], v[24:27], v[192:195], v[156:159]\n"
    "v_exp_f32 v166, v166 clamp\n"
    "v_mfma_f32_16x16x32_bf16 v[188:191], v[28:31], v[196:199], v[188:191]\n"
    "v_exp_f32 v167, v167 clamp\n"
    "v_exp_f32 v168, v168 clamp\n"
    "s_cmp_eq_u32 s66, s80\n"
    "s_cselect_b64 s[66:67], s[64:65], s[66:67]\n"
    "s_add_u32 m0, s68, 6144\n"
    "v_exp_f32 v169, v169 clamp\n"
    "v_add_u32 v251, s66, v244\n"
    "s_waitcnt lgkmcnt(0)\n"
    "v_mfma_f32_4x4x4_16b_bf16 v[224:227], v[228:229], v[208:209], v[224:227] cbsz:4 abid:0\n"
    "v_exp_f32 v170, v170 clamp\n"
    "v_exp_f32 v171, v171 clamp\n"
    "v_mfma_f32_4x4x4_16b_bf16 v[224:227], v[228:229], v[210:211], v[224:227] cbsz:4 abid:1\n"
    "v_exp_f32 v172, v172 clamp\n"
    "v_exp_f32 v173, v173 clamp\n"
    "v_mfma_f32_4x4x4_16b_bf16 v[224:227], v[228:229], v[212:213], v[224:227] cbsz:4 abid:2\n"
    "v_exp_f32 v174, v174 clamp\n"
    "v_exp_f32 v175, v175 clamp\n"
    "v_mfma_f32_4x4x4_16b_bf16 v[224:227], v[228:229], v[214:215], v[224:227] cbsz:4 abid:3\n"
    "s_waitcnt vmcnt(4)\n"
    "s_memrealtime s[34:35]\n"
    "ds_read_b64_tr_b16 v[208:209], v240 offset:6144\n"
    "ds_read_b64_tr_b16 v[210:211], v241 offset:6144\n"
    "ds_read_b64_tr_b16 v[212:213], v242 offset:6144\n"
    "ds_read_b64_tr_b16 v[214:215], v243 offset:6144\n"
    "ds_read_u8 v236, v251 offset:0\n"
    "ds_read_u8 v238, v251 offset:1\n"
    "ds_read_b128 v[192:195], v234 offset:0\n"
    "ds_read_b128 v[196:199], v235 offset:0\n"
    "v_pk_fma_f32 v[160:161], v[160:161], s[70:71], v[128:129]\n"
    "v_pk_fma_f32 v[162:163], v[162:163], s[70:71], v[130:131]\n"
    "v_pk_add_f32 v[230:231], v[160:161], v[162:163]\n"
    "v_pk_mul_f32 v[232:233], v[160:161], v[162:163]\n"
    "v_pk_fma_f32 v[164:165], v[164:165], s[70:71], v[132:133]\n"
    "v_pk_fma_f32 v[230:231], v[230:231], v[164:165], v[232:233]\n"
    "v_pk_mul_f32 v[232:233], v[232:233], v[164:165]\n"
    "v_pk_fma_f32 v[166:167], v[166:167], s[70:71], v[134:135]\n"
    "v_pk_fma_f32 v[230:231], v[230:231], v[166:167], v[232:233]\n"
    "v_pk_mul_f32 v[232:233], v[232:233], v[166:167]\n"
    "v_pk_fma_f32 v[168:169], v[168:169], s[70:71], v[136:137]\n"
    "v_pk_fma_f32 v[230:231], v[230:231], v[168:169], v[232:233]\n"
    "v_pk_mul_f32 v[232:233], v[232:233], v[168:169]\n"
    "v_pk_fma_f32 v[170:171], v[170:171], s[70:71], v[138:139]\n"
    "v_pk_fma_f32 v[230:231], v[230:231], v[170:171], v[232:233]\n"
    "v_pk_mul_f32 v[232:233], v[232:233], v[170:171]\n"
    "v_pk_fma_f32 v[172:173], v[172:173], s[70:71], v[140:141]\n"
    "v_pk_fma_f32 v[230:231], v[230:231], v[172:173], v[232:233]\n"
    "v_pk_mul_f32 v[232:233], v[232:233], v[172:173]\n"
    "v_pk_fma_f32 v[174:175], v[174:175], s[70:71], v[142:143]\n"
    "v_pk_fma_f32 v[230:231], v[230:231], v[174:175], v[232:233]\n"
    "v_pk_mul_f32 v[232:233], v[232:233], v[174:175]\n"
    "v_rcp_f32 v232, v232\n"
    "v_rcp_f32 v233, v233\n"
    "v_fma_f32 v230, v230, v232, v247\n"
    "v_fmac_f32 v230, v231, v233\n"
    "v_mbcnt_lo_u32_b32 v232, -1, 0\n"
    "v_mbcnt_hi_u32_b32 v232, -1, v232\n"
    "v_bfe_u32 v233, v232, 3, 1\n"
    "v_and_b32 v232, 7, v232\n"
    "v_lshl_add_u32 v232, v232, 1, v233\n"
    "v_add_u32 v232, s81, v232\n"
    "ds_read_u8 v232, v232 offset:49664\n"
    "s_add_u32 s81, s81, 16\n"
    "s_waitcnt lgkmcnt(0)\n"
    "buffer_load_ushort v232, v232, s[44:47], s63 idxen\n"
    "s_waitcnt vmcnt(0)\n"
    "v_lshlrev_b32 v232, 16, v232\n"
    "v_mul_f32 v230, v230, v232\n"
    "v_cvt_pk_bf16_f32 v230, v230, v230\n"
    "s_add_u32 s66, s66, 16\n"
    "s_waitcnt lgkmcnt(0)\n"
    "ds_write_b16 v245, v230\n"
    "ds_read_b64 v[228:229], v246\n"
    "s_cmp_lt_u32 s34, s76\n"
    "s_cbranch_scc1 .Ldin_G2s_wait\n"
    ".Ldin_G2s_go:\n"
    "s_sub_u32 s94, s34, 30\n"
    "s_max_u32 s76, s76, s94\n"
    "s_add_u32 s76, s76, s72\n"
    "buffer_load_dwordx4 v[236:237], s[40:43], s67 idxen offen offset:0 nt lds\n"
    "buffer_load_dwordx4 v[238:239], s[40:43], s67 idxen offen offset:1024 nt lds\n"
    "s_cmp_eq_u32 s66, s82\n"
    "s_cbranch_scc1 .Ldin_L3s\n"
    ".Ldin_G3s:\n"
    "v_mfma_f32_16x16x32_bf16 v[160:163], v[0:3], v[192:195], v[144:147]\n"
    "v_exp_f32 v176, v176 clamp\n"
    "v_mfma_f32_16x16x32_bf16 v[160:163], v[4:7], v[196:199], v[160:163]\n"
    "v_exp_f32 v177, v177 clamp\n"
    "v_mfma_f32_16x16x32_bf16 v[164:167], v[8:11], v[192:195], v[148:151]\n"
    "v_exp_f32 v178, v178 clamp\n"
    "v_mfma_f32_16x16x32_bf16 v[164:167], v[12:15], v[196:199], v[164:167]\n"
    "v_exp_f32 v179, v179 clamp\n"
    "v_mfma_f32_16x16x32_bf16 v[168:171], v[16:19], v[192:195], v[152:155]\n"
    "v_exp_f32 v180, v180 clamp\n"
    "v_mfma_f32_16x16x32_bf16 v[168:171], v[20:23], v[196:199], v[168:171]\n"
    "v_exp_f32 v181, v181 clamp\n"
    "v_mfma_f32_16x16x32_bf16 v[172:175], v[24:27], v[192:195], v[156:159]\n"
    "v_exp_f32 v182, v182 clamp\n"
    "v_mfma_f32_16x16x32_bf16 v[172:175], v[28:31], v[196:199], v[172:175]\n"
    "v_exp_f32 v183, v183 clamp\n"
    "v_exp_f32 v184, v184 clamp\n"
    "s_cmp_eq_u32 s66, s80\n"
    "s_cselect_b64 s[66:67], s[64:65], s[66:67]\n"
    "s_add_u32 m0, s68, 0\n"
    "v_exp_f32 v185, v185 clamp\n"
    "v_add_u32_e64 v251, s66, v244\n"
    "s_waitcnt lgkmcnt(0)\n"
    "v_mfma_f32_4x4x4_16b_bf16 v[224:227], v[228:229], v[200:201], v[224:227] cbsz:4 abid:0\n"
    "v_exp_f32 v186, v186 clamp\n"
    "v_exp_f32 v187, v187 clamp\n"
    "v_mfma_f32_4x4x4_16b_bf16 v[224:227], v[228:229], v[202:203], v[224:227] cbsz:4 abid:1\n"
    "v_exp_f32 v188, v188 clamp\n"
    "v_exp_f32 v189, v189 clamp\n"
    "v_mfma_f32_4x4x4_16b_bf16 v[224:227], v[228:229], v[204:205], v[224:227] cbsz:4 abid:2\n"
    "v_exp_f32 v190, v190 clamp\n"
    "v_exp_f32 v191, v191 clamp\n"
    "v_mfma_f32_4x4x4_16b_bf16 v[224:227], v[228:229], v[206:207], v[224:227] cbsz:4 abid:3\n"
    "s_waitcnt vmcnt(4)\n"
    "s_memrealtime s[34:35]\n"
    "ds_read_b64_tr_b16 v[200:201], v240 offset:0\n"
    "ds_read_b64_tr_b16 v[202:203], v241 offset:0\n"
    "ds_read_b64_tr_b16 v[204:205], v242 offset:0\n"
    "ds_read_b64_tr_b16 v[206:207], v243 offset:0\n"
    "ds_read_u8 v236, v251 offset:0\n"
    "ds_read_u8 v238, v251 offset:1\n"
    "ds_read_b128 v[192:195], v234 offset:2048\n"
    "ds_read_b128 v[196:199], v235 offset:2048\n"
    "v_pk_fma_f32 v[176:177], v[176:177], s[70:71], v[128:129]\n"
    "v_pk_fma_f32 v[178:179], v[178:179], s[70:71], v[130:131]\n"
    "v_pk_add_f32 v[230:231], v[176:177], v[178:179]\n"
    "v_pk_mul_f32 v[232:233], v[176:177], v[178:179]\n"
    "v_pk_fma_f32 v[180:181], v[180:181], s[70:71], v[132:133]\n"
    "v_pk_fma_f32 v[230:231], v[230:231], v[180:181], v[232:233]\n"
    "v_pk_mul_f32 v[232:233], v[232:233], v[180:181]\n"
    "v_pk_fma_f32 v[182:183], v[182:183], s[70:71], v[134:135]\n"
    "v_pk_fma_f32 v[230:231], v[230:231], v[182:183], v[232:233]\n"
    "v_pk_mul_f32 v[232:233], v[232:233], v[182:183]\n"
    "v_pk_fma_f32 v[184:185], v[184:185], s[70:71], v[136:137]\n"
    "v_pk_fma_f32 v[230:231], v[230:231], v[184:185], v[232:233]\n"
    "v_pk_mul_f32 v[232:233], v[232:233], v[184:185]\n"
    "v_pk_fma_f32 v[186:187], v[186:187], s[70:71], v[138:139]\n"
    "v_pk_fma_f32 v[230:231], v[230:231], v[186:187], v[232:233]\n"
    "v_pk_mul_f32 v[232:233], v[232:233], v[186:187]\n"
    "v_pk_fma_f32 v[188:189], v[188:189], s[70:71], v[140:141]\n"
    "v_pk_fma_f32 v[230:231], v[230:231], v[188:189], v[232:233]\n"
    "v_pk_mul_f32 v[232:233], v[232:233], v[188:189]\n"
    "v_pk_fma_f32 v[190:191], v[190:191], s[70:71], v[142:143]\n"
    "v_pk_fma_f32 v[230:231], v[230:231], v[190:191], v[232:233]\n"
    "v_pk_mul_f32 v[232:233], v[232:233], v[190:191]\n"
    "v_rcp_f32 v232, v232\n"
    "v_rcp_f32 v233, v233\n"
    "v_fma_f32 v230, v230, v232, v247\n"
    "v_fmac_f32 v230, v231, v233\n"
    "v_mbcnt_lo_u32_b32 v232, -1, 0\n"
    "v_mbcnt_hi_u32_b32 v232, -1, v232\n"
    "v_bfe_u32 v233, v232, 3, 1\n"
    "v_and_b32 v232, 7, v232\n"
    "v_lshl_add_u32 v232, v232, 1, v233\n"
    "v_add_u32 v232, s81, v232\n"
    "ds_read_u8 v232, v232 offset:49664\n"
    "s_add_u32 s81, s81, 16\n"
    "s_waitcnt lgkmcnt(0)\n"
    "buffer_load_ushort v232, v232, s[44:47], s63 idxen\n"
    "s_waitcnt vmcnt(0)\n"
    "v_lshlrev_b32 v232, 16, v232\n"
    "v_mul_f32 v230, v230, v232\n"
    "v_cvt_pk_bf16_f32 v230, v230, v230\n"
    "s_add_u32 s66, s66, 16\n"
    "s_waitcnt lgkmcnt(0)\n"
    "ds_write_b16 v245, v230\n"
    "ds_read_b64 v[228:229], v246\n"
    "s_cmp_lt_u32 s34, s76\n"
    "s_cbranch_scc1 .Ldin_G3s_wait\n"
    ".Ldin_G3s_go:\n"
    "s_add_u32 s76, s76, s72\n"
    "buffer_load_dwordx4 v[236:237], s[40:43], s67 idxen offen offset:0 nt lds\n"
    "buffer_load_dwordx4 v[238:239], s[40:43], s67 idxen offen offset:1024 nt lds\n"
    "s_cmp_eq_u32 s66, s82\n"
    "s_cbranch_scc1 .Ldin_L0s\n"
    ".Ldin_G0s:\n"
    "s_setprio 0\n"
    "v_mfma_f32_16x16x32_bf16 v[176:179], v[0:3], v[192:195], v[144:147]\n"
    "v_exp_f32 v160, v160 clamp\n"
    "v_mfma_f32_16x16x32_bf16 v[176:179], v[4:7], v[196:199], v[176:179]\n"
    "v_exp_f32 v161, v161 clamp\n"
    "v_mfma_f32_16x16x32_bf16 v[180:183], v[8:11], v[192:195], v[148:151]\n"
    "v_exp_f32 v162, v162 clamp\n"
    "v_mfma_f32_16x16x32_bf16 v[180:183], v[12:15], v[196:199], v[180:183]\n"
    "v_exp_f32 v163, v163 clamp\n"
    "v_mfma_f32_16x16x32_bf16 v[184:187], v[16:19], v[192:195], v[152:155]\n"
    "v_exp_f32 v164, v164 clamp\n"
    "v_mfma_f32_16x16x32_bf16 v[184:187], v[20:23], v[196:199], v[184:187]\n"
    "v_exp_f32 v165, v165 clamp\n"
    "v_mfma_f32_16x16x32_bf16 v[188:191], v[24:27], v[192:195], v[156:159]\n"
    "v_exp_f32 v166, v166 clamp\n"
    "v_mfma_f32_16x16x32_bf16 v[188:191], v[28:31], v[196:199], v[188:191]\n"
    "v_exp_f32 v167, v167 clamp\n"
    "v_exp_f32 v168, v168 clamp\n"
    "s_cmp_eq_u32 s66, s80\n"
    "s_cselect_b64 s[66:67], s[64:65], s[66:67]\n"
    "s_add_u32 m0, s68, 2048\n"
    "v_exp_f32 v169, v169 clamp\n"
    "v_add_u32_e64 v251, s66, v244\n"
    "s_waitcnt lgkmcnt(0)\n"
    "v_mfma_f32_4x4x4_16b_bf16 v[224:227], v[228:229], v[208:209], v[224:227] cbsz:4 abid:0\n"
    "v_exp_f32 v170, v170 clamp\n"
    "v_exp_f32 v171, v171 clamp\n"
    "v_mfma_f32_4x4x4_16b_bf16 v[224:227], v[228:229], v[210:211], v[224:227] cbsz:4 abid:1\n"
    "v_exp_f32 v172, v172 clamp\n"
    "v_exp_f32 v173, v173 clamp\n"
    "v_mfma_f32_4x4x4_16b_bf16 v[224:227], v[228:229], v[212:213], v[224:227] cbsz:4 abid:2\n"
    "v_exp_f32 v174, v174 clamp\n"
    "v_exp_f32 v175, v175 clamp\n"
    "v_mfma_f32_4x4x4_16b_bf16 v[224:227], v[228:229], v[214:215], v[224:227] cbsz:4 abid:3\n"
    "s_waitcnt vmcnt(4)\n"
    "s_memrealtime s[34:35]\n"
    "ds_read_b64_tr_b16 v[208:209], v240 offset:2048\n"
    "ds_read_b64_tr_b16 v[210:211], v241 offset:2048\n"
    "ds_read_b64_tr_b16 v[212:213], v242 offset:2048\n"
    "ds_read_b64_tr_b16 v[214:215], v243 offset:2048\n"
    "ds_read_u8 v236, v251 offset:0\n"
    "ds_read_u8 v238, v251 offset:1\n"
    "ds_read_b128 v[192:195], v234 offset:4096\n"
    "ds_read_b128 v[196:199], v235 offset:4096\n"
    "v_pk_fma_f32 v[160:161], v[160:161], s[70:71], v[128:129]\n"
    "v_pk_fma_f32 v[162:163], v[162:163], s[70:71], v[130:131]\n"
    "v_pk_add_f32 v[230:231], v[160:161], v[162:163]\n"
    "v_pk_mul_f32 v[232:233], v[160:161], v[162:163]\n"
    "v_pk_fma_f32 v[164:165], v[164:165], s[70:71], v[132:133]\n"
    "v_pk_fma_f32 v[230:231], v[230:231], v[164:165], v[232:233]\n"
    "v_pk_mul_f32 v[232:233], v[232:233], v[164:165]\n"
    "v_pk_fma_f32 v[166:167], v[166:167], s[70:71], v[134:135]\n"
    "v_pk_fma_f32 v[230:231], v[230:231], v[166:167], v[232:233]\n"
    "v_pk_mul_f32 v[232:233], v[232:233], v[166:167]\n"
    "v_pk_fma_f32 v[168:169], v[168:169], s[70:71], v[136:137]\n"
    "v_pk_fma_f32 v[230:231], v[230:231], v[168:169], v[232:233]\n"
    "v_pk_mul_f32 v[232:233], v[232:233], v[168:169]\n"
    "v_pk_fma_f32 v[170:171], v[170:171], s[70:71], v[138:139]\n"
    "v_pk_fma_f32 v[230:231], v[230:231], v[170:171], v[232:233]\n"
    "v_pk_mul_f32 v[232:233], v[232:233], v[170:171]\n"
    "v_pk_fma_f32 v[172:173], v[172:173], s[70:71], v[140:141]\n"
    "v_pk_fma_f32 v[230:231], v[230:231], v[172:173], v[232:233]\n"
    "v_pk_mul_f32 v[232:233], v[232:233], v[172:173]\n"
    "v_pk_fma_f32 v[174:175], v[174:175], s[70:71], v[142:143]\n"
    "v_pk_fma_f32 v[230:231], v[230:231], v[174:175], v[232:233]\n"
    "v_pk_mul_f32 v[232:233], v[232:233], v[174:175]\n"
    "v_rcp_f32 v232, v232\n"
    "v_rcp_f32 v233, v233\n"
    "v_fma_f32 v230, v230, v232, v247\n"
    "v_fmac_f32 v230, v231, v233\n"
    "v_mbcnt_lo_u32_b32 v232, -1, 0\n"
    "v_mbcnt_hi_u32_b32 v232, -1, v232\n"
    "v_bfe_u32 v233, v232, 3, 1\n"
    "v_and_b32 v232, 7, v232\n"
    "v_lshl_add_u32 v232, v232, 1, v233\n"
    "v_add_u32 v232, s81, v232\n"
    "ds_read_u8 v232, v232 offset:49664\n"
    "s_add_u32 s81, s81, 16\n"
    "s_waitcnt lgkmcnt(0)\n"
    "buffer_load_ushort v232, v232, s[44:47], s63 idxen\n"
    "s_waitcnt vmcnt(0)\n"
    "v_lshlrev_b32 v232, 16, v232\n"
    "v_mul_f32 v230, v230, v232\n"
    "v_cvt_pk_bf16_f32 v230, v230, v230\n"
    "s_add_u32 s66, s66, 16\n"
    "s_waitcnt lgkmcnt(0)\n"
    "ds_write_b16 v245, v230\n"
    "ds_read_b64 v[228:229], v246\n"
    "s_cmp_lt_u32 s34, s76\n"
    "s_cbranch_scc1 .Ldin_G0s_wait\n"
    ".Ldin_G0s_go:\n"
    "s_sub_u32 s94, s34, 30\n"
    "s_max_u32 s76, s76, s94\n"
    "s_add_u32 s76, s76, s72\n"
    "buffer_load_dwordx4 v[236:237], s[40:43], s67 idxen offen offset:0 nt lds\n"
    "buffer_load_dwordx4 v[238:239], s[40:43], s67 idxen offen offset:1024 nt lds\n"
    "s_cmp_eq_u32 s66, s82\n"
    "s_cbranch_scc1 .Ldin_L1s\n"
    "s_branch .Ldin_G1s\n"
    ".Ldin_A0s_poll:\n"
    "s_sleep 1\n"
    "ds_read_b64 v[216:217], v246 offset:128\n"
    "s_waitcnt lgkmcnt(0)\n"
    "v_readlane_b32 s60, v216, s59\n"
    "v_readlane_b32 s61, v217, s59\n"
    "s_cmp_eq_u32 s60, -1\n"
    "s_cbranch_scc1 .Ldin_A0s_poll\n"
    "s_branch .Ldin_A0s_pr\n"
    ".Ldin_G1s_slp:\n"
    "s_sleep 1\n"
    ".Ldin_G1s_wait:\n"
    "s_memrealtime s[34:35]\n"
    "s_waitcnt lgkmcnt(0)\n"
    "s_sub_u32 s94, s34, s76\n"
    "s_cmp_lt_i32 s94, 0\n"
    "s_cbranch_scc1 .Ldin_G1s_slp\n"
    "s_branch .Ldin_G1s_go\n"
    ".Ldin_G2s_slp:\n"
    "s_sleep 1\n"
    ".Ldin_G2s_wait:\n"
    "s_memrealtime s[34:35]\n"
    "s_waitcnt lgkmcnt(0)\n"
    "s_sub_u32 s94, s34, s76\n"
    "s_cmp_lt_i32 s94, 0\n"
    "s_cbranch_scc1 .Ldin_G2s_slp\n"
    "s_branch .Ldin_G2s_go\n"
    ".Ldin_G3s_slp:\n"
    "s_sleep 1\n"
    ".Ldin_G3s_wait:\n"
    "s_memrealtime s[34:35]\n"
    "s_waitcnt lgkmcnt(0)\n"
    "s_sub_u32 s94, s34, s76\n"
    "s_cmp_lt_i32 s94, 0\n"
    "s_cbranch_scc1 .Ldin_G3s_slp\n"
    "s_branch .Ldin_G3s_go\n"
    ".Ldin_G0s_slp:\n"
    "s_sleep 1\n"
    ".Ldin_G0s_wait:\n"
    "s_memrealtime s[34:35]\n"
    "s_waitcnt lgkmcnt(0)\n"
    "s_sub_u32 s94, s34, s76\n"
    "s_cmp_lt_i32 s94, 0\n"
    "s_cbranch_scc1 .Ldin_G0s_slp\n"
    "s_branch .Ldin_G0s_go\n"
    ".p2align 5\n"
    ".Ldin_L2s:\n"
    "s_setprio 0\n"
    "s_cmp_lt_i32 s60, 0\n"
    "s_cbranch_scc1 .Ldin_E0s\n"
    "ds_read_b128 v[144:147], v252 offset:8192\n"
    "ds_read_b128 v[148:151], v252 offset:8256\n"
    "ds_read_b128 v[152:155], v252 offset:16384\n"
    "ds_read_b128 v[156:159], v252 offset:16448\n"
    "s_add_u32 s76, s76, s73\n"
    "s_add_u32 m0, s68, 6144\n"
    "v_and_b32 v216, v248, v216\n"
    "v_and_b32 v217, v249, v217\n"
    "v_and_b32 v218, v248, v218\n"
    "v_and_b32 v219, v249, v219\n"
    "v_mfma_f32_16x16x16_bf16 v[176:179], v[216:217], v[96:97], v[32:35]\n"
    "v_and_b32 v220, v248, v220\n"
    "v_and_b32 v221, v249, v221\n"
    "v_and_b32 v222, v248, v222\n"
    "v_mfma_f32_16x16x16_bf16 v[180:183], v[218:219], v[98:99], v[36:39]\n"
    "v_exp_f32 v160, v160 clamp\n"
    "v_and_b32 v223, v249, v223\n"
    "v_mfma_f32_16x16x16_bf16 v[184:187], v[220:221], v[100:101], v[40:43]\n"
    "v_add_u32 v251, s66, v244\n"
    "v_cvt_pk_bf16_f32 v0, v176, v177\n"
    "v_cvt_pk_bf16_f32 v1, v178, v179\n"
    "s_waitcnt lgkmcnt(4)\n"
    "v_mfma_f32_16x16x16_bf16 v[188:191], v[222:223], v[102:103], v[44:47]\n"
    "v_exp_f32 v161, v161 clamp\n"
    "v_cvt_pk_bf16_f32 v2, v180, v181\n"
    "v_mfma_f32_4x4x4_16b_bf16 v[224:227], v[228:229], v[208:209], v[224:227] cbsz:4 abid:0\n"
    "v_cvt_pk_bf16_f32 v3, v182, v183\n"
    "v_mfma_f32_16x16x16_bf16 v[176:179], v[216:217], v[104:105], v[48:51]\n"
    "v_exp_f32 v162, v162 clamp\n"
    "v_cvt_pk_bf16_f32 v4, v184, v185\n"
    "v_mfma_f32_16x16x16_bf16 v[180:183], v[218:219], v[106:107], v[52:55]\n"
    "v_cvt_pk_bf16_f32 v5, v186, v187\n"
    "v_cvt_pk_bf16_f32 v6, v188, v189\n"
    "v_cvt_pk_bf16_f32 v7, v190, v191\n"
    "v_mfma_f32_16x16x16_bf16 v[184:187], v[220:221], v[108:109], v[56:59]\n"
    "v_exp_f32 v163, v163 clamp\n"
    "v_cvt_pk_bf16_f32 v8, v176, v177\n"
    "v_mfma_f32_4x4x4_16b_bf16 v[224:227], v[228:229], v[210:211], v[224:227] cbsz:4 abid:1\n"
    "v_cvt_pk_bf16_f32 v9, v178, v179\n"
    "v_mfma_f32_16x16x16_bf16 v[188:191], v[222:223], v[110:111], v[60:63]\n"
    "v_exp_f32 v164, v164 clamp\n"
    "v_cvt_pk_bf16_f32 v10, v180, v181\n"
    "v_mfma_f32_16x16x16_bf16 v[176:179], v[216:217], v[112:113], v[64:67]\n"
    "v_cvt_pk_bf16_f32 v11, v182, v183\n"
    "v_cvt_pk_bf16_f32 v12, v184, v185\n"
    "v_cvt_pk_bf16_f32 v13, v186, v187\n"
    "v_mfma_f32_16x16x16_bf16 v[180:183], v[218:219], v[114:115], v[68:71]\n"
    "v_exp_f32 v165, v165 clamp\n"
    "v_cvt_pk_bf16_f32 v14, v188, v189\n"
    "v_mfma_f32_4x4x4_16b_bf16 v[224:227], v[228:229], v[212:213], v[224:227] cbsz:4 abid:2\n"
    "v_cvt_pk_bf16_f32 v15, v190, v191\n"
    "v_mfma_f32_16x16x16_bf16 v[184:187], v[220:221], v[116:117], v[72:75]\n"
    "v_exp_f32 v166, v166 clamp\n"
    "v_cvt_pk_bf16_f32 v16, v176, v177\n"
    "v_mfma_f32_16x16x16_bf16 v[188:191], v[222:223], v[118:119], v[76:79]\n"
    "v_cvt_pk_bf16_f32 v17, v178, v179\n"
    "v_cvt_pk_bf16_f32 v18, v180, v181\n"
    "v_cvt_pk_bf16_f32 v19, v182, v183\n"
    "v_mfma_f32_16x16x16_bf16 v[176:179], v[216:217], v[120:121], v[80:83]\n"
    "v_exp_f32 v167, v167 clamp\n"
    "v_cvt_pk_bf16_f32 v20, v184, v185\n"
    "v_mfma_f32_4x4x4_16b_bf16 v[224:227], v[228:229], v[214:215], v[224:227] cbsz:4 abid:3\n"
    "v_cvt_pk_bf16_f32 v21, v186, v187\n"
    "v_mfma_f32_16x16x16_bf16 v[180:183], v[218:219], v[122:123], v[84:87]\n"
    "v_exp_f32 v168, v168 clamp\n"
    "v_cvt_pk_bf16_f32 v22, v188, v189\n"
    "v_mfma_f32_16x16x16_bf16 v[184:187], v[220:221], v[124:125], v[88:91]\n"
    "v_cvt_pk_bf16_f32 v23, v190, v191\n"
    "v_cvt_pk_bf16_f32 v24, v176, v177\n"
    "v_cvt_pk_bf16_f32 v25, v178, v179\n"
    "v_mfma_f32_16x16x16_bf16 v[188:191], v[222:223], v[126:127], v[92:95]\n"
    "v_exp_f32 v169, v169 clamp\n"
    "v_cvt_pk_bf16_f32 v26, v180, v181\n"
    "s_waitcnt lgkmcnt(0)\n"
    "v_mfma_f32_16x16x32_bf16 v[176:179], v[0:3], v[192:195], v[144:147]\n"
    "v_cvt_pk_bf16_f32 v27, v182, v183\n"
    "v_cvt_pk_bf16_f32 v28, v184, v185\n"
    "v_mfma_f32_16x16x32_bf16 v[176:179], v[4:7], v[196:199], v[176:179]\n"
    "v_cvt_pk_bf16_f32 v29, v186, v187\n"
    "v_cvt_pk_bf16_f32 v30, v188, v189\n"
    "v_mfma_f32_16x16x32_bf16 v[180:183], v[8:11], v[192:195], v[148:151]\n"
    "v_exp_f32 v170, v170 clamp\n"
    "v_mfma_f32_16x16x32_bf16 v[180:183], v[12:15], v[196:199], v[180:183]\n"
    "v_exp_f32 v171, v171 clamp\n"
    "v_mfma_f32_16x16x32_bf16 v[184:187], v[16:19], v[192:195], v[152:155]\n"
    "v_exp_f32 v172, v172 clamp\n"
    "v_mfma_f32_16x16x32_bf16 v[184:187], v[20:23], v[196:199], v[184:187]\n"
    "v_exp_f32 v173, v173 clamp\n"
    "v_cvt_pk_bf16_f32 v31, v190, v191\n"
    "v_mfma_f32_16x16x32_bf16 v[188:191], v[24:27], v[192:195], v[156:159]\n"
    "v_exp_f32 v174, v174 clamp\n"
    "v_mfma_f32_16x16x32_bf16 v[188:191], v[28:31], v[196:199], v[188:191]\n"
    "v_exp_f32 v175, v175 clamp\n"
    "s_waitcnt vmcnt(4)\n"
    "ds_read_b64_tr_b16 v[208:209], v240 offset:6144\n"
    "ds_read_b64_tr_b16 v[210:211], v241 offset:6144\n"
    "ds_read_b64_tr_b16 v[212:213], v242 offset:6144\n"
    "ds_read_b64_tr_b16 v[214:215], v243 offset:6144\n"
    "ds_read_u8 v236, v251 offset:0\n"
    "ds_read_u8 v238, v251 offset:1\n"
    "ds_read_b128 v[192:195], v234 offset:0\n"
    "ds_read_b128 v[196:199], v235 offset:0\n"
    "v_pk_fma_f32 v[160:161], v[160:161], s[70:71], v[128:129]\n"
    "v_pk_fma_f32 v[162:163], v[162:163], s[70:71], v[130:131]\n"
    "v_pk_add_f32 v[230:231], v[160:161], v[162:163]\n"
    "v_pk_mul_f32 v[232:233], v[160:161], v[162:163]\n"
    "v_pk_fma_f32 v[164:165], v[164:165], s[70:71], v[132:133]\n"
    "v_pk_fma_f32 v[230:231], v[230:231], v[164:165], v[232:233]\n"
    "v_pk_mul_f32 v[232:233], v[232:233], v[164:165]\n"
    "v_pk_fma_f32 v[166:167], v[166:167], s[70:71], v[134:135]\n"
    "v_pk_fma_f32 v[230:231], v[230:231], v[166:167], v[232:233]\n"
    "v_pk_mul_f32 v[232:233], v[232:233], v[166:167]\n"
    "v_pk_fma_f32 v[168:169], v[168:169], s[70:71], v[136:137]\n"
    "v_pk_fma_f32 v[230:231], v[230:231], v[168:169], v[232:233]\n"
    "v_pk_mul_f32 v[232:233], v[232:233], v[168:169]\n"
    "v_pk_fma_f32 v[170:171], v[170:171], s[70:71], v[138:139]\n"
    "v_pk_fma_f32 v[230:231], v[230:231], v[170:171], v[232:233]\n"
    "v_pk_mul_f32 v[232:233], v[232:233], v[170:171]\n"
    "v_pk_fma_f32 v[172:173], v[172:173], s[70:71], v[140:141]\n"
    "v_pk_fma_f32 v[230:231], v[230:231], v[172:173], v[232:233]\n"
    "v_pk_mul_f32 v[232:233], v[232:233], v[172:173]\n"
    "v_pk_fma_f32 v[174:175], v[174:175], s[70:71], v[142:143]\n"
    "v_pk_fma_f32 v[230:231], v[230:231], v[174:175], v[232:233]\n"
    "v_pk_mul_f32 v[232:233], v[232:233], v[174:175]\n"
    "v_rcp_f32 v232, v232\n"
    "v_rcp_f32 v233, v233\n"
    "v_fma_f32 v230, v230, v232, v247\n"
    "v_fmac_f32 v230, v231, v233\n"
    "v_mbcnt_lo_u32_b32 v232, -1, 0\n"
    "v_mbcnt_hi_u32_b32 v232, -1, v232\n"
    "v_bfe_u32 v233, v232, 3, 1\n"
    "v_and_b32 v232, 7, v232\n"
    "v_lshl_add_u32 v232, v232, 1, v233\n"
    "v_add_u32 v232, s81, v232\n"
    "ds_read_u8 v232, v232 offset:49664\n"
    "s_add_u32 s81, s81, 16\n"
    "s_waitcnt lgkmcnt(0)\n"
    "buffer_load_ushort v232, v232, s[44:47], s63 idxen\n"
    "s_waitcnt vmcnt(0)\n"
    "v_lshlrev_b32 v232, 16, v232\n"
    "v_mul_f32 v230, v230, v232\n"
    "v_cvt_pk_bf16_f32 v230, v230, v230\n"
    "s_add_u32 s66, s66, 16\n"
    "s_waitcnt lgkmcnt(0)\n"
    "ds_write_b16 v245, v230\n"
    "ds_read_b64 v[228:229], v246\n"
    "s_lshl1_add_u32 s76, s72, s76\n"
    "buffer_load_dwordx4 v[236:237], s[40:43], s67 idxen offen offset:0 nt lds\n"
    "buffer_load_dwordx4 v[238:239], s[40:43], s67 idxen offen offset:1024 nt lds\n"
    "s_mov_b32 s81, s64\n"
    "s_lshr_b32 s63, s65, 6\n"
    "s_mov_b32 s80, s83\n"
    "s_add_u32 s62, s62, 256\n"
    "s_bitcmp1_b32 s60, 30\n"
    "s_cbranch_scc0 .Ldin_A3\n"
    ".Ldin_A3s:\n"
    "s_cbranch_vccz .Ldin_A3s_np\n"
    "s_setprio 1\n"
    ".Ldin_A3s_np:\n"
    "v_mfma_f32_16x16x32_bf16 v[160:163], v[0:3], v[192:195], v[144:147]\n"
    "v_exp_f32 v176, v176 clamp\n"
    "v_mfma_f32_16x16x32_bf16 v[160:163], v[4:7], v[196:199], v[160:163]\n"
    "v_exp_f32 v177, v177 clamp\n"
    "v_mfma_f32_16x16x32_bf16 v[164:167], v[8:11], v[192:195], v[148:151]\n"
    "v_exp_f32 v178, v178 clamp\n"
    "v_mfma_f32_16x16x32_bf16 v[164:167], v[12:15], v[196:199], v[164:167]\n"
    "v_exp_f32 v179, v179 clamp\n"
    "v_mfma_f32_16x16x32_bf16 v[168:171], v[16:19], v[192:195], v[152:155]\n"
    "v_exp_f32 v180, v180 clamp\n"
    "v_mfma_f32_16x16x32_bf16 v[168:171], v[20:23], v[196:199], v[168:171]\n"
    "v_exp_f32 v181, v181 clamp\n"
    "v_mfma_f32_16x16x32_bf16 v[172:175], v[24:27], v[192:195], v[156:159]\n"
    "v_exp_f32 v182, v182 clamp\n"
    "v_mfma_f32_16x16x32_bf16 v[172:175], v[28:31], v[196:199], v[172:175]\n"
    "v_exp_f32 v183, v183 clamp\n"
    "v_exp_f32 v184, v184 clamp\n"
    "s_mov_b32 m0, s62\n"
    "ds_read_b64 v[216:217], v246 offset:128\n"
    "v_exp_f32 v185, v185 clamp\n"
    "v_add_u32_e64 v251, s66, v244\n"
    "s_waitcnt lgkmcnt(1)\n"
    "v_mfma_f32_4x4x4_16b_bf16 v[224:227], v[228:229], v[200:201], v[224:227] cbsz:4 abid:0\n"
    "v_exp_f32 v186, v186 clamp\n"
    "v_exp_f32 v187, v187 clamp\n"
    "v_mfma_f32_4x4x4_16b_bf16 v[224:227], v[228:229], v[202:203], v[224:227] cbsz:4 abid:1\n"
    "v_exp_f32 v188, v188 clamp\n"
    "v_exp_f32 v189, v189 clamp\n"
    "v_mfma_f32_4x4x4_16b_bf16 v[224:227], v[228:229], v[204:205], v[224:227] cbsz:4 abid:2\n"
    "v_exp_f32 v190, v190 clamp\n"
    "v_exp_f32 v191, v191 clamp\n"
    "v_mfma_f32_4x4x4_16b_bf16 v[224:227], v[228:229], v[206:207], v[224:227] cbsz:4 abid:3\n"
    "s_waitcnt vmcnt(4)\n"
    "s_waitcnt lgkmcnt(0)\n"
    "v_readlane_b32 s60, v216, s59\n"
    "v_readlane_b32 s61, v217, s59\n"
    "ds_read_b64_tr_b16 v[200:201], v240 offset:0\n"
    "ds_read_b64_tr_b16 v[202:203], v241 offset:0\n"
    "ds_read_b64_tr_b16 v[204:205], v242 offset:0\n"
    "ds_read_b64_tr_b16 v[206:207], v243 offset:0\n"
    "ds_read_u8 v236, v251 offset:0\n"
    "ds_read_u8 v238, v251 offset:1\n"
    "ds_read_b128 v[192:195], v234 offset:2048\n"
    "ds_read_b128 v[196:199], v235 offset:2048\n"
    "s_cmp_eq_u32 s60, -1\n"
    "s_cbranch_scc1 .Ldin_A3s_poll\n"
    ".Ldin_A3s_pr:\n"
    "s_lshr_b32 s94, s60, 12\n"
    "s_and_b32 s94, s94, 0x1ff0\n"
    "v_xor_b32 v252, s94, v250\n"
    "v_pk_add_f32 v[230:231], v[224:225], v[226:227]\n"
    "v_add_f32 v230, v230, v231\n"
    "ds_write_addtid_b32 v230 offset:49152\n"
    "ds_read_b128 v[216:219], v252 offset:0\n"
    "ds_read_b128 v[220:223], v252 offset:64\n"
    "v_mov_b64 v[224:225], 0\n"
    "v_mov_b64_e64 v[226:227], 0\n"
    "v_pk_fma_f32 v[176:177], v[176:177], s[70:71], v[128:129]\n"
    "v_pk_fma_f32 v[178:179], v[178:179], s[70:71], v[130:131]\n"
    "v_pk_add_f32 v[230:231], v[176:177], v[178:179]\n"
    "v_pk_mul_f32 v[232:233], v[176:177], v[178:179]\n"
    "v_pk_fma_f32 v[180:181], v[180:181], s[70:71], v[132:133]\n"
    "v_pk_fma_f32 v[230:231], v[230:231], v[180:181], v[232:233]\n"
    "v_pk_mul_f32 v[232:233], v[232:233], v[180:181]\n"
    "v_pk_fma_f32 v[182:183], v[182:183], s[70:71], v[134:135]\n"
    "v_pk_fma_f32 v[230:231], v[230:231], v[182:183], v[232:233]\n"
    "v_pk_mul_f32 v[232:233], v[232:233], v[182:183]\n"
    "v_pk_fma_f32 v[184:185], v[184:185], s[70:71], v[136:137]\n"
    "v_pk_fma_f32 v[230:231], v[230:231], v[184:185], v[232:233]\n"
    "v_pk_mul_f32 v[232:233], v[232:233], v[184:185]\n"
    "v_pk_fma_f32 v[186:187], v[186:187], s[70:71], v[138:139]\n"
    "v_pk_fma_f32 v[230:231], v[230:231], v[186:187], v[232:233]\n"
    "v_pk_mul_f32 v[232:233], v[232:233], v[186:187]\n"
    "v_pk_fma_f32 v[188:189], v[188:189], s[70:71], v[140:141]\n"
    "v_pk_fma_f32 v[230:231], v[230:231], v[188:189], v[232:233]\n"
    "v_pk_mul_f32 v[232:233], v[232:233], v[188:189]\n"
    "v_pk_fma_f32 v[190:191], v[190:191], s[70:71], v[142:143]\n"
    "v_pk_fma_f32 v[230:231], v[230:231], v[190:191], v[232:233]\n"
    "v_pk_mul_f32 v[232:233], v[232:233], v[190:191]\n"
    "s_add_u32 m0, s68, 0\n"
    "s_and_b32 s64, s60, 0xffff\n"
    "s_bfe_u32 s94, s60, 0x30013\n"
    "s_bfe_u32 s95, s60, 0x50019\n"
    "s_mul_i32 s94, s94, s56\n"
    "s_lshl4_add_u32 s83, s95, s64\n"
    "s_add_u32 s65, s58, s94\n"
    "s_add_u32 s82, s64, 64\n"
    "s_add_u32 s58, s58, s57\n"
    "s_add_u32 s59, s59, 1\n"
    "s_sub_u32 s95, s61, s76\n"
    "s_lshr_b32 s94, s61, 16\n"
    "s_sext_i32_i16 s95, s95\n"
    "s_mul_i32 s95, s95, s94\n"
    "s_ashr_i32 s95, s95, 16\n"
    "s_max_i32 s95, s95, -8\n"
    "s_min_i32 s95, s95, 4\n"
    "s_add_u32 s72, s77, s95\n"
    "s_cmp_lt_i32 s60, 0\n"
    "s_cselect_b32 s72, 0, s72\n"
    "v_rcp_f32 v232, v232\n"
    "v_rcp_f32 v233, v233\n"
    "v_fma_f32 v230, v230, v232, v247\n"
    "v_fmac_f32 v230, v231, v233\n"
    "v_mbcnt_lo_u32_b32 v232, -1, 0\n"
    "v_mbcnt_hi_u32_b32 v232, -1, v232\n"
    "v_bfe_u32 v233, v232, 3, 1\n"
    "v_and_b32 v232, 7, v232\n"
    "v_lshl_add_u32 v232, v232, 1, v233\n"
    "v_add_u32 v232, s81, v232\n"
    "ds_read_u8 v232, v232 offset:49664\n"
    "s_add_u32 s81, s81, 16\n"
    "s_waitcnt lgkmcnt(0)\n"
    "buffer_load_ushort v232, v232, s[44:47], s63 idxen\n"
    "s_waitcnt vmcnt(0)\n"
    "v_lshlrev_b32 v232, 16, v232\n"
    "v_mul_f32 v230, v230, v232\n"
    "v_cvt_pk_bf16_f32 v230, v230, v230\n"
    "s_add_u32 s66, s66, 16\n"
    "s_waitcnt lgkmcnt(0)\n"
    "ds_write_b16 v245, v230\n"
    "ds_read_b64 v[228:229], v246\n"
    "buffer_load_dwordx4 v[236:237], s[40:43], s67 idxen offen offset:0 nt lds\n"
    "buffer_load_dwordx4 v[238:239], s[40:43], s67 idxen offen offset:1024 nt lds\n"
    "s_branch .Ldin_G0s\n"
    ".Ldin_A3s_poll:\n"
    "s_sleep 1\n"
    "ds_read_b64 v[216:217], v246 offset:128\n"
    "s_waitcnt lgkmcnt(0)\n"
    "v_readlane_b32 s60, v216, s59\n"
    "v_readlane_b32 s61, v217, s59\n"
    "s_cmp_eq_u32 s60, -1\n"
    "s_cbranch_scc1 .Ldin_A3s_poll\n"
    "s_branch .Ldin_A3s_pr\n"
    ".p2align 5\n"
    ".Ldin_L1s:\n"
    "s_setprio 0\n"
    "s_cmp_lt_i32 s60, 0\n"
    "s_cbranch_scc1 .Ldin_E1s\n"
    "ds_read_b128 v[144:147], v252 offset:8192\n"
    "ds_read_b128 v[148:151], v252 offset:8256\n"
    "ds_read_b128 v[152:155], v252 offset:16384\n"
    "ds_read_b128 v[156:159], v252 offset:16448\n"
    "s_add_u32 s76, s76, s73\n"
    "s_add_u32 m0, s68, 4096\n"
    "v_and_b32 v216, v248, v216\n"
    "v_and_b32 v217, v249, v217\n"
    "v_and_b32 v218, v248, v218\n"
    "v_and_b32 v219, v249, v219\n"
    "v_mfma_f32_16x16x16_bf16 v[160:163], v[216:217], v[96:97], v[32:35]\n"
    "v_and_b32 v220, v248, v220\n"
    "v_and_b32 v221, v249, v221\n"
    "v_and_b32 v222, v248, v222\n"
    "v_mfma_f32_16x16x16_bf16 v[164:167], v[218:219], v[98:99], v[36:39]\n"
    "v_exp_f32 v176, v176 clamp\n"
    "v_and_b32 v223, v249, v223\n"
    "v_mfma_f32_16x16x16_bf16 v[168:171], v[220:221], v[100:101], v[40:43]\n"
    "v_add_u32 v251, s66, v244\n"
    "v_cvt_pk_bf16_f32 v0, v160, v161\n"
    "v_cvt_pk_bf16_f32 v1, v162, v163\n"
    "s_waitcnt lgkmcnt(4)\n"
    "v_mfma_f32_16x16x16_bf16 v[172:175], v[222:223], v[102:103], v[44:47]\n"
    "v_exp_f32 v177, v177 clamp\n"
    "v_cvt_pk_bf16_f32 v2, v164, v165\n"
    "v_mfma_f32_4x4x4_16b_bf16 v[224:227], v[228:229], v[200:201], v[224:227] cbsz:4 abid:0\n"
    "v_cvt_pk_bf16_f32 v3, v166, v167\n"
    "v_mfma_f32_16x16x16_bf16 v[160:163], v[216:217], v[104:105], v[48:51]\n"
    "v_exp_f32 v178, v178 clamp\n"
    "v_cvt_pk_bf16_f32 v4, v168, v169\n"
    "v_mfma_f32_16x16x16_bf16 v[164:167], v[218:219], v[106:107], v[52:55]\n"
    "v_cvt_pk_bf16_f32 v5, v170, v171\n"
    "v_cvt_pk_bf16_f32 v6, v172, v173\n"
    "v_cvt_pk_bf16_f32 v7, v174, v175\n"
    "v_mfma_f32_16x16x16_bf16 v[168:171], v[220:221], v[108:109], v[56:59]\n"
    "v_exp_f32 v179, v179 clamp\n"
    "v_cvt_pk_bf16_f32 v8, v160, v161\n"
    "v_mfma_f32_4x4x4_16b_bf16 v[224:227], v[228:229], v[202:203], v[224:227] cbsz:4 abid:1\n"
    "v_cvt_pk_bf16_f32 v9, v162, v163\n"
    "v_mfma_f32_16x16x16_bf16 v[172:175], v[222:223], v[110:111], v[60:63]\n"
    "v_exp_f32 v180, v180 clamp\n"
    "v_cvt_pk_bf16_f32 v10, v164, v165\n"
    "v_mfma_f32_16x16x16_bf16 v[160:163], v[216:217], v[112:113], v[64:67]\n"
    "v_cvt_pk_bf16_f32 v11, v166, v167\n"
    "v_cvt_pk_bf16_f32 v12, v168, v169\n"
    "v_cvt_pk_bf16_f32 v13, v170, v171\n"
    "v_mfma_f32_16x16x16_bf16 v[164:167], v[218:219], v[114:115], v[68:71]\n"
    "v_exp_f32 v181, v181 clamp\n"
    "v_cvt_pk_bf16_f32 v14, v172, v173\n"
    "v_mfma_f32_4x4x4_16b_bf16 v[224:227], v[228:229], v[204:205], v[224:227] cbsz:4 abid:2\n"
    "v_cvt_pk_bf16_f32 v15, v174, v175\n"
    "v_mfma_f32_16x16x16_bf16 v[168:171], v[220:221], v[116:117], v[72:75]\n"
    "v_exp_f32 v182, v182 clamp\n"
    "v_cvt_pk_bf16_f32 v16, v160, v161\n"
    "v_mfma_f32_16x16x16_bf16 v[172:175], v[222:223], v[118:119], v[76:79]\n"
    "v_cvt_pk_bf16_f32 v17, v162, v163\n"
    "v_cvt_pk_bf16_f32 v18, v164, v165\n"
    "v_cvt_pk_bf16_f32 v19, v166, v167\n"
    "v_mfma_f32_16x16x16_bf16 v[160:163], v[216:217], v[120:121], v[80:83]\n"
    "v_exp_f32 v183, v183 clamp\n"
    "v_cvt_pk_bf16_f32 v20, v168, v169\n"
    "v_mfma_f32_4x4x4_16b_bf16 v[224:227], v[228:229], v[206:207], v[224:227] cbsz:4 abid:3\n"
    "v_cvt_pk_bf16_f32 v21, v170, v171\n"
    "v_mfma_f32_16x16x16_bf16 v[164:167], v[218:219], v[122:123], v[84:87]\n"
    "v_exp_f32 v184, v184 clamp\n"
    "v_cvt_pk_bf16_f32 v22, v172, v173\n"
    "v_mfma_f32_16x16x16_bf16 v[168:171], v[220:221], v[124:125], v[88:91]\n"
    "v_cvt_pk_bf16_f32 v23, v174, v175\n"
    "v_cvt_pk_bf16_f32 v24, v160, v161\n"
    "v_cvt_pk_bf16_f32 v25, v162, v163\n"
    "v_mfma_f32_16x16x16_bf16 v[172:175], v[222:223], v[126:127], v[92:95]\n"
    "v_exp_f32 v185, v185 clamp\n"
    "v_cvt_pk_bf16_f32 v26, v164, v165\n"
    "s_waitcnt lgkmcnt(0)\n"
    "v_mfma_f32_16x16x32_bf16 v[160:163], v[0:3], v[192:195], v[144:147]\n"
    "v_cvt_pk_bf16_f32 v27, v166, v167\n"
    "v_cvt_pk_bf16_f32 v28, v168, v169\n"
    "v_mfma_f32_16x16x32_bf16 v[160:163], v[4:7], v[196:199], v[160:163]\n"
    "v_cvt_pk_bf16_f32 v29, v170, v171\n"
    "v_cvt_pk_bf16_f32 v30, v172, v173\n"
    "v_mfma_f32_16x16x32_bf16 v[164:167], v[8:11], v[192:195], v[148:151]\n"
    "v_exp_f32 v186, v186 clamp\n"
    "v_mfma_f32_16x16x32_bf16 v[164:167], v[12:15], v[196:199], v[164:167]\n"
    "v_exp_f32 v187, v187 clamp\n"
    "v_mfma_f32_16x16x32_bf16 v[168:171], v[16:19], v[192:195], v[152:155]\n"
    "v_exp_f32 v188, v188 clamp\n"
    "v_mfma_f32_16x16x32_bf16 v[168:171], v[20:23], v[196:199], v[168:171]\n"
    "v_exp_f32 v189, v189 clamp\n"
    "v_cvt_pk_bf16_f32 v31, v174, v175\n"
    "v_mfma_f32_16x16x32_bf16 v[172:175], v[24:27], v[192:195], v[156:159]\n"
    "v_exp_f32 v190, v190 clamp\n"
    "v_mfma_f32_16x16x32_bf16 v[172:175], v[28:31], v[196:199], v[172:175]\n"
    "v_exp_f32 v191, v191 clamp\n"
    "s_waitcnt vmcnt(4)\n"
    "ds_read_b64_tr_b16 v[200:201], v240 offset:4096\n"
    "ds_read_b64_tr_b16 v[202:203], v241 offset:4096\n"
    "ds_read_b64_tr_b16 v[204:205], v242 offset:4096\n"
    "ds_read_b64_tr_b16 v[206:207], v243 offset:4096\n"
    "ds_read_u8 v236, v251 offset:0\n"
    "ds_read_u8 v238, v251 offset:1\n"
    "ds_read_b128 v[192:195], v234 offset:6144\n"
    "ds_read_b128 v[196:199], v235 offset:6144\n"
    "v_pk_fma_f32 v[176:177], v[176:177], s[70:71], v[128:129]\n"
    "v_pk_fma_f32 v[178:179], v[178:179], s[70:71], v[130:131]\n"
    "v_pk_add_f32 v[230:231], v[176:177], v[178:179]\n"
    "v_pk_mul_f32 v[232:233], v[176:177], v[178:179]\n"
    "v_pk_fma_f32 v[180:181], v[180:181], s[70:71], v[132:133]\n"
    "v_pk_fma_f32 v[230:231], v[230:231], v[180:181], v[232:233]\n"
    "v_pk_mul_f32 v[232:233], v[232:233], v[180:181]\n"
    "v_pk_fma_f32 v[182:183], v[182:183], s[70:71], v[134:135]\n"
    "v_pk_fma_f32 v[230:231], v[230:231], v[182:183], v[232:233]\n"
    "v_pk_mul_f32 v[232:233], v[232:233], v[182:183]\n"
    "v_pk_fma_f32 v[184:185], v[184:185], s[70:71], v[136:137]\n"
    "v_pk_fma_f32 v[230:231], v[230:231], v[184:185], v[232:233]\n"
    "v_pk_mul_f32 v[232:233], v[232:233], v[184:185]\n"
    "v_pk_fma_f32 v[186:187], v[186:187], s[70:71], v[138:139]\n"
    "v_pk_fma_f32 v[230:231], v[230:231], v[186:187], v[232:233]\n"
    "v_pk_mul_f32 v[232:233], v[232:233], v[186:187]\n"
    "v_pk_fma_f32 v[188:189], v[188:189], s[70:71], v[140:141]\n"
    "v_pk_fma_f32 v[230:231], v[230:231], v[188:189], v[232:233]\n"
    "v_pk_mul_f32 v[232:233], v[232:233], v[188:189]\n"
    "v_pk_fma_f32 v[190:191], v[190:191], s[70:71], v[142:143]\n"
    "v_pk_fma_f32 v[230:231], v[230:231], v[190:191], v[232:233]\n"
    "v_pk_mul_f32 v[232:233], v[232:233], v[190:191]\n"
    "v_rcp_f32 v232, v232\n"
    "v_rcp_f32 v233, v233\n"
    "v_fma_f32 v230, v230, v232, v247\n"
    "v_fmac_f32 v230, v231, v233\n"
    "v_mbcnt_lo_u32_b32 v232, -1, 0\n"
    "v_mbcnt_hi_u32_b32 v232, -1, v232\n"
    "v_bfe_u32 v233, v232, 3, 1\n"
    "v_and_b32 v232, 7, v232\n"
    "v_lshl_add_u32 v232, v232, 1, v233\n"
    "v_add_u32 v232, s81, v232\n"
    "ds_read_u8 v232, v232 offset:49664\n"
    "s_add_u32 s81, s81, 16\n"
    "s_waitcnt lgkmcnt(0)\n"
    "buffer_load_ushort v232, v232, s[44:47], s63 idxen\n"
    "s_waitcnt vmcnt(0)\n"
    "v_lshlrev_b32 v232, 16, v232\n"
    "v_mul_f32 v230, v230, v232\n"
    "v_cvt_pk_bf16_f32 v230, v230, v230\n"
    "s_add_u32 s66, s66, 16\n"
    "s_waitcnt lgkmcnt(0)\n"
    "ds_write_b16 v245, v230\n"
    "ds_read_b64 v[228:229], v246\n"
    "s_lshl1_add_u32 s76, s72, s76\n"
    "buffer_load_dwordx4 v[236:237], s[40:43], s67 idxen offen offset:0 nt lds\n"
    "buffer_load_dwordx4 v[238:239], s[40:43], s67 idxen offen offset:1024 nt lds\n"
    "s_mov_b32 s81, s64\n"
    "s_lshr_b32 s63, s65, 6\n"
    "s_mov_b32 s80, s83\n"
    "s_add_u32 s62, s62, 256\n"
    "s_bitcmp1_b32 s60, 30\n"
    "s_cbranch_scc0 .Ldin_A2\n"
    ".Ldin_A2s:\n"
    "s_cbranch_vccz .Ldin_A2s_np\n"
    "s_setprio 1\n"
    ".Ldin_A2s_np:\n"
    "v_mfma_f32_16x16x32_bf16 v[176:179], v[0:3], v[192:195], v[144:147]\n"
    "v_exp_f32 v160, v160 clamp\n"
    "v_mfma_f32_16x16x32_bf16 v[176:179], v[4:7], v[196:199], v[176:179]\n"
    "v_exp_f32 v161, v161 clamp\n"
    "v_mfma_f32_16x16x32_bf16 v[180:183], v[8:11], v[192:195], v[148:151]\n"
    "v_exp_f32 v162, v162 clamp\n"
    "v_mfma_f32_16x16x32_bf16 v[180:183], v[12:15], v[196:199], v[180:183]\n"
    "v_exp_f32 v163, v163 clamp\n"
    "v_mfma_f32_16x16x32_bf16 v[184:187], v[16:19], v[192:195], v[152:155]\n"
    "v_exp_f32 v164, v164 clamp\n"
    "v_mfma_f32_16x16x32_bf16 v[184:187], v[20:23], v[196:199], v[184:187]\n"
    "v_exp_f32 v165, v165 clamp\n"
    "v_mfma_f32_16x16x32_bf16 v[188:191], v[24:27], v[192:195], v[156:159]\n"
    "v_exp_f32 v166, v166 clamp\n"
    "v_mfma_f32_16x16x32_bf16 v[188:191], v[28:31], v[196:199], v[188:191]\n"
    "v_exp_f32 v167, v167 clamp\n"
    "v_exp_f32 v168, v168 clamp\n"
    "s_mov_b32 m0, s62\n"
    "ds_read_b64 v[216:217], v246 offset:128\n"
    "v_exp_f32 v169, v169 clamp\n"
    "v_add_u32_e64 v251, s66, v244\n"
    "s_waitcnt lgkmcnt(1)\n"
    "v_mfma_f32_4x4x4_16b_bf16 v[224:227], v[228:229], v[208:209], v[224:227] cbsz:4 abid:0\n"
    "v_exp_f32 v170, v170 clamp\n"
    "v_exp_f32 v171, v171 clamp\n"
    "v_mfma_f32_4x4x4_16b_bf16 v[224:227], v[228:229], v[210:211], v[224:227] cbsz:4 abid:1\n"
    "v_exp_f32 v172, v172 clamp\n"
    "v_exp_f32 v173, v173 clamp\n"
    "v_mfma_f32_4x4x4_16b_bf16 v[224:227], v[228:229], v[212:213], v[224:227] cbsz:4 abid:2\n"
    "v_exp_f32 v174, v174 clamp\n"
    "v_exp_f32 v175, v175 clamp\n"
    "v_mfma_f32_4x4x4_16b_bf16 v[224:227], v[228:229], v[214:215], v[224:227] cbsz:4 abid:3\n"
    "s_waitcnt vmcnt(4)\n"
    "s_waitcnt lgkmcnt(0)\n"
    "v_readlane_b32 s60, v216, s59\n"
    "v_readlane_b32 s61, v217, s59\n"
    "ds_read_b64_tr_b16 v[208:209], v240 offset:6144\n"
    "ds_read_b64_tr_b16 v[210:211], v241 offset:6144\n"
    "ds_read_b64_tr_b16 v[212:213], v242 offset:6144\n"
    "ds_read_b64_tr_b16 v[214:215], v243 offset:6144\n"
    "ds_read_u8 v236, v251 offset:0\n"
    "ds_read_u8 v238, v251 offset:1\n"
    "ds_read_b128 v[192:195], v234 offset:0\n"
    "ds_read_b128 v[196:199], v235 offset:0\n"
    "s_cmp_eq_u32 s60, -1\n"
    "s_cbranch_scc1 .Ldin_A2s_poll\n"
    ".Ldin_A2s_pr:\n"
    "s_lshr_b32 s94, s60, 12\n"
    "s_and_b32 s94, s94, 0x1ff0\n"
    "v_xor_b32 v252, s94, v250\n"
    "v_pk_add_f32 v[230:231], v[224:225], v[226:227]\n"
    "v_add_f32 v230, v230, v231\n"
    "ds_write_addtid_b32 v230 offset:49152\n"
    "ds_read_b128 v[216:219], v252 offset:0\n"
    "ds_read_b128 v[220:223], v252 offset:64\n"
    "v_mov_b64 v[224:225], 0\n"
    "v_mov_b64_e64 v[226:227], 0\n"
    "v_pk_fma_f32 v[160:161], v[160:161], s[70:71], v[128:129]\n"
    "v_pk_fma_f32 v[162:163], v[162:163], s[70:71], v[130:131]\n"
    "v_pk_add_f32 v[230:231], v[160:161], v[162:163]\n"
    "v_pk_mul_f32 v[232:233], v[160:161], v[162:163]\n"
    "v_pk_fma_f32 v[164:165], v[164:165], s[70:71], v[132:133]\n"
    "v_pk_fma_f32 v[230:231], v[230:231], v[164:165], v[232:233]\n"
    "v_pk_mul_f32 v[232:233], v[232:233], v[164:165]\n"
    "v_pk_fma_f32 v[166:167], v[166:167], s[70:71], v[134:135]\n"
    "v_pk_fma_f32 v[230:231], v[230:231], v[166:167], v[232:233]\n"
    "v_pk_mul_f32 v[232:233], v[232:233], v[166:167]\n"
    "v_pk_fma_f32 v[168:169], v[168:169], s[70:71], v[136:137]\n"
    "v_pk_fma_f32 v[230:231], v[230:231], v[168:169], v[232:233]\n"
    "v_pk_mul_f32 v[232:233], v[232:233], v[168:169]\n"
    "v_pk_fma_f32 v[170:171], v[170:171], s[70:71], v[138:139]\n"
    "v_pk_fma_f32 v[230:231], v[230:231], v[170:171], v[232:233]\n"
    "v_pk_mul_f32 v[232:233], v[232:233], v[170:171]\n"
    "v_pk_fma_f32 v[172:173], v[172:173], s[70:71], v[140:141]\n"
    "v_pk_fma_f32 v[230:231], v[230:231], v[172:173], v[232:233]\n"
    "v_pk_mul_f32 v[232:233], v[232:233], v[172:173]\n"
    "v_pk_fma_f32 v[174:175], v[174:175], s[70:71], v[142:143]\n"
    "v_pk_fma_f32 v[230:231], v[230:231], v[174:175], v[232:233]\n"
    "v_pk_mul_f32 v[232:233], v[232:233], v[174:175]\n"
    "s_add_u32 m0, s68, 6144\n"
    "s_and_b32 s64, s60, 0xffff\n"
    "s_bfe_u32 s94, s60, 0x30013\n"
    "s_bfe_u32 s95, s60, 0x50019\n"
    "s_mul_i32 s94, s94, s56\n"
    "s_lshl4_add_u32 s83, s95, s64\n"
    "s_add_u32 s65, s58, s94\n"
    "s_add_u32 s82, s64, 64\n"
    "s_add_u32 s58, s58, s57\n"
    "s_add_u32 s59, s59, 1\n"
    "s_sub_u32 s95, s61, s76\n"
    "s_lshr_b32 s94, s61, 16\n"
    "s_sext_i32_i16 s95, s95\n"
    "s_mul_i32 s95, s95, s94\n"
    "s_ashr_i32 s95, s95, 16\n"
    "s_max_i32 s95, s95, -8\n"
    "s_min_i32 s95, s95, 4\n"
    "s_add_u32 s72, s77, s95\n"
    "s_cmp_lt_i32 s60, 0\n"
    "s_cselect_b32 s72, 0, s72\n"
    "v_rcp_f32 v232, v232\n"
    "v_rcp_f32 v233, v233\n"
    "v_fma_f32 v230, v230, v232, v247\n"
    "v_fmac_f32 v230, v231, v233\n"
    "v_mbcnt_lo_u32_b32 v232, -1, 0\n"
    "v_mbcnt_hi_u32_b32 v232, -1, v232\n"
    "v_bfe_u32 v233, v232, 3, 1\n"
    "v_and_b32 v232, 7, v232\n"
    "v_lshl_add_u32 v232, v232, 1, v233\n"
    "v_add_u32 v232, s81, v232\n"
    "ds_read_u8 v232, v232 offset:49664\n"
    "s_add_u32 s81, s81, 16\n"
    "s_waitcnt lgkmcnt(0)\n"
    "buffer_load_ushort v232, v232, s[44:47], s63 idxen\n"
    "s_waitcnt vmcnt(0)\n"
    "v_lshlrev_b32 v232, 16, v232\n"
    "v_mul_f32 v230, v230, v232\n"
    "v_cvt_pk_bf16_f32 v230, v230, v230\n"
    "s_add_u32 s66, s66, 16\n"
    "s_waitcnt lgkmcnt(0)\n"
    "ds_write_b16 v245, v230\n"
    "ds_read_b64 v[228:229], v246\n"
    "buffer_load_dwordx4 v[236:237], s[40:43], s67 idxen offen offset:0 nt lds\n"
    "buffer_load_dwordx4 v[238:239], s[40:43], s67 idxen offen offset:1024 nt lds\n"
    "s_branch .Ldin_G3s\n"
    ".Ldin_A2s_poll:\n"
    "s_sleep 1\n"
    "ds_read_b64 v[216:217], v246 offset:128\n"
    "s_waitcnt lgkmcnt(0)\n"
    "v_readlane_b32 s60, v216, s59\n"
    "v_readlane_b32 s61, v217, s59\n"
    "s_cmp_eq_u32 s60, -1\n"
    "s_cbranch_scc1 .Ldin_A2s_poll\n"
    "s_branch .Ldin_A2s_pr\n"
    ".p2align 5\n"
    ".Ldin_L0s:\n"
    "s_setprio 0\n"
    "s_cmp_lt_i32 s60, 0\n"
    "s_cbranch_scc1 .Ldin_E0s\n"
    "ds_read_b128 v[144:147], v252 offset:8192\n"
    "ds_read_b128 v[148:151], v252 offset:8256\n"
    "ds_read_b128 v[152:155], v252 offset:16384\n"
    "ds_read_b128 v[156:159], v252 offset:16448\n"
    "s_add_u32 s76, s76, s73\n"
    "s_add_u32 m0, s68, 2048\n"
    "v_and_b32 v216, v248, v216\n"
    "v_and_b32 v217, v249, v217\n"
    "v_and_b32 v218, v248, v218\n"
    "v_and_b32 v219, v249, v219\n"
    "v_mfma_f32_16x16x16_bf16 v[176:179], v[216:217], v[96:97], v[32:35]\n"
    "v_and_b32 v220, v248, v220\n"
    "v_and_b32 v221, v249, v221\n"
    "v_and_b32 v222, v248, v222\n"
    "v_mfma_f32_16x16x16_bf16 v[180:183], v[218:219], v[98:99], v[36:39]\n"
    "v_exp_f32 v160, v160 clamp\n"
    "v_and_b32 v223, v249, v223\n"
    "v_mfma_f32_16x16x16_bf16 v[184:187], v[220:221], v[100:101], v[40:43]\n"
    "v_add_u32 v251, s66, v244\n"
    "v_cvt_pk_bf16_f32 v0, v176, v177\n"
    "v_cvt_pk_bf16_f32 v1, v178, v179\n"
    "s_waitcnt lgkmcnt(4)\n"
    "v_mfma_f32_16x16x16_bf16 v[188:191], v[222:223], v[102:103], v[44:47]\n"
    "v_exp_f32 v161, v161 clamp\n"
    "v_cvt_pk_bf16_f32 v2, v180, v181\n"
    "v_mfma_f32_4x4x4_16b_bf16 v[224:227], v[228:229], v[208:209], v[224:227] cbsz:4 abid:0\n"
    "v_cvt_pk_bf16_f32 v3, v182, v183\n"
    "v_mfma_f32_16x16x16_bf16 v[176:179], v[216:217], v[104:105], v[48:51]\n"
    "v_exp_f32 v162, v162 clamp\n"
    "v_cvt_pk_bf16_f32 v4, v184, v185\n"
    "v_mfma_f32_16x16x16_bf16 v[180:183], v[218:219], v[106:107], v[52:55]\n"
    "v_cvt_pk_bf16_f32 v5, v186, v187\n"
    "v_cvt_pk_bf16_f32 v6, v188, v189\n"
    "v_cvt_pk_bf16_f32 v7, v190, v191\n"
    "v_mfma_f32_16x16x16_bf16 v[184:187], v[220:221], v[108:109], v[56:59]\n"
    "v_exp_f32 v163, v163 clamp\n"
    "v_cvt_pk_bf16_f32 v8, v176, v177\n"
    "v_mfma_f32_4x4x4_16b_bf16 v[224:227], v[228:229], v[210:211], v[224:227] cbsz:4 abid:1\n"
    "v_cvt_pk_bf16_f32 v9, v178, v179\n"
    "v_mfma_f32_16x16x16_bf16 v[188:191], v[222:223], v[110:111], v[60:63]\n"
    "v_exp_f32 v164, v164 clamp\n"
    "v_cvt_pk_bf16_f32 v10, v180, v181\n"
    "v_mfma_f32_16x16x16_bf16 v[176:179], v[216:217], v[112:113], v[64:67]\n"
    "v_cvt_pk_bf16_f32 v11, v182, v183\n"
    "v_cvt_pk_bf16_f32 v12, v184, v185\n"
    "v_cvt_pk_bf16_f32 v13, v186, v187\n"
    "v_mfma_f32_16x16x16_bf16 v[180:183], v[218:219], v[114:115], v[68:71]\n"
    "v_exp_f32 v165, v165 clamp\n"
    "v_cvt_pk_bf16_f32 v14, v188, v189\n"
    "v_mfma_f32_4x4x4_16b_bf16 v[224:227], v[228:229], v[212:213], v[224:227] cbsz:4 abid:2\n"
    "v_cvt_pk_bf16_f32 v15, v190, v191\n"
    "v_mfma_f32_16x16x16_bf16 v[184:187], v[220:221], v[116:117], v[72:75]\n"
    "v_exp_f32 v166, v166 clamp\n"
    "v_cvt_pk_bf16_f32 v16, v176, v177\n"
    "v_mfma_f32_16x16x16_bf16 v[188:191], v[222:223], v[118:119], v[76:79]\n"
    "v_cvt_pk_bf16_f32 v17, v178, v179\n"
    "v_cvt_pk_bf16_f32 v18, v180, v181\n"
    "v_cvt_pk_bf16_f32 v19, v182, v183\n"
    "v_mfma_f32_16x16x16_bf16 v[176:179], v[216:217], v[120:121], v[80:83]\n"
    "v_exp_f32 v167, v167 clamp\n"
    "v_cvt_pk_bf16_f32 v20, v184, v185\n"
    "v_mfma_f32_4x4x4_16b_bf16 v[224:227], v[228:229], v[214:215], v[224:227] cbsz:4 abid:3\n"
    "v_cvt_pk_bf16_f32 v21, v186, v187\n"
    "v_mfma_f32_16x16x16_bf16 v[180:183], v[218:219], v[122:123], v[84:87]\n"
    "v_exp_f32 v168, v168 clamp\n"
    "v_cvt_pk_bf16_f32 v22, v188, v189\n"
    "v_mfma_f32_16x16x16_bf16 v[184:187], v[220:221], v[124:125], v[88:91]\n"
    "v_cvt_pk_bf16_f32 v23, v190, v191\n"
    "v_cvt_pk_bf16_f32 v24, v176, v177\n"
    "v_cvt_pk_bf16_f32 v25, v178, v179\n"
    "v_mfma_f32_16x16x16_bf16 v[188:191], v[222:223], v[126:127], v[92:95]\n"
    "v_exp_f32 v169, v169 clamp\n"
    "v_cvt_pk_bf16_f32 v26, v180, v181\n"
    "s_waitcnt lgkmcnt(0)\n"
    "v_mfma_f32_16x16x32_bf16 v[176:179], v[0:3], v[192:195], v[144:147]\n"
    "v_cvt_pk_bf16_f32 v27, v182, v183\n"
    "v_cvt_pk_bf16_f32 v28, v184, v185\n"
    "v_mfma_f32_16x16x32_bf16 v[176:179], v[4:7], v[196:199], v[176:179]\n"
    "v_cvt_pk_bf16_f32 v29, v186, v187\n"
    "v_cvt_pk_bf16_f32 v30, v188, v189\n"
    "v_mfma_f32_16x16x32_bf16 v[180:183], v[8:11], v[192:195], v[148:151]\n"
    "v_exp_f32 v170, v170 clamp\n"
    "v_mfma_f32_16x16x32_bf16 v[180:183], v[12:15], v[196:199], v[180:183]\n"
    "v_exp_f32 v171, v171 clamp\n"
    "v_mfma_f32_16x16x32_bf16 v[184:187], v[16:19], v[192:195], v[152:155]\n"
    "v_exp_f32 v172, v172 clamp\n"
    "v_mfma_f32_16x16x32_bf16 v[184:187], v[20:23], v[196:199], v[184:187]\n"
    "v_exp_f32 v173, v173 clamp\n"
    "v_cvt_pk_bf16_f32 v31, v190, v191\n"
    "v_mfma_f32_16x16x32_bf16 v[188:191], v[24:27], v[192:195], v[156:159]\n"
    "v_exp_f32 v174, v174 clamp\n"
    "v_mfma_f32_16x16x32_bf16 v[188:191], v[28:31], v[196:199], v[188:191]\n"
    "v_exp_f32 v175, v175 clamp\n"
    "s_waitcnt vmcnt(4)\n"
    "ds_read_b64_tr_b16 v[208:209], v240 offset:2048\n"
    "ds_read_b64_tr_b16 v[210:211], v241 offset:2048\n"
    "ds_read_b64_tr_b16 v[212:213], v242 offset:2048\n"
    "ds_read_b64_tr_b16 v[214:215], v243 offset:2048\n"
    "ds_read_u8 v236, v251 offset:0\n"
    "ds_read_u8 v238, v251 offset:1\n"
    "ds_read_b128 v[192:195], v234 offset:4096\n"
    "ds_read_b128 v[196:199], v235 offset:4096\n"
    "v_pk_fma_f32 v[160:161], v[160:161], s[70:71], v[128:129]\n"
    "v_pk_fma_f32 v[162:163], v[162:163], s[70:71], v[130:131]\n"
    "v_pk_add_f32 v[230:231], v[160:161], v[162:163]\n"
    "v_pk_mul_f32 v[232:233], v[160:161], v[162:163]\n"
    "v_pk_fma_f32 v[164:165], v[164:165], s[70:71], v[132:133]\n"
    "v_pk_fma_f32 v[230:231], v[230:231], v[164:165], v[232:233]\n"
    "v_pk_mul_f32 v[232:233], v[232:233], v[164:165]\n"
    "v_pk_fma_f32 v[166:167], v[166:167], s[70:71], v[134:135]\n"
    "v_pk_fma_f32 v[230:231], v[230:231], v[166:167], v[232:233]\n"
    "v_pk_mul_f32 v[232:233], v[232:233], v[166:167]\n"
    "v_pk_fma_f32 v[168:169], v[168:169], s[70:71], v[136:137]\n"
    "v_pk_fma_f32 v[230:231], v[230:231], v[168:169], v[232:233]\n"
    "v_pk_mul_f32 v[232:233], v[232:233], v[168:169]\n"
    "v_pk_fma_f32 v[170:171], v[170:171], s[70:71], v[138:139]\n"
    "v_pk_fma_f32 v[230:231], v[230:231], v[170:171], v[232:233]\n"
    "v_pk_mul_f32 v[232:233], v[232:233], v[170:171]\n"
    "v_pk_fma_f32 v[172:173], v[172:173], s[70:71], v[140:141]\n"
    "v_pk_fma_f32 v[230:231], v[230:231], v[172:173], v[232:233]\n"
    "v_pk_mul_f32 v[232:233], v[232:233], v[172:173]\n"
    "v_pk_fma_f32 v[174:175], v[174:175], s[70:71], v[142:143]\n"
    "v_pk_fma_f32 v[230:231], v[230:231], v[174:175], v[232:233]\n"
    "v_pk_mul_f32 v[232:233], v[232:233], v[174:175]\n"
    "v_rcp_f32 v232, v232\n"
    "v_rcp_f32 v233, v233\n"
    "v_fma_f32 v230, v230, v232, v247\n"
    "v_fmac_f32 v230, v231, v233\n"
    "v_mbcnt_lo_u32_b32 v232, -1, 0\n"
    "v_mbcnt_hi_u32_b32 v232, -1, v232\n"
    "v_bfe_u32 v233, v232, 3, 1\n"
    "v_and_b32 v232, 7, v232\n"
    "v_lshl_add_u32 v232, v232, 1, v233\n"
    "v_add_u32 v232, s81, v232\n"
    "ds_read_u8 v232, v232 offset:49664\n"
    "s_add_u32 s81, s81, 16\n"
    "s_waitcnt lgkmcnt(0)\n"
    "buffer_load_ushort v232, v232, s[44:47], s63 idxen\n"
    "s_waitcnt vmcnt(0)\n"
    "v_lshlrev_b32 v232, 16, v232\n"
    "v_mul_f32 v230, v230, v232\n"
    "v_cvt_pk_bf16_f32 v230, v230, v230\n"
    "s_add_u32 s66, s66, 16\n"
    "s_waitcnt lgkmcnt(0)\n"
    "ds_write_b16 v245, v230\n"
    "ds_read_b64 v[228:229], v246\n"
    "s_lshl1_add_u32 s76, s72, s76\n"
    "buffer_load_dwordx4 v[236:237], s[40:43], s67 idxen offen offset:0 nt lds\n"
    "buffer_load_dwordx4 v[238:239], s[40:43], s67 idxen offen offset:1024 nt lds\n"
    "s_mov_b32 s81, s64\n"
    "s_lshr_b32 s63, s65, 6\n"
    "s_mov_b32 s80, s83\n"
    "s_add_u32 s62, s62, 256\n"
    "s_bitcmp1_b32 s60, 30\n"
    "s_cbranch_scc0 .Ldin_A1\n"
    ".Ldin_A1s:\n"
    "s_cbranch_vccz .Ldin_A1s_np\n"
    "s_setprio 1\n"
    ".Ldin_A1s_np:\n"
    "v_mfma_f32_16x16x32_bf16 v[160:163], v[0:3], v[192:195], v[144:147]\n"
    "v_exp_f32 v176, v176 clamp\n"
    "v_mfma_f32_16x16x32_bf16 v[160:163], v[4:7], v[196:199], v[160:163]\n"
    "v_exp_f32 v177, v177 clamp\n"
    "v_mfma_f32_16x16x32_bf16 v[164:167], v[8:11], v[192:195], v[148:151]\n"
    "v_exp_f32 v178, v178 clamp\n"
    "v_mfma_f32_16x16x32_bf16 v[164:167], v[12:15], v[196:199], v[164:167]\n"
    "v_exp_f32 v179, v179 clamp\n"
    "v_mfma_f32_16x16x32_bf16 v[168:171], v[16:19], v[192:195], v[152:155]\n"
    "v_exp_f32 v180, v180 clamp\n"
    "v_mfma_f32_16x16x32_bf16 v[168:171], v[20:23], v[196:199], v[168:171]\n"
    "v_exp_f32 v181, v181 clamp\n"
    "v_mfma_f32_16x16x32_bf16 v[172:175], v[24:27], v[192:195], v[156:159]\n"
    "v_exp_f32 v182, v182 clamp\n"
    "v_mfma_f32_16x16x32_bf16 v[172:175], v[28:31], v[196:199], v[172:175]\n"
    "v_exp_f32 v183, v183 clamp\n"
    "v_exp_f32 v184, v184 clamp\n"
    "s_mov_b32 m0, s62\n"
    "ds_read_b64 v[216:217], v246 offset:128\n"
    "v_exp_f32 v185, v185 clamp\n"
    "v_add_u32_e64 v251, s66, v244\n"
    "s_waitcnt lgkmcnt(1)\n"
    "v_mfma_f32_4x4x4_16b_bf16 v[224:227], v[228:229], v[200:201], v[224:227] cbsz:4 abid:0\n"
    "v_exp_f32 v186, v186 clamp\n"
    "v_exp_f32 v187, v187 clamp\n"
    "v_mfma_f32_4x4x4_16b_bf16 v[224:227], v[228:229], v[202:203], v[224:227] cbsz:4 abid:1\n"
    "v_exp_f32 v188, v188 clamp\n"
    "v_exp_f32 v189, v189 clamp\n"
    "v_mfma_f32_4x4x4_16b_bf16 v[224:227], v[228:229], v[204:205], v[224:227] cbsz:4 abid:2\n"
    "v_exp_f32 v190, v190 clamp\n"
    "v_exp_f32 v191, v191 clamp\n"
    "v_mfma_f32_4x4x4_16b_bf16 v[224:227], v[228:229], v[206:207], v[224:227] cbsz:4 abid:3\n"
    "s_waitcnt vmcnt(4)\n"
    "s_waitcnt lgkmcnt(0)\n"
    "v_readlane_b32 s60, v216, s59\n"
    "v_readlane_b32 s61, v217, s59\n"
    "ds_read_b64_tr_b16 v[200:201], v240 offset:4096\n"
    "ds_read_b64_tr_b16 v[202:203], v241 offset:4096\n"
    "ds_read_b64_tr_b16 v[204:205], v242 offset:4096\n"
    "ds_read_b64_tr_b16 v[206:207], v243 offset:4096\n"
    "ds_read_u8 v236, v251 offset:0\n"
    "ds_read_u8 v238, v251 offset:1\n"
    "ds_read_b128 v[192:195], v234 offset:6144\n"
    "ds_read_b128 v[196:199], v235 offset:6144\n"
    "s_cmp_eq_u32 s60, -1\n"
    "s_cbranch_scc1 .Ldin_A1s_poll\n"
    ".Ldin_A1s_pr:\n"
    "s_lshr_b32 s94, s60, 12\n"
    "s_and_b32 s94, s94, 0x1ff0\n"
    "v_xor_b32 v252, s94, v250\n"
    "v_pk_add_f32 v[230:231], v[224:225], v[226:227]\n"
    "v_add_f32 v230, v230, v231\n"
    "ds_write_addtid_b32 v230 offset:49152\n"
    "ds_read_b128 v[216:219], v252 offset:0\n"
    "ds_read_b128 v[220:223], v252 offset:64\n"
    "v_mov_b64 v[224:225], 0\n"
    "v_mov_b64_e64 v[226:227], 0\n"
    "v_pk_fma_f32 v[176:177], v[176:177], s[70:71], v[128:129]\n"
    "v_pk_fma_f32 v[178:179], v[178:179], s[70:71], v[130:131]\n"
    "v_pk_add_f32 v[230:231], v[176:177], v[178:179]\n"
    "v_pk_mul_f32 v[232:233], v[176:177], v[178:179]\n"
    "v_pk_fma_f32 v[180:181], v[180:181], s[70:71], v[132:133]\n"
    "v_pk_fma_f32 v[230:231], v[230:231], v[180:181], v[232:233]\n"
    "v_pk_mul_f32 v[232:233], v[232:233], v[180:181]\n"
    "v_pk_fma_f32 v[182:183], v[182:183], s[70:71], v[134:135]\n"
    "v_pk_fma_f32 v[230:231], v[230:231], v[182:183], v[232:233]\n"
    "v_pk_mul_f32 v[232:233], v[232:233], v[182:183]\n"
    "v_pk_fma_f32 v[184:185], v[184:185], s[70:71], v[136:137]\n"
    "v_pk_fma_f32 v[230:231], v[230:231], v[184:185], v[232:233]\n"
    "v_pk_mul_f32 v[232:233], v[232:233], v[184:185]\n"
    "v_pk_fma_f32 v[186:187], v[186:187], s[70:71], v[138:139]\n"
    "v_pk_fma_f32 v[230:231], v[230:231], v[186:187], v[232:233]\n"
    "v_pk_mul_f32 v[232:233], v[232:233], v[186:187]\n"
    "v_pk_fma_f32 v[188:189], v[188:189], s[70:71], v[140:141]\n"
    "v_pk_fma_f32 v[230:231], v[230:231], v[188:189], v[232:233]\n"
    "v_pk_mul_f32 v[232:233], v[232:233], v[188:189]\n"
    "v_pk_fma_f32 v[190:191], v[190:191], s[70:71], v[142:143]\n"
    "v_pk_fma_f32 v[230:231], v[230:231], v[190:191], v[232:233]\n"
    "v_pk_mul_f32 v[232:233], v[232:233], v[190:191]\n"
    "s_add_u32 m0, s68, 4096\n"
    "s_and_b32 s64, s60, 0xffff\n"
    "s_bfe_u32 s94, s60, 0x30013\n"
    "s_bfe_u32 s95, s60, 0x50019\n"
    "s_mul_i32 s94, s94, s56\n"
    "s_lshl4_add_u32 s83, s95, s64\n"
    "s_add_u32 s65, s58, s94\n"
    "s_add_u32 s82, s64, 64\n"
    "s_add_u32 s58, s58, s57\n"
    "s_add_u32 s59, s59, 1\n"
    "s_sub_u32 s95, s61, s76\n"
    "s_lshr_b32 s94, s61, 16\n"
    "s_sext_i32_i16 s95, s95\n"
    "s_mul_i32 s95, s95, s94\n"
    "s_ashr_i32 s95, s95, 16\n"
    "s_max_i32 s95, s95, -8\n"
    "s_min_i32 s95, s95, 4\n"
    "s_add_u32 s72, s77, s95\n"
    "s_cmp_lt_i32 s60, 0\n"
    "s_cselect_b32 s72, 0, s72\n"
    "v_rcp_f32 v232, v232\n"
    "v_rcp_f32 v233, v233\n"
    "v_fma_f32 v230, v230, v232, v247\n"
    "v_fmac_f32 v230, v231, v233\n"
    "v_mbcnt_lo_u32_b32 v232, -1, 0\n"
    "v_mbcnt_hi_u32_b32 v232, -1, v232\n"
    "v_bfe_u32 v233, v232, 3, 1\n"
    "v_and_b32 v232, 7, v232\n"
    "v_lshl_add_u32 v232, v232, 1, v233\n"
    "v_add_u32 v232, s81, v232\n"
    "ds_read_u8 v232, v232 offset:49664\n"
    "s_add_u32 s81, s81, 16\n"
    "s_waitcnt lgkmcnt(0)\n"
    "buffer_load_ushort v232, v232, s[44:47], s63 idxen\n"
    "s_waitcnt vmcnt(0)\n"
    "v_lshlrev_b32 v232, 16, v232\n"
    "v_mul_f32 v230, v230, v232\n"
    "v_cvt_pk_bf16_f32 v230, v230, v230\n"
    "s_add_u32 s66, s66, 16\n"
    "s_waitcnt lgkmcnt(0)\n"
    "ds_write_b16 v245, v230\n"
    "ds_read_b64 v[228:229], v246\n"
    "buffer_load_dwordx4 v[236:237], s[40:43], s67 idxen offen offset:0 nt lds\n"
    "buffer_load_dwordx4 v[238:239], s[40:43], s67 idxen offen offset:1024 nt lds\n"
    "s_branch .Ldin_G2s\n"
    ".Ldin_A1s_poll:\n"
    "s_sleep 1\n"
    "ds_read_b64 v[216:217], v246 offset:128\n"
    "s_waitcnt lgkmcnt(0)\n"
    "v_readlane_b32 s60, v216, s59\n"
    "v_readlane_b32 s61, v217, s59\n"
    "s_cmp_eq_u32 s60, -1\n"
    "s_cbranch_scc1 .Ldin_A1s_poll\n"
    "s_branch .Ldin_A1s_pr\n"
    ".p2align 5\n"
    ".Ldin_L3s:\n"
    "s_setprio 0\n"
    "s_cmp_lt_i32 s60, 0\n"
    "s_cbranch_scc1 .Ldin_E1s\n"
    "ds_read_b128 v[144:147], v252 offset:8192\n"
    "ds_read_b128 v[148:151], v252 offset:8256\n"
    "ds_read_b128 v[152:155], v252 offset:16384\n"
    "ds_read_b128 v[156:159], v252 offset:16448\n"
    "s_add_u32 s76, s76, s73\n"
    "s_add_u32 m0, s68, 0\n"
    "v_and_b32 v216, v248, v216\n"
    "v_and_b32 v217, v249, v217\n"
    "v_and_b32 v218, v248, v218\n"
    "v_and_b32 v219, v249, v219\n"
    "v_mfma_f32_16x16x16_bf16 v[160:163], v[216:217], v[96:97], v[32:35]\n"
    "v_and_b32 v220, v248, v220\n"
    "v_and_b32 v221, v249, v221\n"
    "v_and_b32 v222, v248, v222\n"
    "v_mfma_f32_16x16x16_bf16 v[164:167], v[218:219], v[98:99], v[36:39]\n"
    "v_exp_f32 v176, v176 clamp\n"
    "v_and_b32 v223, v249, v223\n"
    "v_mfma_f32_16x16x16_bf16 v[168:171], v[220:221], v[100:101], v[40:43]\n"
    "v_add_u32_e64 v251, s66, v244\n"
    "v_cvt_pk_bf16_f32 v0, v160, v161\n"
    "v_cvt_pk_bf16_f32 v1, v162, v163\n"
    "s_waitcnt lgkmcnt(4)\n"
    "v_mfma_f32_16x16x16_bf16 v[172:175], v[222:223], v[102:103], v[44:47]\n"
    "v_exp_f32 v177, v177 clamp\n"
    "v_cvt_pk_bf16_f32 v2, v164, v165\n"
    "v_mfma_f32_4x4x4_16b_bf16 v[224:227], v[228:229], v[200:201], v[224:227] cbsz:4 abid:0\n"
    "v_cvt_pk_bf16_f32 v3, v166, v167\n"
    "v_mfma_f32_16x16x16_bf16 v[160:163], v[216:217], v[104:105], v[48:51]\n"
    "v_exp_f32 v178, v178 clamp\n"
    "v_cvt_pk_bf16_f32 v4, v168, v169\n"
    "v_mfma_f32_16x16x16_bf16 v[164:167], v[218:219], v[106:107], v[52:55]\n"
    "v_cvt_pk_bf16_f32 v5, v170, v171\n"
    "v_cvt_pk_bf16_f32 v6, v172, v173\n"
    "v_cvt_pk_bf16_f32 v7, v174, v175\n"
    "v_mfma_f32_16x16x16_bf16 v[168:171], v[220:221], v[108:109], v[56:59]\n"
    "v_exp_f32 v179, v179 clamp\n"
    "v_cvt_pk_bf16_f32 v8, v160, v161\n"
    "v_mfma_f32_4x4x4_16b_bf16 v[224:227], v[228:229], v[202:203], v[224:227] cbsz:4 abid:1\n"
    "v_cvt_pk_bf16_f32 v9, v162, v163\n"
    "v_mfma_f32_16x16x16_bf16 v[172:175], v[222:223], v[110:111], v[60:63]\n"
    "v_exp_f32 v180, v180 clamp\n"
    "v_cvt_pk_bf16_f32 v10, v164, v165\n"
    "v_mfma_f32_16x16x16_bf16 v[160:163], v[216:217], v[112:113], v[64:67]\n"
    "v_cvt_pk_bf16_f32 v11, v166, v167\n"
    "v_cvt_pk_bf16_f32 v12, v168, v169\n"
    "v_cvt_pk_bf16_f32 v13, v170, v171\n"
    "v_mfma_f32_16x16x16_bf16 v[164:167], v[218:219], v[114:115], v[68:71]\n"
    "v_exp_f32 v181, v181 clamp\n"
    "v_cvt_pk_bf16_f32 v14, v172, v173\n"
    "v_mfma_f32_4x4x4_16b_bf16 v[224:227], v[228:229], v[204:205], v[224:227] cbsz:4 abid:2\n"
    "v_cvt_pk_bf16_f32 v15, v174, v175\n"
    "v_mfma_f32_16x16x16_bf16 v[168:171], v[220:221], v[116:117], v[72:75]\n"
    "v_exp_f32 v182, v182 clamp\n"
    "v_cvt_pk_bf16_f32 v16, v160, v161\n"
    "v_mfma_f32_16x16x16_bf16 v[172:175], v[222:223], v[118:119], v[76:79]\n"
    "v_cvt_pk_bf16_f32 v17, v162, v163\n"
    "v_cvt_pk_bf16_f32 v18, v164, v165\n"
    "v_cvt_pk_bf16_f32 v19, v166, v167\n"
    "v_mfma_f32_16x16x16_bf16 v[160:163], v[216:217], v[120:121], v[80:83]\n"
    "v_exp_f32 v183, v183 clamp\n"
    "v_cvt_pk_bf16_f32 v20, v168, v169\n"
    "v_mfma_f32_4x4x4_16b_bf16 v[224:227], v[228:229], v[206:207], v[224:227] cbsz:4 abid:3\n"
    "v_cvt_pk_bf16_f32 v21, v170, v171\n"
    "v_mfma_f32_16x16x16_bf16 v[164:167], v[218:219], v[122:123], v[84:87]\n"
    "v_exp_f32 v184, v184 clamp\n"
    "v_cvt_pk_bf16_f32 v22, v172, v173\n"
    "v_mfma_f32_16x16x16_bf16 v[168:171], v[220:221], v[124:125], v[88:91]\n"
    "v_cvt_pk_bf16_f32 v23, v174, v175\n"
    "v_cvt_pk_bf16_f32 v24, v160, v161\n"
    "v_cvt_pk_bf16_f32 v25, v162, v163\n"
    "v_mfma_f32_16x16x16_bf16 v[172:175], v[222:223], v[126:127], v[92:95]\n"
    "v_exp_f32 v185, v185 clamp\n"
    "v_cvt_pk_bf16_f32 v26, v164, v165\n"
    "s_waitcnt lgkmcnt(0)\n"
    "v_mfma_f32_16x16x32_bf16 v[160:163], v[0:3], v[192:195], v[144:147]\n"
    "v_cvt_pk_bf16_f32 v27, v166, v167\n"
    "v_cvt_pk_bf16_f32 v28, v168, v169\n"
    "v_mfma_f32_16x16x32_bf16 v[160:163], v[4:7], v[196:199], v[160:163]\n"
    "v_cvt_pk_bf16_f32 v29, v170, v171\n"
    "v_cvt_pk_bf16_f32 v30, v172, v173\n"
    "v_mfma_f32_16x16x32_bf16 v[164:167], v[8:11], v[192:195], v[148:151]\n"
    "v_exp_f32 v186, v186 clamp\n"
    "v_mfma_f32_16x16x32_bf16 v[164:167], v[12:15], v[196:199], v[164:167]\n"
    "v_exp_f32 v187, v187 clamp\n"
    "v_mfma_f32_16x16x32_bf16 v[168:171], v[16:19], v[192:195], v[152:155]\n"
    "v_exp_f32 v188, v188 clamp\n"
    "v_mfma_f32_16x16x32_bf16 v[168:171], v[20:23], v[196:199], v[168:171]\n"
    "v_exp_f32 v189, v189 clamp\n"
    "v_cvt_pk_bf16_f32 v31, v174, v175\n"
    "v_mfma_f32_16x16x32_bf16 v[172:175], v[24:27], v[192:195], v[156:159]\n"
    "v_exp_f32 v190, v190 clamp\n"
    "v_mfma_f32_16x16x32_bf16 v[172:175], v[28:31], v[196:199], v[172:175]\n"
    "v_exp_f32 v191, v191 clamp\n"
    "s_waitcnt vmcnt(4)\n"
    "ds_read_b64_tr_b16 v[200:201], v240 offset:0\n"
    "ds_read_b64_tr_b16 v[202:203], v241 offset:0\n"
    "ds_read_b64_tr_b16 v[204:205], v242 offset:0\n"
    "ds_read_b64_tr_b16 v[206:207], v243 offset:0\n"
    "ds_read_u8 v236, v251 offset:0\n"
    "ds_read_u8 v238, v251 offset:1\n"
    "ds_read_b128 v[192:195], v234 offset:2048\n"
    "ds_read_b128 v[196:199], v235 offset:2048\n"
    "v_pk_fma_f32 v[176:177], v[176:177], s[70:71], v[128:129]\n"
    "v_pk_fma_f32 v[178:179], v[178:179], s[70:71], v[130:131]\n"
    "v_pk_add_f32 v[230:231], v[176:177], v[178:179]\n"
    "v_pk_mul_f32 v[232:233], v[176:177], v[178:179]\n"
    "v_pk_fma_f32 v[180:181], v[180:181], s[70:71], v[132:133]\n"
    "v_pk_fma_f32 v[230:231], v[230:231], v[180:181], v[232:233]\n"
    "v_pk_mul_f32 v[232:233], v[232:233], v[180:181]\n"
    "v_pk_fma_f32 v[182:183], v[182:183], s[70:71], v[134:135]\n"
    "v_pk_fma_f32 v[230:231], v[230:231], v[182:183], v[232:233]\n"
    "v_pk_mul_f32 v[232:233], v[232:233], v[182:183]\n"
    "v_pk_fma_f32 v[184:185], v[184:185], s[70:71], v[136:137]\n"
    "v_pk_fma_f32 v[230:231], v[230:231], v[184:185], v[232:233]\n"
    "v_pk_mul_f32 v[232:233], v[232:233], v[184:185]\n"
    "v_pk_fma_f32 v[186:187], v[186:187], s[70:71], v[138:139]\n"
    "v_pk_fma_f32 v[230:231], v[230:231], v[186:187], v[232:233]\n"
    "v_pk_mul_f32 v[232:233], v[232:233], v[186:187]\n"
    "v_pk_fma_f32 v[188:189], v[188:189], s[70:71], v[140:141]\n"
    "v_pk_fma_f32 v[230:231], v[230:231], v[188:189], v[232:233]\n"
    "v_pk_mul_f32 v[232:233], v[232:233], v[188:189]\n"
    "v_pk_fma_f32 v[190:191], v[190:191], s[70:71], v[142:143]\n"
    "v_pk_fma_f32 v[230:231], v[230:231], v[190:191], v[232:233]\n"
    "v_pk_mul_f32 v[232:233], v[232:233], v[190:191]\n"
    "v_rcp_f32 v232, v232\n"
    "v_rcp_f32 v233, v233\n"
    "v_fma_f32 v230, v230, v232, v247\n"
    "v_fmac_f32 v230, v231, v233\n"
    "v_mbcnt_lo_u32_b32 v232, -1, 0\n"
    "v_mbcnt_hi_u32_b32 v232, -1, v232\n"
    "v_bfe_u32 v233, v232, 3, 1\n"
    "v_and_b32 v232, 7, v232\n"
    "v_lshl_add_u32 v232, v232, 1, v233\n"
    "v_add_u32 v232, s81, v232\n"
    "ds_read_u8 v232, v232 offset:49664\n"
    "s_add_u32 s81, s81, 16\n"
    "s_waitcnt lgkmcnt(0)\n"
    "buffer_load_ushort v232, v232, s[44:47], s63 idxen\n"
    "s_waitcnt vmcnt(0)\n"
    "v_lshlrev_b32 v232, 16, v232\n"
    "v_mul_f32 v230, v230, v232\n"
    "v_cvt_pk_bf16_f32 v230, v230, v230\n"
    "s_add_u32 s66, s66, 16\n"
    "s_waitcnt lgkmcnt(0)\n"
    "ds_write_b16 v245, v230\n"
    "ds_read_b64 v[228:229], v246\n"
    "s_lshl1_add_u32 s76, s72, s76\n"
    "buffer_load_dwordx4 v[236:237], s[40:43], s67 idxen offen offset:0 nt lds\n"
    "buffer_load_dwordx4 v[238:239], s[40:43], s67 idxen offen offset:1024 nt lds\n"
    "s_mov_b32 s81, s64\n"
    "s_lshr_b32 s63, s65, 6\n"
    "s_mov_b32 s80, s83\n"
    "s_add_u32 s62, s62, 256\n"
    "s_bitcmp1_b32 s60, 30\n"
    "s_cbranch_scc0 .Ldin_A0\n"
    "s_branch .Ldin_A0s\n"
    ".Ldin_E0s:\n"
    "v_exp_f32 v160, v160 clamp\n"
    "v_exp_f32 v161, v161 clamp\n"
    "v_exp_f32 v162, v162 clamp\n"
    "v_exp_f32 v163, v163 clamp\n"
    "v_exp_f32 v164, v164 clamp\n"
    "v_exp_f32 v165, v165 clamp\n"
    "v_exp_f32 v166, v166 clamp\n"
    "v_exp_f32 v167, v167 clamp\n"
    "s_add_u32 m0, s62, 256\n"
    "s_waitcnt lgkmcnt(0)\n"
    "v_mfma_f32_4x4x4_16b_bf16 v[224:227], v[228:229], v[208:209], v[224:227] cbsz:4 abid:0\n"
    "v_exp_f32 v168, v168 clamp\n"
    "v_exp_f32 v169, v169 clamp\n"
    "v_mfma_f32_4x4x4_16b_bf16 v[224:227], v[228:229], v[210:211], v[224:227] cbsz:4 abid:1\n"
    "v_exp_f32 v170, v170 clamp\n"
    "v_exp_f32 v171, v171 clamp\n"
    "v_mfma_f32_4x4x4_16b_bf16 v[224:227], v[228:229], v[212:213], v[224:227] cbsz:4 abid:2\n"
    "v_exp_f32 v172, v172 clamp\n"
    "v_exp_f32 v173, v173 clamp\n"
    "v_mfma_f32_4x4x4_16b_bf16 v[224:227], v[228:229], v[214:215], v[224:227] cbsz:4 abid:3\n"
    "v_exp_f32 v174, v174 clamp\n"
    "v_exp_f32 v175, v175 clamp\n"
    "v_pk_fma_f32 v[160:161], v[160:161], s[70:71], v[128:129]\n"
    "v_pk_fma_f32 v[162:163], v[162:163], s[70:71], v[130:131]\n"
    "v_pk_add_f32 v[230:231], v[160:161], v[162:163]\n"
    "v_pk_mul_f32 v[232:233], v[160:161], v[162:163]\n"
    "v_pk_fma_f32 v[164:165], v[164:165], s[70:71], v[132:133]\n"
    "v_pk_fma_f32 v[230:231], v[230:231], v[164:165], v[232:233]\n"
    "v_pk_mul_f32 v[232:233], v[232:233], v[164:165]\n"
    "v_pk_fma_f32 v[166:167], v[166:167], s[70:71], v[134:135]\n"
    "v_pk_fma_f32 v[230:231], v[230:231], v[166:167], v[232:233]\n"
    "v_pk_mul_f32 v[232:233], v[232:233], v[166:167]\n"
    "v_pk_fma_f32 v[168:169], v[168:169], s[70:71], v[136:137]\n"
    "v_pk_fma_f32 v[230:231], v[230:231], v[168:169], v[232:233]\n"
    "v_pk_mul_f32 v[232:233], v[232:233], v[168:169]\n"
    "v_pk_fma_f32 v[170:171], v[170:171], s[70:71], v[138:139]\n"
    "v_pk_fma_f32 v[230:231], v[230:231], v[170:171], v[232:233]\n"
    "v_pk_mul_f32 v[232:233], v[232:233], v[170:171]\n"
    "v_pk_fma_f32 v[172:173], v[172:173], s[70:71], v[140:141]\n"
    "v_pk_fma_f32 v[230:231], v[230:231], v[172:173], v[232:233]\n"
    "v_pk_mul_f32 v[232:233], v[232:233], v[172:173]\n"
    "v_pk_fma_f32 v[174:175], v[174:175], s[70:71], v[142:143]\n"
    "v_pk_fma_f32 v[230:231], v[230:231], v[174:175], v[232:233]\n"
    "v_pk_mul_f32 v[232:233], v[232:233], v[174:175]\n"
    "v_rcp_f32 v232, v232\n"
    "v_rcp_f32 v233, v233\n"
    "v_fma_f32 v230, v230, v232, v247\n"
    "v_fmac_f32 v230, v231, v233\n"
    "v_mbcnt_lo_u32_b32 v232, -1, 0\n"
    "v_mbcnt_hi_u32_b32 v232, -1, v232\n"
    "v_bfe_u32 v233, v232, 3, 1\n"
    "v_and_b32 v232, 7, v232\n"
    "v_lshl_add_u32 v232, v232, 1, v233\n"
    "v_add_u32 v232, s81, v232\n"
    "ds_read_u8 v232, v232 offset:49664\n"
    "s_add_u32 s81, s81, 16\n"
    "s_waitcnt lgkmcnt(0)\n"
    "buffer_load_ushort v232, v232, s[44:47], s63 idxen\n"
    "s_waitcnt vmcnt(0)\n"
    "v_lshlrev_b32 v232, 16, v232\n"
    "v_mul_f32 v230, v230, v232\n"
    "v_cvt_pk_bf16_f32 v230, v230, v230\n"
    "ds_write_b16 v245, v230\n"
    "ds_read_b64 v[228:229], v246\n"
    "s_branch .Ldin_drain0\n"
    ".Ldin_E1s:\n"
    "v_exp_f32 v176, v176 clamp\n"
    "v_exp_f32 v177, v177 clamp\n"
    "v_exp_f32 v178, v178 clamp\n"
    "v_exp_f32 v179, v179 clamp\n"
    "v_exp_f32 v180, v180 clamp\n"
    "v_exp_f32 v181, v181 clamp\n"
    "v_exp_f32 v182, v182 clamp\n"
    "v_exp_f32 v183, v183 clamp\n"
    "s_add_u32 m0, s62, 256\n"
    "s_waitcnt lgkmcnt(0)\n"
    "v_mfma_f32_4x4x4_16b_bf16 v[224:227], v[228:229], v[200:201], v[224:227] cbsz:4 abid:0\n"
    "v_exp_f32 v184, v184 clamp\n"
    "v_exp_f32 v185, v185 clamp\n"
    "v_mfma_f32_4x4x4_16b_bf16 v[224:227], v[228:229], v[202:203], v[224:227] cbsz:4 abid:1\n"
    "v_exp_f32 v186, v186 clamp\n"
    "v_exp_f32 v187, v187 clamp\n"
    "v_mfma_f32_4x4x4_16b_bf16 v[224:227], v[228:229], v[204:205], v[224:227] cbsz:4 abid:2\n"
    "v_exp_f32 v188, v188 clamp\n"
    "v_exp_f32 v189, v189 clamp\n"
    "v_mfma_f32_4x4x4_16b_bf16 v[224:227], v[228:229], v[206:207], v[224:227] cbsz:4 abid:3\n"
    "v_exp_f32 v190, v190 clamp\n"
    "v_exp_f32 v191, v191 clamp\n"
    "v_pk_fma_f32 v[176:177], v[176:177], s[70:71], v[128:129]\n"
    "v_pk_fma_f32 v[178:179], v[178:179], s[70:71], v[130:131]\n"
    "v_pk_add_f32 v[230:231], v[176:177], v[178:179]\n"
    "v_pk_mul_f32 v[232:233], v[176:177], v[178:179]\n"
    "v_pk_fma_f32 v[180:181], v[180:181], s[70:71], v[132:133]\n"
    "v_pk_fma_f32 v[230:231], v[230:231], v[180:181], v[232:233]\n"
    "v_pk_mul_f32 v[232:233], v[232:233], v[180:181]\n"
    "v_pk_fma_f32 v[182:183], v[182:183], s[70:71], v[134:135]\n"
    "v_pk_fma_f32 v[230:231], v[230:231], v[182:183], v[232:233]\n"
    "v_pk_mul_f32 v[232:233], v[232:233], v[182:183]\n"
    "v_pk_fma_f32 v[184:185], v[184:185], s[70:71], v[136:137]\n"
    "v_pk_fma_f32 v[230:231], v[230:231], v[184:185], v[232:233]\n"
    "v_pk_mul_f32 v[232:233], v[232:233], v[184:185]\n"
    "v_pk_fma_f32 v[186:187], v[186:187], s[70:71], v[138:139]\n"
    "v_pk_fma_f32 v[230:231], v[230:231], v[186:187], v[232:233]\n"
    "v_pk_mul_f32 v[232:233], v[232:233], v[186:187]\n"
    "v_pk_fma_f32 v[188:189], v[188:189], s[70:71], v[140:141]\n"
    "v_pk_fma_f32 v[230:231], v[230:231], v[188:189], v[232:233]\n"
    "v_pk_mul_f32 v[232:233], v[232:233], v[188:189]\n"
    "v_pk_fma_f32 v[190:191], v[190:191], s[70:71], v[142:143]\n"
    "v_pk_fma_f32 v[230:231], v[230:231], v[190:191], v[232:233]\n"
    "v_pk_mul_f32 v[232:233], v[232:233], v[190:191]\n"
    "v_rcp_f32 v232, v232\n"
    "v_rcp_f32 v233, v233\n"
    "v_fma_f32 v230, v230, v232, v247\n"
    "v_fmac_f32 v230, v231, v233\n"
    "v_mbcnt_lo_u32_b32 v232, -1, 0\n"
    "v_mbcnt_hi_u32_b32 v232, -1, v232\n"
    "v_bfe_u32 v233, v232, 3, 1\n"
    "v_and_b32 v232, 7, v232\n"
    "v_lshl_add_u32 v232, v232, 1, v233\n"
    "v_add_u32 v232, s81, v232\n"
    "ds_read_u8 v232, v232 offset:49664\n"
    "s_add_u32 s81, s81, 16\n"
    "s_waitcnt lgkmcnt(0)\n"
    "buffer_load_ushort v232, v232, s[44:47], s63 idxen\n"
    "s_waitcnt vmcnt(0)\n"
    "v_lshlrev_b32 v232, 16, v232\n"
    "v_mul_f32 v230, v230, v232\n"
    "v_cvt_pk_bf16_f32 v230, v230, v230\n"
    "ds_write_b16 v245, v230\n"
    "ds_read_b64 v[228:229], v246\n"
    "s_branch .Ldin_drain1\n"
    ".Ldin_idle:\n"
    "s_waitcnt vmcnt(0)\n"
    ".Ldin_exit:\n"

      :
      : "{s[0:1]}"(__builtin_amdgcn_kernarg_segment_ptr()), "{s2}"(blockIdx.x), "{v0}"(threadIdx.x), "{s97}"((uint32_t)(uintptr_t)smem)
      : "memory", "vcc", "scc", "v1", "v2", "v3", "v4", "v5", "v6", "v7", "v8", "v9", "v10", "v11", "v12", "v13", "v14", "v15", "v16", "v17", "v18", "v19", "v20", "v21", "v22", "v23", "v24", "v25", "v26", "v27", "v28", "v29", "v30", "v31", "v32", "v33", "v34", "v35", "v36", "v37", "v38", "v39", "v40", "v41", "v42", "v43", "v44", "v45", "v46", "v47", "v48", "v49", "v50", "v51", "v52", "v53", "v54", "v55", "v56", "v57", "v58", "v59", "v60", "v61", "v62", "v63", "v64", "v65", "v66", "v67", "v68", "v69", "v70", "v71", "v72", "v73", "v74", "v75", "v76", "v77", "v78", "v79", "v80", "v81", "v82", "v83", "v84", "v85", "v86", "v87", "v88", "v89", "v90", "v91", "v92", "v93", "v94", "v95", "v96", "v97", "v98", "v99", "v100", "v101", "v102", "v103", "v104", "v105", "v106", "v107", "v108", "v109", "v110", "v111", "v112", "v113", "v114", "v115", "v116", "v117", "v118", "v119", "v120", "v121", "v122", "v123", "v124", "v125", "v126", "v127", "v128", "v129", "v130", "v131", "v132", "v133", "v134", "v135", "v136", "v137", "v138", "v139", "v140", "v141", "v142", "v143", "v144", "v145", "v146", "v147", "v148", "v149", "v150", "v151", "v152", "v153", "v154", "v155", "v156", "v157", "v158", "v159", "v160", "v161", "v162", "v163", "v164", "v165", "v166", "v167", "v168", "v169", "v170", "v171", "v172", "v173", "v174", "v175", "v176", "v177", "v178", "v179", "v180", "v181", "v182", "v183", "v184", "v185", "v186", "v187", "v188", "v189", "v190", "v191", "v192", "v193", "v194", "v195", "v196", "v197", "v198", "v199", "v200", "v201", "v202", "v203", "v204", "v205", "v206", "v207", "v208", "v209", "v210", "v211", "v212", "v213", "v214", "v215", "v216", "v217", "v218", "v219", "v220", "v221", "v222", "v223", "v224", "v225", "v226", "v227", "v228", "v229", "v230", "v231", "v232", "v233", "v234", "v235", "v236", "v237", "v238", "v239", "v240", "v241", "v242", "v243", "v244", "v245", "v246", "v247", "v248", "v249", "v250", "v251", "v252", "v253", "s3", "s4", "s5", "s6", "s7", "s8", "s9", "s10", "s11", "s12", "s13", "s14", "s15", "s16", "s17", "s18", "s19", "s20", "s21", "s22", "s23", "s24", "s25", "s26", "s27", "s28", "s29", "s30", "s31", "s33", "s34", "s35", "s36", "s37", "s38", "s39", "s40", "s41", "s42", "s43", "s44", "s45", "s46", "s47", "s48", "s49", "s50", "s51", "s52", "s53", "s54", "s55", "s56", "s57", "s58", "s59", "s60", "s61", "s62", "s63", "s64", "s65", "s66", "s67", "s68", "s69", "s70", "s71", "s72", "s73", "s74", "s75", "s76", "s77", "s78", "s79", "s80", "s81", "s82", "s83", "s84", "s85", "s86", "s87", "s88", "s89", "s90", "s91", "s92", "s93", "s94", "s95", "s96");
}

__global__ __launch_bounds__(64) void din_fallback_kernel(
    const unsigned short* __restrict__ target, const unsigned short* __restrict__ hist,
    const unsigned short* __restrict__ mask, const unsigned short* __restrict__ w1,
    const unsigned short* __restrict__ b1, const unsigned short* __restrict__ w2,
    const unsigned short* __restrict__ b2, unsigned short* __restrict__ out, int B, int L) {
  __shared__ float s_w[64 * 64];
  __shared__ float s_x[64];
  const int b = blockIdx.x, h = threadIdx.x;
  float bias = bf2f(b1[h]);
  for (int d = 0; d < 64; ++d) {
    const float t = bf2f(target[(size_t)b * 64 + d]);
    const float wc = bf2f(w1[(128 + d) * 64 + h]);
    bias += t * (bf2f(w1[(64 + d) * 64 + h]) - wc);
    s_w[d * 64 + h] = bf2f(w1[d * 64 + h]) + wc + t * bf2f(w1[(192 + d) * 64 + h]);
  }
  const float w2h = bf2f(w2[h]), b2v = bf2f(b2[0]);
  float acc = 0.f;
  for (int l = 0; l < L; ++l) {
    const float m = bf2f(mask[(size_t)b * L + l]);
    if (m == 0.f) continue;
    const float x = bf2f(hist[((size_t)b * L + l) * 64 + h]);
    __syncthreads();
    s_x[h] = x;
    __syncthreads();
    float z = bias;
    for (int d = 0; d < 64; ++d) z += s_x[d] * s_w[d * 64 + h];
    float s = w2h / (1.0f + __expf(-z));
    for (int o = 32; o > 0; o >>= 1) s += __shfl_xor(s, o);
    acc += (s + b2v) * m * x;
  }
  const uint32_t u = __float_as_uint(acc);
  out[(size_t)b * 64 + h] = (unsigned short)((u + 0x7fffu + ((u >> 16) & 1u)) >> 16);
}

extern "C" void kernel_launch(void* const* d_in, const int* in_sizes, int n_in, void* d_out,
                              int out_size, void* d_ws, size_t ws_size, hipStream_t stream) {
  const unsigned short* target = (const unsigned short*)d_in[0];
  const unsigned short* hist = (const unsigned short*)d_in[1];
  const unsigned short* mask = (const unsigned short*)d_in[2];
  const unsigned short* w1 = (const unsigned short*)d_in[3];
  const unsigned short* b1 = (const unsigned short*)d_in[4];
  const unsigned short* w2 = (const unsigned short*)d_in[5];
  const unsigned short* b2 = (const unsigned short*)d_in[6];
  int B = in_sizes[0] / 64;
  int L = in_sizes[2] / B;
  if ((L & 1) || L > 254 || B >= (1 << 24) - 16) {
    din_fallback_kernel<<<B, 64, 0, stream>>>(target, hist, mask, w1, b1, w2, b2, (unsigned short*)d_out, B, L);
    return;
  }
  const uint32_t G = (B + 63) / 64, L2 = 2u * L, lanes = (L2 + 15) / 16, F = 0x20000u;
  din_kernel<<<G, 512, 0, stream>>>(hist, (uint32_t)L, F, mask, (uint32_t)B * L2, F, w1, 32768u, F, (unsigned short*)d_out, (uint32_t)B * 128u, F,
                                    target, (uint32_t)B * 128u, F, b1, w2, b2, L2, G * 8u * L2, lanes >= 32 ? 0xffffffffu : (1u << lanes) - 1u,
                                    G * 8u * 128u, G >= 224 ? 0xffffffffu : 0u, L2 + 31u);
}
```

```cpp
#include <hip/hip_runtime.h>
#include <stdint.h>

__device__ __forceinline__ float bf2f(unsigned short u) { return __uint_as_float(((uint32_t)u) << 16); }

__global__ __launch_bounds__(512, 2) void din_kernel(
    const unsigned short* hist, uint32_t hn, uint32_t hf, const unsigned short* mask, uint32_t mn, uint32_t mf,
    const unsigned short* w1, uint32_t wn, uint32_t wf, unsigned short* out, uint32_t on, uint32_t of,
    const unsigned short* target, uint32_t tn, uint32_t tf, const unsigned short* b1, const unsigned short* w2, const unsigned short* b2,
    uint32_t row_bytes, uint32_t round_bytes, uint32_t row_lanes, uint32_t round_out, uint32_t paced, uint32_t usual_units) {
  __shared__ __attribute__((aligned(1024))) char smem[154624];
  asm volatile(
    ".p2align 3\n"
    "s_load_dwordx16 s[40:55], s[0:1], 0x0\n"
    "s_load_dwordx16 s[4:19], s[0:1], 0x40\n"
    "s_memrealtime s[76:77]\n"
    "v_readfirstlane_b32 s3, v0\n"
    "v_and_b32 v1, 63, v0\n"
    "v_and_b32 v2, 48, v0\n"
    "v_lshlrev_b32 v251, 4, v1\n"
    "s_lshr_b32 s20, s3, 6\n"
    "s_lshl_b32 s78, s2, 3\n"
    "s_and_b32 s21, s20, 3\n"
    "s_lshr_b32 s75, s3, 8\n"
    "s_cselect_b64 vcc, -1, 0\n"
    "s_cbranch_scc0 .Ldin_p0\n"
    "s_setprio 1\n"
    ".Ldin_p0:\n"
    "s_cmp_lt_u32 s2, 32\n"
    "s_cbranch_scc0 .Ldin_ncp\n"
    "s_getpc_b64 s[34:35]\n"
    "v_lshlrev_b32 v5, 7, v1\n"
    "s_mul_i32 s23, s20, 3072\n"
    "s_add_u32 s34, s34, s23\n"
    "s_addc_u32 s35, s35, 0\n"
    "s_mov_b64 exec, 0xffffff\n"
    "global_load_dword v253, v5, s[34:35] offset:1024\n"
    "s_mov_b64 exec, -1\n"
    ".Ldin_ncp:\n"
    "v_lshlrev_b32 v6, 2, v0\n"
    "v_and_b32 v7, 32, v0\n"
    "v_and_b32 v6, 64, v6\n"
    "v_xor_b32 v5, v251, v2\n"
    "v_bitop3_b32 v6, v6, v7, v251 bitop3:0x96\n"
    "v_and_b32 v3, 0x1f0, v251\n"
    "v_xor_b32 v7, 32, v6\n"
    "v_mov_b64 v[40:41], 0\n"
    "v_mov_b64 v[42:43], 0\n"
    "v_mov_b64 v[44:45], 0\n"
    "v_mov_b64 v[46:47], 0\n"
    "v_bfe_u32 v50, v251, 3, 5\n"
    "s_lshl_b32 s23, s21, 5\n"
    "v_lshrrev_b32_e64 v51, 1, v2\n"
    "s_lshl_b32 s24, s20, 12\n"
    "v_or_b32 v51, s23, v51\n"
    "s_add_u32 m0, s24, 54144\n"
    "s_add_u32 s22, s78, s20\n"
    "s_lshl_b32 s25, s20, 10\n"
    "s_lshl_b32 s26, s2, 10\n"
    "s_waitcnt lgkmcnt(0)\n"
    "s_mul_i32 s67, s22, s14\n"
    "s_cbranch_vccz .Ldin_pm\n"
    "global_load_short_d16_hi v40, v50, s[10:11] offset:0\n"
    "global_load_short_d16_hi v41, v50, s[10:11] offset:32\n"
    "global_load_short_d16_hi v42, v50, s[10:11] offset:64\n"
    "global_load_short_d16_hi v43, v50, s[10:11] offset:96\n"
    "global_load_short_d16_hi v44, v45, s[12:13]\n"
    "global_load_dwordx2 v[46:47], v51, s[10:11]\n"
    "global_load_dwordx2 v[48:49], v51, s[8:9]\n"
    "s_branch .Ldin_p1\n"
    ".Ldin_pm:\n"
    "s_mov_b32 exec_lo, s16\n"
    "s_mov_b32 exec_hi, 0\n"
    "s_lshl2_add_u32 s28, s14, s67\n"
    "buffer_load_dwordx4 v[40:43], v3, s[44:47], s67 offen nt\n"
    "s_add_u32 s29, s67, s15\n"
    "buffer_load_dwordx4 v[44:47], v3, s[44:47], s28 offen nt\n"
    "s_mov_b32 exec_hi, s16\n"
    "s_mov_b32 exec_lo, 0\n"
    "s_add_u32 s30, s28, s15\n"
    "buffer_load_dwordx4 v[40:43], v3, s[44:47], s29 offen nt\n"
    "buffer_load_dwordx4 v[44:47], v3, s[44:47], s30 offen nt\n"
    "s_mov_b64 exec, -1\n"
    ".Ldin_p1:\n"
    "s_mul_i32 s23, s20, s17\n"
    "buffer_load_dwordx4 v6, s[48:51], s24 offen offset:0 lds\n"
    "buffer_load_dwordx4 v7, s[48:51], s24 offen offset:1024 lds\n"
    "buffer_load_dwordx4 v6, s[48:51], s24 offen offset:2048 lds\n"
    "buffer_load_dwordx4 v7, s[48:51], s24 offen offset:3072 lds\n"
    "s_add_u32 m0, s25, 24576\n"
    "s_add_u32 s23, s23, s26\n"
    "buffer_load_dwordx4 v5, s[4:7], s23 offen nt lds\n"
    ".Ldin_p2:\n"
    "s_lshl_b32 s67, s67, 6\n"
    "s_lshl_b32 s68, s20, 13\n"
    "s_lshl_b32 s23, s20, 8\n"
    "s_add_u32 s68, s68, 89088\n"
    "s_add_u32 s23, s23, 86912\n"
    "v_and_b32 v32, 0x70, v5\n"
    "v_lshrrev_b32 v4, 1, v3\n"
    "v_or_b32 v237, 0x400, v32\n"
    "v_xor_b32 v239, 64, v32\n"
    "v_and_b32 v32, 0x70, v4\n"
    "v_lshlrev_b32 v33, 6, v50\n"
    "v_bitop3_b32 v36, v32, v2, v33 bitop3:0x96\n"
    "v_and_b32 v32, 8, v0\n"
    "v_add_u32 v234, s68, v36\n"
    "v_mul_u32_u24 v33, 0x88, v32\n"
    "v_xor_b32 v235, 64, v234\n"
    "v_lshrrev_b32 v245, 1, v32\n"
    "v_lshlrev_b32 v32, 1, v2\n"
    "v_and_b32 v34, 0x18, v4\n"
    "v_bitop3_b32 v33, v33, v32, v34 bitop3:0x96\n"
    "v_or3_b32 v245, v245, v32, s23\n"
    "v_and_b32 v32, 0x40, v251\n"
    "v_lshl_or_b32 v246, v34, 2, s23\n"
    "v_lshl_or_b32 v33, v32, 1, v33\n"
    "v_and_b32 v32, 24, v50\n"
    "v_add_u32 v240, s68, v33\n"
    "v_or_b32 v246, v246, v32\n"
    "v_xor_b32 v241, 0x110, v240\n"
    "v_xor_b32 v242, 0x220, v240\n"
    "v_xor_b32 v243, 0x330, v240\n"
    "v_and_b32 v32, 12, v50\n"
    "v_and_b32 v33, 2, v50\n"
    "v_lshl_or_b32 v32, v32, 1, v33\n"
    "v_bfe_u32 v244, v0, 3, 3\n"
    "v_or_b32 v245, v245, v32\n"
    "s_mov_b32 s24, 49664\n"
    "v_lshl_add_u32 v244, v244, 1, s24\n"
    "v_or_b32 v250, 0x6000, v2\n"
    "s_mov_b64 s[26:27], 0xffff\n"
    "v_mov_b64 v[248:249], 0\n"
    "s_mov_b32 exec_lo, 0x00f0000f\n"
    "s_mov_b32 exec_hi, 0xf0000f00\n"
    "v_lshlrev_b64 v[248:249], v251, s[26:27]\n"
    "s_mov_b64 exec, -1\n"
    "v_add_u32 v13, 1, v4\n"
    "v_mov_b64 v[14:15], -1\n"
    "v_lshl_add_u32 v8, v13, 16, v4\n"
    "v_add_u32 v9, 0x20002, v8\n"
    "v_add_u32 v10, 0x40004, v8\n"
    "v_add_u32 v11, 0x60006, v8\n"
    "v_mov_b32 v17, v237\n"
    "v_mov_b32 v19, v239\n"
    "v_mov_b32 v21, v237\n"
    "v_mov_b32 v23, v239\n"
    "v_mov_b32 v25, v237\n"
    "v_mov_b32 v27, v239\n"
    "v_mov_b32 v29, v237\n"
    "v_mov_b32 v31, v239\n"
    "v_mov_b32 v32, -1\n"
    "v_mov_b32 v33, 0\n"
    "s_mov_b64 exec, 0xff00\n"
    "v_mov_b32 v32, 0x8c000000\n"
    "s_mov_b64 exec, 0xfffc\n"
    "ds_write_b64 v246, v[32:33] offset:128\n"
    "s_mov_b64 exec, 0xfff\n"
    "ds_write_b64 v4, v[14:15] offset:49664\n"
    "ds_write_b32 v33, v33 offset:49416\n"
    "s_mov_b64 exec, -1\n"
    "s_mov_b32 s28, 0xc080c080\n"
    "s_mov_b32 s29, 0xffff0001\n"
    "s_mov_b32 s30, 0x01000100\n"
    "s_mov_b32 s31, 0x00010001\n"
    "s_add_u32 s40, s40, 0xfffffc00\n"
    "s_addc_u32 s41, s41, 0x7fffff\n"
    "s_lshl_b32 s56, s14, 6\n"
    "s_lshl_b32 s57, s15, 6\n"
    "s_lshr_b32 s79, s17, 7\n"
    "s_mul_i32 s58, s78, s56\n"
    "s_mov_b32 s59, 1\n"
    "s_add_u32 s58, s58, s57\n"
    "s_mul_i32 s62, s20, 2304\n"
    "s_mov_b32 s70, 0x47000000\n"
    "s_mov_b32 s71, 0x47000000\n"
    "s_add_u32 s62, s62, 18016\n"
    "s_and_b32 s77, s18, 53\n"
    "s_and_b32 s73, s18, 8\n"
    "s_lshr_b32 s74, s6, 7\n"
    "s_cbranch_vccnz .Ldin_py\n"
    "v_lshrrev_b32 v32, 5, v1\n"
    "s_mul_i32 s24, s20, 272\n"
    "v_mul_u32_u24 v12, 2176, v32\n"
    "v_and_b32 v60, 32, v0\n"
    "v_add3_u32 v12, v12, s24, 15\n"
    "v_add_u32 v60, s23, v60\n"
    "v_add_u32 v13, v13, v12\n"
    "v_mov_b32 v57, 0\n"
    "v_mov_b32_e64 v59, 0\n"
    "ds_write_b64 v13, v[14:15] offset:49760\n"
    "ds_write_b64 v13, v[14:15] offset:50848\n"
    "s_mul_i32 s23, s20, 0x80110\n"
    "s_lshr_b32 s24, s20, 1\n"
    "s_lshl_b32 s24, s24, 16\n"
    "s_add_u32 s23, s23, s24\n"
    "s_sub_u32 s24, s74, s22\n"
    "s_sub_u32 s25, s24, s79\n"
    "s_add_u32 s26, s23, 0x70\n"
    "s_cmp_gt_i32 s24, 0\n"
    "s_cselect_b32 s26, s26, 0x80000000\n"
    "v_writelane_b32 v56, s26, 31\n"
    "s_add_u32 s26, s23, 0x4008f0\n"
    "s_cmp_gt_i32 s25, 0\n"
    "s_cselect_b32 s26, s26, 0x80000000\n"
    "v_writelane_b32 v56, s26, 63\n"
    "s_add_u32 s26, s23, 0x2204b0\n"
    "s_cmp_gt_i32 s24, 4\n"
    "s_cselect_b32 s26, s26, 0x80000000\n"
    "v_writelane_b32 v58, s26, 31\n"
    "s_add_u32 s26, s23, 0x620d30\n"
    "s_cmp_gt_i32 s25, 4\n"
    "s_cselect_b32 s26, s26, 0x80000000\n"
    "v_writelane_b32 v58, s26, 63\n"
    "s_cmp_lg_u32 s21, 0\n"
    "s_cbranch_scc1 .Ldin_tw0\n"
    "s_call_b64 s[34:35], .Ldin_stub_G1\n"
    "s_call_b64 s[34:35], .Ldin_stub_A0_poll\n"
    "s_call_b64 s[34:35], .Ldin_stub_L2\n"
    "s_branch .Ldin_twe\n"
    ".Ldin_tw0:\n"
    "s_cmp_lg_u32 s21, 1\n"
    "s_cbranch_scc1 .Ldin_tw1\n"
    "s_call_b64 s[34:35], .Ldin_stub_L1\n"
    "s_call_b64 s[34:35], .Ldin_stub_L0\n"
    "s_branch .Ldin_twe\n"
    ".Ldin_tw1:\n"
    "s_cmp_lg_u32 s21, 2\n"
    "s_cbranch_scc1 .Ldin_tw2\n"
    "s_call_b64 s[34:35], .Ldin_stub_L3\n"
    "s_call_b64 s[34:35], .Ldin_stub_E0\n"
    "s_branch .Ldin_twe\n"
    ".Ldin_tw2:\n"
    "s_call_b64 s[34:35], .Ldin_stub_E1\n"
    "s_call_b64 s[34:35], .Ldin_stub_ps\n"
    ".Ldin_twe:\n"
    "s_and_b32 s23, s14, 14\n"
    "s_waitcnt vmcnt(5)\n"
    "s_cbranch_scc1 .Ldin_trimo\n"
    ".Ldin_trimo_r:\n"
    "v_pk_min_u16 v48, v40, 1 op_sel_hi:[1,0]\n"
    "v_pk_min_u16 v49, v41, 1 op_sel_hi:[1,0]\n"
    "v_pk_min_u16 v50, v42, 1 op_sel_hi:[1,0]\n"
    "v_pk_min_u16 v51, v43, 1 op_sel_hi:[1,0]\n"
    "v_add3_u32 v64, v48, v49, v50\n"
    "v_add_u32_e64 v64, v64, v51\n"
    "v_dot2_u32_u16 v62, v64, s31, 0\n"
    "v_pk_min_u16 v52, v44, 1 op_sel_hi:[1,0]\n"
    "v_pk_min_u16 v53, v45, 1 op_sel_hi:[1,0]\n"
    "v_pk_min_u16 v54, v46, 1 op_sel_hi:[1,0]\n"
    "v_pk_min_u16 v55, v47, 1 op_sel_hi:[1,0]\n"
    "v_add3_u32 v64, v52, v53, v54\n"
    "v_add_u32_e64 v64, v64, v55\n"
    "v_dot2_u32_u16 v62, v64, s30, v62\n"
    "v_pk_mad_u16 v40, v48, s28, v40\n"
    "v_pk_mad_u16 v41, v49, s28, v41\n"
    "v_pk_mad_u16 v42, v50, s28, v42\n"
    "v_add_u32_dpp v63, v62, v62 row_shr:1 row_mask:0xf bank_mask:0xf bound_ctrl:1\n"
    "v_sub_u32 v64, 0x10101, v62\n"
    "v_pk_mad_u16 v43, v51, s28, v43\n"
    "v_add_u32_dpp v63, v63, v63 row_shr:2 row_mask:0xf bank_mask:0xf bound_ctrl:1\n"
    "v_or3_b32 v65, v40, v41, 0\n"
    "v_or3_b32 v65, v42, v43, v65\n"
    "v_add_u32_dpp v63, v63, v63 row_shr:4 row_mask:0xf bank_mask:0xf bound_ctrl:1\n"
    "v_pk_mad_u16 v44, v52, s28, v44\n"
    "v_pk_mad_u16 v45, v53, s28, v45\n"
    "v_add_u32_dpp v63, v63, v63 row_shr:8 row_mask:0xf bank_mask:0xf bound_ctrl:1\n"
    "v_pk_mad_u16 v46, v54, s28, v46\n"
    "v_pk_mad_u16 v47, v55, s28, v47\n"
    "v_add_u32_dpp v63, v63, v63 row_bcast:15 row_mask:0xa bank_mask:0xf\n"
    "v_or3_b32 v65, v44, v45, v65\n"
    "v_or3_b32 v65, v46, v47, v65\n"
    "v_add_u32_e64 v64, v64, v63\n"
    "s_mov_b32 s26, 0xc040c00\n"
    "v_perm_b32 v66, v63, v64, s26\n"
    "v_mad_u32_u16 v68, v48, v66, v12\n"
    "v_mad_u32_u16 v69, v48, v66, v12 op_sel:[1,1,0,0]\n"
    "v_pk_mad_u16 v66, v48, s29, v66\n"
    "ds_write_b8 v68, v8 offset:49760\n"
    "ds_write_b8_d16_hi v69, v8 offset:49760\n"
    "v_mad_u32_u16 v70, v49, v66, v12\n"
    "v_mad_u32_u16 v71, v49, v66, v12 op_sel:[1,1,0,0]\n"
    "v_pk_mad_u16 v66, v49, s29, v66\n"
    "ds_write_b8 v70, v9 offset:49760\n"
    "ds_write_b8_d16_hi v71, v9 offset:49760\n"
    "v_mad_u32_u16 v68, v50, v66, v12\n"
    "v_mad_u32_u16 v69, v50, v66, v12 op_sel:[1,1,0,0]\n"
    "v_pk_mad_u16 v66, v50, s29, v66\n"
    "ds_write_b8 v68, v10 offset:49760\n"
    "ds_write_b8_d16_hi v69, v10 offset:49760\n"
    "v_mad_u32_u16 v70, v51, v66, v12\n"
    "v_mad_u32_u16 v71, v51, v66, v12 op_sel:[1,1,0,0]\n"
    "ds_write_b8 v70, v11 offset:49760\n"
    "ds_write_b8_d16_hi v71, v11 offset:49760\n"
    "s_waitcnt vmcnt(0)\n"
    "s_barrier\n"
    "s_mul_i32 s23, s20, 272\n"
    "v_add_u32_e64 v66, s23, v244\n"
    "ds_read_u8 v16, v66 offset:112\n"
    "ds_read_u8 v18, v66 offset:113\n"
    "ds_read_u8 v20, v66 offset:128\n"
    "ds_read_u8 v22, v66 offset:129\n"
    "ds_read_u8 v24, v66 offset:144\n"
    "ds_read_u8 v26, v66 offset:145\n"
    "ds_read_u8 v28, v66 offset:160\n"
    "ds_read_u8 v30, v66 offset:161\n"
    "v_cmp_ne_u32 s[26:27], 0, v65\n"
    "s_mov_b32 s24, 0x0c010c00\n"
    "v_perm_b32 v62, v63, v63, s24\n"
    "s_cmp_lg_u64 s[26:27], 0\n"
    "v_pk_add_u16 v62, v62, 15 op_sel_hi:[1,0]\n"
    "s_cselect_b32 s24, 0x200020, 0\n"
    "v_pk_lshrrev_b16 v62, 4, v62 op_sel_hi:[0,1]\n"
    "v_pk_max_u16 v62, v62, 6 op_sel_hi:[1,0]\n"
    "v_or_b32 v62, s24, v62\n"
    "v_lshl_or_b32 v56, v62, 25, v56\n"
    "v_lshrrev_b32 v62, 16, v62\n"
    "s_add_u32 m0, s68, 0\n"
    "s_waitcnt lgkmcnt(6)\n"
    "buffer_load_dwordx4 v[16:17], s[40:43], s67 idxen offen offset:0 nt lds\n"
    "buffer_load_dwordx4 v[18:19], s[40:43], s67 idxen offen offset:1024 nt lds\n"
    "s_add_u32 m0, s68, 2048\n"
    "s_waitcnt lgkmcnt(4)\n"
    "buffer_load_dwordx4 v[20:21], s[40:43], s67 idxen offen offset:0 nt lds\n"
    "buffer_load_dwordx4 v[22:23], s[40:43], s67 idxen offen offset:1024 nt lds\n"
    "s_add_u32 m0, s68, 4096\n"
    "s_waitcnt lgkmcnt(2)\n"
    "buffer_load_dwordx4 v[24:25], s[40:43], s67 idxen offen offset:0 nt lds\n"
    "buffer_load_dwordx4 v[26:27], s[40:43], s67 idxen offen offset:1024 nt lds\n"
    "s_add_u32 m0, s68, 6144\n"
    "s_waitcnt lgkmcnt(0)\n"
    "buffer_load_dwordx4 v[28:29], s[40:43], s67 idxen offen offset:0 nt lds\n"
    "buffer_load_dwordx4 v[30:31], s[40:43], s67 idxen offen offset:1024 nt lds\n"
    "v_lshl_or_b32 v58, v62, 25, v58\n"
    "v_readlane_b32 s60, v56, 31\n"
    "s_mov_b32 s26, 0xc050c01\n"
    "v_perm_b32 v66, v63, v64, s26\n"
    "v_mad_u32_u16 v68, v52, v66, v12\n"
    "v_mad_u32_u16 v69, v52, v66, v12 op_sel:[1,1,0,0]\n"
    "v_pk_mad_u16 v66, v52, s29, v66\n"
    "ds_write_b8 v68, v8 offset:50848\n"
    "ds_write_b8_d16_hi v69, v8 offset:50848\n"
    "v_mad_u32_u16 v70, v53, v66, v12\n"
    "v_mad_u32_u16 v71, v53, v66, v12 op_sel:[1,1,0,0]\n"
    "v_pk_mad_u16 v66, v53, s29, v66\n"
    "ds_write_b8 v70, v9 offset:50848\n"
    "ds_write_b8_d16_hi v71, v9 offset:50848\n"
    "v_mad_u32_u16 v68, v54, v66, v12\n"
    "v_mad_u32_u16 v69, v54, v66, v12 op_sel:[1,1,0,0]\n"
    "v_pk_mad_u16 v66, v54, s29, v66\n"
    "ds_write_b8 v68, v10 offset:50848\n"
    "ds_write_b8_d16_hi v69, v10 offset:50848\n"
    "v_mad_u32_u16 v70, v55, v66, v12\n"
    "v_mad_u32_u16 v71, v55, v66, v12 op_sel:[1,1,0,0]\n"
    "ds_write_b8 v70, v11 offset:50848\n"
    "ds_write_b8_d16_hi v71, v11 offset:50848\n"
    "s_brev_b32 exec_lo, 1\n"
    "s_brev_b32 exec_hi, 1\n"
    "ds_write_b64 v60, v[56:57] offset:128\n"
    "ds_write_b64 v60, v[58:59] offset:1152\n"
    "s_mov_b64 exec, -1\n"
    "s_waitcnt lgkmcnt(0)\n"
    "s_barrier\n"
    ".Ldin_go:\n"
    "s_or_b32 s45, s45, 0x20000\n"
    "s_mov_b32 s46, s42\n"
    "s_cmp_lt_i32 s60, 0\n"
    "s_cbranch_scc1 .Ldin_idle\n"
    "s_lshr_b32 s94, s60, 12\n"
    "s_and_b32 s94, s94, 0x1ff0\n"
    "v_xor_b32 v252, s94, v250\n"
    "ds_read_b128 v[216:219], v252 offset:0\n"
    "ds_read_b128 v[220:223], v252 offset:64\n"
    "ds_read_b128 v[96:99], v251 offset:16384\n"
    "ds_read_b128 v[32:35], v251 offset:0\n"
    "ds_read_b128 v[36:39], v251 offset:1024\n"
    "ds_read_b128 v[100:103], v251 offset:17408\n"
    "ds_read_b128 v[40:43], v251 offset:2048\n"
    "ds_read_b128 v[44:47], v251 offset:3072\n"
    "ds_read_b128 v[104:107], v251 offset:18432\n"
    "ds_read_b128 v[48:51], v251 offset:4096\n"
    "ds_read_b128 v[52:55], v251 offset:5120\n"
    "ds_read_b128 v[108:111], v251 offset:19456\n"
    "ds_read_b128 v[56:59], v251 offset:6144\n"
    "ds_read_b128 v[60:63], v251 offset:7168\n"
    "s_and_b32 s66, s60, 0xffff\n"
    "s_bfe_u32 s95, s60, 0x50019\n"
    "s_mov_b32 s81, s66\n"
    "s_lshr_b32 s63, s67, 6\n"
    "s_lshl4_add_u32 s80, s95, s66\n"
    "v_add_u32 v230, s66, v244\n"
    "s_add_u32 s66, s66, 80\n"
    "s_add_u32 m0, s68, 0\n"
    "s_cmp_lg_u32 s75, 0\n"
    "s_cselect_b64 vcc, -1, 0\n"
    "s_waitcnt lgkmcnt(12)\n"
    "v_and_b32 v216, v248, v216\n"
    "v_and_b32 v217, v249, v217\n"
    "v_and_b32 v218, v248, v218\n"
    "v_and_b32 v219, v249, v219\n"
    "s_waitcnt lgkmcnt(9)\n"
    "ds_read_b128 v[112:115], v251 offset:20480\n"
    "ds_read_b128 v[64:67], v251 offset:8192\n"
    "ds_read_b128 v[68:71], v251 offset:9216\n"
    "v_mfma_f32_16x16x16_bf16 v[176:179], v[216:217], v[96:97], v[32:35]\n"
    "v_and_b32 v220, v248, v220\n"
    "v_and_b32 v221, v249, v221\n"
    "v_and_b32 v222, v248, v222\n"
    "v_mfma_f32_16x16x16_bf16 v[180:183], v[218:219], v[98:99], v[36:39]\n"
    "v_and_b32 v223, v249, v223\n"
    "s_waitcnt lgkmcnt(9)\n"
    "ds_read_b128 v[116:119], v251 offset:21504\n"
    "ds_read_b128 v[72:75], v251 offset:10240\n"
    "ds_read_b128 v[76:79], v251 offset:11264\n"
    "v_mfma_f32_16x16x16_bf16 v[184:187], v[220:221], v[100:101], v[40:43]\n"
    "v_cvt_pk_bf16_f32 v0, v176, v177\n"
    "v_cvt_pk_bf16_f32 v1, v178, v179\n"
    "v_cvt_pk_bf16_f32 v2, v180, v181\n"
    "v_mfma_f32_16x16x16_bf16 v[188:191], v[222:223], v[102:103], v[44:47]\n"
    "v_cvt_pk_bf16_f32 v3, v182, v183\n"
    "s_waitcnt lgkmcnt(9)\n"
    "s_nop 0\n"
    "ds_read_b128 v[120:123], v251 offset:22528\n"
    "ds_read_b128 v[80:83], v251 offset:12288\n"
    "ds_read_b128 v[84:87], v251 offset:13312\n"
    "v_mfma_f32_16x16x16_bf16 v[176:179], v[216:217], v[104:105], v[48:51]\n"
    "v_cvt_pk_bf16_f32 v4, v184, v185\n"
    "v_cvt_pk_bf16_f32 v5, v186, v187\n"
    "v_cvt_pk_bf16_f32 v6, v188, v189\n"
    "v_mfma_f32_16x16x16_bf16 v[180:183], v[218:219], v[106:107], v[52:55]\n"
    "v_cvt_pk_bf16_f32 v7, v190, v191\n"
    "s_waitcnt lgkmcnt(9)\n"
    "s_nop 0\n"
    "ds_read_b128 v[124:127], v251 offset:23552\n"
    "ds_read_b128 v[88:91], v251 offset:14336\n"
    "ds_read_b128 v[92:95], v251 offset:15360\n"
    "v_mfma_f32_16x16x16_bf16 v[184:187], v[220:221], v[108:109], v[56:59]\n"
    "v_cvt_pk_bf16_f32 v8, v176, v177\n"
    "v_cvt_pk_bf16_f32 v9, v178, v179\n"
    "v_cvt_pk_bf16_f32 v10, v180, v181\n"
    "v_mfma_f32_16x16x16_bf16 v[188:191], v[222:223], v[110:111], v[60:63]\n"
    "v_cvt_pk_bf16_f32 v11, v182, v183\n"
    "s_waitcnt lgkmcnt(9)\n"
    "s_nop 0\n"
    "ds_read_b128 v[144:147], v252 offset:8192\n"
    "ds_read_b128 v[148:151], v252 offset:8256\n"
    "ds_read_b128 v[152:155], v252 offset:16384\n"
    "ds_read_b128 v[156:159], v252 offset:16448\n"
    "ds_read_u8 v236, v230 offset:64\n"
    "ds_read_u8 v238, v230 offset:65\n"
    "v_mfma_f32_16x16x16_bf16 v[176:179], v[216:217], v[112:113], v[64:67]\n"
    "v_cvt_pk_bf16_f32 v12, v184, v185\n"
    "v_cvt_pk_bf16_f32 v13, v186, v187\n"
    "v_cvt_pk_bf16_f32 v14, v188, v189\n"
    "v_mfma_f32_16x16x16_bf16 v[180:183], v[218:219], v[114:115], v[68:71]\n"
    "v_cvt_pk_bf16_f32 v15, v190, v191\n"
    "s_waitcnt lgkmcnt(12)\n"
    "s_waitcnt vmcnt(4)\n"
    "ds_read_b128 v[192:195], v234 offset:0\n"
    "ds_read_b128 v[196:199], v235 offset:0\n"
    "ds_read_b64_tr_b16 v[200:201], v240 offset:0\n"
    "ds_read_b64_tr_b16 v[202:203], v241 offset:0\n"
    "ds_read_b64_tr_b16 v[204:205], v242 offset:0\n"
    "ds_read_b64_tr_b16 v[206:207], v243 offset:0\n"
    "v_mfma_f32_16x16x16_bf16 v[184:187], v[220:221], v[116:117], v[72:75]\n"
    "v_cvt_pk_bf16_f32 v16, v176, v177\n"
    "v_cvt_pk_bf16_f32 v17, v178, v179\n"
    "v_cvt_pk_bf16_f32 v18, v180, v181\n"
    "v_mfma_f32_16x16x16_bf16 v[188:191], v[222:223], v[118:119], v[76:79]\n"
    "v_cvt_pk_bf16_f32 v19, v182, v183\n"
    "s_waitcnt lgkmcnt(15)\n"
    "s_nop 0\n"
    "ds_read_b128 v[128:131], v250 offset:24576\n"
    "ds_read_b128 v[132:135], v250 offset:24640\n"
    "ds_read_b128 v[136:139], v250 offset:24704\n"
    "ds_read_b128 v[140:143], v250 offset:24768\n"
    "ds_read_b64 v[232:233], v250 offset:24832\n"
    "v_mfma_f32_16x16x16_bf16 v[176:179], v[216:217], v[120:121], v[80:83]\n"
    "v_cvt_pk_bf16_f32 v20, v184, v185\n"
    "v_cvt_pk_bf16_f32 v21, v186, v187\n"
    "v_cvt_pk_bf16_f32 v22, v188, v189\n"
    "v_mfma_f32_16x16x16_bf16 v[180:183], v[218:219], v[122:123], v[84:87]\n"
    "v_cvt_pk_bf16_f32 v23, v190, v191\n"
    "s_waitcnt lgkmcnt(15)\n"
    "v_mfma_f32_16x16x16_bf16 v[184:187], v[220:221], v[124:125], v[88:91]\n"
    "v_mfma_f32_16x16x16_bf16 v[188:191], v[222:223], v[126:127], v[92:95]\n"
    "v_cvt_pk_bf16_f32 v24, v176, v177\n"
    "v_cvt_pk_bf16_f32 v25, v178, v179\n"
    "s_waitcnt lgkmcnt(5)\n"
    "buffer_load_dwordx4 v[236:237], s[40:43], s67 idxen offen offset:0 nt lds\n"
    "buffer_load_dwordx4 v[238:239], s[40:43], s67 idxen offen offset:1024 nt lds\n"
    "v_mfma_f32_16x16x32_bf16 v[160:163], v[0:3], v[192:195], v[144:147]\n"
    "v_cvt_pk_bf16_f32 v26, v180, v181\n"
    "v_cvt_pk_bf16_f32 v27, v182, v183\n"
    "v_mfma_f32_16x16x32_bf16 v[160:163], v[4:7], v[196:199], v[160:163]\n"
    "v_cvt_pk_bf16_f32 v28, v184, v185\n"
    "v_cvt_pk_bf16_f32 v29, v186, v187\n"
    "v_mfma_f32_16x16x32_bf16 v[164:167], v[8:11], v[192:195], v[148:151]\n"
    "v_cvt_pk_bf16_f32 v30, v188, v189\n"
    "v_cvt_pk_bf16_f32 v31, v190, v191\n"
    "v_mfma_f32_16x16x32_bf16 v[164:167], v[12:15], v[196:199], v[164:167]\n"
    "v_mfma_f32_16x16x32_bf16 v[168:171], v[16:19], v[192:195], v[152:155]\n"
    "v_mfma_f32_16x16x32_bf16 v[168:171], v[20:23], v[196:199], v[168:171]\n"
    "v_mfma_f32_16x16x32_bf16 v[172:175], v[24:27], v[192:195], v[156:159]\n"
    "v_mfma_f32_16x16x32_bf16 v[172:175], v[28:31], v[196:199], v[172:175]\n"
    "ds_read_b128 v[192:195], v234 offset:2048\n"
    "ds_read_b128 v[196:199], v235 offset:2048\n"
    "s_memrealtime s[34:35]\n"
    "s_waitcnt lgkmcnt(0)\n"
    "s_mul_i32 s94, s75, 70\n"
    "s_add_u32 s94, s94, 0\n"
    "s_and_b32 s94, s18, s94\n"
    "s_add_u32 s76, s34, s94\n"
    "v_mov_b32 v247, v232\n"
    "v_readfirstlane_b32 s69, v233\n"
    "s_bitcmp1_b32 s60, 30\n"
    "s_nop 0\n"
    "s_cbranch_scc1 .Ldin_A0s\n"
    ".Ldin_A0:\n"
    "s_cbranch_vccz .Ldin_A0_np\n"
    "s_setprio 1\n"
    ".Ldin_A0_np:\n"
    "v_mfma_f32_16x16x32_bf16 v[176:179], v[0:3], v[192:195], v[144:147]\n"
    "v_exp_f32 v160, v160 clamp\n"
    "v_mfma_f32_16x16x32_bf16 v[176:179], v[4:7], v[196:199], v[176:179]\n"
    "v_exp_f32 v161, v161 clamp\n"
    "v_mfma_f32_16x16x32_bf16 v[180:183], v[8:11], v[192:195], v[148:151]\n"
    "v_exp_f32 v162, v162 clamp\n"
    "v_mfma_f32_16x16x32_bf16 v[180:183], v[12:15], v[196:199], v[180:183]\n"
    "v_exp_f32 v163, v163 clamp\n"
    "v_mfma_f32_16x16x32_bf16 v[184:187], v[16:19], v[192:195], v[152:155]\n"
    "v_exp_f32 v164, v164 clamp\n"
    "v_mfma_f32_16x16x32_bf16 v[184:187], v[20:23], v[196:199], v[184:187]\n"
    "v_exp_f32 v165, v165 clamp\n"
    "v_mfma_f32_16x16x32_bf16 v[188:191], v[24:27], v[192:195], v[156:159]\n"
    "v_exp_f32 v166, v166 clamp\n"
    "v_mfma_f32_16x16x32_bf16 v[188:191], v[28:31], v[196:199], v[188:191]\n"
    "v_exp_f32 v167, v167 clamp\n"
    "v_exp_f32 v168, v168 clamp\n"
    "s_mov_b32 m0, s62\n"
    "ds_read_b64 v[216:217], v246 offset:128\n"
    "v_exp_f32 v169, v169 clamp\n"
    "v_add_u32 v251, s66, v244\n"
    "s_waitcnt lgkmcnt(1)\n"
    "v_mfma_f32_4x4x4_16b_bf16 v[224:227], v[228:229], v[208:209], v[224:227] cbsz:4 abid:0\n"
    "v_exp_f32 v170, v170 clamp\n"
    "v_exp_f32 v171, v171 clamp\n"
    "v_mfma_f32_4x4x4_16b_bf16 v[224:227], v[228:229], v[210:211], v[224:227] cbsz:4 abid:1\n"
    "v_exp_f32 v172, v172 clamp\n"
    "v_exp_f32 v173, v173 clamp\n"
    "v_mfma_f32_4x4x4_16b_bf16 v[224:227], v[228:229], v[212:213], v[224:227] cbsz:4 abid:2\n"
    "v_exp_f32 v174, v174 clamp\n"
    "v_exp_f32 v175, v175 clamp\n"
    "v_mfma_f32_4x4x4_16b_bf16 v[224:227], v[228:229], v[214:215], v[224:227] cbsz:4 abid:3\n"
    "s_waitcnt vmcnt(4)\n"
    "s_waitcnt lgkmcnt(0)\n"
    "v_readlane_b32 s60, v216, s59\n"
    "v_readlane_b32 s61, v217, s59\n"
    "ds_read_b64_tr_b16 v[208:209], v240 offset:2048\n"
    "ds_read_b64_tr_b16 v[210:211], v241 offset:2048\n"
    "ds_read_b64_tr_b16 v[212:213], v242 offset:2048\n"
    "ds_read_b64_tr_b16 v[214:215], v243 offset:2048\n"
    "ds_read_u8 v236, v251 offset:0\n"
    "ds_read_u8 v238, v251 offset:1\n"
    "ds_read_b128 v[192:195], v234 offset:4096\n"
    "ds_read_b128 v[196:199], v235 offset:4096\n"
    "s_cmp_eq_u32 s60, -1\n"
    "s_cbranch_scc1 .Ldin_A0_poll\n"
    ".Ldin_A0_pr:\n"
    "s_lshr_b32 s94, s60, 12\n"
    "s_and_b32 s94, s94, 0x1ff0\n"
    "v_xor_b32 v252, s94, v250\n"
    "v_pk_add_f32 v[230:231], v[224:225], v[226:227]\n"
    "v_add_f32 v230, v230, v231\n"
    "ds_write_addtid_b32 v230 offset:49152\n"
    "ds_read_b128 v[216:219], v252 offset:0\n"
    "ds_read_b128 v[220:223], v252 offset:64\n"
    "v_mov_b64 v[224:225], 0\n"
    "v_mov_b64_e64 v[226:227], 0\n"
    "v_pk_fma_f32 v[160:161], v[160:161], s[70:71], v[128:129]\n"
    "v_pk_fma_f32 v[162:163], v[162:163], s[70:71], v[130:131]\n"
    "v_pk_add_f32 v[230:231], v[160:161], v[162:163]\n"
    "v_pk_mul_f32 v[232:233], v[160:161], v[162:163]\n"
    "v_pk_fma_f32 v[164:165], v[164:165], s[70:71], v[132:133]\n"
    "v_pk_fma_f32 v[230:231], v[230:231], v[164:165], v[232:233]\n"
    "v_pk_mul_f32 v[232:233], v[232:233], v[164:165]\n"
    "v_pk_fma_f32 v[166:167], v[166:167], s[70:71], v[134:135]\n"
    "v_pk_fma_f32 v[230:231], v[230:231], v[166:167], v[232:233]\n"
    "v_pk_mul_f32 v[232:233], v[232:233], v[166:167]\n"
    "v_pk_fma_f32 v[168:169], v[168:169], s[70:71], v[136:137]\n"
    "v_pk_fma_f32 v[230:231], v[230:231], v[168:169], v[232:233]\n"
    "v_pk_mul_f32 v[232:233], v[232:233], v[168:169]\n"
    "v_pk_fma_f32 v[170:171], v[170:171], s[70:71], v[138:139]\n"
    "v_pk_fma_f32 v[230:231], v[230:231], v[170:171], v[232:233]\n"
    "v_pk_mul_f32 v[232:233], v[232:233], v[170:171]\n"
    "v_pk_fma_f32 v[172:173], v[172:173], s[70:71], v[140:141]\n"
    "v_pk_fma_f32 v[230:231], v[230:231], v[172:173], v[232:233]\n"
    "v_pk_mul_f32 v[232:233], v[232:233], v[172:173]\n"
    "v_pk_fma_f32 v[174:175], v[174:175], s[70:71], v[142:143]\n"
    "v_pk_fma_f32 v[230:231], v[230:231], v[174:175], v[232:233]\n"
    "v_pk_mul_f32 v[232:233], v[232:233], v[174:175]\n"
    "s_add_u32 m0, s68, 2048\n"
    "s_and_b32 s64, s60, 0xffff\n"
    "s_bfe_u32 s94, s60, 0x30013\n"
    "s_bfe_u32 s95, s60, 0x50019\n"
    "s_mul_i32 s94, s94, s56\n"
    "s_lshl4_add_u32 s83, s95, s64\n"
    "s_add_u32 s65, s58, s94\n"
    "s_add_u32 s82, s64, 64\n"
    "s_add_u32 s58, s58, s57\n"
    "s_add_u32 s59, s59, 1\n"
    "s_sub_u32 s95, s61, s76\n"
    "s_lshr_b32 s94, s61, 16\n"
    "s_sext_i32_i16 s95, s95\n"
    "s_cmp_lt_i32 s95, -400\n"
    "s_cselect_b32 s94, 0, s94\n"
    "s_mul_i32 s95, s95, s94\n"
    "s_ashr_i32 s95, s95, 16\n"
    "s_max_i32 s95, s95, -10\n"
    "s_min_i32 s95, s95, 10\n"
    "s_add_u32 s72, s77, s95\n"
    "s_cmp_lt_i32 s60, 0\n"
    "s_cselect_b32 s72, 0, s72\n"
    "v_rcp_f32 v232, v232\n"
    "v_rcp_f32 v233, v233\n"
    "v_fma_f32 v230, v230, v232, v247\n"
    "v_fmac_f32 v230, v231, v233\n"
    "s_add_u32 s66, s66, 16\n"
    "s_waitcnt lgkmcnt(0)\n"
    "ds_write_b16_d16_hi v245, v230\n"
    "ds_read_b64 v[228:229], v246\n"
    "buffer_load_dwordx4 v[236:237], s[40:43], s67 idxen offen offset:0 nt lds\n"
    "buffer_load_dwordx4 v[238:239], s[40:43], s67 idxen offen offset:1024 nt lds\n"
    "s_branch .Ldin_G1\n"
    ".p2align 12\n"
    ".Ldin_stub_G1:\n"
    "s_setpc_b64 s[34:35]\n"
    ".p2align 5\n"
    ".Ldin_G1:\n"
    "s_cbranch_vccz .Ldin_G1_np\n"
    "s_setprio 1\n"
    ".Ldin_G1_np:\n"
    "v_mfma_f32_16x16x32_bf16 v[160:163], v[0:3], v[192:195], v[144:147]\n"
    "v_exp_f32 v176, v176 clamp\n"
    "v_mfma_f32_16x16x32_bf16 v[160:163], v[4:7], v[196:199], v[160:163]\n"
    "v_exp_f32 v177, v177 clamp\n"
    "v_mfma_f32_16x16x32_bf16 v[164:167], v[8:11], v[192:195], v[148:151]\n"
    "v_exp_f32 v178, v178 clamp\n"
    "v_mfma_f32_16x16x32_bf16 v[164:167], v[12:15], v[196:199], v[164:167]\n"
    "v_exp_f32 v179, v179 clamp\n"
    "v_mfma_f32_16x16x32_bf16 v[168:171], v[16:19], v[192:195], v[152:155]\n"
    "v_exp_f32 v180, v180 clamp\n"
    "v_mfma_f32_16x16x32_bf16 v[168:171], v[20:23], v[196:199], v[168:171]\n"
    "v_exp_f32 v181, v181 clamp\n"
    "v_mfma_f32_16x16x32_bf16 v[172:175], v[24:27], v[192:195], v[156:159]\n"
    "v_exp_f32 v182, v182 clamp\n"
    "v_mfma_f32_16x16x32_bf16 v[172:175], v[28:31], v[196:199], v[172:175]\n"
    "v_exp_f32 v183, v183 clamp\n"
    "v_exp_f32 v184, v184 clamp\n"
    "s_cmp_eq_u32 s66, s80\n"
    "s_cselect_b64 s[66:67], s[64:65], s[66:67]\n"
    "s_add_u32 m0, s68, 4096\n"
    "v_exp_f32 v185, v185 clamp\n"
    "v_add_u32_e64 v251, s66, v244\n"
    "s_waitcnt lgkmcnt(0)\n"
    "v_mfma_f32_4x4x4_16b_bf16 v[224:227], v[228:229], v[200:201], v[224:227] cbsz:4 abid:0\n"
    "v_exp_f32 v186, v186 clamp\n"
    "v_exp_f32 v187, v187 clamp\n"
    "v_mfma_f32_4x4x4_16b_bf16 v[224:227], v[228:229], v[202:203], v[224:227] cbsz:4 abid:1\n"
    "v_exp_f32 v188, v188 clamp\n"
    "v_exp_f32 v189, v189 clamp\n"
    "v_mfma_f32_4x4x4_16b_bf16 v[224:227], v[228:229], v[204:205], v[224:227] cbsz:4 abid:2\n"
    "v_exp_f32 v190, v190 clamp\n"
    "v_exp_f32 v191, v191 clamp\n"
    "v_mfma_f32_4x4x4_16b_bf16 v[224:227], v[228:229], v[206:207], v[224:227] cbsz:4 abid:3\n"
    "s_waitcnt vmcnt(4)\n"
    "s_memrealtime s[34:35]\n"
    "ds_read_b64_tr_b16 v[200:201], v240 offset:4096\n"
    "ds_read_b64_tr_b16 v[202:203], v241 offset:4096\n"
    "ds_read_b64_tr_b16 v[204:205], v242 offset:4096\n"
    "ds_read_b64_tr_b16 v[206:207], v243 offset:4096\n"
    "ds_read_u8 v236, v251 offset:0\n"
    "ds_read_u8 v238, v251 offset:1\n"
    "ds_read_b128 v[192:195], v234 offset:6144\n"
    "ds_read_b128 v[196:199], v235 offset:6144\n"
    "v_pk_fma_f32 v[176:177], v[176:177], s[70:71], v[128:129]\n"
    "v_pk_fma_f32 v[178:179], v[178:179], s[70:71], v[130:131]\n"
    "v_pk_add_f32 v[230:231], v[176:177], v[178:179]\n"
    "v_pk_mul_f32 v[232:233], v[176:177], v[178:179]\n"
    "v_pk_fma_f32 v[180:181], v[180:181], s[70:71], v[132:133]\n"
    "v_pk_fma_f32 v[230:231], v[230:231], v[180:181], v[232:233]\n"
    "v_pk_mul_f32 v[232:233], v[232:233], v[180:181]\n"
    "v_pk_fma_f32 v[182:183], v[182:183], s[70:71], v[134:135]\n"
    "v_pk_fma_f32 v[230:231], v[230:231], v[182:183], v[232:233]\n"
    "v_pk_mul_f32 v[232:233], v[232:233], v[182:183]\n"
    "v_pk_fma_f32 v[184:185], v[184:185], s[70:71], v[136:137]\n"
    "v_pk_fma_f32 v[230:231], v[230:231], v[184:185], v[232:233]\n"
    "v_pk_mul_f32 v[232:233], v[232:233], v[184:185]\n"
    "v_pk_fma_f32 v[186:187], v[186:187], s[70:71], v[138:139]\n"
    "v_pk_fma_f32 v[230:231], v[230:231], v[186:187], v[232:233]\n"
    "v_pk_mul_f32 v[232:233], v[232:233], v[186:187]\n"
    "v_pk_fma_f32 v[188:189], v[188:189], s[70:71], v[140:141]\n"
    "v_pk_fma_f32 v[230:231], v[230:231], v[188:189], v[232:233]\n"
    "v_pk_mul_f32 v[232:233], v[232:233], v[188:189]\n"
    "v_pk_fma_f32 v[190:191], v[190:191], s[70:71], v[142:143]\n"
    "v_pk_fma_f32 v[230:231], v[230:231], v[190:191], v[232:233]\n"
    "v_pk_mul_f32 v[232:233], v[232:233], v[190:191]\n"
    "v_rcp_f32 v232, v232\n"
    "v_rcp_f32 v233, v233\n"
    "v_fma_f32 v230, v230, v232, v247\n"
    "v_fmac_f32 v230, v231, v233\n"
    "s_add_u32 s66, s66, 16\n"
    "s_waitcnt lgkmcnt(0)\n"
    "ds_write_b16_d16_hi v245, v230\n"
    "ds_read_b64 v[228:229], v246\n"
    "s_cmp_lt_u32 s34, s76\n"
    "s_cbranch_scc1 .Ldin_G1_wait\n"
    ".Ldin_G1_go:\n"
    "s_add_u32 s76, s76, s72\n"
    "buffer_load_dwordx4 v[236:237], s[40:43], s67 idxen offen offset:0 nt lds\n"
    "buffer_load_dwordx4 v[238:239], s[40:43], s67 idxen offen offset:1024 nt lds\n"
    "s_cmp_eq_u32 s66, s82\n"
    "s_cbranch_scc1 .Ldin_L2\n"
    ".Ldin_G2:\n"
    "v_mfma_f32_16x16x32_bf16 v[176:179], v[0:3], v[192:195], v[144:147]\n"
    "v_exp_f32 v160, v160 clamp\n"
    "v_mfma_f32_16x16x32_bf16 v[176:179], v[4:7], v[196:199], v[176:179]\n"
    "v_exp_f32 v161, v161 clamp\n"
    "v_mfma_f32_16x16x32_bf16 v[180:183], v[8:11], v[192:195], v[148:151]\n"
    "v_exp_f32 v162, v162 clamp\n"
    "v_mfma_f32_16x16x32_bf16 v[180:183], v[12:15], v[196:199], v[180:183]\n"
    "v_exp_f32 v163, v163 clamp\n"
    "v_mfma_f32_16x16x32_bf16 v[184:187], v[16:19], v[192:195], v[152:155]\n"
    "v_exp_f32 v164, v164 clamp\n"
    "v_mfma_f32_16x16x32_bf16 v[184:187], v[20:23], v[196:199], v[184:187]\n"
    "v_exp_f32 v165, v165 clamp\n"
    "v_mfma_f32_16x16x32_bf16 v[188:191], v[24:27], v[192:195], v[156:159]\n"
    "v_exp_f32 v166, v166 clamp\n"
    "v_mfma_f32_16x16x32_bf16 v[188:191], v[28:31], v[196:199], v[188:191]\n"
    "v_exp_f32 v167, v167 clamp\n"
    "v_exp_f32 v168, v168 clamp\n"
    "s_cmp_eq_u32 s66, s80\n"
    "s_cselect_b64 s[66:67], s[64:65], s[66:67]\n"
    "s_add_u32 m0, s68, 6144\n"
    "v_exp_f32 v169, v169 clamp\n"
    "v_add_u32_e64 v251, s66, v244\n"
    "s_waitcnt lgkmcnt(0)\n"
    "v_mfma_f32_4x4x4_16b_bf16 v[224:227], v[228:229], v[208:209], v[224:227] cbsz:4 abid:0\n"
    "v_exp_f32 v170, v170 clamp\n"
    "v_exp_f32 v171, v171 clamp\n"
    "v_mfma_f32_4x4x4_16b_bf16 v[224:227], v[228:229], v[210:211], v[224:227] cbsz:4 abid:1\n"
    "v_exp_f32 v172, v172 clamp\n"
    "v_exp_f32 v173, v173 clamp\n"
    "v_mfma_f32_4x4x4_16b_bf16 v[224:227], v[228:229], v[212:213], v[224:227] cbsz:4 abid:2\n"
    "v_exp_f32 v174, v174 clamp\n"
    "v_exp_f32 v175, v175 clamp\n"
    "v_mfma_f32_4x4x4_16b_bf16 v[224:227], v[228:229], v[214:215], v[224:227] cbsz:4 abid:3\n"
    "s_waitcnt vmcnt(4)\n"
    "s_memrealtime s[34:35]\n"
    "ds_read_b64_tr_b16 v[208:209], v240 offset:6144\n"
    "ds_read_b64_tr_b16 v[210:211], v241 offset:6144\n"
    "ds_read_b64_tr_b16 v[212:213], v242 offset:6144\n"
    "ds_read_b64_tr_b16 v[214:215], v243 offset:6144\n"
    "ds_read_u8 v236, v251 offset:0\n"
    "ds_read_u8 v238, v251 offset:1\n"
    "ds_read_b128 v[192:195], v234 offset:0\n"
    "ds_read_b128 v[196:199], v235 offset:0\n"
    "v_pk_fma_f32 v[160:161], v[160:161], s[70:71], v[128:129]\n"
    "v_pk_fma_f32 v[162:163], v[162:163], s[70:71], v[130:131]\n"
    "v_pk_add_f32 v[230:231], v[160:161], v[162:163]\n"
    "v_pk_mul_f32 v[232:233], v[160:161], v[162:163]\n"
    "v_pk_fma_f32 v[164:165], v[164:165], s[70:71], v[132:133]\n"
    "v_pk_fma_f32 v[230:231], v[230:231], v[164:165], v[232:233]\n"
    "v_pk_mul_f32 v[232:233], v[232:233], v[164:165]\n"
    "v_pk_fma_f32 v[166:167], v[166:167], s[70:71], v[134:135]\n"
    "v_pk_fma_f32 v[230:231], v[230:231], v[166:167], v[232:233]\n"
    "v_pk_mul_f32 v[232:233], v[232:233], v[166:167]\n"
    "v_pk_fma_f32 v[168:169], v[168:169], s[70:71], v[136:137]\n"
    "v_pk_fma_f32 v[230:231], v[230:231], v[168:169], v[232:233]\n"
    "v_pk_mul_f32 v[232:233], v[232:233], v[168:169]\n"
    "v_pk_fma_f32 v[170:171], v[170:171], s[70:71], v[138:139]\n"
    "v_pk_fma_f32 v[230:231], v[230:231], v[170:171], v[232:233]\n"
    "v_pk_mul_f32 v[232:233], v[232:233], v[170:171]\n"
    "v_pk_fma_f32 v[172:173], v[172:173], s[70:71], v[140:141]\n"
    "v_pk_fma_f32 v[230:231], v[230:231], v[172:173], v[232:233]\n"
    "v_pk_mul_f32 v[232:233], v[232:233], v[172:173]\n"
    "v_pk_fma_f32 v[174:175], v[174:175], s[70:71], v[142:143]\n"
    "v_pk_fma_f32 v[230:231], v[230:231], v[174:175], v[232:233]\n"
    "v_pk_mul_f32 v[232:233], v[232:233], v[174:175]\n"
    "v_rcp_f32 v232, v232\n"
    "v_rcp_f32 v233, v233\n"
    "v_fma_f32 v230, v230, v232, v247\n"
    "v_fmac_f32 v230, v231, v233\n"
    "s_add_u32 s66, s66, 16\n"
    "s_waitcnt lgkmcnt(0)\n"
    "ds_write_b16_d16_hi v245, v230\n"
    "ds_read_b64 v[228:229], v246\n"
    "s_cmp_lt_u32 s34, s76\n"
    "s_cbranch_scc1 .Ldin_G2_wait\n"
    ".Ldin_G2_go:\n"
    "s_sub_u32 s94, s34, 30\n"
    "s_max_u32 s76, s76, s94\n"
    "s_add_u32 s76, s76, s72\n"
    "buffer_load_dwordx4 v[236:237], s[40:43], s67 idxen offen offset:0 nt lds\n"
    "buffer_load_dwordx4 v[238:239], s[40:43], s67 idxen offen offset:1024 nt lds\n"
    "s_cmp_eq_u32 s66, s82\n"
    "s_cbranch_scc1 .Ldin_L3\n"
    ".Ldin_G3:\n"
    "v_mfma_f32_16x16x32_bf16 v[160:163], v[0:3], v[192:195], v[144:147]\n"
    "v_exp_f32 v176, v176 clamp\n"
    "v_mfma_f32_16x16x32_bf16 v[160:163], v[4:7], v[196:199], v[160:163]\n"
    "v_exp_f32 v177, v177 clamp\n"
    "v_mfma_f32_16x16x32_bf16 v[164:167], v[8:11], v[192:195], v[148:151]\n"
    "v_exp_f32 v178, v178 clamp\n"
    "v_mfma_f32_16x16x32_bf16 v[164:167], v[12:15], v[196:199], v[164:167]\n"
    "v_exp_f32 v179, v179 clamp\n"
    "v_mfma_f32_16x16x32_bf16 v[168:171], v[16:19], v[192:195], v[152:155]\n"
    "v_exp_f32 v180, v180 clamp\n"
    "v_mfma_f32_16x16x32_bf16 v[168:171], v[20:23], v[196:199], v[168:171]\n"
    "v_exp_f32 v181, v181 clamp\n"
    "v_mfma_f32_16x16x32_bf16 v[172:175], v[24:27], v[192:195], v[156:159]\n"
    "v_exp_f32 v182, v182 clamp\n"
    "v_mfma_f32_16x16x32_bf16 v[172:175], v[28:31], v[196:199], v[172:175]\n"
    "v_exp_f32 v183, v183 clamp\n"
    "v_exp_f32 v184, v184 clamp\n"
    "s_cmp_eq_u32 s66, s80\n"
    "s_cselect_b64 s[66:67], s[64:65], s[66:67]\n"
    "s_add_u32 m0, s68, 0\n"
    "v_exp_f32 v185, v185 clamp\n"
    "v_add_u32 v251, s66, v244\n"
    "s_waitcnt lgkmcnt(0)\n"
    "v_mfma_f32_4x4x4_16b_bf16 v[224:227], v[228:229], v[200:201], v[224:227] cbsz:4 abid:0\n"
    "v_exp_f32 v186, v186 clamp\n"
    "v_exp_f32 v187, v187 clamp\n"
    "v_mfma_f32_4x4x4_16b_bf16 v[224:227], v[228:229], v[202:203], v[224:227] cbsz:4 abid:1\n"
    "v_exp_f32 v188, v188 clamp\n"
    "v_exp_f32 v189, v189 clamp\n"
    "v_mfma_f32_4x4x4_16b_bf16 v[224:227], v[228:229], v[204:205], v[224:227] cbsz:4 abid:2\n"
    "v_exp_f32 v190, v190 clamp\n"
    "v_exp_f32 v191, v191 clamp\n"
    "v_mfma_f32_4x4x4_16b_bf16 v[224:227], v[228:229], v[206:207], v[224:227] cbsz:4 abid:3\n"
    "s_waitcnt vmcnt(4)\n"
    "s_memrealtime s[34:35]\n"
    "ds_read_b64_tr_b16 v[200:201], v240 offset:0\n"
    "ds_read_b64_tr_b16 v[202:203], v241 offset:0\n"
    "ds_read_b64_tr_b16 v[204:205], v242 offset:0\n"
    "ds_read_b64_tr_b16 v[206:207], v243 offset:0\n"
    "ds_read_u8 v236, v251 offset:0\n"
    "ds_read_u8 v238, v251 offset:1\n"
    "ds_read_b128 v[192:195], v234 offset:2048\n"
    "ds_read_b128 v[196:199], v235 offset:2048\n"
    "v_pk_fma_f32 v[176:177], v[176:177], s[70:71], v[128:129]\n"
    "v_pk_fma_f32 v[178:179], v[178:179], s[70:71], v[130:131]\n"
    "v_pk_add_f32 v[230:231], v[176:177], v[178:179]\n"
    "v_pk_mul_f32 v[232:233], v[176:177], v[178:179]\n"
    "v_pk_fma_f32 v[180:181], v[180:181], s[70:71], v[132:133]\n"
    "v_pk_fma_f32 v[230:231], v[230:231], v[180:181], v[232:233]\n"
    "v_pk_mul_f32 v[232:233], v[232:233], v[180:181]\n"
    "v_pk_fma_f32 v[182:183], v[182:183], s[70:71], v[134:135]\n"
    "v_pk_fma_f32 v[230:231], v[230:231], v[182:183], v[232:233]\n"
    "v_pk_mul_f32 v[232:233], v[232:233], v[182:183]\n"
    "v_pk_fma_f32 v[184:185], v[184:185], s[70:71], v[136:137]\n"
    "v_pk_fma_f32 v[230:231], v[230:231], v[184:185], v[232:233]\n"
    "v_pk_mul_f32 v[232:233], v[232:233], v[184:185]\n"
    "v_pk_fma_f32 v[186:187], v[186:187], s[70:71], v[138:139]\n"
    "v_pk_fma_f32 v[230:231], v[230:231], v[186:187], v[232:233]\n"
    "v_pk_mul_f32 v[232:233], v[232:233], v[186:187]\n"
    "v_pk_fma_f32 v[188:189], v[188:189], s[70:71], v[140:141]\n"
    "v_pk_fma_f32 v[230:231], v[230:231], v[188:189], v[232:233]\n"
    "v_pk_mul_f32 v[232:233], v[232:233], v[188:189]\n"
    "v_pk_fma_f32 v[190:191], v[190:191], s[70:71], v[142:143]\n"
    "v_pk_fma_f32 v[230:231], v[230:231], v[190:191], v[232:233]\n"
    "v_pk_mul_f32 v[232:233], v[232:233], v[190:191]\n"
    "v_rcp_f32 v232, v232\n"
    "v_rcp_f32 v233, v233\n"
    "v_fma_f32 v230, v230, v232, v247\n"
    "v_fmac_f32 v230, v231, v233\n"
    "s_add_u32 s66, s66, 16\n"
    "s_waitcnt lgkmcnt(0)\n"
    "ds_write_b16_d16_hi v245, v230\n"
    "ds_read_b64 v[228:229], v246\n"
    "s_cmp_lt_u32 s34, s76\n"
    "s_cbranch_scc1 .Ldin_G3_wait\n"
    ".Ldin_G3_go:\n"
    "s_add_u32 s76, s76, s72\n"
    "buffer_load_dwordx4 v[236:237], s[40:43], s67 idxen offen offset:0 nt lds\n"
    "buffer_load_dwordx4 v[238:239], s[40:43], s67 idxen offen offset:1024 nt lds\n"
    "s_cmp_eq_u32 s66, s82\n"
    "s_cbranch_scc1 .Ldin_L0\n"
    ".Ldin_G0:\n"
    "s_setprio 0\n"
    "v_mfma_f32_16x16x32_bf16 v[176:179], v[0:3], v[192:195], v[144:147]\n"
    "v_exp_f32 v160, v160 clamp\n"
    "v_mfma_f32_16x16x32_bf16 v[176:179], v[4:7], v[196:199], v[176:179]\n"
    "v_exp_f32 v161, v161 clamp\n"
    "v_mfma_f32_16x16x32_bf16 v[180:183], v[8:11], v[192:195], v[148:151]\n"
    "v_exp_f32 v162, v162 clamp\n"
    "v_mfma_f32_16x16x32_bf16 v[180:183], v[12:15], v[196:199], v[180:183]\n"
    "v_exp_f32 v163, v163 clamp\n"
    "v_mfma_f32_16x16x32_bf16 v[184:187], v[16:19], v[192:195], v[152:155]\n"
    "v_exp_f32 v164, v164 clamp\n"
    "v_mfma_f32_16x16x32_bf16 v[184:187], v[20:23], v[196:199], v[184:187]\n"
    "v_exp_f32 v165, v165 clamp\n"
    "v_mfma_f32_16x16x32_bf16 v[188:191], v[24:27], v[192:195], v[156:159]\n"
    "v_exp_f32 v166, v166 clamp\n"
    "v_mfma_f32_16x16x32_bf16 v[188:191], v[28:31], v[196:199], v[188:191]\n"
    "v_exp_f32 v167, v167 clamp\n"
    "v_exp_f32 v168, v168 clamp\n"
    "s_cmp_eq_u32 s66, s80\n"
    "s_cselect_b64 s[66:67], s[64:65], s[66:67]\n"
    "s_add_u32 m0, s68, 2048\n"
    "v_exp_f32 v169, v169 clamp\n"
    "v_add_u32 v251, s66, v244\n"
    "s_waitcnt lgkmcnt(0)\n"
    "v_mfma_f32_4x4x4_16b_bf16 v[224:227], v[228:229], v[208:209], v[224:227] cbsz:4 abid:0\n"
    "v_exp_f32 v170, v170 clamp\n"
    "v_exp_f32 v171, v171 clamp\n"
    "v_mfma_f32_4x4x4_16b_bf16 v[224:227], v[228:229], v[210:211], v[224:227] cbsz:4 abid:1\n"
    "v_exp_f32 v172, v172 clamp\n"
    "v_exp_f32 v173, v173 clamp\n"
    "v_mfma_f32_4x4x4_16b_bf16 v[224:227], v[228:229], v[212:213], v[224:227] cbsz:4 abid:2\n"
    "v_exp_f32 v174, v174 clamp\n"
    "v_exp_f32 v175, v175 clamp\n"
    "v_mfma_f32_4x4x4_16b_bf16 v[224:227], v[228:229], v[214:215], v[224:227] cbsz:4 abid:3\n"
    "s_waitcnt vmcnt(4)\n"
    "s_memrealtime s[34:35]\n"
    "ds_read_b64_tr_b16 v[208:209], v240 offset:2048\n"
    "ds_read_b64_tr_b16 v[210:211], v241 offset:2048\n"
    "ds_read_b64_tr_b16 v[212:213], v242 offset:2048\n"
    "ds_read_b64_tr_b16 v[214:215], v243 offset:2048\n"
    "ds_read_u8 v236, v251 offset:0\n"
    "ds_read_u8 v238, v251 offset:1\n"
    "ds_read_b128 v[192:195], v234 offset:4096\n"
    "ds_read_b128 v[196:199], v235 offset:4096\n"
    "v_pk_fma_f32 v[160:161], v[160:161], s[70:71], v[128:129]\n"
    "v_pk_fma_f32 v[162:163], v[162:163], s[70:71], v[130:131]\n"
    "v_pk_add_f32 v[230:231], v[160:161], v[162:163]\n"
    "v_pk_mul_f32 v[232:233], v[160:161], v[162:163]\n"
    "v_pk_fma_f32 v[164:165], v[164:165], s[70:71], v[132:133]\n"
    "v_pk_fma_f32 v[230:231], v[230:231], v[164:165], v[232:233]\n"
    "v_pk_mul_f32 v[232:233], v[232:233], v[164:165]\n"
    "v_pk_fma_f32 v[166:167], v[166:167], s[70:71], v[134:135]\n"
    "v_pk_fma_f32 v[230:231], v[230:231], v[166:167], v[232:233]\n"
    "v_pk_mul_f32 v[232:233], v[232:233], v[166:167]\n"
    "v_pk_fma_f32 v[168:169], v[168:169], s[70:71], v[136:137]\n"
    "v_pk_fma_f32 v[230:231], v[230:231], v[168:169], v[232:233]\n"
    "v_pk_mul_f32 v[232:233], v[232:233], v[168:169]\n"
    "v_pk_fma_f32 v[170:171], v[170:171], s[70:71], v[138:139]\n"
    "v_pk_fma_f32 v[230:231], v[230:231], v[170:171], v[232:233]\n"
    "v_pk_mul_f32 v[232:233], v[232:233], v[170:171]\n"
    "v_pk_fma_f32 v[172:173], v[172:173], s[70:71], v[140:141]\n"
    "v_pk_fma_f32 v[230:231], v[230:231], v[172:173], v[232:233]\n"
    "v_pk_mul_f32 v[232:233], v[232:233], v[172:173]\n"
    "v_pk_fma_f32 v[174:175], v[174:175], s[70:71], v[142:143]\n"
    "v_pk_fma_f32 v[230:231], v[230:231], v[174:175], v[232:233]\n"
    "v_pk_mul_f32 v[232:233], v[232:233], v[174:175]\n"
    "v_rcp_f32 v232, v232\n"
    "v_rcp_f32 v233, v233\n"
    "v_fma_f32 v230, v230, v232, v247\n"
    "v_fmac_f32 v230, v231, v233\n"
    "s_add_u32 s66, s66, 16\n"
    "s_waitcnt lgkmcnt(0)\n"
    "ds_write_b16_d16_hi v245, v230\n"
    "ds_read_b64 v[228:229], v246\n"
    "s_cmp_lt_u32 s34, s76\n"
    "s_cbranch_scc1 .Ldin_G0_wait\n"
    ".Ldin_G0_go:\n"
    "s_sub_u32 s94, s34, 30\n"
    "s_max_u32 s76, s76, s94\n"
    "s_add_u32 s76, s76, s72\n"
    "buffer_load_dwordx4 v[236:237], s[40:43], s67 idxen offen offset:0 nt lds\n"
    "buffer_load_dwordx4 v[238:239], s[40:43], s67 idxen offen offset:1024 nt lds\n"
    "s_cmp_eq_u32 s66, s82\n"
    "s_cbranch_scc1 .Ldin_L1\n"
    "s_branch .Ldin_G1\n"
    ".Ldin_stub_A0_poll:\n"
    "s_setpc_b64 s[34:35]\n"
    ".Ldin_A0_poll:\n"
    "s_sleep 1\n"
    "ds_read_b64 v[216:217], v246 offset:128\n"
    "s_waitcnt lgkmcnt(0)\n"
    "v_readlane_b32 s60, v216, s59\n"
    "v_readlane_b32 s61, v217, s59\n"
    "s_cmp_eq_u32 s60, -1\n"
    "s_cbranch_scc1 .Ldin_A0_poll\n"
    "s_branch .Ldin_A0_pr\n"
    ".Ldin_G1_slp:\n"
    "s_sleep 1\n"
    ".Ldin_G1_wait:\n"
    "s_memrealtime s[34:35]\n"
    "s_waitcnt lgkmcnt(0)\n"
    "s_sub_u32 s94, s34, s76\n"
    "s_cmp_lt_i32 s94, 0\n"
    "s_cbranch_scc1 .Ldin_G1_slp\n"
    "s_branch .Ldin_G1_go\n"
    ".Ldin_G2_slp:\n"
    "s_sleep 1\n"
    ".Ldin_G2_wait:\n"
    "s_memrealtime s[34:35]\n"
    "s_waitcnt lgkmcnt(0)\n"
    "s_sub_u32 s94, s34, s76\n"
    "s_cmp_lt_i32 s94, 0\n"
    "s_cbranch_scc1 .Ldin_G2_slp\n"
    "s_branch .Ldin_G2_go\n"
    ".Ldin_G3_slp:\n"
    "s_sleep 1\n"
    ".Ldin_G3_wait:\n"
    "s_memrealtime s[34:35]\n"
    "s_waitcnt lgkmcnt(0)\n"
    "s_sub_u32 s94, s34, s76\n"
    "s_cmp_lt_i32 s94, 0\n"
    "s_cbranch_scc1 .Ldin_G3_slp\n"
    "s_branch .Ldin_G3_go\n"
    ".Ldin_G0_slp:\n"
    "s_sleep 1\n"
    ".Ldin_G0_wait:\n"
    "s_memrealtime s[34:35]\n"
    "s_waitcnt lgkmcnt(0)\n"
    "s_sub_u32 s94, s34, s76\n"
    "s_cmp_lt_i32 s94, 0\n"
    "s_cbranch_scc1 .Ldin_G0_slp\n"
    "s_branch .Ldin_G0_go\n"
    ".Ldin_stub_E0:\n"
    "s_setpc_b64 s[34:35]\n"
    ".p2align 5\n"
    ".Ldin_E0:\n"
    "v_exp_f32 v160, v160 clamp\n"
    "v_exp_f32 v161, v161 clamp\n"
    "v_exp_f32 v162, v162 clamp\n"
    "v_exp_f32 v163, v163 clamp\n"
    "v_exp_f32 v164, v164 clamp\n"
    "v_exp_f32 v165, v165 clamp\n"
    "v_exp_f32 v166, v166 clamp\n"
    "v_exp_f32 v167, v167 clamp\n"
    "s_add_u32 m0, s62, 256\n"
    "s_waitcnt lgkmcnt(0)\n"
    "v_mfma_f32_4x4x4_16b_bf16 v[224:227], v[228:229], v[208:209], v[224:227] cbsz:4 abid:0\n"
    "v_exp_f32 v168, v168 clamp\n"
    "v_exp_f32 v169, v169 clamp\n"
    "v_mfma_f32_4x4x4_16b_bf16 v[224:227], v[228:229], v[210:211], v[224:227] cbsz:4 abid:1\n"
    "v_exp_f32 v170, v170 clamp\n"
    "v_exp_f32 v171, v171 clamp\n"
    "v_mfma_f32_4x4x4_16b_bf16 v[224:227], v[228:229], v[212:213], v[224:227] cbsz:4 abid:2\n"
    "v_exp_f32 v172, v172 clamp\n"
    "v_exp_f32 v173, v173 clamp\n"
    "v_mfma_f32_4x4x4_16b_bf16 v[224:227], v[228:229], v[214:215], v[224:227] cbsz:4 abid:3\n"
    "v_exp_f32 v174, v174 clamp\n"
    "v_exp_f32 v175, v175 clamp\n"
    "s_nop 0\n"
    "v_pk_fma_f32 v[160:161], v[160:161], s[70:71], v[128:129]\n"
    "v_pk_fma_f32 v[162:163], v[162:163], s[70:71], v[130:131]\n"
    "v_pk_add_f32 v[230:231], v[160:161], v[162:163]\n"
    "v_pk_mul_f32 v[232:233], v[160:161], v[162:163]\n"
    "v_pk_fma_f32 v[164:165], v[164:165], s[70:71], v[132:133]\n"
    "v_pk_fma_f32 v[230:231], v[230:231], v[164:165], v[232:233]\n"
    "v_pk_mul_f32 v[232:233], v[232:233], v[164:165]\n"
    "v_pk_fma_f32 v[166:167], v[166:167], s[70:71], v[134:135]\n"
    "v_pk_fma_f32 v[230:231], v[230:231], v[166:167], v[232:233]\n"
    "v_pk_mul_f32 v[232:233], v[232:233], v[166:167]\n"
    "v_pk_fma_f32 v[168:169], v[168:169], s[70:71], v[136:137]\n"
    "v_pk_fma_f32 v[230:231], v[230:231], v[168:169], v[232:233]\n"
    "v_pk_mul_f32 v[232:233], v[232:233], v[168:169]\n"
    "v_pk_fma_f32 v[170:171], v[170:171], s[70:71], v[138:139]\n"
    "v_pk_fma_f32 v[230:231], v[230:231], v[170:171], v[232:233]\n"
    "v_pk_mul_f32 v[232:233], v[232:233], v[170:171]\n"
    "v_pk_fma_f32 v[172:173], v[172:173], s[70:71], v[140:141]\n"
    "v_pk_fma_f32 v[230:231], v[230:231], v[172:173], v[232:233]\n"
    "v_pk_mul_f32 v[232:233], v[232:233], v[172:173]\n"
    "v_pk_fma_f32 v[174:175], v[174:175], s[70:71], v[142:143]\n"
    "v_pk_fma_f32 v[230:231], v[230:231], v[174:175], v[232:233]\n"
    "v_pk_mul_f32 v[232:233], v[232:233], v[174:175]\n"
    "v_rcp_f32 v232, v232\n"
    "v_rcp_f32 v233, v233\n"
    "v_fma_f32 v230, v230, v232, v247\n"
    "v_fmac_f32 v230, v231, v233\n"
    "ds_write_b16_d16_hi v245, v230\n"
    "ds_read_b64 v[228:229], v246\n"
    ".Ldin_drain0:\n"
    "v_mbcnt_lo_u32_b32 v0, -1, 0\n"
    "v_mbcnt_hi_u32_b32 v0, -1, v0\n"
    "v_lshrrev_b32 v1, 3, v0\n"
    "v_lshlrev_b32 v2, 2, v1\n"
    "ds_read_b32 v3, v246 offset:128\n"
    "s_lshl_b32 s94, s59, 8\n"
    "s_sub_u32 s94, s62, s94\n"
    "s_waitcnt lgkmcnt(0)\n"
    "ds_bpermute_b32 v3, v2, v3\n"
    "v_mfma_f32_4x4x4_16b_bf16 v[224:227], v[228:229], v[200:201], v[224:227] cbsz:4 abid:0\n"
    "v_lshlrev_b32 v2, 5, v0\n"
    "v_mul_lo_u32 v1, v1, s79\n"
    "v_mfma_f32_4x4x4_16b_bf16 v[224:227], v[228:229], v[202:203], v[224:227] cbsz:4 abid:1\n"
    "v_add_u32 v2, s94, v2\n"
    "v_and_b32 v0, 7, v0\n"
    "v_mfma_f32_4x4x4_16b_bf16 v[224:227], v[228:229], v[204:205], v[224:227] cbsz:4 abid:2\n"
    "v_add_u32 v1, s78, v1\n"
    "v_lshlrev_b32 v0, 4, v0\n"
    "v_mfma_f32_4x4x4_16b_bf16 v[224:227], v[228:229], v[206:207], v[224:227] cbsz:4 abid:3\n"
    "s_waitcnt lgkmcnt(0)\n"
    "v_bfe_u32 v4, v3, 19, 3\n"
    "v_cmp_le_i32 vcc, 0, v3\n"
    "v_add_u32 v1, v1, v4\n"
    "s_mov_b32 s70, s69\n"
    "s_mov_b32 s71, s69\n"
    "v_pk_add_f32 v[232:233], v[224:225], v[226:227]\n"
    "v_add_f32 v232, v232, v233\n"
    "ds_write_addtid_b32 v232 offset:49152\n"
    "ds_read_b128 v[8:11], v2 offset:49920\n"
    "ds_read_b128 v[12:15], v2 offset:49936\n"
    "v_lshl_add_u32 v1, v1, 7, v0\n"
    "v_bfrev_b32 v4, 1\n"
    "v_cndmask_b32 v1, v4, v1, vcc\n"
    "s_waitcnt vmcnt(0) lgkmcnt(0)\n"
    "v_pk_mul_f32 v[8:9], v[8:9], s[70:71]\n"
    "v_pk_mul_f32 v[10:11], v[10:11], s[70:71]\n"
    "v_pk_mul_f32 v[12:13], v[12:13], s[70:71]\n"
    "v_pk_mul_f32 v[14:15], v[14:15], s[70:71]\n"
    "v_cvt_pk_bf16_f32 v16, v8, v9\n"
    "v_cvt_pk_bf16_f32 v17, v10, v11\n"
    "v_cvt_pk_bf16_f32 v18, v12, v13\n"
    "v_cvt_pk_bf16_f32 v19, v14, v15\n"
    "buffer_store_dwordx4 v[16:19], v1, s[52:55], 0 offen sc1\n"
    "s_endpgm\n"
    ".Ldin_stub_E1:\n"
    "s_setpc_b64 s[34:35]\n"
    ".p2align 5\n"
    ".Ldin_E1:\n"
    "v_exp_f32 v176, v176 clamp\n"
    "v_exp_f32 v177, v177 clamp\n"
    "v_exp_f32 v178, v178 clamp\n"
    "v_exp_f32 v179, v179 clamp\n"
    "v_exp_f32 v180, v180 clamp\n"
    "v_exp_f32 v181, v181 clamp\n"
    "v_exp_f32 v182, v182 clamp\n"
    "v_exp_f32 v183, v183 clamp\n"
    "s_add_u32 m0, s62, 256\n"
    "s_waitcnt lgkmcnt(0)\n"
    "v_mfma_f32_4x4x4_16b_bf16 v[224:227], v[228:229], v[200:201], v[224:227] cbsz:4 abid:0\n"
    "v_exp_f32 v184, v184 clamp\n"
    "v_exp_f32 v185, v185 clamp\n"
    "v_mfma_f32_4x4x4_16b_bf16 v[224:227], v[228:229], v[202:203], v[224:227] cbsz:4 abid:1\n"
    "v_exp_f32 v186, v186 clamp\n"
    "v_exp_f32 v187, v187 clamp\n"
    "v_mfma_f32_4x4x4_16b_bf16 v[224:227], v[228:229], v[204:205], v[224:227] cbsz:4 abid:2\n"
    "v_exp_f32 v188, v188 clamp\n"
    "v_exp_f32 v189, v189 clamp\n"
    "v_mfma_f32_4x4x4_16b_bf16 v[224:227], v[228:229], v[206:207], v[224:227] cbsz:4 abid:3\n"
    "v_exp_f32 v190, v190 clamp\n"
    "v_exp_f32 v191, v191 clamp\n"
    "s_nop 0\n"
    "v_pk_fma_f32 v[176:177], v[176:177], s[70:71], v[128:129]\n"
    "v_pk_fma_f32 v[178:179], v[178:179], s[70:71], v[130:131]\n"
    "v_pk_add_f32 v[230:231], v[176:177], v[178:179]\n"
    "v_pk_mul_f32 v[232:233], v[176:177], v[178:179]\n"
    "v_pk_fma_f32 v[180:181], v[180:181], s[70:71], v[132:133]\n"
    "v_pk_fma_f32 v[230:231], v[230:231], v[180:181], v[232:233]\n"
    "v_pk_mul_f32 v[232:233], v[232:233], v[180:181]\n"
    "v_pk_fma_f32 v[182:183], v[182:183], s[70:71], v[134:135]\n"
    "v_pk_fma_f32 v[230:231], v[230:231], v[182:183], v[232:233]\n"
    "v_pk_mul_f32 v[232:233], v[232:233], v[182:183]\n"
    "v_pk_fma_f32 v[184:185], v[184:185], s[70:71], v[136:137]\n"
    "v_pk_fma_f32 v[230:231], v[230:231], v[184:185], v[232:233]\n"
    "v_pk_mul_f32 v[232:233], v[232:233], v[184:185]\n"
    "v_pk_fma_f32 v[186:187], v[186:187], s[70:71], v[138:139]\n"
    "v_pk_fma_f32 v[230:231], v[230:231], v[186:187], v[232:233]\n"
    "v_pk_mul_f32 v[232:233], v[232:233], v[186:187]\n"
    "v_pk_fma_f32 v[188:189], v[188:189], s[70:71], v[140:141]\n"
    "v_pk_fma_f32 v[230:231], v[230:231], v[188:189], v[232:233]\n"
    "v_pk_mul_f32 v[232:233], v[232:233], v[188:189]\n"
    "v_pk_fma_f32 v[190:191], v[190:191], s[70:71], v[142:143]\n"
    "v_pk_fma_f32 v[230:231], v[230:231], v[190:191], v[232:233]\n"
    "v_pk_mul_f32 v[232:233], v[232:233], v[190:191]\n"
    "v_rcp_f32 v232, v232\n"
    "v_rcp_f32 v233, v233\n"
    "v_fma_f32 v230, v230, v232, v247\n"
    "v_fmac_f32 v230, v231, v233\n"
    "ds_write_b16_d16_hi v245, v230\n"
    "ds_read_b64 v[228:229], v246\n"
    ".Ldin_drain1:\n"
    "v_mbcnt_lo_u32_b32 v0, -1, 0\n"
    "v_mbcnt_hi_u32_b32 v0, -1, v0\n"
    "v_lshrrev_b32 v1, 3, v0\n"
    "v_lshlrev_b32 v2, 2, v1\n"
    "ds_read_b32 v3, v246 offset:128\n"
    "s_lshl_b32 s94, s59, 8\n"
    "s_sub_u32 s94, s62, s94\n"
    "s_waitcnt lgkmcnt(0)\n"
    "ds_bpermute_b32 v3, v2, v3\n"
    "v_mfma_f32_4x4x4_16b_bf16 v[224:227], v[228:229], v[208:209], v[224:227] cbsz:4 abid:0\n"
    "v_lshlrev_b32 v2, 5, v0\n"
    "v_mul_lo_u32 v1, v1, s79\n"
    "v_mfma_f32_4x4x4_16b_bf16 v[224:227], v[228:229], v[210:211], v[224:227] cbsz:4 abid:1\n"
    "v_add_u32 v2, s94, v2\n"
    "v_and_b32 v0, 7, v0\n"
    "v_mfma_f32_4x4x4_16b_bf16 v[224:227], v[228:229], v[212:213], v[224:227] cbsz:4 abid:2\n"
    "v_add_u32 v1, s78, v1\n"
    "v_lshlrev_b32 v0, 4, v0\n"
    "v_mfma_f32_4x4x4_16b_bf16 v[224:227], v[228:229], v[214:215], v[224:227] cbsz:4 abid:3\n"
    "s_waitcnt lgkmcnt(0)\n"
    "v_bfe_u32 v4, v3, 19, 3\n"
    "v_cmp_le_i32 vcc, 0, v3\n"
    "v_add_u32 v1, v1, v4\n"
    "s_mov_b32 s70, s69\n"
    "s_mov_b32 s71, s69\n"
    "v_pk_add_f32 v[232:233], v[224:225], v[226:227]\n"
    "v_add_f32 v232, v232, v233\n"
    "ds_write_addtid_b32 v232 offset:49152\n"
    "ds_read_b128 v[8:11], v2 offset:49920\n"
    "ds_read_b128 v[12:15], v2 offset:49936\n"
    "v_lshl_add_u32 v1, v1, 7, v0\n"
    "v_bfrev_b32 v4, 1\n"
    "v_cndmask_b32 v1, v4, v1, vcc\n"
    "s_waitcnt vmcnt(0) lgkmcnt(0)\n"
    "v_pk_mul_f32 v[8:9], v[8:9], s[70:71]\n"
    "v_pk_mul_f32 v[10:11], v[10:11], s[70:71]\n"
    "v_pk_mul_f32 v[12:13], v[12:13], s[70:71]\n"
    "v_pk_mul_f32 v[14:15], v[14:15], s[70:71]\n"
    "v_cvt_pk_bf16_f32 v16, v8, v9\n"
    "v_cvt_pk_bf16_f32 v17, v10, v11\n"
    "v_cvt_pk_bf16_f32 v18, v12, v13\n"
    "v_cvt_pk_bf16_f32 v19, v14, v15\n"
    "buffer_store_dwordx4 v[16:19], v1, s[52:55], 0 offen sc1\n"
    "s_endpgm\n"
    ".p2align 12\n"
    ".Ldin_stub_L2:\n"
    "s_setpc_b64 s[34:35]\n"
    ".p2align 5\n"
    ".Ldin_L2:\n"
    "s_setprio 0\n"
    "s_cmp_lt_i32 s60, 0\n"
    "s_cbranch_scc1 .Ldin_E0\n"
    "ds_read_b128 v[144:147], v252 offset:8192\n"
    "ds_read_b128 v[148:151], v252 offset:8256\n"
    "ds_read_b128 v[152:155], v252 offset:16384\n"
    "ds_read_b128 v[156:159], v252 offset:16448\n"
    "s_add_u32 s76, s76, s73\n"
    "s_add_u32 m0, s68, 6144\n"
    "v_and_b32 v216, v248, v216\n"
    "v_and_b32 v217, v249, v217\n"
    "v_and_b32 v218, v248, v218\n"
    "v_and_b32 v219, v249, v219\n"
    "v_mfma_f32_16x16x16_bf16 v[176:179], v[216:217], v[96:97], v[32:35]\n"
    "v_and_b32 v220, v248, v220\n"
    "v_and_b32 v221, v249, v221\n"
    "v_and_b32 v222, v248, v222\n"
    "v_mfma_f32_16x16x16_bf16 v[180:183], v[218:219], v[98:99], v[36:39]\n"
    "v_exp_f32 v160, v160 clamp\n"
    "v_and_b32 v223, v249, v223\n"
    "v_mfma_f32_16x16x16_bf16 v[184:187], v[220:221], v[100:101], v[40:43]\n"
    "v_add_u32 v251, s66, v244\n"
    "v_cvt_pk_bf16_f32 v0, v176, v177\n"
    "v_cvt_pk_bf16_f32 v1, v178, v179\n"
    "s_waitcnt lgkmcnt(4)\n"
    "v_mfma_f32_16x16x16_bf16 v[188:191], v[222:223], v[102:103], v[44:47]\n"
    "v_exp_f32 v161, v161 clamp\n"
    "v_cvt_pk_bf16_f32 v2, v180, v181\n"
    "v_mfma_f32_4x4x4_16b_bf16 v[224:227], v[228:229], v[208:209], v[224:227] cbsz:4 abid:0\n"
    "v_cvt_pk_bf16_f32 v3, v182, v183\n"
    "v_mfma_f32_16x16x16_bf16 v[176:179], v[216:217], v[104:105], v[48:51]\n"
    "v_exp_f32 v162, v162 clamp\n"
    "v_cvt_pk_bf16_f32 v4, v184, v185\n"
    "v_mfma_f32_16x16x16_bf16 v[180:183], v[218:219], v[106:107], v[52:55]\n"
    "v_cvt_pk_bf16_f32 v5, v186, v187\n"
    "v_cvt_pk_bf16_f32 v6, v188, v189\n"
    "v_cvt_pk_bf16_f32 v7, v190, v191\n"
    "v_mfma_f32_16x16x16_bf16 v[184:187], v[220:221], v[108:109], v[56:59]\n"
    "v_exp_f32 v163, v163 clamp\n"
    "v_cvt_pk_bf16_f32 v8, v176, v177\n"
    "v_mfma_f32_4x4x4_16b_bf16 v[224:227], v[228:229], v[210:211], v[224:227] cbsz:4 abid:1\n"
    "v_cvt_pk_bf16_f32 v9, v178, v179\n"
    "v_mfma_f32_16x16x16_bf16 v[188:191], v[222:223], v[110:111], v[60:63]\n"
    "v_exp_f32 v164, v164 clamp\n"
    "v_cvt_pk_bf16_f32 v10, v180, v181\n"
    "v_mfma_f32_16x16x16_bf16 v[176:179], v[216:217], v[112:113], v[64:67]\n"
    "v_cvt_pk_bf16_f32 v11, v182, v183\n"
    "v_cvt_pk_bf16_f32 v12, v184, v185\n"
    "v_cvt_pk_bf16_f32 v13, v186, v187\n"
    "v_mfma_f32_16x16x16_bf16 v[180:183], v[218:219], v[114:115], v[68:71]\n"
    "v_exp_f32 v165, v165 clamp\n"
    "v_cvt_pk_bf16_f32 v14, v188, v189\n"
    "v_mfma_f32_4x4x4_16b_bf16 v[224:227], v[228:229], v[212:213], v[224:227] cbsz:4 abid:2\n"
    "v_cvt_pk_bf16_f32 v15, v190, v191\n"
    "v_mfma_f32_16x16x16_bf16 v[184:187], v[220:221], v[116:117], v[72:75]\n"
    "v_exp_f32 v166, v166 clamp\n"
    "v_cvt_pk_bf16_f32 v16, v176, v177\n"
    "v_mfma_f32_16x16x16_bf16 v[188:191], v[222:223], v[118:119], v[76:79]\n"
    "v_cvt_pk_bf16_f32 v17, v178, v179\n"
    "v_cvt_pk_bf16_f32 v18, v180, v181\n"
    "v_cvt_pk_bf16_f32 v19, v182, v183\n"
    "v_mfma_f32_16x16x16_bf16 v[176:179], v[216:217], v[120:121], v[80:83]\n"
    "v_exp_f32 v167, v167 clamp\n"
    "v_cvt_pk_bf16_f32 v20, v184, v185\n"
    "v_mfma_f32_4x4x4_16b_bf16 v[224:227], v[228:229], v[214:215], v[224:227] cbsz:4 abid:3\n"
    "v_cvt_pk_bf16_f32 v21, v186, v187\n"
    "v_mfma_f32_16x16x16_bf16 v[180:183], v[218:219], v[122:123], v[84:87]\n"
    "v_exp_f32 v168, v168 clamp\n"
    "v_cvt_pk_bf16_f32 v22, v188, v189\n"
    "v_mfma_f32_16x16x16_bf16 v[184:187], v[220:221], v[124:125], v[88:91]\n"
    "v_cvt_pk_bf16_f32 v23, v190, v191\n"
    "v_cvt_pk_bf16_f32 v24, v176, v177\n"
    "v_cvt_pk_bf16_f32 v25, v178, v179\n"
    "v_mfma_f32_16x16x16_bf16 v[188:191], v[222:223], v[126:127], v[92:95]\n"
    "v_exp_f32 v169, v169 clamp\n"
    "v_cvt_pk_bf16_f32 v26, v180, v181\n"
    "s_waitcnt lgkmcnt(0)\n"
    "v_mfma_f32_16x16x32_bf16 v[176:179], v[0:3], v[192:195], v[144:147]\n"
    "v_cvt_pk_bf16_f32 v27, v182, v183\n"
    "v_cvt_pk_bf16_f32 v28, v184, v185\n"
    "v_mfma_f32_16x16x32_bf16 v[176:179], v[4:7], v[196:199], v[176:179]\n"
    "v_cvt_pk_bf16_f32 v29, v186, v187\n"
    "v_cvt_pk_bf16_f32 v30, v188, v189\n"
    "v_mfma_f32_16x16x32_bf16 v[180:183], v[8:11], v[192:195], v[148:151]\n"
    "v_exp_f32 v170, v170 clamp\n"
    "v_mfma_f32_16x16x32_bf16 v[180:183], v[12:15], v[196:199], v[180:183]\n"
    "v_exp_f32 v171, v171 clamp\n"
    "v_mfma_f32_16x16x32_bf16 v[184:187], v[16:19], v[192:195], v[152:155]\n"
    "v_exp_f32 v172, v172 clamp\n"
    "v_mfma_f32_16x16x32_bf16 v[184:187], v[20:23], v[196:199], v[184:187]\n"
    "v_exp_f32 v173, v173 clamp\n"
    "v_cvt_pk_bf16_f32 v31, v190, v191\n"
    "v_mfma_f32_16x16x32_bf16 v[188:191], v[24:27], v[192:195], v[156:159]\n"
    "v_exp_f32 v174, v174 clamp\n"
    "v_mfma_f32_16x16x32_bf16 v[188:191], v[28:31], v[196:199], v[188:191]\n"
    "v_exp_f32 v175, v175 clamp\n"
    "s_waitcnt vmcnt(4)\n"
    "ds_read_b64_tr_b16 v[208:209], v240 offset:6144\n"
    "ds_read_b64_tr_b16 v[210:211], v241 offset:6144\n"
    "ds_read_b64_tr_b16 v[212:213], v242 offset:6144\n"
    "ds_read_b64_tr_b16 v[214:215], v243 offset:6144\n"
    "ds_read_u8 v236, v251 offset:0\n"
    "ds_read_u8 v238, v251 offset:1\n"
    "ds_read_b128 v[192:195], v234 offset:0\n"
    "ds_read_b128 v[196:199], v235 offset:0\n"
    "v_pk_fma_f32 v[160:161], v[160:161], s[70:71], v[128:129]\n"
    "v_pk_fma_f32 v[162:163], v[162:163], s[70:71], v[130:131]\n"
    "v_pk_add_f32 v[230:231], v[160:161], v[162:163]\n"
    "v_pk_mul_f32 v[232:233], v[160:161], v[162:163]\n"
    "v_pk_fma_f32 v[164:165], v[164:165], s[70:71], v[132:133]\n"
    "v_pk_fma_f32 v[230:231], v[230:231], v[164:165], v[232:233]\n"
    "v_pk_mul_f32 v[232:233], v[232:233], v[164:165]\n"
    "v_pk_fma_f32 v[166:167], v[166:167], s[70:71], v[134:135]\n"
    "v_pk_fma_f32 v[230:231], v[230:231], v[166:167], v[232:233]\n"
    "v_pk_mul_f32 v[232:233], v[232:233], v[166:167]\n"
    "v_pk_fma_f32 v[168:169], v[168:169], s[70:71], v[136:137]\n"
    "v_pk_fma_f32 v[230:231], v[230:231], v[168:169], v[232:233]\n"
    "v_pk_mul_f32 v[232:233], v[232:233], v[168:169]\n"
    "v_pk_fma_f32 v[170:171], v[170:171], s[70:71], v[138:139]\n"
    "v_pk_fma_f32 v[230:231], v[230:231], v[170:171], v[232:233]\n"
    "v_pk_mul_f32 v[232:233], v[232:233], v[170:171]\n"
    "v_pk_fma_f32 v[172:173], v[172:173], s[70:71], v[140:141]\n"
    "v_pk_fma_f32 v[230:231], v[230:231], v[172:173], v[232:233]\n"
    "v_pk_mul_f32 v[232:233], v[232:233], v[172:173]\n"
    "v_pk_fma_f32 v[174:175], v[174:175], s[70:71], v[142:143]\n"
    "v_pk_fma_f32 v[230:231], v[230:231], v[174:175], v[232:233]\n"
    "v_pk_mul_f32 v[232:233], v[232:233], v[174:175]\n"
    "v_rcp_f32 v232, v232\n"
    "v_rcp_f32 v233, v233\n"
    "v_fma_f32 v230, v230, v232, v247\n"
    "v_fmac_f32 v230, v231, v233\n"
    "s_add_u32 s66, s66, 16\n"
    "s_waitcnt lgkmcnt(0)\n"
    "ds_write_b16_d16_hi v245, v230\n"
    "ds_read_b64 v[228:229], v246\n"
    "s_lshl1_add_u32 s76, s72, s76\n"
    "buffer_load_dwordx4 v[236:237], s[40:43], s67 idxen offen offset:0 nt lds\n"
    "buffer_load_dwordx4 v[238:239], s[40:43], s67 idxen offen offset:1024 nt lds\n"
    "s_mov_b32 s81, s64\n"
    "s_lshr_b32 s63, s65, 6\n"
    "s_mov_b32 s80, s83\n"
    "s_add_u32 s62, s62, 256\n"
    "s_bitcmp1_b32 s60, 30\n"
    "s_cbranch_scc1 .Ldin_A3s\n"
    ".Ldin_A3:\n"
    "s_cbranch_vccz .Ldin_A3_np\n"
    "s_setprio 1\n"
    ".Ldin_A3_np:\n"
    "v_mfma_f32_16x16x32_bf16 v[160:163], v[0:3], v[192:195], v[144:147]\n"
    "v_exp_f32 v176, v176 clamp\n"
    "v_mfma_f32_16x16x32_bf16 v[160:163], v[4:7], v[196:199], v[160:163]\n"
    "v_exp_f32 v177, v177 clamp\n"
    "v_mfma_f32_16x16x32_bf16 v[164:167], v[8:11], v[192:195], v[148:151]\n"
    "v_exp_f32 v178, v178 clamp\n"
    "v_mfma_f32_16x16x32_bf16 v[164:167], v[12:15], v[196:199], v[164:167]\n"
    "v_exp_f32 v179, v179 clamp\n"
    "v_mfma_f32_16x16x32_bf16 v[168:171], v[16:19], v[192:195], v[152:155]\n"
    "v_exp_f32 v180, v180 clamp\n"
    "v_mfma_f32_16x16x32_bf16 v[168:171], v[20:23], v[196:199], v[168:171]\n"
    "v_exp_f32 v181, v181 clamp\n"
    "v_mfma_f32_16x16x32_bf16 v[172:175], v[24:27], v[192:195], v[156:159]\n"
    "v_exp_f32 v182, v182 clamp\n"
    "v_mfma_f32_16x16x32_bf16 v[172:175], v[28:31], v[196:199], v[172:175]\n"
    "v_exp_f32 v183, v183 clamp\n"
    "v_exp_f32 v184, v184 clamp\n"
    "s_mov_b32 m0, s62\n"
    "ds_read_b64 v[216:217], v246 offset:128\n"
    "v_exp_f32 v185, v185 clamp\n"
    "v_add_u32 v251, s66, v244\n"
    "s_waitcnt lgkmcnt(1)\n"
    "v_mfma_f32_4x4x4_16b_bf16 v[224:227], v[228:229], v[200:201], v[224:227] cbsz:4 abid:0\n"
    "v_exp_f32 v186, v186 clamp\n"
    "v_exp_f32 v187, v187 clamp\n"
    "v_mfma_f32_4x4x4_16b_bf16 v[224:227], v[228:229], v[202:203], v[224:227] cbsz:4 abid:1\n"
    "v_exp_f32 v188, v188 clamp\n"
    "v_exp_f32 v189, v189 clamp\n"
    "v_mfma_f32_4x4x4_16b_bf16 v[224:227], v[228:229], v[204:205], v[224:227] cbsz:4 abid:2\n"
    "v_exp_f32 v190, v190 clamp\n"
    "v_exp_f32 v191, v191 clamp\n"
    "v_mfma_f32_4x4x4_16b_bf16 v[224:227], v[228:229], v[206:207], v[224:227] cbsz:4 abid:3\n"
    "s_waitcnt vmcnt(4)\n"
    "s_waitcnt lgkmcnt(0)\n"
    "v_readlane_b32 s60, v216, s59\n"
    "v_readlane_b32 s61, v217, s59\n"
    "ds_read_b64_tr_b16 v[200:201], v240 offset:0\n"
    "ds_read_b64_tr_b16 v[202:203], v241 offset:0\n"
    "ds_read_b64_tr_b16 v[204:205], v242 offset:0\n"
    "ds_read_b64_tr_b16 v[206:207], v243 offset:0\n"
    "ds_read_u8 v236, v251 offset:0\n"
    "ds_read_u8 v238, v251 offset:1\n"
    "ds_read_b128 v[192:195], v234 offset:2048\n"
    "ds_read_b128 v[196:199], v235 offset:2048\n"
    "s_cmp_eq_u32 s60, -1\n"
    "s_cbranch_scc1 .Ldin_A3_poll\n"
    ".Ldin_A3_pr:\n"
    "s_lshr_b32 s94, s60, 12\n"
    "s_and_b32 s94, s94, 0x1ff0\n"
    "v_xor_b32 v252, s94, v250\n"
    "v_pk_add_f32 v[230:231], v[224:225], v[226:227]\n"
    "v_add_f32 v230, v230, v231\n"
    "ds_write_addtid_b32 v230 offset:49152\n"
    "ds_read_b128 v[216:219], v252 offset:0\n"
    "ds_read_b128 v[220:223], v252 offset:64\n"
    "v_mov_b64 v[224:225], 0\n"
    "v_mov_b64_e64 v[226:227], 0\n"
    "v_pk_fma_f32 v[176:177], v[176:177], s[70:71], v[128:129]\n"
    "v_pk_fma_f32 v[178:179], v[178:179], s[70:71], v[130:131]\n"
    "v_pk_add_f32 v[230:231], v[176:177], v[178:179]\n"
    "v_pk_mul_f32 v[232:233], v[176:177], v[178:179]\n"
    "v_pk_fma_f32 v[180:181], v[180:181], s[70:71], v[132:133]\n"
    "v_pk_fma_f32 v[230:231], v[230:231], v[180:181], v[232:233]\n"
    "v_pk_mul_f32 v[232:233], v[232:233], v[180:181]\n"
    "v_pk_fma_f32 v[182:183], v[182:183], s[70:71], v[134:135]\n"
    "v_pk_fma_f32 v[230:231], v[230:231], v[182:183], v[232:233]\n"
    "v_pk_mul_f32 v[232:233], v[232:233], v[182:183]\n"
    "v_pk_fma_f32 v[184:185], v[184:185], s[70:71], v[136:137]\n"
    "v_pk_fma_f32 v[230:231], v[230:231], v[184:185], v[232:233]\n"
    "v_pk_mul_f32 v[232:233], v[232:233], v[184:185]\n"
    "v_pk_fma_f32 v[186:187], v[186:187], s[70:71], v[138:139]\n"
    "v_pk_fma_f32 v[230:231], v[230:231], v[186:187], v[232:233]\n"
    "v_pk_mul_f32 v[232:233], v[232:233], v[186:187]\n"
    "v_pk_fma_f32 v[188:189], v[188:189], s[70:71], v[140:141]\n"
    "v_pk_fma_f32 v[230:231], v[230:231], v[188:189], v[232:233]\n"
    "v_pk_mul_f32 v[232:233], v[232:233], v[188:189]\n"
    "v_pk_fma_f32 v[190:191], v[190:191], s[70:71], v[142:143]\n"
    "v_pk_fma_f32 v[230:231], v[230:231], v[190:191], v[232:233]\n"
    "v_pk_mul_f32 v[232:233], v[232:233], v[190:191]\n"
    "s_add_u32 m0, s68, 0\n"
    "s_and_b32 s64, s60, 0xffff\n"
    "s_bfe_u32 s94, s60, 0x30013\n"
    "s_bfe_u32 s95, s60, 0x50019\n"
    "s_mul_i32 s94, s94, s56\n"
    "s_lshl4_add_u32 s83, s95, s64\n"
    "s_add_u32 s65, s58, s94\n"
    "s_add_u32 s82, s64, 64\n"
    "s_add_u32 s58, s58, s57\n"
    "s_add_u32 s59, s59, 1\n"
    "s_sub_u32 s95, s61, s76\n"
    "s_lshr_b32 s94, s61, 16\n"
    "s_sext_i32_i16 s95, s95\n"
    "s_cmp_lt_i32 s95, -400\n"
    "s_cselect_b32 s94, 0, s94\n"
    "s_mul_i32 s95, s95, s94\n"
    "s_ashr_i32 s95, s95, 16\n"
    "s_max_i32 s95, s95, -10\n"
    "s_min_i32 s95, s95, 10\n"
    "s_add_u32 s72, s77, s95\n"
    "s_cmp_lt_i32 s60, 0\n"
    "s_cselect_b32 s72, 0, s72\n"
    "v_rcp_f32 v232, v232\n"
    "v_rcp_f32 v233, v233\n"
    "v_fma_f32 v230, v230, v232, v247\n"
    "v_fmac_f32 v230, v231, v233\n"
    "s_add_u32 s66, s66, 16\n"
    "s_waitcnt lgkmcnt(0)\n"
    "ds_write_b16_d16_hi v245, v230\n"
    "ds_read_b64 v[228:229], v246\n"
    "buffer_load_dwordx4 v[236:237], s[40:43], s67 idxen offen offset:0 nt lds\n"
    "buffer_load_dwordx4 v[238:239], s[40:43], s67 idxen offen offset:1024 nt lds\n"
    "s_branch .Ldin_G0\n"
    ".Ldin_A3_poll:\n"
    "s_sleep 1\n"
    "ds_read_b64 v[216:217], v246 offset:128\n"
    "s_waitcnt lgkmcnt(0)\n"
    "v_readlane_b32 s60, v216, s59\n"
    "v_readlane_b32 s61, v217, s59\n"
    "s_cmp_eq_u32 s60, -1\n"
    "s_cbranch_scc1 .Ldin_A3_poll\n"
    "s_branch .Ldin_A3_pr\n"
    ".Ldin_stub_L1:\n"
    "s_setpc_b64 s[34:35]\n"
    ".p2align 5\n"
    ".Ldin_L1:\n"
    "s_setprio 0\n"
    "s_cmp_lt_i32 s60, 0\n"
    "s_cbranch_scc1 .Ldin_E1\n"
    "ds_read_b128 v[144:147], v252 offset:8192\n"
    "ds_read_b128 v[148:151], v252 offset:8256\n"
    "ds_read_b128 v[152:155], v252 offset:16384\n"
    "ds_read_b128 v[156:159], v252 offset:16448\n"
    "s_add_u32 s76, s76, s73\n"
    "s_add_u32 m0, s68, 4096\n"
    "v_and_b32 v216, v248, v216\n"
    "v_and_b32 v217, v249, v217\n"
    "v_and_b32 v218, v248, v218\n"
    "v_and_b32 v219, v249, v219\n"
    "v_mfma_f32_16x16x16_bf16 v[160:163], v[216:217], v[96:97], v[32:35]\n"
    "v_and_b32 v220, v248, v220\n"
    "v_and_b32 v221, v249, v221\n"
    "v_and_b32 v222, v248, v222\n"
    "v_mfma_f32_16x16x16_bf16 v[164:167], v[218:219], v[98:99], v[36:39]\n"
    "v_exp_f32 v176, v176 clamp\n"
    "v_and_b32 v223, v249, v223\n"
    "v_mfma_f32_16x16x16_bf16 v[168:171], v[220:221], v[100:101], v[40:43]\n"
    "v_add_u32 v251, s66, v244\n"
    "v_cvt_pk_bf16_f32 v0, v160, v161\n"
    "v_cvt_pk_bf16_f32 v1, v162, v163\n"
    "s_waitcnt lgkmcnt(4)\n"
    "v_mfma_f32_16x16x16_bf16 v[172:175], v[222:223], v[102:103], v[44:47]\n"
    "v_exp_f32 v177, v177 clamp\n"
    "v_cvt_pk_bf16_f32 v2, v164, v165\n"
    "v_mfma_f32_4x4x4_16b_bf16 v[224:227], v[228:229], v[200:201], v[224:227] cbsz:4 abid:0\n"
    "v_cvt_pk_bf16_f32 v3, v166, v167\n"
    "v_mfma_f32_16x16x16_bf16 v[160:163], v[216:217], v[104:105], v[48:51]\n"
    "v_exp_f32 v178, v178 clamp\n"
    "v_cvt_pk_bf16_f32 v4, v168, v169\n"
    "v_mfma_f32_16x16x16_bf16 v[164:167], v[218:219], v[106:107], v[52:55]\n"
    "v_cvt_pk_bf16_f32 v5, v170, v171\n"
    "v_cvt_pk_bf16_f32 v6, v172, v173\n"
    "v_cvt_pk_bf16_f32 v7, v174, v175\n"
    "v_mfma_f32_16x16x16_bf16 v[168:171], v[220:221], v[108:109], v[56:59]\n"
    "v_exp_f32 v179, v179 clamp\n"
    "v_cvt_pk_bf16_f32 v8, v160, v161\n"
    "v_mfma_f32_4x4x4_16b_bf16 v[224:227], v[228:229], v[202:203], v[224:227] cbsz:4 abid:1\n"
    "v_cvt_pk_bf16_f32 v9, v162, v163\n"
    "v_mfma_f32_16x16x16_bf16 v[172:175], v[222:223], v[110:111], v[60:63]\n"
    "v_exp_f32 v180, v180 clamp\n"
    "v_cvt_pk_bf16_f32 v10, v164, v165\n"
    "v_mfma_f32_16x16x16_bf16 v[160:163], v[216:217], v[112:113], v[64:67]\n"
    "v_cvt_pk_bf16_f32 v11, v166, v167\n"
    "v_cvt_pk_bf16_f32 v12, v168, v169\n"
    "v_cvt_pk_bf16_f32 v13, v170, v171\n"
    "v_mfma_f32_16x16x16_bf16 v[164:167], v[218:219], v[114:115], v[68:71]\n"
    "v_exp_f32 v181, v181 clamp\n"
    "v_cvt_pk_bf16_f32 v14, v172, v173\n"
    "v_mfma_f32_4x4x4_16b_bf16 v[224:227], v[228:229], v[204:205], v[224:227] cbsz:4 abid:2\n"
    "v_cvt_pk_bf16_f32 v15, v174, v175\n"
    "v_mfma_f32_16x16x16_bf16 v[168:171], v[220:221], v[116:117], v[72:75]\n"
    "v_exp_f32 v182, v182 clamp\n"
    "v_cvt_pk_bf16_f32 v16, v160, v161\n"
    "v_mfma_f32_16x16x16_bf16 v[172:175], v[222:223], v[118:119], v[76:79]\n"
    "v_cvt_pk_bf16_f32 v17, v162, v163\n"
    "v_cvt_pk_bf16_f32 v18, v164, v165\n"
    "v_cvt_pk_bf16_f32 v19, v166, v167\n"
    "v_mfma_f32_16x16x16_bf16 v[160:163], v[216:217], v[120:121], v[80:83]\n"
    "v_exp_f32 v183, v183 clamp\n"
    "v_cvt_pk_bf16_f32 v20, v168, v169\n"
    "v_mfma_f32_4x4x4_16b_bf16 v[224:227], v[228:229], v[206:207], v[224:227] cbsz:4 abid:3\n"
    "v_cvt_pk_bf16_f32 v21, v170, v171\n"
    "v_mfma_f32_16x16x16_bf16 v[164:167], v[218:219], v[122:123], v[84:87]\n"
    "v_exp_f32 v184, v184 clamp\n"
    "v_cvt_pk_bf16_f32 v22, v172, v173\n"
    "v_mfma_f32_16x16x16_bf16 v[168:171], v[220:221], v[124:125], v[88:91]\n"
    "v_cvt_pk_bf16_f32 v23, v174, v175\n"
    "v_cvt_pk_bf16_f32 v24, v160, v161\n"
    "v_cvt_pk_bf16_f32 v25, v162, v163\n"
    "v_mfma_f32_16x16x16_bf16 v[172:175], v[222:223], v[126:127], v[92:95]\n"
    "v_exp_f32 v185, v185 clamp\n"
    "v_cvt_pk_bf16_f32 v26, v164, v165\n"
    "s_waitcnt lgkmcnt(0)\n"
    "v_mfma_f32_16x16x32_bf16 v[160:163], v[0:3], v[192:195], v[144:147]\n"
    "v_cvt_pk_bf16_f32 v27, v166, v167\n"
    "v_cvt_pk_bf16_f32 v28, v168, v169\n"
    "v_mfma_f32_16x16x32_bf16 v[160:163], v[4:7], v[196:199], v[160:163]\n"
    "v_cvt_pk_bf16_f32 v29, v170, v171\n"
    "v_cvt_pk_bf16_f32 v30, v172, v173\n"
    "v_mfma_f32_16x16x32_bf16 v[164:167], v[8:11], v[192:195], v[148:151]\n"
    "v_exp_f32 v186, v186 clamp\n"
    "v_mfma_f32_16x16x32_bf16 v[164:167], v[12:15], v[196:199], v[164:167]\n"
    "v_exp_f32 v187, v187 clamp\n"
    "v_mfma_f32_16x16x32_bf16 v[168:171], v[16:19], v[192:195], v[152:155]\n"
    "v_exp_f32 v188, v188 clamp\n"
    "v_mfma_f32_16x16x32_bf16 v[168:171], v[20:23], v[196:199], v[168:171]\n"
    "v_exp_f32 v189, v189 clamp\n"
    "v_cvt_pk_bf16_f32 v31, v174, v175\n"
    "v_mfma_f32_16x16x32_bf16 v[172:175], v[24:27], v[192:195], v[156:159]\n"
    "v_exp_f32 v190, v190 clamp\n"
    "v_mfma_f32_16x16x32_bf16 v[172:175], v[28:31], v[196:199], v[172:175]\n"
    "v_exp_f32 v191, v191 clamp\n"
    "s_waitcnt vmcnt(4)\n"
    "ds_read_b64_tr_b16 v[200:201], v240 offset:4096\n"
    "ds_read_b64_tr_b16 v[202:203], v241 offset:4096\n"
    "ds_read_b64_tr_b16 v[204:205], v242 offset:4096\n"
    "ds_read_b64_tr_b16 v[206:207], v243 offset:4096\n"
    "ds_read_u8 v236, v251 offset:0\n"
    "ds_read_u8 v238, v251 offset:1\n"
    "ds_read_b128 v[192:195], v234 offset:6144\n"
    "ds_read_b128 v[196:199], v235 offset:6144\n"
    "v_pk_fma_f32 v[176:177], v[176:177], s[70:71], v[128:129]\n"
    "v_pk_fma_f32 v[178:179], v[178:179], s[70:71], v[130:131]\n"
    "v_pk_add_f32 v[230:231], v[176:177], v[178:179]\n"
    "v_pk_mul_f32 v[232:233], v[176:177], v[178:179]\n"
    "v_pk_fma_f32 v[180:181], v[180:181], s[70:71], v[132:133]\n"
    "v_pk_fma_f32 v[230:231], v[230:231], v[180:181], v[232:233]\n"
    "v_pk_mul_f32 v[232:233], v[232:233], v[180:181]\n"
    "v_pk_fma_f32 v[182:183], v[182:183], s[70:71], v[134:135]\n"
    "v_pk_fma_f32 v[230:231], v[230:231], v[182:183], v[232:233]\n"
    "v_pk_mul_f32 v[232:233], v[232:233], v[182:183]\n"
    "v_pk_fma_f32 v[184:185], v[184:185], s[70:71], v[136:137]\n"
    "v_pk_fma_f32 v[230:231], v[230:231], v[184:185], v[232:233]\n"
    "v_pk_mul_f32 v[232:233], v[232:233], v[184:185]\n"
    "v_pk_fma_f32 v[186:187], v[186:187], s[70:71], v[138:139]\n"
    "v_pk_fma_f32 v[230:231], v[230:231], v[186:187], v[232:233]\n"
    "v_pk_mul_f32 v[232:233], v[232:233], v[186:187]\n"
    "v_pk_fma_f32 v[188:189], v[188:189], s[70:71], v[140:141]\n"
    "v_pk_fma_f32 v[230:231], v[230:231], v[188:189], v[232:233]\n"
    "v_pk_mul_f32 v[232:233], v[232:233], v[188:189]\n"
    "v_pk_fma_f32 v[190:191], v[190:191], s[70:71], v[142:143]\n"
    "v_pk_fma_f32 v[230:231], v[230:231], v[190:191], v[232:233]\n"
    "v_pk_mul_f32 v[232:233], v[232:233], v[190:191]\n"
    "v_rcp_f32 v232, v232\n"
    "v_rcp_f32 v233, v233\n"
    "v_fma_f32 v230, v230, v232, v247\n"
    "v_fmac_f32 v230, v231, v233\n"
    "s_add_u32 s66, s66, 16\n"
    "s_waitcnt lgkmcnt(0)\n"
    "ds_write_b16_d16_hi v245, v230\n"
    "ds_read_b64 v[228:229], v246\n"
    "s_lshl1_add_u32 s76, s72, s76\n"
    "buffer_load_dwordx4 v[236:237], s[40:43], s67 idxen offen offset:0 nt lds\n"
    "buffer_load_dwordx4 v[238:239], s[40:43], s67 idxen offen offset:1024 nt lds\n"
    "s_mov_b32 s81, s64\n"
    "s_lshr_b32 s63, s65, 6\n"
    "s_mov_b32 s80, s83\n"
    "s_add_u32 s62, s62, 256\n"
    "s_bitcmp1_b32 s60, 30\n"
    "s_cbranch_scc1 .Ldin_A2s\n"
    ".Ldin_A2:\n"
    "s_cbranch_vccz .Ldin_A2_np\n"
    "s_setprio 1\n"
    ".Ldin_A2_np:\n"
    "v_mfma_f32_16x16x32_bf16 v[176:179], v[0:3], v[192:195], v[144:147]\n"
    "v_exp_f32 v160, v160 clamp\n"
    "v_mfma_f32_16x16x32_bf16 v[176:179], v[4:7], v[196:199], v[176:179]\n"
    "v_exp_f32 v161, v161 clamp\n"
    "v_mfma_f32_16x16x32_bf16 v[180:183], v[8:11], v[192:195], v[148:151]\n"
    "v_exp_f32 v162, v162 clamp\n"
    "v_mfma_f32_16x16x32_bf16 v[180:183], v[12:15], v[196:199], v[180:183]\n"
    "v_exp_f32 v163, v163 clamp\n"
    "v_mfma_f32_16x16x32_bf16 v[184:187], v[16:19], v[192:195], v[152:155]\n"
    "v_exp_f32 v164, v164 clamp\n"
    "v_mfma_f32_16x16x32_bf16 v[184:187], v[20:23], v[196:199], v[184:187]\n"
    "v_exp_f32 v165, v165 clamp\n"
    "v_mfma_f32_16x16x32_bf16 v[188:191], v[24:27], v[192:195], v[156:159]\n"
    "v_exp_f32 v166, v166 clamp\n"
    "v_mfma_f32_16x16x32_bf16 v[188:191], v[28:31], v[196:199], v[188:191]\n"
    "v_exp_f32 v167, v167 clamp\n"
    "v_exp_f32 v168, v168 clamp\n"
    "s_mov_b32 m0, s62\n"
    "ds_read_b64 v[216:217], v246 offset:128\n"
    "v_exp_f32 v169, v169 clamp\n"
    "v_add_u32 v251, s66, v244\n"
    "s_waitcnt lgkmcnt(1)\n"
    "v_mfma_f32_4x4x4_16b_bf16 v[224:227], v[228:229], v[208:209], v[224:227] cbsz:4 abid:0\n"
    "v_exp_f32 v170, v170 clamp\n"
    "v_exp_f32 v171, v171 clamp\n"
    "v_mfma_f32_4x4x4_16b_bf16 v[224:227], v[228:229], v[210:211], v[224:227] cbsz:4 abid:1\n"
    "v_exp_f32 v172, v172 clamp\n"
    "v_exp_f32 v173, v173 clamp\n"
    "v_mfma_f32_4x4x4_16b_bf16 v[224:227], v[228:229], v[212:213], v[224:227] cbsz:4 abid:2\n"
    "v_exp_f32 v174, v174 clamp\n"
    "v_exp_f32 v175, v175 clamp\n"
    "v_mfma_f32_4x4x4_16b_bf16 v[224:227], v[228:229], v[214:215], v[224:227] cbsz:4 abid:3\n"
    "s_waitcnt vmcnt(4)\n"
    "s_waitcnt lgkmcnt(0)\n"
    "v_readlane_b32 s60, v216, s59\n"
    "v_readlane_b32 s61, v217, s59\n"
    "ds_read_b64_tr_b16 v[208:209], v240 offset:6144\n"
    "ds_read_b64_tr_b16 v[210:211], v241 offset:6144\n"
    "ds_read_b64_tr_b16 v[212:213], v242 offset:6144\n"
    "ds_read_b64_tr_b16 v[214:215], v243 offset:6144\n"
    "ds_read_u8 v236, v251 offset:0\n"
    "ds_read_u8 v238, v251 offset:1\n"
    "ds_read_b128 v[192:195], v234 offset:0\n"
    "ds_read_b128 v[196:199], v235 offset:0\n"
    "s_cmp_eq_u32 s60, -1\n"
    "s_cbranch_scc1 .Ldin_A2_poll\n"
    ".Ldin_A2_pr:\n"
    "s_lshr_b32 s94, s60, 12\n"
    "s_and_b32 s94, s94, 0x1ff0\n"
    "v_xor_b32 v252, s94, v250\n"
    "v_pk_add_f32 v[230:231], v[224:225], v[226:227]\n"
    "v_add_f32 v230, v230, v231\n"
    "ds_write_addtid_b32 v230 offset:49152\n"
    "ds_read_b128 v[216:219], v252 offset:0\n"
    "ds_read_b128 v[220:223], v252 offset:64\n"
    "v_mov_b64 v[224:225], 0\n"
    "v_mov_b64_e64 v[226:227], 0\n"
    "v_pk_fma_f32 v[160:161], v[160:161], s[70:71], v[128:129]\n"
    "v_pk_fma_f32 v[162:163], v[162:163], s[70:71], v[130:131]\n"
    "v_pk_add_f32 v[230:231], v[160:161], v[162:163]\n"
    "v_pk_mul_f32 v[232:233], v[160:161], v[162:163]\n"
    "v_pk_fma_f32 v[164:165], v[164:165], s[70:71], v[132:133]\n"
    "v_pk_fma_f32 v[230:231], v[230:231], v[164:165], v[232:233]\n"
    "v_pk_mul_f32 v[232:233], v[232:233], v[164:165]\n"
    "v_pk_fma_f32 v[166:167], v[166:167], s[70:71], v[134:135]\n"
    "v_pk_fma_f32 v[230:231], v[230:231], v[166:167], v[232:233]\n"
    "v_pk_mul_f32 v[232:233], v[232:233], v[166:167]\n"
    "v_pk_fma_f32 v[168:169], v[168:169], s[70:71], v[136:137]\n"
    "v_pk_fma_f32 v[230:231], v[230:231], v[168:169], v[232:233]\n"
    "v_pk_mul_f32 v[232:233], v[232:233], v[168:169]\n"
    "v_pk_fma_f32 v[170:171], v[170:171], s[70:71], v[138:139]\n"
    "v_pk_fma_f32 v[230:231], v[230:231], v[170:171], v[232:233]\n"
    "v_pk_mul_f32 v[232:233], v[232:233], v[170:171]\n"
    "v_pk_fma_f32 v[172:173], v[172:173], s[70:71], v[140:141]\n"
    "v_pk_fma_f32 v[230:231], v[230:231], v[172:173], v[232:233]\n"
    "v_pk_mul_f32 v[232:233], v[232:233], v[172:173]\n"
    "v_pk_fma_f32 v[174:175], v[174:175], s[70:71], v[142:143]\n"
    "v_pk_fma_f32 v[230:231], v[230:231], v[174:175], v[232:233]\n"
    "v_pk_mul_f32 v[232:233], v[232:233], v[174:175]\n"
    "s_add_u32 m0, s68, 6144\n"
    "s_and_b32 s64, s60, 0xffff\n"
    "s_bfe_u32 s94, s60, 0x30013\n"
    "s_bfe_u32 s95, s60, 0x50019\n"
    "s_mul_i32 s94, s94, s56\n"
    "s_lshl4_add_u32 s83, s95, s64\n"
    "s_add_u32 s65, s58, s94\n"
    "s_add_u32 s82, s64, 64\n"
    "s_add_u32 s58, s58, s57\n"
    "s_add_u32 s59, s59, 1\n"
    "s_sub_u32 s95, s61, s76\n"
    "s_lshr_b32 s94, s61, 16\n"
    "s_sext_i32_i16 s95, s95\n"
    "s_cmp_lt_i32 s95, -400\n"
    "s_cselect_b32 s94, 0, s94\n"
    "s_mul_i32 s95, s95, s94\n"
    "s_ashr_i32 s95, s95, 16\n"
    "s_max_i32 s95, s95, -10\n"
    "s_min_i32 s95, s95, 10\n"
    "s_add_u32 s72, s77, s95\n"
    "s_cmp_lt_i32 s60, 0\n"
    "s_cselect_b32 s72, 0, s72\n"
    "v_rcp_f32 v232, v232\n"
    "v_rcp_f32 v233, v233\n"
    "v_fma_f32 v230, v230, v232, v247\n"
    "v_fmac_f32 v230, v231, v233\n"
    "s_add_u32 s66, s66, 16\n"
    "s_waitcnt lgkmcnt(0)\n"
    "ds_write_b16_d16_hi v245, v230\n"
    "ds_read_b64 v[228:229], v246\n"
    "buffer_load_dwordx4 v[236:237], s[40:43], s67 idxen offen offset:0 nt lds\n"
    "buffer_load_dwordx4 v[238:239], s[40:43], s67 idxen offen offset:1024 nt lds\n"
    "s_branch .Ldin_G3\n"
    ".Ldin_A2_poll:\n"
    "s_sleep 1\n"
    "ds_read_b64 v[216:217], v246 offset:128\n"
    "s_waitcnt lgkmcnt(0)\n"
    "v_readlane_b32 s60, v216, s59\n"
    "v_readlane_b32 s61, v217, s59\n"
    "s_cmp_eq_u32 s60, -1\n"
    "s_cbranch_scc1 .Ldin_A2_poll\n"
    "s_branch .Ldin_A2_pr\n"
    ".p2align 12\n"
    ".Ldin_stub_L0:\n"
    "s_setpc_b64 s[34:35]\n"
    ".p2align 5\n"
    ".Ldin_L0:\n"
    "s_setprio 0\n"
    "s_cmp_lt_i32 s60, 0\n"
    "s_cbranch_scc1 .Ldin_E0\n"
    "ds_read_b128 v[144:147], v252 offset:8192\n"
    "ds_read_b128 v[148:151], v252 offset:8256\n"
    "ds_read_b128 v[152:155], v252 offset:16384\n"
    "ds_read_b128 v[156:159], v252 offset:16448\n"
    "s_add_u32 s76, s76, s73\n"
    "s_add_u32 m0, s68, 2048\n"
    "v_and_b32 v216, v248, v216\n"
    "v_and_b32 v217, v249, v217\n"
    "v_and_b32 v218, v248, v218\n"
    "v_and_b32 v219, v249, v219\n"
    "v_mfma_f32_16x16x16_bf16 v[176:179], v[216:217], v[96:97], v[32:35]\n"
    "v_and_b32 v220, v248, v220\n"
    "v_and_b32 v221, v249, v221\n"
    "v_and_b32 v222, v248, v222\n"
    "v_mfma_f32_16x16x16_bf16 v[180:183], v[218:219], v[98:99], v[36:39]\n"
    "v_exp_f32 v160, v160 clamp\n"
    "v_and_b32 v223, v249, v223\n"
    "v_mfma_f32_16x16x16_bf16 v[184:187], v[220:221], v[100:101], v[40:43]\n"
    "v_add_u32 v251, s66, v244\n"
    "v_cvt_pk_bf16_f32 v0, v176, v177\n"
    "v_cvt_pk_bf16_f32 v1, v178, v179\n"
    "s_waitcnt lgkmcnt(4)\n"
    "v_mfma_f32_16x16x16_bf16 v[188:191], v[222:223], v[102:103], v[44:47]\n"
    "v_exp_f32 v161, v161 clamp\n"
    "v_cvt_pk_bf16_f32 v2, v180, v181\n"
    "v_mfma_f32_4x4x4_16b_bf16 v[224:227], v[228:229], v[208:209], v[224:227] cbsz:4 abid:0\n"
    "v_cvt_pk_bf16_f32 v3, v182, v183\n"
    "v_mfma_f32_16x16x16_bf16 v[176:179], v[216:217], v[104:105], v[48:51]\n"
    "v_exp_f32 v162, v162 clamp\n"
    "v_cvt_pk_bf16_f32 v4, v184, v185\n"
    "v_mfma_f32_16x16x16_bf16 v[180:183], v[218:219], v[106:107], v[52:55]\n"
    "v_cvt_pk_bf16_f32 v5, v186, v187\n"
    "v_cvt_pk_bf16_f32 v6, v188, v189\n"
    "v_cvt_pk_bf16_f32 v7, v190, v191\n"
    "v_mfma_f32_16x16x16_bf16 v[184:187], v[220:221], v[108:109], v[56:59]\n"
    "v_exp_f32 v163, v163 clamp\n"
    "v_cvt_pk_bf16_f32 v8, v176, v177\n"
    "v_mfma_f32_4x4x4_16b_bf16 v[224:227], v[228:229], v[210:211], v[224:227] cbsz:4 abid:1\n"
    "v_cvt_pk_bf16_f32 v9, v178, v179\n"
    "v_mfma_f32_16x16x16_bf16 v[188:191], v[222:223], v[110:111], v[60:63]\n"
    "v_exp_f32 v164, v164 clamp\n"
    "v_cvt_pk_bf16_f32 v10, v180, v181\n"
    "v_mfma_f32_16x16x16_bf16 v[176:179], v[216:217], v[112:113], v[64:67]\n"
    "v_cvt_pk_bf16_f32 v11, v182, v183\n"
    "v_cvt_pk_bf16_f32 v12, v184, v185\n"
    "v_cvt_pk_bf16_f32 v13, v186, v187\n"
    "v_mfma_f32_16x16x16_bf16 v[180:183], v[218:219], v[114:115], v[68:71]\n"
    "v_exp_f32 v165, v165 clamp\n"
    "v_cvt_pk_bf16_f32 v14, v188, v189\n"
    "v_mfma_f32_4x4x4_16b_bf16 v[224:227], v[228:229], v[212:213], v[224:227] cbsz:4 abid:2\n"
    "v_cvt_pk_bf16_f32 v15, v190, v191\n"
    "v_mfma_f32_16x16x16_bf16 v[184:187], v[220:221], v[116:117], v[72:75]\n"
    "v_exp_f32 v166, v166 clamp\n"
    "v_cvt_pk_bf16_f32 v16, v176, v177\n"
    "v_mfma_f32_16x16x16_bf16 v[188:191], v[222:223], v[118:119], v[76:79]\n"
    "v_cvt_pk_bf16_f32 v17, v178, v179\n"
    "v_cvt_pk_bf16_f32 v18, v180, v181\n"
    "v_cvt_pk_bf16_f32 v19, v182, v183\n"
    "v_mfma_f32_16x16x16_bf16 v[176:179], v[216:217], v[120:121], v[80:83]\n"
    "v_exp_f32 v167, v167 clamp\n"
    "v_cvt_pk_bf16_f32 v20, v184, v185\n"
    "v_mfma_f32_4x4x4_16b_bf16 v[224:227], v[228:229], v[214:215], v[224:227] cbsz:4 abid:3\n"
    "v_cvt_pk_bf16_f32 v21, v186, v187\n"
    "v_mfma_f32_16x16x16_bf16 v[180:183], v[218:219], v[122:123], v[84:87]\n"
    "v_exp_f32 v168, v168 clamp\n"
    "v_cvt_pk_bf16_f32 v22, v188, v189\n"
    "v_mfma_f32_16x16x16_bf16 v[184:187], v[220:221], v[124:125], v[88:91]\n"
    "v_cvt_pk_bf16_f32 v23, v190, v191\n"
    "v_cvt_pk_bf16_f32 v24, v176, v177\n"
    "v_cvt_pk_bf16_f32 v25, v178, v179\n"
    "v_mfma_f32_16x16x16_bf16 v[188:191], v[222:223], v[126:127], v[92:95]\n"
    "v_exp_f32 v169, v169 clamp\n"
    "v_cvt_pk_bf16_f32 v26, v180, v181\n"
    "s_waitcnt lgkmcnt(0)\n"
    "v_mfma_f32_16x16x32_bf16 v[176:179], v[0:3], v[192:195], v[144:147]\n"
    "v_cvt_pk_bf16_f32 v27, v182, v183\n"
    "v_cvt_pk_bf16_f32 v28, v184, v185\n"
    "v_mfma_f32_16x16x32_bf16 v[176:179], v[4:7], v[196:199], v[176:179]\n"
    "v_cvt_pk_bf16_f32 v29, v186, v187\n"
    "v_cvt_pk_bf16_f32 v30, v188, v189\n"
    "v_mfma_f32_16x16x32_bf16 v[180:183], v[8:11], v[192:195], v[148:151]\n"
    "v_exp_f32 v170, v170 clamp\n"
    "v_mfma_f32_16x16x32_bf16 v[180:183], v[12:15], v[196:199], v[180:183]\n"
    "v_exp_f32 v171, v171 clamp\n"
    "v_mfma_f32_16x16x32_bf16 v[184:187], v[16:19], v[192:195], v[152:155]\n"
    "v_exp_f32 v172, v172 clamp\n"
    "v_mfma_f32_16x16x32_bf16 v[184:187], v[20:23], v[196:199], v[184:187]\n"
    "v_exp_f32 v173, v173 clamp\n"
    "v_cvt_pk_bf16_f32 v31, v190, v191\n"
    "v_mfma_f32_16x16x32_bf16 v[188:191], v[24:27], v[192:195], v[156:159]\n"
    "v_exp_f32 v174, v174 clamp\n"
    "v_mfma_f32_16x16x32_bf16 v[188:191], v[28:31], v[196:199], v[188:191]\n"
    "v_exp_f32 v175, v175 clamp\n"
    "s_waitcnt vmcnt(4)\n"
    "ds_read_b64_tr_b16 v[208:209], v240 offset:2048\n"
    "ds_read_b64_tr_b16 v[210:211], v241 offset:2048\n"
    "ds_read_b64_tr_b16 v[212:213], v242 offset:2048\n"
    "ds_read_b64_tr_b16 v[214:215], v243 offset:2048\n"
    "ds_read_u8 v236, v251 offset:0\n"
    "ds_read_u8 v238, v251 offset:1\n"
    "ds_read_b128 v[192:195], v234 offset:4096\n"
    "ds_read_b128 v[196:199], v235 offset:4096\n"
    "v_pk_fma_f32 v[160:161], v[160:161], s[70:71], v[128:129]\n"
    "v_pk_fma_f32 v[162:163], v[162:163], s[70:71], v[130:131]\n"
    "v_pk_add_f32 v[230:231], v[160:161], v[162:163]\n"
    "v_pk_mul_f32 v[232:233], v[160:161], v[162:163]\n"
    "v_pk_fma_f32 v[164:165], v[164:165], s[70:71], v[132:133]\n"
    "v_pk_fma_f32 v[230:231], v[230:231], v[164:165], v[232:233]\n"
    "v_pk_mul_f32 v[232:233], v[232:233], v[164:165]\n"
    "v_pk_fma_f32 v[166:167], v[166:167], s[70:71], v[134:135]\n"
    "v_pk_fma_f32 v[230:231], v[230:231], v[166:167], v[232:233]\n"
    "v_pk_mul_f32 v[232:233], v[232:233], v[166:167]\n"
    "v_pk_fma_f32 v[168:169], v[168:169], s[70:71], v[136:137]\n"
    "v_pk_fma_f32 v[230:231], v[230:231], v[168:169], v[232:233]\n"
    "v_pk_mul_f32 v[232:233], v[232:233], v[168:169]\n"
    "v_pk_fma_f32 v[170:171], v[170:171], s[70:71], v[138:139]\n"
    "v_pk_fma_f32 v[230:231], v[230:231], v[170:171], v[232:233]\n"
    "v_pk_mul_f32 v[232:233], v[232:233], v[170:171]\n"
    "v_pk_fma_f32 v[172:173], v[172:173], s[70:71], v[140:141]\n"
    "v_pk_fma_f32 v[230:231], v[230:231], v[172:173], v[232:233]\n"
    "v_pk_mul_f32 v[232:233], v[232:233], v[172:173]\n"
    "v_pk_fma_f32 v[174:175], v[174:175], s[70:71], v[142:143]\n"
    "v_pk_fma_f32 v[230:231], v[230:231], v[174:175], v[232:233]\n"
    "v_pk_mul_f32 v[232:233], v[232:233], v[174:175]\n"
    "v_rcp_f32 v232, v232\n"
    "v_rcp_f32 v233, v233\n"
    "v_fma_f32 v230, v230, v232, v247\n"
    "v_fmac_f32 v230, v231, v233\n"
    "s_add_u32 s66, s66, 16\n"
    "s_waitcnt lgkmcnt(0)\n"
    "ds_write_b16_d16_hi v245, v230\n"
    "ds_read_b64 v[228:229], v246\n"
    "s_lshl1_add_u32 s76, s72, s76\n"
    "buffer_load_dwordx4 v[236:237], s[40:43], s67 idxen offen offset:0 nt lds\n"
    "buffer_load_dwordx4 v[238:239], s[40:43], s67 idxen offen offset:1024 nt lds\n"
    "s_mov_b32 s81, s64\n"
    "s_lshr_b32 s63, s65, 6\n"
    "s_mov_b32 s80, s83\n"
    "s_add_u32 s62, s62, 256\n"
    "s_bitcmp1_b32 s60, 30\n"
    "s_cbranch_scc1 .Ldin_A1s\n"
    ".Ldin_A1:\n"
    "s_cbranch_vccz .Ldin_A1_np\n"
    "s_setprio 1\n"
    ".Ldin_A1_np:\n"
    "v_mfma_f32_16x16x32_bf16 v[160:163], v[0:3], v[192:195], v[144:147]\n"
    "v_exp_f32 v176, v176 clamp\n"
    "v_mfma_f32_16x16x32_bf16 v[160:163], v[4:7], v[196:199], v[160:163]\n"
    "v_exp_f32 v177, v177 clamp\n"
    "v_mfma_f32_16x16x32_bf16 v[164:167], v[8:11], v[192:195], v[148:151]\n"
    "v_exp_f32 v178, v178 clamp\n"
    "v_mfma_f32_16x16x32_bf16 v[164:167], v[12:15], v[196:199], v[164:167]\n"
    "v_exp_f32 v179, v179 clamp\n"
    "v_mfma_f32_16x16x32_bf16 v[168:171], v[16:19], v[192:195], v[152:155]\n"
    "v_exp_f32 v180, v180 clamp\n"
    "v_mfma_f32_16x16x32_bf16 v[168:171], v[20:23], v[196:199], v[168:171]\n"
    "v_exp_f32 v181, v181 clamp\n"
    "v_mfma_f32_16x16x32_bf16 v[172:175], v[24:27], v[192:195], v[156:159]\n"
    "v_exp_f32 v182, v182 clamp\n"
    "v_mfma_f32_16x16x32_bf16 v[172:175], v[28:31], v[196:199], v[172:175]\n"
    "v_exp_f32 v183, v183 clamp\n"
    "v_exp_f32 v184, v184 clamp\n"
    "s_mov_b32 m0, s62\n"
    "ds_read_b64 v[216:217], v246 offset:128\n"
    "v_exp_f32 v185, v185 clamp\n"
    "v_add_u32 v251, s66, v244\n"
    "s_waitcnt lgkmcnt(1)\n"
    "v_mfma_f32_4x4x4_16b_bf16 v[224:227], v[228:229], v[200:201], v[224:227] cbsz:4 abid:0\n"
    "v_exp_f32 v186, v186 clamp\n"
    "v_exp_f32 v187, v187 clamp\n"
    "v_mfma_f32_4x4x4_16b_bf16 v[224:227], v[228:229], v[202:203], v[224:227] cbsz:4 abid:1\n"
    "v_exp_f32 v188, v188 clamp\n"
    "v_exp_f32 v189, v189 clamp\n"
    "v_mfma_f32_4x4x4_16b_bf16 v[224:227], v[228:229], v[204:205], v[224:227] cbsz:4 abid:2\n"
    "v_exp_f32 v190, v190 clamp\n"
    "v_exp_f32 v191, v191 clamp\n"
    "v_mfma_f32_4x4x4_16b_bf16 v[224:227], v[228:229], v[206:207], v[224:227] cbsz:4 abid:3\n"
    "s_waitcnt vmcnt(4)\n"
    "s_waitcnt lgkmcnt(0)\n"
    "v_readlane_b32 s60, v216, s59\n"
    "v_readlane_b32 s61, v217, s59\n"
    "ds_read_b64_tr_b16 v[200:201], v240 offset:4096\n"
    "ds_read_b64_tr_b16 v[202:203], v241 offset:4096\n"
    "ds_read_b64_tr_b16 v[204:205], v242 offset:4096\n"
    "ds_read_b64_tr_b16 v[206:207], v243 offset:4096\n"
    "ds_read_u8 v236, v251 offset:0\n"
    "ds_read_u8 v238, v251 offset:1\n"
    "ds_read_b128 v[192:195], v234 offset:6144\n"
    "ds_read_b128 v[196:199], v235 offset:6144\n"
    "s_cmp_eq_u32 s60, -1\n"
    "s_cbranch_scc1 .Ldin_A1_poll\n"
    ".Ldin_A1_pr:\n"
    "s_lshr_b32 s94, s60, 12\n"
    "s_and_b32 s94, s94, 0x1ff0\n"
    "v_xor_b32 v252, s94, v250\n"
    "v_pk_add_f32 v[230:231], v[224:225], v[226:227]\n"
    "v_add_f32 v230, v230, v231\n"
    "ds_write_addtid_b32 v230 offset:49152\n"
    "ds_read_b128 v[216:219], v252 offset:0\n"
    "ds_read_b128 v[220:223], v252 offset:64\n"
    "v_mov_b64 v[224:225], 0\n"
    "v_mov_b64_e64 v[226:227], 0\n"
    "v_pk_fma_f32 v[176:177], v[176:177], s[70:71], v[128:129]\n"
    "v_pk_fma_f32 v[178:179], v[178:179], s[70:71], v[130:131]\n"
    "v_pk_add_f32 v[230:231], v[176:177], v[178:179]\n"
    "v_pk_mul_f32 v[232:233], v[176:177], v[178:179]\n"
    "v_pk_fma_f32 v[180:181], v[180:181], s[70:71], v[132:133]\n"
    "v_pk_fma_f32 v[230:231], v[230:231], v[180:181], v[232:233]\n"
    "v_pk_mul_f32 v[232:233], v[232:233], v[180:181]\n"
    "v_pk_fma_f32 v[182:183], v[182:183], s[70:71], v[134:135]\n"
    "v_pk_fma_f32 v[230:231], v[230:231], v[182:183], v[232:233]\n"
    "v_pk_mul_f32 v[232:233], v[232:233], v[182:183]\n"
    "v_pk_fma_f32 v[184:185], v[184:185], s[70:71], v[136:137]\n"
    "v_pk_fma_f32 v[230:231], v[230:231], v[184:185], v[232:233]\n"
    "v_pk_mul_f32 v[232:233], v[232:233], v[184:185]\n"
    "v_pk_fma_f32 v[186:187], v[186:187], s[70:71], v[138:139]\n"
    "v_pk_fma_f32 v[230:231], v[230:231], v[186:187], v[232:233]\n"
    "v_pk_mul_f32 v[232:233], v[232:233], v[186:187]\n"
    "v_pk_fma_f32 v[188:189], v[188:189], s[70:71], v[140:141]\n"
    "v_pk_fma_f32 v[230:231], v[230:231], v[188:189], v[232:233]\n"
    "v_pk_mul_f32 v[232:233], v[232:233], v[188:189]\n"
    "v_pk_fma_f32 v[190:191], v[190:191], s[70:71], v[142:143]\n"
    "v_pk_fma_f32 v[230:231], v[230:231], v[190:191], v[232:233]\n"
    "v_pk_mul_f32 v[232:233], v[232:233], v[190:191]\n"
    "s_add_u32 m0, s68, 4096\n"
    "s_and_b32 s64, s60, 0xffff\n"
    "s_bfe_u32 s94, s60, 0x30013\n"
    "s_bfe_u32 s95, s60, 0x50019\n"
    "s_mul_i32 s94, s94, s56\n"
    "s_lshl4_add_u32 s83, s95, s64\n"
    "s_add_u32 s65, s58, s94\n"
    "s_add_u32 s82, s64, 64\n"
    "s_add_u32 s58, s58, s57\n"
    "s_add_u32 s59, s59, 1\n"
    "s_sub_u32 s95, s61, s76\n"
    "s_lshr_b32 s94, s61, 16\n"
    "s_sext_i32_i16 s95, s95\n"
    "s_cmp_lt_i32 s95, -400\n"
    "s_cselect_b32 s94, 0, s94\n"
    "s_mul_i32 s95, s95, s94\n"
    "s_ashr_i32 s95, s95, 16\n"
    "s_max_i32 s95, s95, -10\n"
    "s_min_i32 s95, s95, 10\n"
    "s_add_u32 s72, s77, s95\n"
    "s_cmp_lt_i32 s60, 0\n"
    "s_cselect_b32 s72, 0, s72\n"
    "v_rcp_f32 v232, v232\n"
    "v_rcp_f32 v233, v233\n"
    "v_fma_f32 v230, v230, v232, v247\n"
    "v_fmac_f32 v230, v231, v233\n"
    "s_add_u32 s66, s66, 16\n"
    "s_waitcnt lgkmcnt(0)\n"
    "ds_write_b16_d16_hi v245, v230\n"
    "ds_read_b64 v[228:229], v246\n"
    "buffer_load_dwordx4 v[236:237], s[40:43], s67 idxen offen offset:0 nt lds\n"
    "buffer_load_dwordx4 v[238:239], s[40:43], s67 idxen offen offset:1024 nt lds\n"
    "s_branch .Ldin_G2\n"
    ".Ldin_A1_poll:\n"
    "s_sleep 1\n"
    "ds_read_b64 v[216:217], v246 offset:128\n"
    "s_waitcnt lgkmcnt(0)\n"
    "v_readlane_b32 s60, v216, s59\n"
    "v_readlane_b32 s61, v217, s59\n"
    "s_cmp_eq_u32 s60, -1\n"
    "s_cbranch_scc1 .Ldin_A1_poll\n"
    "s_branch .Ldin_A1_pr\n"
    ".Ldin_stub_L3:\n"
    "s_setpc_b64 s[34:35]\n"
    ".p2align 5\n"
    ".Ldin_L3:\n"
    "s_setprio 0\n"
    "s_cmp_lt_i32 s60, 0\n"
    "s_cbranch_scc1 .Ldin_E1\n"
    "ds_read_b128 v[144:147], v252 offset:8192\n"
    "ds_read_b128 v[148:151], v252 offset:8256\n"
    "ds_read_b128 v[152:155], v252 offset:16384\n"
    "ds_read_b128 v[156:159], v252 offset:16448\n"
    "s_add_u32 s76, s76, s73\n"
    "s_add_u32 m0, s68, 0\n"
    "v_and_b32 v216, v248, v216\n"
    "v_and_b32 v217, v249, v217\n"
    "v_and_b32 v218, v248, v218\n"
    "v_and_b32 v219, v249, v219\n"
    "v_mfma_f32_16x16x16_bf16 v[160:163], v[216:217], v[96:97], v[32:35]\n"
    "v_and_b32 v220, v248, v220\n"
    "v_and_b32 v221, v249, v221\n"
    "v_and_b32 v222, v248, v222\n"
    "v_mfma_f32_16x16x16_bf16 v[164:167], v[218:219], v[98:99], v[36:39]\n"
    "v_exp_f32 v176, v176 clamp\n"
    "v_and_b32 v223, v249, v223\n"
    "v_mfma_f32_16x16x16_bf16 v[168:171], v[220:221], v[100:101], v[40:43]\n"
    "v_add_u32_e64 v251, s66, v244\n"
    "v_cvt_pk_bf16_f32 v0, v160, v161\n"
    "v_cvt_pk_bf16_f32 v1, v162, v163\n"
    "s_waitcnt lgkmcnt(4)\n"
    "v_mfma_f32_16x16x16_bf16 v[172:175], v[222:223], v[102:103], v[44:47]\n"
    "v_exp_f32 v177, v177 clamp\n"
    "v_cvt_pk_bf16_f32 v2, v164, v165\n"
    "v_mfma_f32_4x4x4_16b_bf16 v[224:227], v[228:229], v[200:201], v[224:227] cbsz:4 abid:0\n"
    "v_cvt_pk_bf16_f32 v3, v166, v167\n"
    "v_mfma_f32_16x16x16_bf16 v[160:163], v[216:217], v[104:105], v[48:51]\n"
    "v_exp_f32 v178, v178 clamp\n"
    "v_cvt_pk_bf16_f32 v4, v168, v169\n"
    "v_mfma_f32_16x16x16_bf16 v[164:167], v[218:219], v[106:107], v[52:55]\n"
    "v_cvt_pk_bf16_f32 v5, v170, v171\n"
    "v_cvt_pk_bf16_f32 v6, v172, v173\n"
    "v_cvt_pk_bf16_f32 v7, v174, v175\n"
    "v_mfma_f32_16x16x16_bf16 v[168:171], v[220:221], v[108:109], v[56:59]\n"
    "v_exp_f32 v179, v179 clamp\n"
    "v_cvt_pk_bf16_f32 v8, v160, v161\n"
    "v_mfma_f32_4x4x4_16b_bf16 v[224:227], v[228:229], v[202:203], v[224:227] cbsz:4 abid:1\n"
    "v_cvt_pk_bf16_f32 v9, v162, v163\n"
    "v_mfma_f32_16x16x16_bf16 v[172:175], v[222:223], v[110:111], v[60:63]\n"
    "v_exp_f32 v180, v180 clamp\n"
    "v_cvt_pk_bf16_f32 v10, v164, v165\n"
    "v_mfma_f32_16x16x16_bf16 v[160:163], v[216:217], v[112:113], v[64:67]\n"
    "v_cvt_pk_bf16_f32 v11, v166, v167\n"
    "v_cvt_pk_bf16_f32 v12, v168, v169\n"
    "v_cvt_pk_bf16_f32 v13, v170, v171\n"
    "v_mfma_f32_16x16x16_bf16 v[164:167], v[218:219], v[114:115], v[68:71]\n"
    "v_exp_f32 v181, v181 clamp\n"
    "v_cvt_pk_bf16_f32 v14, v172, v173\n"
    "v_mfma_f32_4x4x4_16b_bf16 v[224:227], v[228:229], v[204:205], v[224:227] cbsz:4 abid:2\n"
    "v_cvt_pk_bf16_f32 v15, v174, v175\n"
    "v_mfma_f32_16x16x16_bf16 v[168:171], v[220:221], v[116:117], v[72:75]\n"
    "v_exp_f32 v182, v182 clamp\n"
    "v_cvt_pk_bf16_f32 v16, v160, v161\n"
    "v_mfma_f32_16x16x16_bf16 v[172:175], v[222:223], v[118:119], v[76:79]\n"
    "v_cvt_pk_bf16_f32 v17, v162, v163\n"
    "v_cvt_pk_bf16_f32 v18, v164, v165\n"
    "v_cvt_pk_bf16_f32 v19, v166, v167\n"
    "v_mfma_f32_16x16x16_bf16 v[160:163], v[216:217], v[120:121], v[80:83]\n"
    "v_exp_f32 v183, v183 clamp\n"
    "v_cvt_pk_bf16_f32 v20, v168, v169\n"
    "v_mfma_f32_4x4x4_16b_bf16 v[224:227], v[228:229], v[206:207], v[224:227] cbsz:4 abid:3\n"
    "v_cvt_pk_bf16_f32 v21, v170, v171\n"
    "v_mfma_f32_16x16x16_bf16 v[164:167], v[218:219], v[122:123], v[84:87]\n"
    "v_exp_f32 v184, v184 clamp\n"
    "v_cvt_pk_bf16_f32 v22, v172, v173\n"
    "v_mfma_f32_16x16x16_bf16 v[168:171], v[220:221], v[124:125], v[88:91]\n"
    "v_cvt_pk_bf16_f32 v23, v174, v175\n"
    "v_cvt_pk_bf16_f32 v24, v160, v161\n"
    "v_cvt_pk_bf16_f32 v25, v162, v163\n"
    "v_mfma_f32_16x16x16_bf16 v[172:175], v[222:223], v[126:127], v[92:95]\n"
    "v_exp_f32 v185, v185 clamp\n"
    "v_cvt_pk_bf16_f32 v26, v164, v165\n"
    "s_waitcnt lgkmcnt(0)\n"
    "v_mfma_f32_16x16x32_bf16 v[160:163], v[0:3], v[192:195], v[144:147]\n"
    "v_cvt_pk_bf16_f32 v27, v166, v167\n"
    "v_cvt_pk_bf16_f32 v28, v168, v169\n"
    "v_mfma_f32_16x16x32_bf16 v[160:163], v[4:7], v[196:199], v[160:163]\n"
    "v_cvt_pk_bf16_f32 v29, v170, v171\n"
    "v_cvt_pk_bf16_f32 v30, v172, v173\n"
    "v_mfma_f32_16x16x32_bf16 v[164:167], v[8:11], v[192:195], v[148:151]\n"
    "v_exp_f32 v186, v186 clamp\n"
    "v_mfma_f32_16x16x32_bf16 v[164:167], v[12:15], v[196:199], v[164:167]\n"
    "v_exp_f32 v187, v187 clamp\n"
    "v_mfma_f32_16x16x32_bf16 v[168:171], v[16:19], v[192:195], v[152:155]\n"
    "v_exp_f32 v188, v188 clamp\n"
    "v_mfma_f32_16x16x32_bf16 v[168:171], v[20:23], v[196:199], v[168:171]\n"
    "v_exp_f32 v189, v189 clamp\n"
    "v_cvt_pk_bf16_f32 v31, v174, v175\n"
    "v_mfma_f32_16x16x32_bf16 v[172:175], v[24:27], v[192:195], v[156:159]\n"
    "v_exp_f32 v190, v190 clamp\n"
    "v_mfma_f32_16x16x32_bf16 v[172:175], v[28:31], v[196:199], v[172:175]\n"
    "v_exp_f32 v191, v191 clamp\n"
    "s_waitcnt vmcnt(4)\n"
    "ds_read_b64_tr_b16 v[200:201], v240 offset:0\n"
    "ds_read_b64_tr_b16 v[202:203], v241 offset:0\n"
    "ds_read_b64_tr_b16 v[204:205], v242 offset:0\n"
    "ds_read_b64_tr_b16 v[206:207], v243 offset:0\n"
    "ds_read_u8 v236, v251 offset:0\n"
    "ds_read_u8 v238, v251 offset:1\n"
    "ds_read_b128 v[192:195], v234 offset:2048\n"
    "ds_read_b128 v[196:199], v235 offset:2048\n"
    "v_pk_fma_f32 v[176:177], v[176:177], s[70:71], v[128:129]\n"
    "v_pk_fma_f32 v[178:179], v[178:179], s[70:71], v[130:131]\n"
    "v_pk_add_f32 v[230:231], v[176:177], v[178:179]\n"
    "v_pk_mul_f32 v[232:233], v[176:177], v[178:179]\n"
    "v_pk_fma_f32 v[180:181], v[180:181], s[70:71], v[132:133]\n"
    "v_pk_fma_f32 v[230:231], v[230:231], v[180:181], v[232:233]\n"
    "v_pk_mul_f32 v[232:233], v[232:233], v[180:181]\n"
    "v_pk_fma_f32 v[182:183], v[182:183], s[70:71], v[134:135]\n"
    "v_pk_fma_f32 v[230:231], v[230:231], v[182:183], v[232:233]\n"
    "v_pk_mul_f32 v[232:233], v[232:233], v[182:183]\n"
    "v_pk_fma_f32 v[184:185], v[184:185], s[70:71], v[136:137]\n"
    "v_pk_fma_f32 v[230:231], v[230:231], v[184:185], v[232:233]\n"
    "v_pk_mul_f32 v[232:233], v[232:233], v[184:185]\n"
    "v_pk_fma_f32 v[186:187], v[186:187], s[70:71], v[138:139]\n"
    "v_pk_fma_f32 v[230:231], v[230:231], v[186:187], v[232:233]\n"
    "v_pk_mul_f32 v[232:233], v[232:233], v[186:187]\n"
    "v_pk_fma_f32 v[188:189], v[188:189], s[70:71], v[140:141]\n"
    "v_pk_fma_f32 v[230:231], v[230:231], v[188:189], v[232:233]\n"
    "v_pk_mul_f32 v[232:233], v[232:233], v[188:189]\n"
    "v_pk_fma_f32 v[190:191], v[190:191], s[70:71], v[142:143]\n"
    "v_pk_fma_f32 v[230:231], v[230:231], v[190:191], v[232:233]\n"
    "v_pk_mul_f32 v[232:233], v[232:233], v[190:191]\n"
    "v_rcp_f32 v232, v232\n"
    "v_rcp_f32 v233, v233\n"
    "v_fma_f32 v230, v230, v232, v247\n"
    "v_fmac_f32 v230, v231, v233\n"
    "s_add_u32 s66, s66, 16\n"
    "s_waitcnt lgkmcnt(0)\n"
    "ds_write_b16_d16_hi v245, v230\n"
    "ds_read_b64 v[228:229], v246\n"
    "s_lshl1_add_u32 s76, s72, s76\n"
    "buffer_load_dwordx4 v[236:237], s[40:43], s67 idxen offen offset:0 nt lds\n"
    "buffer_load_dwordx4 v[238:239], s[40:43], s67 idxen offen offset:1024 nt lds\n"
    "s_mov_b32 s81, s64\n"
    "s_lshr_b32 s63, s65, 6\n"
    "s_mov_b32 s80, s83\n"
    "s_add_u32 s62, s62, 256\n"
    "s_bitcmp1_b32 s60, 30\n"
    "s_cbranch_scc1 .Ldin_A0s\n"
    "s_branch .Ldin_A0\n"
    ".p2align 12\n"
    ".Ldin_py:\n"
    "v_and_b32 v32, 0x58, v4\n"
    "v_lshlrev_b32 v33, 1, v2\n"
    "v_and_b32 v34, 0x3c0, v251\n"
    "v_bitop3_b32 v35, v33, v0, 32 bitop3:0x28\n"
    "v_lshl_or_b32 v34, v34, 1, v32\n"
    "s_lshl_b32 s23, s21, 11\n"
    "s_add_u32 s23, s23, 54144\n"
    "v_add3_u32 v52, v34, v35, s23\n"
    "v_and_or_b32 v32, v33, 32, v32\n"
    "v_xor_b32 v53, 32, v52\n"
    "v_xor_b32 v54, 64, v52\n"
    "v_xor_b32 v55, 96, v52\n"
    "v_and_b32 v34, 0xc0, v251\n"
    "v_lshl_or_b32 v32, v2, 6, v32\n"
    "s_lshl_b32 s23, s21, 5\n"
    "v_lshl_or_b32 v32, v34, 1, v32\n"
    "v_and_b32 v58, 0x7bf, v36\n"
    "v_xor_b32 v32, s23, v32\n"
    "s_mul_i32 s23, s21, 0x1040\n"
    "v_add_u32 v56, 54144, v32\n"
    "s_and_b32 s23, s23, 0x2040\n"
    "v_xor_b32 v57, 32, v56\n"
    "v_add_u32 v59, s23, v58\n"
    "v_and_b32 v32, 0xf0, v251\n"
    "v_and_b32 v33, 32, v0\n"
    "v_and_b32 v34, 16, v0\n"
    "v_lshl_or_b32 v32, v33, 3, v32\n"
    "s_lshl_b32 s24, s21, 9\n"
    "s_and_b32 s23, s24, 0x400\n"
    "v_lshl_or_b32 v60, v34, 6, v32\n"
    "s_add_u32 s23, s23, s24\n"
    "v_lshrrev_b32 v34, 1, v34\n"
    "v_or_b32 v60, s23, v60\n"
    "v_or3_b32 v61, v32, v34, s24\n"
    "v_and_b32 v62, 0x3f803f80, v248\n"
    "v_and_b32 v63, 0x3f803f80, v249\n"
    "v_lshrrev_b32 v32, 5, v1\n"
    "s_mul_i32 s23, s21, 544\n"
    "v_mul_u32_u24 v12, 272, v32\n"
    "v_mad_u32_u24 v64, v32, s14, v3\n"
    "v_add3_u32 v12, v12, s23, 15\n"
    "s_not_b32 exec_lo, s16\n"
    "s_not_b32 exec_hi, s16\n"
    "v_bfrev_b32 v64, 1\n"
    "s_mov_b64 exec, -1\n"
    "v_add_u32 v13, v13, v12\n"
    "s_lshl1_add_u32 s23, s21, s78\n"
    "s_mul_i32 s23, s23, s14\n"
    "s_lshl1_add_u32 s22, s15, s23\n"
    "s_mov_b32 s26, 0xbfb8aa3b\n"
    "v_bfrev_b32 v35, 1\n"
    "s_waitcnt vmcnt(5)\n"
    "v_mul_f32 v32, 0x35800000, v44\n"
    "v_max3_f32 v33, |v40|, |v41|, |v42|\n"
    "v_max3_f32 v33, v33, |v43|, |v32|\n"
    "v_lshlrev_b32 v76, 16, v46\n"
    "v_and_b32 v77, 0xffff0000, v46\n"
    "v_max_f32_dpp v33, v33, v33 quad_perm:[1,0,3,2] row_mask:0xf bank_mask:0xf bound_ctrl:1\n"
    "v_lshlrev_b32_e64 v78, 16, v47\n"
    "v_and_b32 v79, 0xffff0000, v47\n"
    "v_max_f32_dpp v33, v33, v33 quad_perm:[2,3,0,1] row_mask:0xf bank_mask:0xf bound_ctrl:1\n"
    "v_bitop3_b32 v68, v76, s26, v35 bitop3:0x6c\n"
    "v_bitop3_b32 v69, v77, s26, v35 bitop3:0x6c\n"
    "v_max_f32_dpp v33, v33, v33 row_half_mirror row_mask:0xf bank_mask:0xf bound_ctrl:1\n"
    "v_bitop3_b32 v70, v78, s26, v35 bitop3:0x6c\n"
    "v_bitop3_b32 v71, v79, s26, v35 bitop3:0x6c\n"
    "v_max_f32_dpp v33, v33, v33 row_mirror row_mask:0xf bank_mask:0xf bound_ctrl:1\n"
    "v_rcp_f32_e64 v76, |v76|\n"
    "v_rcp_f32_e64 v77, |v77|\n"
    "v_rcp_f32_e64 v78, |v78|\n"
    "v_rcp_f32_e64 v79, |v79|\n"
    "v_lshrrev_b32 v33, 23, v33\n"
    "s_movk_i32 s23, 253\n"
    "v_med3_u32 v33, v33, 15, s23\n"
    "v_add_lshl_u32 v33, v33, -14, 23\n"
    "s_lshl_b32 s23, s21, 6\n"
    "v_pk_mul_f32 v[76:77], v[76:77], v[32:33] op_sel:[0,1] op_sel_hi:[1,1]\n"
    "v_pk_mul_f32 v[78:79], v[78:79], v[32:33] op_sel:[0,1] op_sel_hi:[1,1]\n"
    "v_or_b32 v34, s23, v2\n"
    "v_min_f32 v76, 0x46800000, v76\n"
    "v_min_f32 v77, 0x46800000, v77\n"
    "v_min_f32 v78, 0x46800000, v78\n"
    "v_min_f32 v79, 0x46800000, v79\n"
    "ds_write_b128 v34, v[76:79] offset:49152\n"
    "v_log_f32 v72, v76\n"
    "v_log_f32 v73, v77\n"
    "v_log_f32 v74, v78\n"
    "v_log_f32 v75, v79\n"
    "v_bitop3_b32 v80, v40, s26, v35 bitop3:0x6c\n"
    "v_bitop3_b32 v81, v41, s26, v35 bitop3:0x6c\n"
    "v_bitop3_b32 v82, v42, s26, v35 bitop3:0x6c\n"
    "v_bitop3_b32 v83, v43, s26, v35 bitop3:0x6c\n"
    "s_cmp_eq_u32 s21, 0\n"
    "v_add_f32 v72, 0xc1700000, v72\n"
    "v_add_f32 v73, 0xc1700000, v73\n"
    "v_add_f32 v74, 0xc1700000, v74\n"
    "v_add_f32 v75, 0xc1700000, v75\n"
    "v_lshlrev_b32 v84, 16, v48\n"
    "v_and_b32 v85, 0xffff0000, v48\n"
    "v_lshlrev_b32 v86, 16, v49\n"
    "v_and_b32 v87, 0xffff0000, v49\n"
    "s_cbranch_scc0 .Ldin_cg_r\n"
    "v_min_f32 v40, 0, v40\n"
    "v_min_f32 v41, 0, v41\n"
    "v_min_f32 v42, 0, v42\n"
    "v_min_f32 v43, 0, v43\n"
    "v_pk_add_f32 v[40:41], v[40:41], v[42:43]\n"
    "v_sub_u32 v34, 0x7f000000, v33\n"
    "v_add_f32 v32, v40, v41\n"
    "v_and_b32 v42, 12, v0\n"
    "v_lshlrev_b32 v42, 2, v42\n"
    "v_add_f32_dpp v32, v32, v32 quad_perm:[1,0,3,2] row_mask:0xf bank_mask:0xf bound_ctrl:1\n"
    "v_or_b32 v33, 23600, v33\n"
    "s_nop 0\n"
    "v_add_f32_dpp v32, v32, v32 quad_perm:[2,3,0,1] row_mask:0xf bank_mask:0xf bound_ctrl:1\n"
    "s_mov_b64 exec, 15\n"
    "v_add_f32 v32, v32, v44\n"
    "s_mov_b64 exec, 0xffff\n"
    "v_mul_f32_e64 v32, v32, v34\n"
    "ds_write_b64 v42, v[32:33] offset:49408\n"
    "s_mov_b64 exec, -1\n"
    ".Ldin_cg_r:\n"
    "s_waitcnt vmcnt(0) lgkmcnt(0)\n"
    "buffer_load_dwordx4 v[96:99], v64, s[44:47], s22 offen nt\n"
    "s_add_u32 s22, s22, s15\n"
    "buffer_load_dwordx4 v[100:103], v64, s[44:47], s22 offen nt\n"
    "s_add_u32 s22, s22, s15\n"
    "buffer_load_dwordx4 v[104:107], v64, s[44:47], s22 offen nt\n"
    "s_add_u32 s22, s22, s15\n"
    "buffer_load_dwordx4 v[108:111], v64, s[44:47], s22 offen nt\n"
    "s_add_u32 s22, s22, s15\n"
    "buffer_load_dwordx4 v[112:115], v64, s[44:47], s22 offen nt\n"
    "s_add_u32 s22, s22, s15\n"
    "buffer_load_dwordx4 v[116:119], v64, s[44:47], s22 offen nt\n"
    "s_barrier\n"
    "ds_read_b64_tr_b16 v[120:121], v52 offset:0\n"
    "ds_read_b64_tr_b16 v[122:123], v52 offset:16384\n"
    "ds_read_b64_tr_b16 v[124:125], v52 offset:24576\n"
    "ds_read_b64_tr_b16 v[126:127], v53 offset:0\n"
    "ds_read_b64_tr_b16 v[128:129], v53 offset:16384\n"
    "ds_read_b64_tr_b16 v[130:131], v53 offset:24576\n"
    "ds_read_b64_tr_b16 v[132:133], v54 offset:0\n"
    "ds_read_b64_tr_b16 v[134:135], v54 offset:16384\n"
    "ds_read_b64_tr_b16 v[136:137], v54 offset:24576\n"
    "ds_read_b64_tr_b16 v[138:139], v55 offset:0\n"
    "ds_read_b64_tr_b16 v[140:141], v55 offset:16384\n"
    "ds_read_b64_tr_b16 v[142:143], v55 offset:24576\n"
    "s_waitcnt lgkmcnt(9)\n"
    "v_mfma_f32_16x16x16_bf16 v[192:195], v[62:63], v[120:121], 0\n"
    "v_mfma_f32_16x16x16_bf16 v[192:195], v[62:63], v[122:123], v[192:195]\n"
    "v_mfma_f32_16x16x16_bf16 v[196:199], v[62:63], v[124:125], 0\n"
    "ds_read_b64_tr_b16 v[144:145], v56 offset:8192\n"
    "ds_read_b64_tr_b16 v[146:147], v57 offset:8704\n"
    "ds_read_b64_tr_b16 v[148:149], v56 offset:12288\n"
    "ds_read_b64_tr_b16 v[150:151], v57 offset:12800\n"
    "s_waitcnt lgkmcnt(10)\n"
    "v_mfma_f32_16x16x16_bf16 v[200:203], v[62:63], v[126:127], 0\n"
    "v_mfma_f32_16x16x16_bf16 v[200:203], v[62:63], v[128:129], v[200:203]\n"
    "v_mfma_f32_16x16x16_bf16 v[204:207], v[62:63], v[130:131], 0\n"
    "ds_read_b128 v[160:163], v58 offset:24576\n"
    "ds_read_b128 v[164:167], v58 offset:24640\n"
    "s_waitcnt lgkmcnt(9)\n"
    "v_mfma_f32_16x16x16_bf16 v[208:211], v[62:63], v[132:133], 0\n"
    "v_mfma_f32_16x16x16_bf16 v[208:211], v[62:63], v[134:135], v[208:211]\n"
    "v_mfma_f32_16x16x16_bf16 v[212:215], v[62:63], v[136:137], 0\n"
    "ds_read_b64_tr_b16 v[152:153], v56 offset:16384\n"
    "ds_read_b64_tr_b16 v[154:155], v57 offset:16896\n"
    "ds_read_b64_tr_b16 v[156:157], v56 offset:20480\n"
    "ds_read_b64_tr_b16 v[158:159], v57 offset:20992\n"
    "s_waitcnt lgkmcnt(10)\n"
    "v_mfma_f32_16x16x16_bf16 v[216:219], v[62:63], v[138:139], 0\n"
    "v_mfma_f32_16x16x16_bf16 v[216:219], v[62:63], v[140:141], v[216:219]\n"
    "v_mfma_f32_16x16x16_bf16 v[220:223], v[62:63], v[142:143], 0\n"
    "s_waitcnt lgkmcnt(4)\n"
    "v_mfma_f32_16x16x32_bf16 v[224:227], v[144:147], v[160:163], v[84:87]\n"
    "v_mfma_f32_16x16x32_bf16 v[224:227], v[148:151], v[164:167], v[224:227]\n"
    "s_waitcnt lgkmcnt(0)\n"
    "v_mfma_f32_16x16x32_bf16 v[228:231], v[152:155], v[160:163], 0\n"
    "v_mfma_f32_16x16x32_bf16 v[228:231], v[156:159], v[164:167], v[228:231]\n"
    "v_pk_mul_f32 v[192:193], v[192:193], v[80:81] op_sel_hi:[1,0]\n"
    "v_pk_mul_f32 v[194:195], v[194:195], v[80:81] op_sel_hi:[1,0]\n"
    "ds_write_b128 v60, v[192:195] offset:0\n"
    "v_pk_mul_f32 v[196:197], v[196:197], v[80:81] op_sel_hi:[1,0]\n"
    "v_pk_mul_f32 v[198:199], v[198:199], v[80:81] op_sel_hi:[1,0]\n"
    "v_cvt_pk_bf16_f32 v196, v196, v197\n"
    "v_cvt_pk_bf16_f32 v197, v198, v199\n"
    "ds_write_b64 v61, v[196:197] offset:16384\n"
    "v_pk_mul_f32 v[200:201], v[200:201], v[80:81] op_sel:[0,1] op_sel_hi:[1,1]\n"
    "v_pk_mul_f32 v[202:203], v[202:203], v[80:81] op_sel:[0,1] op_sel_hi:[1,1]\n"
    "ds_write_b128 v60, v[200:203] offset:4096\n"
    "v_pk_mul_f32 v[204:205], v[204:205], v[80:81] op_sel:[0,1] op_sel_hi:[1,1]\n"
    "v_pk_mul_f32 v[206:207], v[206:207], v[80:81] op_sel:[0,1] op_sel_hi:[1,1]\n"
    "v_cvt_pk_bf16_f32 v204, v204, v205\n"
    "v_cvt_pk_bf16_f32 v205, v206, v207\n"
    "ds_write_b64 v61, v[204:205] offset:18432\n"
    "v_pk_mul_f32 v[208:209], v[208:209], v[82:83] op_sel_hi:[1,0]\n"
    "v_pk_mul_f32 v[210:211], v[210:211], v[82:83] op_sel_hi:[1,0]\n"
    "ds_write_b128 v60, v[208:211] offset:8192\n"
    "v_pk_mul_f32 v[212:213], v[212:213], v[82:83] op_sel_hi:[1,0]\n"
    "v_pk_mul_f32 v[214:215], v[214:215], v[82:83] op_sel_hi:[1,0]\n"
    "v_cvt_pk_bf16_f32 v212, v212, v213\n"
    "v_cvt_pk_bf16_f32 v213, v214, v215\n"
    "ds_write_b64 v61, v[212:213] offset:20480\n"
    "v_pk_mul_f32 v[216:217], v[216:217], v[82:83] op_sel:[0,1] op_sel_hi:[1,1]\n"
    "v_pk_mul_f32 v[218:219], v[218:219], v[82:83] op_sel:[0,1] op_sel_hi:[1,1]\n"
    "ds_write_b128 v60, v[216:219] offset:12288\n"
    "v_pk_mul_f32 v[220:221], v[220:221], v[82:83] op_sel:[0,1] op_sel_hi:[1,1]\n"
    "v_pk_mul_f32 v[222:223], v[222:223], v[82:83] op_sel:[0,1] op_sel_hi:[1,1]\n"
    "v_cvt_pk_bf16_f32 v220, v220, v221\n"
    "v_cvt_pk_bf16_f32 v221, v222, v223\n"
    "ds_write_b64 v61, v[220:221] offset:22528\n"
    "v_pk_add_f32 v[224:225], v[224:225], v[228:229] neg_lo:[0,1] neg_hi:[0,1]\n"
    "v_pk_add_f32 v[226:227], v[226:227], v[230:231] neg_lo:[0,1] neg_hi:[0,1]\n"
    "v_pk_fma_f32 v[224:225], v[224:225], v[68:69], v[72:73]\n"
    "v_pk_fma_f32 v[226:227], v[226:227], v[70:71], v[74:75]\n"
    "ds_write_b128 v59, v[224:227] offset:32768\n"
    "s_waitcnt lgkmcnt(0)\n"
    "s_barrier\n"
    "s_setprio 0\n"
    "s_mul_i32 s23, s20, 272\n"
    "v_add_u32 v32, s23, v244\n"
    "ds_read_u8 v16, v32 offset:112\n"
    "ds_read_u8 v18, v32 offset:113\n"
    "ds_read_u8 v20, v32 offset:128\n"
    "ds_read_u8 v22, v32 offset:129\n"
    "ds_read_u8 v24, v32 offset:144\n"
    "ds_read_u8 v26, v32 offset:145\n"
    "ds_read_u8 v28, v32 offset:160\n"
    "ds_read_u8 v30, v32 offset:161\n"
    "ds_read_b32 v33, v246 offset:128\n"
    "ds_read_b128 v[168:171], v58 offset:26624\n"
    "ds_read_b128 v[172:175], v58 offset:26688\n"
    "ds_read_b128 v[176:179], v58 offset:28672\n"
    "ds_read_b128 v[180:183], v58 offset:28736\n"
    "ds_read_b128 v[184:187], v58 offset:30720\n"
    "ds_read_b128 v[188:191], v58 offset:30784\n"
    "s_add_u32 m0, s68, 0\n"
    "s_waitcnt lgkmcnt(13)\n"
    "buffer_load_dwordx4 v[16:17], s[40:43], s67 idxen offen offset:0 nt lds\n"
    "buffer_load_dwordx4 v[18:19], s[40:43], s67 idxen offen offset:1024 nt lds\n"
    "s_add_u32 m0, s68, 2048\n"
    "s_waitcnt lgkmcnt(11)\n"
    "buffer_load_dwordx4 v[20:21], s[40:43], s67 idxen offen offset:0 nt lds\n"
    "buffer_load_dwordx4 v[22:23], s[40:43], s67 idxen offen offset:1024 nt lds\n"
    "s_add_u32 m0, s68, 4096\n"
    "s_waitcnt lgkmcnt(9)\n"
    "buffer_load_dwordx4 v[24:25], s[40:43], s67 idxen offen offset:0 nt lds\n"
    "buffer_load_dwordx4 v[26:27], s[40:43], s67 idxen offen offset:1024 nt lds\n"
    "s_add_u32 m0, s68, 6144\n"
    "s_waitcnt lgkmcnt(7)\n"
    "s_nop 0\n"
    "buffer_load_dwordx4 v[28:29], s[40:43], s67 idxen offen offset:0 nt lds\n"
    "buffer_load_dwordx4 v[30:31], s[40:43], s67 idxen offen offset:1024 nt lds\n"
    "ds_write_b64 v13, v[14:15] offset:54112\n"
    "ds_write_b64 v13, v[14:15] offset:56288\n"
    "ds_write_b64 v13, v[14:15] offset:58464\n"
    "ds_write_b64 v13, v[14:15] offset:60640\n"
    "ds_write_b64 v13, v[14:15] offset:62816\n"
    "ds_write_b64 v13, v[14:15] offset:64992\n"
    "s_waitcnt lgkmcnt(6)\n"
    "v_readfirstlane_b32 s60, v33\n"
    "v_mfma_f32_16x16x32_bf16 v[120:123], v[144:147], v[168:171], v[84:87]\n"
    "v_mfma_f32_16x16x32_bf16 v[120:123], v[148:151], v[172:175], v[120:123]\n"
    "v_mfma_f32_16x16x32_bf16 v[124:127], v[152:155], v[168:171], 0\n"
    "v_mfma_f32_16x16x32_bf16 v[124:127], v[156:159], v[172:175], v[124:127]\n"
    "v_mfma_f32_16x16x32_bf16 v[128:131], v[144:147], v[176:179], v[84:87]\n"
    "v_mfma_f32_16x16x32_bf16 v[128:131], v[148:151], v[180:183], v[128:131]\n"
    "v_mfma_f32_16x16x32_bf16 v[132:135], v[152:155], v[176:179], 0\n"
    "v_mfma_f32_16x16x32_bf16 v[132:135], v[156:159], v[180:183], v[132:135]\n"
    "v_mfma_f32_16x16x32_bf16 v[136:139], v[144:147], v[184:187], v[84:87]\n"
    "v_mfma_f32_16x16x32_bf16 v[136:139], v[148:151], v[188:191], v[136:139]\n"
    "v_mfma_f32_16x16x32_bf16 v[140:143], v[152:155], v[184:187], 0\n"
    "v_mfma_f32_16x16x32_bf16 v[140:143], v[156:159], v[188:191], v[140:143]\n"
    "s_and_b32 s23, s14, 14\n"
    "s_waitcnt vmcnt(8)\n"
    "s_cbranch_scc1 .Ldin_trimy\n"
    ".Ldin_trimy_r:\n"
    "v_pk_min_u16 v192, v96, 1 op_sel_hi:[1,0]\n"
    "v_pk_min_u16 v193, v97, 1 op_sel_hi:[1,0]\n"
    "v_pk_min_u16 v194, v98, 1 op_sel_hi:[1,0]\n"
    "v_pk_min_u16 v195, v99, 1 op_sel_hi:[1,0]\n"
    "v_add3_u32 v44, v192, v193, v194\n"
    "v_add_u32 v44, v44, v195\n"
    "v_dot2_u32_u16 v40, v44, s31, 0\n"
    "v_pk_min_u16 v196, v100, 1 op_sel_hi:[1,0]\n"
    "v_pk_min_u16 v197, v101, 1 op_sel_hi:[1,0]\n"
    "v_pk_min_u16 v198, v102, 1 op_sel_hi:[1,0]\n"
    "v_pk_min_u16 v199, v103, 1 op_sel_hi:[1,0]\n"
    "v_add3_u32 v44, v196, v197, v198\n"
    "v_add_u32_e64 v44, v44, v199\n"
    "v_dot2_u32_u16 v40, v44, s30, v40\n"
    "v_pk_min_u16 v200, v104, 1 op_sel_hi:[1,0]\n"
    "v_pk_min_u16 v201, v105, 1 op_sel_hi:[1,0]\n"
    "v_pk_min_u16 v202, v106, 1 op_sel_hi:[1,0]\n"
    "v_pk_min_u16 v203, v107, 1 op_sel_hi:[1,0]\n"
    "v_add3_u32 v42, v200, v201, v202\n"
    "v_add_u32 v42, v42, v203\n"
    "v_lshl_add_u32 v42, v42, 16, v42\n"
    "v_and_b32 v42, 0xffff0000, v42\n"
    "v_or_b32 v40, v40, v42\n"
    "v_pk_min_u16 v204, v108, 1 op_sel_hi:[1,0]\n"
    "v_pk_min_u16 v205, v109, 1 op_sel_hi:[1,0]\n"
    "v_pk_min_u16 v206, v110, 1 op_sel_hi:[1,0]\n"
    "v_pk_min_u16 v207, v111, 1 op_sel_hi:[1,0]\n"
    "v_add3_u32 v45, v204, v205, v206\n"
    "v_add_u32_e64 v45, v45, v207\n"
    "v_dot2_u32_u16 v41, v45, s31, 0\n"
    "v_pk_min_u16 v208, v112, 1 op_sel_hi:[1,0]\n"
    "v_pk_min_u16 v209, v113, 1 op_sel_hi:[1,0]\n"
    "v_pk_min_u16 v210, v114, 1 op_sel_hi:[1,0]\n"
    "v_pk_min_u16 v211, v115, 1 op_sel_hi:[1,0]\n"
    "v_add3_u32 v45, v208, v209, v210\n"
    "v_add_u32_e64 v45, v45, v211\n"
    "v_dot2_u32_u16 v41, v45, s30, v41\n"
    "v_pk_min_u16 v212, v116, 1 op_sel_hi:[1,0]\n"
    "v_pk_min_u16 v213, v117, 1 op_sel_hi:[1,0]\n"
    "v_pk_min_u16 v214, v118, 1 op_sel_hi:[1,0]\n"
    "v_pk_min_u16 v215, v119, 1 op_sel_hi:[1,0]\n"
    "v_add3_u32 v43, v212, v213, v214\n"
    "v_add_u32 v43, v43, v215\n"
    "v_lshl_add_u32 v43, v43, 16, v43\n"
    "v_and_b32 v43, 0xffff0000, v43\n"
    "v_or_b32 v41, v41, v43\n"
    "v_pk_add_f32 v[120:121], v[120:121], v[124:125] neg_lo:[0,1] neg_hi:[0,1]\n"
    "v_pk_add_f32 v[122:123], v[122:123], v[126:127] neg_lo:[0,1] neg_hi:[0,1]\n"
    "v_pk_fma_f32 v[120:121], v[120:121], v[68:69], v[72:73]\n"
    "v_pk_fma_f32 v[122:123], v[122:123], v[70:71], v[74:75]\n"
    "ds_write_b128 v59, v[120:123] offset:34816\n"
    "v_pk_add_f32 v[128:129], v[128:129], v[132:133] neg_lo:[0,1] neg_hi:[0,1]\n"
    "v_pk_add_f32 v[130:131], v[130:131], v[134:135] neg_lo:[0,1] neg_hi:[0,1]\n"
    "v_pk_fma_f32 v[128:129], v[128:129], v[68:69], v[72:73]\n"
    "v_pk_fma_f32 v[130:131], v[130:131], v[70:71], v[74:75]\n"
    "ds_write_b128 v59, v[128:131] offset:36864\n"
    "v_pk_add_f32 v[136:137], v[136:137], v[140:141] neg_lo:[0,1] neg_hi:[0,1]\n"
    "v_pk_add_f32 v[138:139], v[138:139], v[142:143] neg_lo:[0,1] neg_hi:[0,1]\n"
    "v_pk_fma_f32 v[136:137], v[136:137], v[68:69], v[72:73]\n"
    "v_pk_fma_f32 v[138:139], v[138:139], v[70:71], v[74:75]\n"
    "ds_write_b128 v59, v[136:139] offset:38912\n"
    "v_sub_u32 v44, 0x10101, v40\n"
    "v_sub_u32 v45, 0x10101, v41\n"
    "v_add_u32_dpp v42, v40, v40 row_shr:1 row_mask:0xf bank_mask:0xf bound_ctrl:1\n"
    "v_add_u32_dpp v43, v41, v41 row_shr:1 row_mask:0xf bank_mask:0xf bound_ctrl:1\n"
    "v_pk_mad_u16 v96, v192, s28, v96\n"
    "v_add_u32_dpp v42, v42, v42 row_shr:2 row_mask:0xf bank_mask:0xf bound_ctrl:1\n"
    "v_add_u32_dpp v43, v43, v43 row_shr:2 row_mask:0xf bank_mask:0xf bound_ctrl:1\n"
    "v_pk_mad_u16 v97, v193, s28, v97\n"
    "v_add_u32_dpp v42, v42, v42 row_shr:4 row_mask:0xf bank_mask:0xf bound_ctrl:1\n"
    "v_add_u32_dpp v43, v43, v43 row_shr:4 row_mask:0xf bank_mask:0xf bound_ctrl:1\n"
    "v_pk_mad_u16 v98, v194, s28, v98\n"
    "v_add_u32_dpp v42, v42, v42 row_shr:8 row_mask:0xf bank_mask:0xf bound_ctrl:1\n"
    "v_add_u32_dpp v43, v43, v43 row_shr:8 row_mask:0xf bank_mask:0xf bound_ctrl:1\n"
    "v_pk_mad_u16 v99, v195, s28, v99\n"
    "v_add_u32_dpp v42, v42, v42 row_bcast:15 row_mask:0xa bank_mask:0xf\n"
    "v_add_u32_dpp v43, v43, v43 row_bcast:15 row_mask:0xa bank_mask:0xf\n"
    "v_or3_b32 v46, v96, v97, 0\n"
    "v_add_u32 v44, v44, v42\n"
    "v_add_u32 v45, v45, v43\n"
    "v_lshrrev_b32 v32, 3, v1\n"
    "v_and_b32_e64 v32, 4, v32\n"
    "v_lshl_add_u32 v32, s21, 3, v32\n"
    "s_brev_b32 exec_lo, 1\n"
    "s_brev_b32 exec_hi, 1\n"
    "ds_write_b32 v32, v42 offset:49472\n"
    "ds_write_b32 v32, v43 offset:49504\n"
    "s_mov_b64 exec, -1\n"
    "s_mov_b32 s26, 0xc040c00\n"
    "v_perm_b32 v47, v42, v44, s26\n"
    "v_mad_u32_u16 v48, v192, v47, v12\n"
    "v_mad_u32_u16 v49, v192, v47, v12 op_sel:[1,1,0,0]\n"
    "v_pk_mad_u16 v47, v192, s29, v47\n"
    "ds_write_b8 v48, v8 offset:54112\n"
    "ds_write_b8_d16_hi v49, v8 offset:54112\n"
    "v_mad_u32_u16 v50, v193, v47, v12\n"
    "v_mad_u32_u16 v51, v193, v47, v12 op_sel:[1,1,0,0]\n"
    "v_pk_mad_u16 v47, v193, s29, v47\n"
    "ds_write_b8 v50, v9 offset:54112\n"
    "ds_write_b8_d16_hi v51, v9 offset:54112\n"
    "v_mad_u32_u16 v48, v194, v47, v12\n"
    "v_mad_u32_u16 v49, v194, v47, v12 op_sel:[1,1,0,0]\n"
    "v_pk_mad_u16 v47, v194, s29, v47\n"
    "ds_write_b8 v48, v10 offset:54112\n"
    "ds_write_b8_d16_hi v49, v10 offset:54112\n"
    "v_mad_u32_u16 v50, v195, v47, v12\n"
    "v_mad_u32_u16 v51, v195, v47, v12 op_sel:[1,1,0,0]\n"
    "ds_write_b8 v50, v11 offset:54112\n"
    "ds_write_b8_d16_hi v51, v11 offset:54112\n"
    "v_or3_b32 v46, v98, v99, v46\n"
    "v_pk_mad_u16 v100, v196, s28, v100\n"
    "v_pk_mad_u16 v101, v197, s28, v101\n"
    "v_pk_mad_u16 v102, v198, s28, v102\n"
    "v_pk_mad_u16 v103, v199, s28, v103\n"
    "v_or3_b32 v46, v100, v101, v46\n"
    "s_mov_b32 s26, 0xc050c01\n"
    "v_perm_b32 v47, v42, v44, s26\n"
    "v_mad_u32_u16 v48, v196, v47, v12\n"
    "v_mad_u32_u16 v49, v196, v47, v12 op_sel:[1,1,0,0]\n"
    "v_pk_mad_u16 v47, v196, s29, v47\n"
    "ds_write_b8 v48, v8 offset:56288\n"
    "ds_write_b8_d16_hi v49, v8 offset:56288\n"
    "v_mad_u32_u16 v50, v197, v47, v12\n"
    "v_mad_u32_u16 v51, v197, v47, v12 op_sel:[1,1,0,0]\n"
    "v_pk_mad_u16 v47, v197, s29, v47\n"
    "ds_write_b8 v50, v9 offset:56288\n"
    "ds_write_b8_d16_hi v51, v9 offset:56288\n"
    "v_mad_u32_u16 v48, v198, v47, v12\n"
    "v_mad_u32_u16 v49, v198, v47, v12 op_sel:[1,1,0,0]\n"
    "v_pk_mad_u16 v47, v198, s29, v47\n"
    "ds_write_b8 v48, v10 offset:56288\n"
    "ds_write_b8_d16_hi v49, v10 offset:56288\n"
    "v_mad_u32_u16 v50, v199, v47, v12\n"
    "v_mad_u32_u16 v51, v199, v47, v12 op_sel:[1,1,0,0]\n"
    "ds_write_b8 v50, v11 offset:56288\n"
    "ds_write_b8_d16_hi v51, v11 offset:56288\n"
    "v_or3_b32 v46, v102, v103, v46\n"
    "v_pk_mad_u16 v104, v200, s28, v104\n"
    "v_pk_mad_u16 v105, v201, s28, v105\n"
    "v_pk_mad_u16 v106, v202, s28, v106\n"
    "v_pk_mad_u16 v107, v203, s28, v107\n"
    "v_or3_b32 v46, v104, v105, v46\n"
    "s_mov_b32 s26, 0xc060c02\n"
    "v_perm_b32 v47, v42, v44, s26\n"
    "v_mad_u32_u16 v48, v200, v47, v12\n"
    "v_mad_u32_u16 v49, v200, v47, v12 op_sel:[1,1,0,0]\n"
    "v_pk_mad_u16 v47, v200, s29, v47\n"
    "ds_write_b8 v48, v8 offset:58464\n"
    "ds_write_b8_d16_hi v49, v8 offset:58464\n"
    "v_mad_u32_u16 v50, v201, v47, v12\n"
    "v_mad_u32_u16 v51, v201, v47, v12 op_sel:[1,1,0,0]\n"
    "v_pk_mad_u16 v47, v201, s29, v47\n"
    "ds_write_b8 v50, v9 offset:58464\n"
    "ds_write_b8_d16_hi v51, v9 offset:58464\n"
    "v_mad_u32_u16 v48, v202, v47, v12\n"
    "v_mad_u32_u16 v49, v202, v47, v12 op_sel:[1,1,0,0]\n"
    "v_pk_mad_u16 v47, v202, s29, v47\n"
    "ds_write_b8 v48, v10 offset:58464\n"
    "ds_write_b8_d16_hi v49, v10 offset:58464\n"
    "v_mad_u32_u16 v50, v203, v47, v12\n"
    "v_mad_u32_u16 v51, v203, v47, v12 op_sel:[1,1,0,0]\n"
    "ds_write_b8 v50, v11 offset:58464\n"
    "ds_write_b8_d16_hi v51, v11 offset:58464\n"
    "v_or3_b32 v46, v106, v107, v46\n"
    "v_pk_mad_u16 v108, v204, s28, v108\n"
    "v_pk_mad_u16 v109, v205, s28, v109\n"
    "v_pk_mad_u16 v110, v206, s28, v110\n"
    "v_pk_mad_u16 v111, v207, s28, v111\n"
    "v_or3_b32 v46, v108, v109, v46\n"
    "s_mov_b32 s26, 0xc040c00\n"
    "v_perm_b32 v47, v43, v45, s26\n"
    "v_mad_u32_u16 v48, v204, v47, v12\n"
    "v_mad_u32_u16 v49, v204, v47, v12 op_sel:[1,1,0,0]\n"
    "v_pk_mad_u16 v47, v204, s29, v47\n"
    "ds_write_b8 v48, v8 offset:60640\n"
    "ds_write_b8_d16_hi v49, v8 offset:60640\n"
    "v_mad_u32_u16 v50, v205, v47, v12\n"
    "v_mad_u32_u16 v51, v205, v47, v12 op_sel:[1,1,0,0]\n"
    "v_pk_mad_u16 v47, v205, s29, v47\n"
    "ds_write_b8 v50, v9 offset:60640\n"
    "ds_write_b8_d16_hi v51, v9 offset:60640\n"
    "v_mad_u32_u16 v48, v206, v47, v12\n"
    "v_mad_u32_u16 v49, v206, v47, v12 op_sel:[1,1,0,0]\n"
    "v_pk_mad_u16 v47, v206, s29, v47\n"
    "ds_write_b8 v48, v10 offset:60640\n"
    "ds_write_b8_d16_hi v49, v10 offset:60640\n"
    "v_mad_u32_u16 v50, v207, v47, v12\n"
    "v_mad_u32_u16 v51, v207, v47, v12 op_sel:[1,1,0,0]\n"
    "ds_write_b8 v50, v11 offset:60640\n"
    "ds_write_b8_d16_hi v51, v11 offset:60640\n"
    "v_or3_b32 v46, v110, v111, v46\n"
    "v_pk_mad_u16 v112, v208, s28, v112\n"
    "v_pk_mad_u16 v113, v209, s28, v113\n"
    "v_pk_mad_u16 v114, v210, s28, v114\n"
    "v_pk_mad_u16 v115, v211, s28, v115\n"
    "v_or3_b32 v46, v112, v113, v46\n"
    "s_mov_b32 s26, 0xc050c01\n"
    "v_perm_b32 v47, v43, v45, s26\n"
    "v_mad_u32_u16 v48, v208, v47, v12\n"
    "v_mad_u32_u16 v49, v208, v47, v12 op_sel:[1,1,0,0]\n"
    "v_pk_mad_u16 v47, v208, s29, v47\n"
    "ds_write_b8 v48, v8 offset:62816\n"
    "ds_write_b8_d16_hi v49, v8 offset:62816\n"
    "v_mad_u32_u16 v50, v209, v47, v12\n"
    "v_mad_u32_u16 v51, v209, v47, v12 op_sel:[1,1,0,0]\n"
    "v_pk_mad_u16 v47, v209, s29, v47\n"
    "ds_write_b8 v50, v9 offset:62816\n"
    "ds_write_b8_d16_hi v51, v9 offset:62816\n"
    "v_mad_u32_u16 v48, v210, v47, v12\n"
    "v_mad_u32_u16 v49, v210, v47, v12 op_sel:[1,1,0,0]\n"
    "v_pk_mad_u16 v47, v210, s29, v47\n"
    "ds_write_b8 v48, v10 offset:62816\n"
    "ds_write_b8_d16_hi v49, v10 offset:62816\n"
    "v_mad_u32_u16 v50, v211, v47, v12\n"
    "v_mad_u32_u16 v51, v211, v47, v12 op_sel:[1,1,0,0]\n"
    "ds_write_b8 v50, v11 offset:62816\n"
    "ds_write_b8_d16_hi v51, v11 offset:62816\n"
    "v_or3_b32 v46, v114, v115, v46\n"
    "v_pk_mad_u16 v116, v212, s28, v116\n"
    "v_pk_mad_u16 v117, v213, s28, v117\n"
    "v_pk_mad_u16 v118, v214, s28, v118\n"
    "v_pk_mad_u16 v119, v215, s28, v119\n"
    "v_or3_b32 v46, v116, v117, v46\n"
    "s_mov_b32 s26, 0xc060c02\n"
    "v_perm_b32 v47, v43, v45, s26\n"
    "v_mad_u32_u16 v48, v212, v47, v12\n"
    "v_mad_u32_u16 v49, v212, v47, v12 op_sel:[1,1,0,0]\n"
    "v_pk_mad_u16 v47, v212, s29, v47\n"
    "ds_write_b8 v48, v8 offset:64992\n"
    "ds_write_b8_d16_hi v49, v8 offset:64992\n"
    "v_mad_u32_u16 v50, v213, v47, v12\n"
    "v_mad_u32_u16 v51, v213, v47, v12 op_sel:[1,1,0,0]\n"
    "v_pk_mad_u16 v47, v213, s29, v47\n"
    "ds_write_b8 v50, v9 offset:64992\n"
    "ds_write_b8_d16_hi v51, v9 offset:64992\n"
    "v_mad_u32_u16 v48, v214, v47, v12\n"
    "v_mad_u32_u16 v49, v214, v47, v12 op_sel:[1,1,0,0]\n"
    "v_pk_mad_u16 v47, v214, s29, v47\n"
    "ds_write_b8 v48, v10 offset:64992\n"
    "ds_write_b8_d16_hi v49, v10 offset:64992\n"
    "v_mad_u32_u16 v50, v215, v47, v12\n"
    "v_mad_u32_u16 v51, v215, v47, v12 op_sel:[1,1,0,0]\n"
    "ds_write_b8 v50, v11 offset:64992\n"
    "ds_write_b8_d16_hi v51, v11 offset:64992\n"
    "v_or3_b32 v46, v118, v119, v46\n"
    "v_cmp_ne_u32 vcc, 0, v46\n"
    "v_mov_b32 v32, 0\n"
    "s_cmp_lg_u64 vcc, 0\n"
    "s_cselect_b32 s23, 0x101, 1\n"
    "s_mov_b64 exec, 1\n"
    "v_mov_b32 v33, s23\n"
    "ds_add_rtn_u32 v33, v32, v33 offset:49416\n"
    "s_mov_b64 exec, -1\n"
    "s_waitcnt lgkmcnt(0)\n"
    "v_readfirstlane_b32 s24, v33\n"
    "s_add_u32 s24, s24, s23\n"
    "s_bitcmp1_b32 s24, 2\n"
    "s_cbranch_scc0 .Ldin_go\n"
    "s_branch .Ldin_ps\n"
    ".p2align 12\n"
    ".Ldin_stub_ps:\n"
    "s_setpc_b64 s[34:35]\n"
    ".Ldin_ps:\n"
    "s_setprio 1\n"
    "v_lshrrev_b32 v40, 3, v1\n"
    "v_and_b32 v41, 7, v0\n"
    "v_lshl_add_u32 v60, v41, 2, v40\n"
    "s_mov_b32 s23, 86912\n"
    "s_mov_b32 exec_lo, 0xff000000\n"
    "v_add_u32 v60, 29, v60\n"
    "s_mov_b32 exec_lo, -1\n"
    "v_lshl_add_u32 v62, v40, 8, s23\n"
    "v_lshl_add_u32 v55, v41, 8, s23\n"
    "ds_read_u8 v44, v60 offset:49472\n"
    "ds_read2_b32 v[46:47], v62 offset0:32 offset1:40\n"
    "ds_read2_b32 v[48:49], v55 offset0:32 offset1:40\n"
    "s_cmp_gt_u32 s24, 255\n"
    "s_cselect_b32 s24, 2.0, 0\n"
    "s_add_u32 s24, s24, 0x801170\n"
    "v_mul_u32_u24 v52, 0x80110, v1\n"
    "v_bfe_u32 v60, v0, 1, 2\n"
    "v_mul_lo_u32 v62, v40, s79\n"
    "v_lshl_add_u32 v52, v60, 16, v52\n"
    "s_lshl1_add_u32 s23, s79, s78\n"
    "v_add_u32 v52, s24, v52\n"
    "v_add3_u32 v62, v62, v41, s23\n"
    "v_and_b32 v42, 56, v0\n"
    "v_cmp_gt_u32 vcc, s74, v62\n"
    "v_lshlrev_b32 v43, 3, v41\n"
    "s_waitcnt lgkmcnt(2)\n"
    "v_add_u32 v44, 15, v44\n"
    "v_bfrev_b32 v60, 1\n"
    "v_lshrrev_b32 v44, 4, v44\n"
    "v_cndmask_b32 v52, v60, v52, vcc\n"
    "v_max_u32 v44, 6, v44\n"
    "v_lshl_or_b32 v52, v44, 25, v52\n"
    "v_add_u32_e64 v44, -5, v44\n"
    "v_cndmask_b32 v44, 0, v44, vcc\n"
    "v_lshl_or_b32 v53, v44, 3, v41\n"
    "s_waitcnt lgkmcnt(0)\n"
    "v_bfe_u32 v46, v46, 25, 5\n"
    "v_bfe_u32 v47, v47, 25, 5\n"
    "v_mov_b32_dpp v54, v53 row_half_mirror row_mask:0xf bank_mask:0xf bound_ctrl:1\n"
    "v_sub_u32_dpp v64, v53, v53 quad_perm:[1,0,3,2] row_mask:0xf bank_mask:0xf bound_ctrl:1\n"
    "v_sub_u32_dpp v65, v53, v53 quad_perm:[2,3,0,1] row_mask:0xf bank_mask:0xf bound_ctrl:1\n"
    "v_sub_u32_dpp v66, v53, v53 quad_perm:[3,2,1,0] row_mask:0xf bank_mask:0xf bound_ctrl:1\n"
    "v_sub_u32 v67, v54, v53\n"
    "v_sub_u32_dpp v68, v54, v53 quad_perm:[1,0,3,2] row_mask:0xf bank_mask:0xf bound_ctrl:1\n"
    "v_sub_u32_dpp v69, v54, v53 quad_perm:[2,3,0,1] row_mask:0xf bank_mask:0xf bound_ctrl:1\n"
    "v_sub_u32_dpp v70, v54, v53 quad_perm:[3,2,1,0] row_mask:0xf bank_mask:0xf bound_ctrl:1\n"
    "v_lshrrev_b32 v56, 31, v64\n"
    "v_alignbit_b32 v56, v56, v65, 31\n"
    "v_alignbit_b32 v56, v56, v66, 31\n"
    "v_alignbit_b32 v56, v56, v67, 31\n"
    "v_alignbit_b32 v56, v56, v68, 31\n"
    "v_alignbit_b32 v56, v56, v69, 31\n"
    "v_alignbit_b32 v56, v56, v70, 31\n"
    "v_bcnt_u32_b32 v56, v56, -7\n"
    "v_mul_i32_i24 v56, -4, v56\n"
    "v_lshlrev_b32 v44, v56, v44\n"
    "v_lshl_add_u32 v56, v42, 2, v56\n"
    "ds_permute_b32 v52, v56, v52\n"
    "v_or_b32_dpp v44, v44, v44 quad_perm:[1,0,3,2] row_mask:0xf bank_mask:0xf bound_ctrl:1\n"
    "v_and_b32_e64 v50, 4, v40\n"
    "v_mad_u32_u24 v50, v50, 6, v40\n"
    "v_or_b32_dpp v44, v44, v44 quad_perm:[2,3,0,1] row_mask:0xf bank_mask:0xf bound_ctrl:1\n"
    "v_lshl_add_u32 v50, v46, 3, v50\n"
    "v_bfe_u32 v48, v48, 25, 5\n"
    "v_or_b32_dpp v44, v44, v44 row_half_mirror row_mask:0xf bank_mask:0xf bound_ctrl:1\n"
    "v_bfe_u32 v49, v49, 25, 5\n"
    "v_and_b32_e64 v51, 4, v41\n"
    "v_mad_u32_u24 v51, v51, 6, v41\n"
    "v_lshl_add_u32 v51, v48, 3, v51\n"
    "v_readlane_b32 s4, v44, 0\n"
    "v_readlane_b32 s5, v44, 8\n"
    "v_readlane_b32 s6, v44, 16\n"
    "v_readlane_b32 s7, v44, 24\n"
    "v_readlane_b32 s8, v44, 32\n"
    "v_readlane_b32 s9, v44, 40\n"
    "v_cmp_eq_u32 vcc, s20, v40\n"
    "s_nop 1\n"
    "v_cndmask_b32 v60, 0, 8, vcc\n"
    "v_add_u32 v50, v50, v60\n"
    "v_cmp_eq_u32 vcc, s20, v41\n"
    "s_nop 1\n"
    "v_cndmask_b32 v60, 0, 8, vcc\n"
    "v_add_u32 v51, v51, v60\n"
    "v_add_u32 v57, 40, v51\n"
    "v_lshl_add_u32 v50, v47, 3, v50\n"
    "v_lshl_add_u32 v51, v49, 3, v51\n"
    "v_cmp_lt_u32 vcc, v51, v50\n"
    "s_lshl_b64 exec, 0xff, 8\n"
    "v_mov_b32 v57, v51\n"
    "s_mov_b64 exec, -1\n"
    "v_lshrrev_b64 v[62:63], v43, vcc\n"
    "v_lshrrev_b64 v[60:61], v42, vcc\n"
    "v_and_b32 v62, 0xff, v62\n"
    "v_and_b32 v60, 0xff, v60\n"
    "v_bcnt_u32_b32 v62, v62, 0\n"
    "v_bcnt_u32_b32 v60, v60, 0\n"
    "v_lshlrev_b32 v62, 2, v62\n"
    "v_lshlrev_b32 v60, 2, v60\n"
    "v_bfe_u32 v63, s4, v62, 4\n"
    "v_bfe_u32 v61, s4, v60, 4\n"
    "s_lshl_b64 exec, 0xff, 0\n"
    "v_lshl_add_u32 v58, v42, 2, v62\n"
    "s_mov_b64 exec, -1\n"
    "v_lshl_add_u32 v51, v63, 3, v51\n"
    "v_lshl_add_u32 v50, v61, 3, v50\n"
    "v_cmp_lt_u32 vcc, v51, v50\n"
    "s_lshl_b64 exec, 0xff, 16\n"
    "v_mov_b32 v57, v51\n"
    "s_mov_b64 exec, -1\n"
    "v_lshrrev_b64 v[62:63], v43, vcc\n"
    "v_lshrrev_b64 v[60:61], v42, vcc\n"
    "v_and_b32 v62, 0xff, v62\n"
    "v_and_b32 v60, 0xff, v60\n"
    "v_bcnt_u32_b32 v62, v62, 0\n"
    "v_bcnt_u32_b32 v60, v60, 0\n"
    "v_lshlrev_b32 v62, 2, v62\n"
    "v_lshlrev_b32 v60, 2, v60\n"
    "v_bfe_u32 v63, s5, v62, 4\n"
    "v_bfe_u32 v61, s5, v60, 4\n"
    "s_lshl_b64 exec, 0xff, 8\n"
    "v_lshl_add_u32 v58, v42, 2, v62\n"
    "s_mov_b64 exec, -1\n"
    "v_lshl_add_u32 v51, v63, 3, v51\n"
    "v_lshl_add_u32 v50, v61, 3, v50\n"
    "v_cmp_lt_u32 vcc, v51, v50\n"
    "s_lshl_b64 exec, 0xff, 24\n"
    "v_mov_b32 v57, v51\n"
    "s_mov_b64 exec, -1\n"
    "v_lshrrev_b64 v[62:63], v43, vcc\n"
    "v_lshrrev_b64 v[60:61], v42, vcc\n"
    "v_and_b32 v62, 0xff, v62\n"
    "v_and_b32 v60, 0xff, v60\n"
    "v_bcnt_u32_b32 v62, v62, 0\n"
    "v_bcnt_u32_b32 v60, v60, 0\n"
    "v_lshlrev_b32 v62, 2, v62\n"
    "v_lshlrev_b32 v60, 2, v60\n"
    "v_bfe_u32 v63, s6, v62, 4\n"
    "v_bfe_u32 v61, s6, v60, 4\n"
    "s_lshl_b64 exec, 0xff, 16\n"
    "v_lshl_add_u32 v58, v42, 2, v62\n"
    "s_mov_b64 exec, -1\n"
    "v_lshl_add_u32 v51, v63, 3, v51\n"
    "v_lshl_add_u32 v50, v61, 3, v50\n"
    "v_cmp_lt_u32 vcc, v51, v50\n"
    "s_lshl_b64 exec, 0xff, 32\n"
    "v_mov_b32 v57, v51\n"
    "s_mov_b64 exec, -1\n"
    "v_lshrrev_b64 v[62:63], v43, vcc\n"
    "v_lshrrev_b64 v[60:61], v42, vcc\n"
    "v_and_b32 v62, 0xff, v62\n"
    "v_and_b32 v60, 0xff, v60\n"
    "v_bcnt_u32_b32 v62, v62, 0\n"
    "v_bcnt_u32_b32 v60, v60, 0\n"
    "v_lshlrev_b32 v62, 2, v62\n"
    "v_lshlrev_b32 v60, 2, v60\n"
    "v_bfe_u32 v63, s7, v62, 4\n"
    "v_bfe_u32 v61, s7, v60, 4\n"
    "s_lshl_b64 exec, 0xff, 24\n"
    "v_lshl_add_u32 v58, v42, 2, v62\n"
    "s_mov_b64 exec, -1\n"
    "v_lshl_add_u32 v51, v63, 3, v51\n"
    "v_lshl_add_u32 v50, v61, 3, v50\n"
    "v_cmp_lt_u32 vcc, v51, v50\n"
    "s_lshl_b64 exec, 0xff, 40\n"
    "v_mov_b32 v57, v51\n"
    "s_mov_b64 exec, -1\n"
    "v_lshrrev_b64 v[62:63], v43, vcc\n"
    "v_lshrrev_b64 v[60:61], v42, vcc\n"
    "v_and_b32 v62, 0xff, v62\n"
    "v_and_b32 v60, 0xff, v60\n"
    "v_bcnt_u32_b32 v62, v62, 0\n"
    "v_bcnt_u32_b32 v60, v60, 0\n"
    "v_lshlrev_b32 v62, 2, v62\n"
    "v_lshlrev_b32 v60, 2, v60\n"
    "v_bfe_u32 v63, s8, v62, 4\n"
    "v_bfe_u32 v61, s8, v60, 4\n"
    "s_lshl_b64 exec, 0xff, 32\n"
    "v_lshl_add_u32 v58, v42, 2, v62\n"
    "s_mov_b64 exec, -1\n"
    "v_lshl_add_u32 v51, v63, 3, v51\n"
    "v_lshl_add_u32 v50, v61, 3, v50\n"
    "v_cmp_lt_u32 vcc, v51, v50\n"
    "s_nop 1\n"
    "v_lshrrev_b64 v[62:63], v43, vcc\n"
    "v_and_b32 v62, 0xff, v62\n"
    "v_bcnt_u32_b32 v62, v62, 0\n"
    "v_lshlrev_b32 v62, 2, v62\n"
    "v_bfe_u32 v63, s9, v62, 4\n"
    "s_lshl_b64 exec, 0xff, 40\n"
    "v_lshl_add_u32 v58, v42, 2, v62\n"
    "s_mov_b64 exec, -1\n"
    "s_waitcnt lgkmcnt(0)\n"
    "ds_bpermute_b32 v52, v58, v52\n"
    "v_lshl_add_u32 v60, v63, 3, v51\n"
    "v_sub_u32 v57, v51, v57\n"
    "v_sub_u32 v50, 6, v40\n"
    "v_add_u32_dpp v60, v60, v60 quad_perm:[1,0,3,2] row_mask:0xf bank_mask:0xf bound_ctrl:1\n"
    "v_mad_u32_u24 v57, v50, 40, v57\n"
    "v_mul_i32_i24 v50, 8, v50\n"
    "v_add_u32_dpp v60, v60, v60 quad_perm:[2,3,0,1] row_mask:0xf bank_mask:0xf bound_ctrl:1\n"
    "v_cvt_f32_u32 v54, v57\n"
    "v_mul_u32_u24 v57, 105, v57\n"
    "v_add_u32_dpp v60, v60, v60 row_half_mirror row_mask:0xf bank_mask:0xf bound_ctrl:1\n"
    "v_rcp_f32 v54, v54\n"
    "v_mad_u32_u24 v50, v63, 30, v50\n"
    "v_lshrrev_b32 v60, 3, v60\n"
    "v_lshrrev_b32 v57, 4, v57\n"
    "v_add_u32 v60, 224, v60\n"
    "v_mul_f32 v54, 0x49000000, v54\n"
    "v_lshrrev_b32 v56, 5, v60\n"
    "v_cmp_lt_u32 vcc, 0, v63\n"
    "v_cvt_u32_f32 v54, v54\n"
    "v_sub_u32 v44, v60, v56\n"
    "v_add_u32 v60, v60, v56\n"
    "v_cndmask_b32 v54, 0, v54, vcc\n"
    "v_med3_u32 v60, s19, v44, v60\n"
    "v_and_b32 v54, s18, v54\n"
    "v_mul_u32_u24 v60, 401, v60\n"
    "s_add_u32 s23, s76, 395\n"
    "v_add_u32 v50, v50, v57\n"
    "s_mov_b32 s4, 0x4b4b4b00\n"
    "s_mov_b32 s5, 0x284b\n"
    "v_lshrrev_b64 v[62:63], v42, s[4:5]\n"
    "s_mov_b32 s6, 0xffff1111\n"
    "v_lshlrev_b32 v57, 2, v41\n"
    "v_and_b32 v62, 0xff, v62\n"
    "v_bfe_i32 v57, s6, v57, 4\n"
    "v_mad_i32_i24 v50, v62, v57, v50\n"
    "v_lshrrev_b32 v60, 6, v60\n"
    "v_add_u32 v56, 16, v42\n"
    "v_sub_u32 v60, v60, v50\n"
    "v_and_b32 v44, 24, v56\n"
    "v_add_u32 v60, s23, v60\n"
    "v_lshl_add_u32 v55, v44, 2, v55\n"
    "v_and_b32 v60, 0xffff, v60\n"
    "v_lshrrev_b32 v56, 5, v56\n"
    "v_lshl_or_b32 v53, v54, 16, v60\n"
    "v_lshl_add_u32 v55, v56, 3, v55\n"
    "s_mov_b32 exec_hi, 0xffff\n"
    "s_waitcnt lgkmcnt(0)\n"
    "ds_write_b64 v55, v[52:53] offset:128\n"
    "s_mov_b32 exec_hi, -1\n"
    "s_branch .Ldin_go\n"
    ".Ldin_trimo:\n"
    "v_add_u32 v66, 4, v3\n"
    "v_cmp_gt_u32 vcc, s14, v66\n"
    "s_nop 1\n"
    "v_cndmask_b32 v41, 0, v41, vcc\n"
    "v_cndmask_b32 v45, 0, v45, vcc\n"
    "v_add_u32 v66, 8, v3\n"
    "v_cmp_gt_u32 vcc, s14, v66\n"
    "s_nop 1\n"
    "v_cndmask_b32 v42, 0, v42, vcc\n"
    "v_cndmask_b32 v46, 0, v46, vcc\n"
    "v_add_u32 v66, 12, v3\n"
    "v_cmp_gt_u32 vcc, s14, v66\n"
    "s_nop 1\n"
    "v_cndmask_b32 v43, 0, v43, vcc\n"
    "v_cndmask_b32 v47, 0, v47, vcc\n"
    "s_branch .Ldin_trimo_r\n"
    ".Ldin_trimy:\n"
    "v_add_u32 v32, 4, v3\n"
    "v_cmp_gt_u32 vcc, s14, v32\n"
    "s_nop 1\n"
    "v_cndmask_b32 v97, 0, v97, vcc\n"
    "v_cndmask_b32 v101, 0, v101, vcc\n"
    "v_cndmask_b32 v105, 0, v105, vcc\n"
    "v_cndmask_b32 v109, 0, v109, vcc\n"
    "v_cndmask_b32 v113, 0, v113, vcc\n"
    "v_cndmask_b32 v117, 0, v117, vcc\n"
    "v_add_u32 v32, 8, v3\n"
    "v_cmp_gt_u32 vcc, s14, v32\n"
    "s_nop 1\n"
    "v_cndmask_b32 v98, 0, v98, vcc\n"
    "v_cndmask_b32 v102, 0, v102, vcc\n"
    "v_cndmask_b32 v106, 0, v106, vcc\n"
    "v_cndmask_b32 v110, 0, v110, vcc\n"
    "v_cndmask_b32 v114, 0, v114, vcc\n"
    "v_cndmask_b32 v118, 0, v118, vcc\n"
    "v_add_u32 v32, 12, v3\n"
    "v_cmp_gt_u32 vcc, s14, v32\n"
    "s_nop 1\n"
    "v_cndmask_b32 v99, 0, v99, vcc\n"
    "v_cndmask_b32 v103, 0, v103, vcc\n"
    "v_cndmask_b32 v107, 0, v107, vcc\n"
    "v_cndmask_b32 v111, 0, v111, vcc\n"
    "v_cndmask_b32 v115, 0, v115, vcc\n"
    "v_cndmask_b32 v119, 0, v119, vcc\n"
    "s_branch .Ldin_trimy_r\n"
    ".Ldin_A0s:\n"
    "s_cbranch_vccz .Ldin_A0s_np\n"
    "s_setprio 1\n"
    ".Ldin_A0s_np:\n"
    "v_mfma_f32_16x16x32_bf16 v[176:179], v[0:3], v[192:195], v[144:147]\n"
    "v_exp_f32 v160, v160 clamp\n"
    "v_mfma_f32_16x16x32_bf16 v[176:179], v[4:7], v[196:199], v[176:179]\n"
    "v_exp_f32 v161, v161 clamp\n"
    "v_mfma_f32_16x16x32_bf16 v[180:183], v[8:11], v[192:195], v[148:151]\n"
    "v_exp_f32 v162, v162 clamp\n"
    "v_mfma_f32_16x16x32_bf16 v[180:183], v[12:15], v[196:199], v[180:183]\n"
    "v_exp_f32 v163, v163 clamp\n"
    "v_mfma_f32_16x16x32_bf16 v[184:187], v[16:19], v[192:195], v[152:155]\n"
    "v_exp_f32 v164, v164 clamp\n"
    "v_mfma_f32_16x16x32_bf16 v[184:187], v[20:23], v[196:199], v[184:187]\n"
    "v_exp_f32 v165, v165 clamp\n"
    "v_mfma_f32_16x16x32_bf16 v[188:191], v[24:27], v[192:195], v[156:159]\n"
    "v_exp_f32 v166, v166 clamp\n"
    "v_mfma_f32_16x16x32_bf16 v[188:191], v[28:31], v[196:199], v[188:191]\n"
    "v_exp_f32 v167, v167 clamp\n"
    "v_exp_f32 v168, v168 clamp\n"
    "s_mov_b32 m0, s62\n"
    "ds_read_b64 v[216:217], v246 offset:128\n"
    "v_exp_f32 v169, v169 clamp\n"
    "v_add_u32 v251, s66, v244\n"
    "s_waitcnt lgkmcnt(1)\n"
    "v_mfma_f32_4x4x4_16b_bf16 v[224:227], v[228:229], v[208:209], v[224:227] cbsz:4 abid:0\n"
    "v_exp_f32 v170, v170 clamp\n"
    "v_exp_f32 v171, v171 clamp\n"
    "v_mfma_f32_4x4x4_16b_bf16 v[224:227], v[228:229], v[210:211], v[224:227] cbsz:4 abid:1\n"
    "v_exp_f32 v172, v172 clamp\n"
    "v_exp_f32 v173, v173 clamp\n"
    "v_mfma_f32_4x4x4_16b_bf16 v[224:227], v[228:229], v[212:213], v[224:227] cbsz:4 abid:2\n"
    "v_exp_f32 v174, v174 clamp\n"
    "v_exp_f32 v175, v175 clamp\n"
    "v_mfma_f32_4x4x4_16b_bf16 v[224:227], v[228:229], v[214:215], v[224:227] cbsz:4 abid:3\n"
    "s_waitcnt vmcnt(4)\n"
    "s_waitcnt lgkmcnt(0)\n"
    "v_readlane_b32 s60, v216, s59\n"
    "v_readlane_b32 s61, v217, s59\n"
    "ds_read_b64_tr_b16 v[208:209], v240 offset:2048\n"
    "ds_read_b64_tr_b16 v[210:211], v241 offset:2048\n"
    "ds_read_b64_tr_b16 v[212:213], v242 offset:2048\n"
    "ds_read_b64_tr_b16 v[214:215], v243 offset:2048\n"
    "ds_read_u8 v236, v251 offset:0\n"
    "ds_read_u8 v238, v251 offset:1\n"
    "ds_read_b128 v[192:195], v234 offset:4096\n"
    "ds_read_b128 v[196:199], v235 offset:4096\n"
    "s_cmp_eq_u32 s60, -1\n"
    "s_cbranch_scc1 .Ldin_A0s_poll\n"
    ".Ldin_A0s_pr:\n"
    "s_lshr_b32 s94, s60, 12\n"
    "s_and_b32 s94, s94, 0x1ff0\n"
    "v_xor_b32 v252, s94, v250\n"
    "v_pk_add_f32 v[230:231], v[224:225], v[226:227]\n"
    "v_add_f32 v230, v230, v231\n"
    "ds_write_addtid_b32 v230 offset:49152\n"
    "ds_read_b128 v[216:219], v252 offset:0\n"
    "ds_read_b128 v[220:223], v252 offset:64\n"
    "v_mov_b64 v[224:225], 0\n"
    "v_mov_b64_e64 v[226:227], 0\n"
    "v_pk_fma_f32 v[160:161], v[160:161], s[70:71], v[128:129]\n"
    "v_pk_fma_f32 v[162:163], v[162:163], s[70:71], v[130:131]\n"
    "v_pk_add_f32 v[230:231], v[160:161], v[162:163]\n"
    "v_pk_mul_f32 v[232:233], v[160:161], v[162:163]\n"
    "v_pk_fma_f32 v[164:165], v[164:165], s[70:71], v[132:133]\n"
    "v_pk_fma_f32 v[230:231], v[230:231], v[164:165], v[232:233]\n"
    "v_pk_mul_f32 v[232:233], v[232:233], v[164:165]\n"
    "v_pk_fma_f32 v[166:167], v[166:167], s[70:71], v[134:135]\n"
    "v_pk_fma_f32 v[230:231], v[230:231], v[166:167], v[232:233]\n"
    "v_pk_mul_f32 v[232:233], v[232:233], v[166:167]\n"
    "v_pk_fma_f32 v[168:169], v[168:169], s[70:71], v[136:137]\n"
    "v_pk_fma_f32 v[230:231], v[230:231], v[168:169], v[232:233]\n"
    "v_pk_mul_f32 v[232:233], v[232:233], v[168:169]\n"
    "v_pk_fma_f32 v[170:171], v[170:171], s[70:71], v[138:139]\n"
    "v_pk_fma_f32 v[230:231], v[230:231], v[170:171], v[232:233]\n"
    "v_pk_mul_f32 v[232:233], v[232:233], v[170:171]\n"
    "v_pk_fma_f32 v[172:173], v[172:173], s[70:71], v[140:141]\n"
    "v_pk_fma_f32 v[230:231], v[230:231], v[172:173], v[232:233]\n"
    "v_pk_mul_f32 v[232:233], v[232:233], v[172:173]\n"
    "v_pk_fma_f32 v[174:175], v[174:175], s[70:71], v[142:143]\n"
    "v_pk_fma_f32 v[230:231], v[230:231], v[174:175], v[232:233]\n"
    "v_pk_mul_f32 v[232:233], v[232:233], v[174:175]\n"
    "s_add_u32 m0, s68, 2048\n"
    "s_and_b32 s64, s60, 0xffff\n"
    "s_bfe_u32 s94, s60, 0x30013\n"
    "s_bfe_u32 s95, s60, 0x50019\n"
    "s_mul_i32 s94, s94, s56\n"
    "s_lshl4_add_u32 s83, s95, s64\n"
    "s_add_u32 s65, s58, s94\n"
    "s_add_u32 s82, s64, 64\n"
    "s_add_u32 s58, s58, s57\n"
    "s_add_u32 s59, s59, 1\n"
    "s_sub_u32 s95, s61, s76\n"
    "s_lshr_b32 s94, s61, 16\n"
    "s_sext_i32_i16 s95, s95\n"
    "s_cmp_lt_i32 s95, -400\n"
    "s_cselect_b32 s94, 0, s94\n"
    "s_mul_i32 s95, s95, s94\n"
    "s_ashr_i32 s95, s95, 16\n"
    "s_max_i32 s95, s95, -10\n"
    "s_min_i32 s95, s95, 10\n"
    "s_add_u32 s72, s77, s95\n"
    "s_cmp_lt_i32 s60, 0\n"
    "s_cselect_b32 s72, 0, s72\n"
    "v_rcp_f32 v232, v232\n"
    "v_rcp_f32 v233, v233\n"
    "v_fma_f32 v230, v230, v232, v247\n"
    "v_fmac_f32 v230, v231, v233\n"
    "v_mbcnt_lo_u32_b32 v232, -1, 0\n"
    "v_mbcnt_hi_u32_b32 v232, -1, v232\n"
    "v_bfe_u32 v233, v232, 3, 1\n"
    "v_and_b32 v232, 7, v232\n"
    "v_lshl_add_u32 v232, v232, 1, v233\n"
    "v_add_u32 v232, s81, v232\n"
    "ds_read_u8 v232, v232 offset:49664\n"
    "s_add_u32 s81, s81, 16\n"
    "s_waitcnt lgkmcnt(0)\n"
    "buffer_load_ushort v232, v232, s[44:47], s63 idxen\n"
    "s_waitcnt vmcnt(0)\n"
    "v_lshlrev_b32 v232, 16, v232\n"
    "v_mul_f32 v230, v230, v232\n"
    "s_add_u32 s66, s66, 16\n"
    "s_waitcnt lgkmcnt(0)\n"
    "ds_write_b16_d16_hi v245, v230\n"
    "ds_read_b64 v[228:229], v246\n"
    "buffer_load_dwordx4 v[236:237], s[40:43], s67 idxen offen offset:0 nt lds\n"
    "buffer_load_dwordx4 v[238:239], s[40:43], s67 idxen offen offset:1024 nt lds\n"
    ".Ldin_G1s:\n"
    "s_cbranch_vccz .Ldin_G1s_np\n"
    "s_setprio 1\n"
    ".Ldin_G1s_np:\n"
    "v_mfma_f32_16x16x32_bf16 v[160:163], v[0:3], v[192:195], v[144:147]\n"
    "v_exp_f32 v176, v176 clamp\n"
    "v_mfma_f32_16x16x32_bf16 v[160:163], v[4:7], v[196:199], v[160:163]\n"
    "v_exp_f32 v177, v177 clamp\n"
    "v_mfma_f32_16x16x32_bf16 v[164:167], v[8:11], v[192:195], v[148:151]\n"
    "v_exp_f32 v178, v178 clamp\n"
    "v_mfma_f32_16x16x32_bf16 v[164:167], v[12:15], v[196:199], v[164:167]\n"
    "v_exp_f32 v179, v179 clamp\n"
    "v_mfma_f32_16x16x32_bf16 v[168:171], v[16:19], v[192:195], v[152:155]\n"
    "v_exp_f32 v180, v180 clamp\n"
    "v_mfma_f32_16x16x32_bf16 v[168:171], v[20:23], v[196:199], v[168:171]\n"
    "v_exp_f32 v181, v181 clamp\n"
    "v_mfma_f32_16x16x32_bf16 v[172:175], v[24:27], v[192:195], v[156:159]\n"
    "v_exp_f32 v182, v182 clamp\n"
    "v_mfma_f32_16x16x32_bf16 v[172:175], v[28:31], v[196:199], v[172:175]\n"
    "v_exp_f32 v183, v183 clamp\n"
    "v_exp_f32 v184, v184 clamp\n"
    "s_cmp_eq_u32 s66, s80\n"
    "s_cselect_b64 s[66:67], s[64:65], s[66:67]\n"
    "s_add_u32 m0, s68, 4096\n"
    "v_exp_f32 v185, v185 clamp\n"
    "v_add_u32 v251, s66, v244\n"
    "s_waitcnt lgkmcnt(0)\n"
    "v_mfma_f32_4x4x4_16b_bf16 v[224:227], v[228:229], v[200:201], v[224:227] cbsz:4 abid:0\n"
    "v_exp_f32 v186, v186 clamp\n"
    "v_exp_f32 v187, v187 clamp\n"
    "v_mfma_f32_4x4x4_16b_bf16 v[224:227], v[228:229], v[202:203], v[224:227] cbsz:4 abid:1\n"
    "v_exp_f32 v188, v188 clamp\n"
    "v_exp_f32 v189, v189 clamp\n"
    "v_mfma_f32_4x4x4_16b_bf16 v[224:227], v[228:229], v[204:205], v[224:227] cbsz:4 abid:2\n"
    "v_exp_f32 v190, v190 clamp\n"
    "v_exp_f32 v191, v191 clamp\n"
    "v_mfma_f32_4x4x4_16b_bf16 v[224:227], v[228:229], v[206:207], v[224:227] cbsz:4 abid:3\n"
    "s_waitcnt vmcnt(4)\n"
    "s_memrealtime s[34:35]\n"
    "ds_read_b64_tr_b16 v[200:201], v240 offset:4096\n"
    "ds_read_b64_tr_b16 v[202:203], v241 offset:4096\n"
    "ds_read_b64_tr_b16 v[204:205], v242 offset:4096\n"
    "ds_read_b64_tr_b16 v[206:207], v243 offset:4096\n"
    "ds_read_u8 v236, v251 offset:0\n"
    "ds_read_u8 v238, v251 offset:1\n"
    "ds_read_b128 v[192:195], v234 offset:6144\n"
    "ds_read_b128 v[196:199], v235 offset:6144\n"
    "v_pk_fma_f32 v[176:177], v[176:177], s[70:71], v[128:129]\n"
    "v_pk_fma_f32 v[178:179], v[178:179], s[70:71], v[130:131]\n"
    "v_pk_add_f32 v[230:231], v[176:177], v[178:179]\n"
    "v_pk_mul_f32 v[232:233], v[176:177], v[178:179]\n"
    "v_pk_fma_f32 v[180:181], v[180:181], s[70:71], v[132:133]\n"
    "v_pk_fma_f32 v[230:231], v[230:231], v[180:181], v[232:233]\n"
    "v_pk_mul_f32 v[232:233], v[232:233], v[180:181]\n"
    "v_pk_fma_f32 v[182:183], v[182:183], s[70:71], v[134:135]\n"
    "v_pk_fma_f32 v[230:231], v[230:231], v[182:183], v[232:233]\n"
    "v_pk_mul_f32 v[232:233], v[232:233], v[182:183]\n"
    "v_pk_fma_f32 v[184:185], v[184:185], s[70:71], v[136:137]\n"
    "v_pk_fma_f32 v[230:231], v[230:231], v[184:185], v[232:233]\n"
    "v_pk_mul_f32 v[232:233], v[232:233], v[184:185]\n"
    "v_pk_fma_f32 v[186:187], v[186:187], s[70:71], v[138:139]\n"
    "v_pk_fma_f32 v[230:231], v[230:231], v[186:187], v[232:233]\n"
    "v_pk_mul_f32 v[232:233], v[232:233], v[186:187]\n"
    "v_pk_fma_f32 v[188:189], v[188:189], s[70:71], v[140:141]\n"
    "v_pk_fma_f32 v[230:231], v[230:231], v[188:189], v[232:233]\n"
    "v_pk_mul_f32 v[232:233], v[232:233], v[188:189]\n"
    "v_pk_fma_f32 v[190:191], v[190:191], s[70:71], v[142:143]\n"
    "v_pk_fma_f32 v[230:231], v[230:231], v[190:191], v[232:233]\n"
    "v_pk_mul_f32 v[232:233], v[232:233], v[190:191]\n"
    "v_rcp_f32 v232, v232\n"
    "v_rcp_f32 v233, v233\n"
    "v_fma_f32 v230, v230, v232, v247\n"
    "v_fmac_f32 v230, v231, v233\n"
    "v_mbcnt_lo_u32_b32 v232, -1, 0\n"
    "v_mbcnt_hi_u32_b32 v232, -1, v232\n"
    "v_bfe_u32 v233, v232, 3, 1\n"
    "v_and_b32 v232, 7, v232\n"
    "v_lshl_add_u32 v232, v232, 1, v233\n"
    "v_add_u32 v232, s81, v232\n"
    "ds_read_u8 v232, v232 offset:49664\n"
    "s_add_u32 s81, s81, 16\n"
    "s_waitcnt lgkmcnt(0)\n"
    "buffer_load_ushort v232, v232, s[44:47], s63 idxen\n"
    "s_waitcnt vmcnt(0)\n"
    "v_lshlrev_b32 v232, 16, v232\n"
    "v_mul_f32 v230, v230, v232\n"
    "s_add_u32 s66, s66, 16\n"
    "s_waitcnt lgkmcnt(0)\n"
    "ds_write_b16_d16_hi v245, v230\n"
    "ds_read_b64 v[228:229], v246\n"
    "s_cmp_lt_u32 s34, s76\n"
    "s_cbranch_scc1 .Ldin_G1s_wait\n"
    ".Ldin_G1s_go:\n"
    "s_add_u32 s76, s76, s72\n"
    "buffer_load_dwordx4 v[236:237], s[40:43], s67 idxen offen offset:0 nt lds\n"
    "buffer_load_dwordx4 v[238:239], s[40:43], s67 idxen offen offset:1024 nt lds\n"
    "s_cmp_eq_u32 s66, s82\n"
    "s_cbranch_scc1 .Ldin_L2s\n"
    ".Ldin_G2s:\n"
    "v_mfma_f32_16x16x32_bf16 v[176:179], v[0:3], v[192:195], v[144:147]\n"
    "v_exp_f32 v160, v160 clamp\n"
    "v_mfma_f32_16x16x32_bf16 v[176:179], v[4:7], v[196:199], v[176:179]\n"
    "v_exp_f32 v161, v161 clamp\n"
    "v_mfma_f32_16x16x32_bf16 v[180:183], v[8:11], v[192:195], v[148:151]\n"
    "v_exp_f32 v162, v162 clamp\n"
    "v_mfma_f32_16x16x32_bf16 v[180:183], v[12:15], v[196:199], v[180:183]\n"
    "v_exp_f32 v163, v163 clamp\n"
    "v_mfma_f32_16x16x32_bf16 v[184:187], v[16:19], v[192:195], v[152:155]\n"
    "v_exp_f32 v164, v164 clamp\n"
    "v_mfma_f32_16x16x32_bf16 v[184:187], v[20:23], v[196:199], v[184:187]\n"
    "v_exp_f32 v165, v165 clamp\n"
    "v_mfma_f32_16x16x32_bf16 v[188:191], v[24:27], v[192:195], v[156:159]\n"
    "v_exp_f32 v166, v166 clamp\n"
    "v_mfma_f32_16x16x32_bf16 v[188:191], v[28:31], v[196:199], v[188:191]\n"
    "v_exp_f32 v167, v167 clamp\n"
    "v_exp_f32 v168, v168 clamp\n"
    "s_cmp_eq_u32 s66, s80\n"
    "s_cselect_b64 s[66:67], s[64:65], s[66:67]\n"
    "s_add_u32 m0, s68, 6144\n"
    "v_exp_f32 v169, v169 clamp\n"
    "v_add_u32 v251, s66, v244\n"
    "s_waitcnt lgkmcnt(0)\n"
    "v_mfma_f32_4x4x4_16b_bf16 v[224:227], v[228:229], v[208:209], v[224:227] cbsz:4 abid:0\n"
    "v_exp_f32 v170, v170 clamp\n"
    "v_exp_f32 v171, v171 clamp\n"
    "v_mfma_f32_4x4x4_16b_bf16 v[224:227], v[228:229], v[210:211], v[224:227] cbsz:4 abid:1\n"
    "v_exp_f32 v172, v172 clamp\n"
    "v_exp_f32 v173, v173 clamp\n"
    "v_mfma_f32_4x4x4_16b_bf16 v[224:227], v[228:229], v[212:213], v[224:227] cbsz:4 abid:2\n"
    "v_exp_f32 v174, v174 clamp\n"
    "v_exp_f32 v175, v175 clamp\n"
    "v_mfma_f32_4x4x4_16b_bf16 v[224:227], v[228:229], v[214:215], v[224:227] cbsz:4 abid:3\n"
    "s_waitcnt vmcnt(4)\n"
    "s_memrealtime s[34:35]\n"
    "ds_read_b64_tr_b16 v[208:209], v240 offset:6144\n"
    "ds_read_b64_tr_b16 v[210:211], v241 offset:6144\n"
    "ds_read_b64_tr_b16 v[212:213], v242 offset:6144\n"
    "ds_read_b64_tr_b16 v[214:215], v243 offset:6144\n"
    "ds_read_u8 v236, v251 offset:0\n"
    "ds_read_u8 v238, v251 offset:1\n"
    "ds_read_b128 v[192:195], v234 offset:0\n"
    "ds_read_b128 v[196:199], v235 offset:0\n"
    "v_pk_fma_f32 v[160:161], v[160:161], s[70:71], v[128:129]\n"
    "v_pk_fma_f32 v[162:163], v[162:163], s[70:71], v[130:131]\n"
    "v_pk_add_f32 v[230:231], v[160:161], v[162:163]\n"
    "v_pk_mul_f32 v[232:233], v[160:161], v[162:163]\n"
    "v_pk_fma_f32 v[164:165], v[164:165], s[70:71], v[132:133]\n"
    "v_pk_fma_f32 v[230:231], v[230:231], v[164:165], v[232:233]\n"
    "v_pk_mul_f32 v[232:233], v[232:233], v[164:165]\n"
    "v_pk_fma_f32 v[166:167], v[166:167], s[70:71], v[134:135]\n"
    "v_pk_fma_f32 v[230:231], v[230:231], v[166:167], v[232:233]\n"
    "v_pk_mul_f32 v[232:233], v[232:233], v[166:167]\n"
    "v_pk_fma_f32 v[168:169], v[168:169], s[70:71], v[136:137]\n"
    "v_pk_fma_f32 v[230:231], v[230:231], v[168:169], v[232:233]\n"
    "v_pk_mul_f32 v[232:233], v[232:233], v[168:169]\n"
    "v_pk_fma_f32 v[170:171], v[170:171], s[70:71], v[138:139]\n"
    "v_pk_fma_f32 v[230:231], v[230:231], v[170:171], v[232:233]\n"
    "v_pk_mul_f32 v[232:233], v[232:233], v[170:171]\n"
    "v_pk_fma_f32 v[172:173], v[172:173], s[70:71], v[140:141]\n"
    "v_pk_fma_f32 v[230:231], v[230:231], v[172:173], v[232:233]\n"
    "v_pk_mul_f32 v[232:233], v[232:233], v[172:173]\n"
    "v_pk_fma_f32 v[174:175], v[174:175], s[70:71], v[142:143]\n"
    "v_pk_fma_f32 v[230:231], v[230:231], v[174:175], v[232:233]\n"
    "v_pk_mul_f32 v[232:233], v[232:233], v[174:175]\n"
    "v_rcp_f32 v232, v232\n"
    "v_rcp_f32 v233, v233\n"
    "v_fma_f32 v230, v230, v232, v247\n"
    "v_fmac_f32 v230, v231, v233\n"
    "v_mbcnt_lo_u32_b32 v232, -1, 0\n"
    "v_mbcnt_hi_u32_b32 v232, -1, v232\n"
    "v_bfe_u32 v233, v232, 3, 1\n"
    "v_and_b32 v232, 7, v232\n"
    "v_lshl_add_u32 v232, v232, 1, v233\n"
    "v_add_u32 v232, s81, v232\n"
    "ds_read_u8 v232, v232 offset:49664\n"
    "s_add_u32 s81, s81, 16\n"
    "s_waitcnt lgkmcnt(0)\n"
    "buffer_load_ushort v232, v232, s[44:47], s63 idxen\n"
    "s_waitcnt vmcnt(0)\n"
    "v_lshlrev_b32 v232, 16, v232\n"
    "v_mul_f32 v230, v230, v232\n"
    "s_add_u32 s66, s66, 16\n"
    "s_waitcnt lgkmcnt(0)\n"
    "ds_write_b16_d16_hi v245, v230\n"
    "ds_read_b64 v[228:229], v246\n"
    "s_cmp_lt_u32 s34, s76\n"
    "s_cbranch_scc1 .Ldin_G2s_wait\n"
    ".Ldin_G2s_go:\n"
    "s_sub_u32 s94, s34, 30\n"
    "s_max_u32 s76, s76, s94\n"
    "s_add_u32 s76, s76, s72\n"
    "buffer_load_dwordx4 v[236:237], s[40:43], s67 idxen offen offset:0 nt lds\n"
    "buffer_load_dwordx4 v[238:239], s[40:43], s67 idxen offen offset:1024 nt lds\n"
    "s_cmp_eq_u32 s66, s82\n"
    "s_cbranch_scc1 .Ldin_L3s\n"
    ".Ldin_G3s:\n"
    "v_mfma_f32_16x16x32_bf16 v[160:163], v[0:3], v[192:195], v[144:147]\n"
    "v_exp_f32 v176, v176 clamp\n"
    "v_mfma_f32_16x16x32_bf16 v[160:163], v[4:7], v[196:199], v[160:163]\n"
    "v_exp_f32 v177, v177 clamp\n"
    "v_mfma_f32_16x16x32_bf16 v[164:167], v[8:11], v[192:195], v[148:151]\n"
    "v_exp_f32 v178, v178 clamp\n"
    "v_mfma_f32_16x16x32_bf16 v[164:167], v[12:15], v[196:199], v[164:167]\n"
    "v_exp_f32 v179, v179 clamp\n"
    "v_mfma_f32_16x16x32_bf16 v[168:171], v[16:19], v[192:195], v[152:155]\n"
    "v_exp_f32 v180, v180 clamp\n"
    "v_mfma_f32_16x16x32_bf16 v[168:171], v[20:23], v[196:199], v[168:171]\n"
    "v_exp_f32 v181, v181 clamp\n"
    "v_mfma_f32_16x16x32_bf16 v[172:175], v[24:27], v[192:195], v[156:159]\n"
    "v_exp_f32 v182, v182 clamp\n"
    "v_mfma_f32_16x16x32_bf16 v[172:175], v[28:31], v[196:199], v[172:175]\n"
    "v_exp_f32 v183, v183 clamp\n"
    "v_exp_f32 v184, v184 clamp\n"
    "s_cmp_eq_u32 s66, s80\n"
    "s_cselect_b64 s[66:67], s[64:65], s[66:67]\n"
    "s_add_u32 m0, s68, 0\n"
    "v_exp_f32 v185, v185 clamp\n"
    "v_add_u32_e64 v251, s66, v244\n"
    "s_waitcnt lgkmcnt(0)\n"
    "v_mfma_f32_4x4x4_16b_bf16 v[224:227], v[228:229], v[200:201], v[224:227] cbsz:4 abid:0\n"
    "v_exp_f32 v186, v186 clamp\n"
    "v_exp_f32 v187, v187 clamp\n"
    "v_mfma_f32_4x4x4_16b_bf16 v[224:227], v[228:229], v[202:203], v[224:227] cbsz:4 abid:1\n"
    "v_exp_f32 v188, v188 clamp\n"
    "v_exp_f32 v189, v189 clamp\n"
    "v_mfma_f32_4x4x4_16b_bf16 v[224:227], v[228:229], v[204:205], v[224:227] cbsz:4 abid:2\n"
    "v_exp_f32 v190, v190 clamp\n"
    "v_exp_f32 v191, v191 clamp\n"
    "v_mfma_f32_4x4x4_16b_bf16 v[224:227], v[228:229], v[206:207], v[224:227] cbsz:4 abid:3\n"
    "s_waitcnt vmcnt(4)\n"
    "s_memrealtime s[34:35]\n"
    "ds_read_b64_tr_b16 v[200:201], v240 offset:0\n"
    "ds_read_b64_tr_b16 v[202:203], v241 offset:0\n"
    "ds_read_b64_tr_b16 v[204:205], v242 offset:0\n"
    "ds_read_b64_tr_b16 v[206:207], v243 offset:0\n"
    "ds_read_u8 v236, v251 offset:0\n"
    "ds_read_u8 v238, v251 offset:1\n"
    "ds_read_b128 v[192:195], v234 offset:2048\n"
    "ds_read_b128 v[196:199], v235 offset:2048\n"
    "v_pk_fma_f32 v[176:177], v[176:177], s[70:71], v[128:129]\n"
    "v_pk_fma_f32 v[178:179], v[178:179], s[70:71], v[130:131]\n"
    "v_pk_add_f32 v[230:231], v[176:177], v[178:179]\n"
    "v_pk_mul_f32 v[232:233], v[176:177], v[178:179]\n"
    "v_pk_fma_f32 v[180:181], v[180:181], s[70:71], v[132:133]\n"
    "v_pk_fma_f32 v[230:231], v[230:231], v[180:181], v[232:233]\n"
    "v_pk_mul_f32 v[232:233], v[232:233], v[180:181]\n"
    "v_pk_fma_f32 v[182:183], v[182:183], s[70:71], v[134:135]\n"
    "v_pk_fma_f32 v[230:231], v[230:231], v[182:183], v[232:233]\n"
    "v_pk_mul_f32 v[232:233], v[232:233], v[182:183]\n"
    "v_pk_fma_f32 v[184:185], v[184:185], s[70:71], v[136:137]\n"
    "v_pk_fma_f32 v[230:231], v[230:231], v[184:185], v[232:233]\n"
    "v_pk_mul_f32 v[232:233], v[232:233], v[184:185]\n"
    "v_pk_fma_f32 v[186:187], v[186:187], s[70:71], v[138:139]\n"
    "v_pk_fma_f32 v[230:231], v[230:231], v[186:187], v[232:233]\n"
    "v_pk_mul_f32 v[232:233], v[232:233], v[186:187]\n"
    "v_pk_fma_f32 v[188:189], v[188:189], s[70:71], v[140:141]\n"
    "v_pk_fma_f32 v[230:231], v[230:231], v[188:189], v[232:233]\n"
    "v_pk_mul_f32 v[232:233], v[232:233], v[188:189]\n"
    "v_pk_fma_f32 v[190:191], v[190:191], s[70:71], v[142:143]\n"
    "v_pk_fma_f32 v[230:231], v[230:231], v[190:191], v[232:233]\n"
    "v_pk_mul_f32 v[232:233], v[232:233], v[190:191]\n"
    "v_rcp_f32 v232, v232\n"
    "v_rcp_f32 v233, v233\n"
    "v_fma_f32 v230, v230, v232, v247\n"
    "v_fmac_f32 v230, v231, v233\n"
    "v_mbcnt_lo_u32_b32 v232, -1, 0\n"
    "v_mbcnt_hi_u32_b32 v232, -1, v232\n"
    "v_bfe_u32 v233, v232, 3, 1\n"
    "v_and_b32 v232, 7, v232\n"
    "v_lshl_add_u32 v232, v232, 1, v233\n"
    "v_add_u32 v232, s81, v232\n"
    "ds_read_u8 v232, v232 offset:49664\n"
    "s_add_u32 s81, s81, 16\n"
    "s_waitcnt lgkmcnt(0)\n"
    "buffer_load_ushort v232, v232, s[44:47], s63 idxen\n"
    "s_waitcnt vmcnt(0)\n"
    "v_lshlrev_b32 v232, 16, v232\n"
    "v_mul_f32 v230, v230, v232\n"
    "s_add_u32 s66, s66, 16\n"
    "s_waitcnt lgkmcnt(0)\n"
    "ds_write_b16_d16_hi v245, v230\n"
    "ds_read_b64 v[228:229], v246\n"
    "s_cmp_lt_u32 s34, s76\n"
    "s_cbranch_scc1 .Ldin_G3s_wait\n"
    ".Ldin_G3s_go:\n"
    "s_add_u32 s76, s76, s72\n"
    "buffer_load_dwordx4 v[236:237], s[40:43], s67 idxen offen offset:0 nt lds\n"
    "buffer_load_dwordx4 v[238:239], s[40:43], s67 idxen offen offset:1024 nt lds\n"
    "s_cmp_eq_u32 s66, s82\n"
    "s_cbranch_scc1 .Ldin_L0s\n"
    ".Ldin_G0s:\n"
    "s_setprio 0\n"
    "v_mfma_f32_16x16x32_bf16 v[176:179], v[0:3], v[192:195], v[144:147]\n"
    "v_exp_f32 v160, v160 clamp\n"
    "v_mfma_f32_16x16x32_bf16 v[176:179], v[4:7], v[196:199], v[176:179]\n"
    "v_exp_f32 v161, v161 clamp\n"
    "v_mfma_f32_16x16x32_bf16 v[180:183], v[8:11], v[192:195], v[148:151]\n"
    "v_exp_f32 v162, v162 clamp\n"
    "v_mfma_f32_16x16x32_bf16 v[180:183], v[12:15], v[196:199], v[180:183]\n"
    "v_exp_f32 v163, v163 clamp\n"
    "v_mfma_f32_16x16x32_bf16 v[184:187], v[16:19], v[192:195], v[152:155]\n"
    "v_exp_f32 v164, v164 clamp\n"
    "v_mfma_f32_16x16x32_bf16 v[184:187], v[20:23], v[196:199], v[184:187]\n"
    "v_exp_f32 v165, v165 clamp\n"
    "v_mfma_f32_16x16x32_bf16 v[188:191], v[24:27], v[192:195], v[156:159]\n"
    "v_exp_f32 v166, v166 clamp\n"
    "v_mfma_f32_16x16x32_bf16 v[188:191], v[28:31], v[196:199], v[188:191]\n"
    "v_exp_f32 v167, v167 clamp\n"
    "v_exp_f32 v168, v168 clamp\n"
    "s_cmp_eq_u32 s66, s80\n"
    "s_cselect_b64 s[66:67], s[64:65], s[66:67]\n"
    "s_add_u32 m0, s68, 2048\n"
    "v_exp_f32 v169, v169 clamp\n"
    "v_add_u32_e64 v251, s66, v244\n"
    "s_waitcnt lgkmcnt(0)\n"
    "v_mfma_f32_4x4x4_16b_bf16 v[224:227], v[228:229], v[208:209], v[224:227] cbsz:4 abid:0\n"
    "v_exp_f32 v170, v170 clamp\n"
    "v_exp_f32 v171, v171 clamp\n"
    "v_mfma_f32_4x4x4_16b_bf16 v[224:227], v[228:229], v[210:211], v[224:227] cbsz:4 abid:1\n"
    "v_exp_f32 v172, v172 clamp\n"
    "v_exp_f32 v173, v173 clamp\n"
    "v_mfma_f32_4x4x4_16b_bf16 v[224:227], v[228:229], v[212:213], v[224:227] cbsz:4 abid:2\n"
    "v_exp_f32 v174, v174 clamp\n"
    "v_exp_f32 v175, v175 clamp\n"
    "v_mfma_f32_4x4x4_16b_bf16 v[224:227], v[228:229], v[214:215], v[224:227] cbsz:4 abid:3\n"
    "s_waitcnt vmcnt(4)\n"
    "s_memrealtime s[34:35]\n"
    "ds_read_b64_tr_b16 v[208:209], v240 offset:2048\n"
    "ds_read_b64_tr_b16 v[210:211], v241 offset:2048\n"
    "ds_read_b64_tr_b16 v[212:213], v242 offset:2048\n"
    "ds_read_b64_tr_b16 v[214:215], v243 offset:2048\n"
    "ds_read_u8 v236, v251 offset:0\n"
    "ds_read_u8 v238, v251 offset:1\n"
    "ds_read_b128 v[192:195], v234 offset:4096\n"
    "ds_read_b128 v[196:199], v235 offset:4096\n"
    "v_pk_fma_f32 v[160:161], v[160:161], s[70:71], v[128:129]\n"
    "v_pk_fma_f32 v[162:163], v[162:163], s[70:71], v[130:131]\n"
    "v_pk_add_f32 v[230:231], v[160:161], v[162:163]\n"
    "v_pk_mul_f32 v[232:233], v[160:161], v[162:163]\n"
    "v_pk_fma_f32 v[164:165], v[164:165], s[70:71], v[132:133]\n"
    "v_pk_fma_f32 v[230:231], v[230:231], v[164:165], v[232:233]\n"
    "v_pk_mul_f32 v[232:233], v[232:233], v[164:165]\n"
    "v_pk_fma_f32 v[166:167], v[166:167], s[70:71], v[134:135]\n"
    "v_pk_fma_f32 v[230:231], v[230:231], v[166:167], v[232:233]\n"
    "v_pk_mul_f32 v[232:233], v[232:233], v[166:167]\n"
    "v_pk_fma_f32 v[168:169], v[168:169], s[70:71], v[136:137]\n"
    "v_pk_fma_f32 v[230:231], v[230:231], v[168:169], v[232:233]\n"
    "v_pk_mul_f32 v[232:233], v[232:233], v[168:169]\n"
    "v_pk_fma_f32 v[170:171], v[170:171], s[70:71], v[138:139]\n"
    "v_pk_fma_f32 v[230:231], v[230:231], v[170:171], v[232:233]\n"
    "v_pk_mul_f32 v[232:233], v[232:233], v[170:171]\n"
    "v_pk_fma_f32 v[172:173], v[172:173], s[70:71], v[140:141]\n"
    "v_pk_fma_f32 v[230:231], v[230:231], v[172:173], v[232:233]\n"
    "v_pk_mul_f32 v[232:233], v[232:233], v[172:173]\n"
    "v_pk_fma_f32 v[174:175], v[174:175], s[70:71], v[142:143]\n"
    "v_pk_fma_f32 v[230:231], v[230:231], v[174:175], v[232:233]\n"
    "v_pk_mul_f32 v[232:233], v[232:233], v[174:175]\n"
    "v_rcp_f32 v232, v232\n"
    "v_rcp_f32 v233, v233\n"
    "v_fma_f32 v230, v230, v232, v247\n"
    "v_fmac_f32 v230, v231, v233\n"
    "v_mbcnt_lo_u32_b32 v232, -1, 0\n"
    "v_mbcnt_hi_u32_b32 v232, -1, v232\n"
    "v_bfe_u32 v233, v232, 3, 1\n"
    "v_and_b32 v232, 7, v232\n"
    "v_lshl_add_u32 v232, v232, 1, v233\n"
    "v_add_u32 v232, s81, v232\n"
    "ds_read_u8 v232, v232 offset:49664\n"
    "s_add_u32 s81, s81, 16\n"
    "s_waitcnt lgkmcnt(0)\n"
    "buffer_load_ushort v232, v232, s[44:47], s63 idxen\n"
    "s_waitcnt vmcnt(0)\n"
    "v_lshlrev_b32 v232, 16, v232\n"
    "v_mul_f32 v230, v230, v232\n"
    "s_add_u32 s66, s66, 16\n"
    "s_waitcnt lgkmcnt(0)\n"
    "ds_write_b16_d16_hi v245, v230\n"
    "ds_read_b64 v[228:229], v246\n"
    "s_cmp_lt_u32 s34, s76\n"
    "s_cbranch_scc1 .Ldin_G0s_wait\n"
    ".Ldin_G0s_go:\n"
    "s_sub_u32 s94, s34, 30\n"
    "s_max_u32 s76, s76, s94\n"
    "s_add_u32 s76, s76, s72\n"
    "buffer_load_dwordx4 v[236:237], s[40:43], s67 idxen offen offset:0 nt lds\n"
    "buffer_load_dwordx4 v[238:239], s[40:43], s67 idxen offen offset:1024 nt lds\n"
    "s_cmp_eq_u32 s66, s82\n"
    "s_cbranch_scc1 .Ldin_L1s\n"
    "s_branch .Ldin_G1s\n"
    ".Ldin_A0s_poll:\n"
    "s_sleep 1\n"
    "ds_read_b64 v[216:217], v246 offset:128\n"
    "s_waitcnt lgkmcnt(0)\n"
    "v_readlane_b32 s60, v216, s59\n"
    "v_readlane_b32 s61, v217, s59\n"
    "s_cmp_eq_u32 s60, -1\n"
    "s_cbranch_scc1 .Ldin_A0s_poll\n"
    "s_branch .Ldin_A0s_pr\n"
    ".Ldin_G1s_slp:\n"
    "s_sleep 1\n"
    ".Ldin_G1s_wait:\n"
    "s_memrealtime s[34:35]\n"
    "s_waitcnt lgkmcnt(0)\n"
    "s_sub_u32 s94, s34, s76\n"
    "s_cmp_lt_i32 s94, 0\n"
    "s_cbranch_scc1 .Ldin_G1s_slp\n"
    "s_branch .Ldin_G1s_go\n"
    ".Ldin_G2s_slp:\n"
    "s_sleep 1\n"
    ".Ldin_G2s_wait:\n"
    "s_memrealtime s[34:35]\n"
    "s_waitcnt lgkmcnt(0)\n"
    "s_sub_u32 s94, s34, s76\n"
    "s_cmp_lt_i32 s94, 0\n"
    "s_cbranch_scc1 .Ldin_G2s_slp\n"
    "s_branch .Ldin_G2s_go\n"
    ".Ldin_G3s_slp:\n"
    "s_sleep 1\n"
    ".Ldin_G3s_wait:\n"
    "s_memrealtime s[34:35]\n"
    "s_waitcnt lgkmcnt(0)\n"
    "s_sub_u32 s94, s34, s76\n"
    "s_cmp_lt_i32 s94, 0\n"
    "s_cbranch_scc1 .Ldin_G3s_slp\n"
    "s_branch .Ldin_G3s_go\n"
    ".Ldin_G0s_slp:\n"
    "s_sleep 1\n"
    ".Ldin_G0s_wait:\n"
    "s_memrealtime s[34:35]\n"
    "s_waitcnt lgkmcnt(0)\n"
    "s_sub_u32 s94, s34, s76\n"
    "s_cmp_lt_i32 s94, 0\n"
    "s_cbranch_scc1 .Ldin_G0s_slp\n"
    "s_branch .Ldin_G0s_go\n"
    ".p2align 5\n"
    ".Ldin_L2s:\n"
    "s_setprio 0\n"
    "s_cmp_lt_i32 s60, 0\n"
    "s_cbranch_scc1 .Ldin_E0s\n"
    "ds_read_b128 v[144:147], v252 offset:8192\n"
    "ds_read_b128 v[148:151], v252 offset:8256\n"
    "ds_read_b128 v[152:155], v252 offset:16384\n"
    "ds_read_b128 v[156:159], v252 offset:16448\n"
    "s_add_u32 s76, s76, s73\n"
    "s_add_u32 m0, s68, 6144\n"
    "v_and_b32 v216, v248, v216\n"
    "v_and_b32 v217, v249, v217\n"
    "v_and_b32 v218, v248, v218\n"
    "v_and_b32 v219, v249, v219\n"
    "v_mfma_f32_16x16x16_bf16 v[176:179], v[216:217], v[96:97], v[32:35]\n"
    "v_and_b32 v220, v248, v220\n"
    "v_and_b32 v221, v249, v221\n"
    "v_and_b32 v222, v248, v222\n"
    "v_mfma_f32_16x16x16_bf16 v[180:183], v[218:219], v[98:99], v[36:39]\n"
    "v_exp_f32 v160, v160 clamp\n"
    "v_and_b32 v223, v249, v223\n"
    "v_mfma_f32_16x16x16_bf16 v[184:187], v[220:221], v[100:101], v[40:43]\n"
    "v_add_u32 v251, s66, v244\n"
    "v_cvt_pk_bf16_f32 v0, v176, v177\n"
    "v_cvt_pk_bf16_f32 v1, v178, v179\n"
    "s_waitcnt lgkmcnt(4)\n"
    "v_mfma_f32_16x16x16_bf16 v[188:191], v[222:223], v[102:103], v[44:47]\n"
    "v_exp_f32 v161, v161 clamp\n"
    "v_cvt_pk_bf16_f32 v2, v180, v181\n"
    "v_mfma_f32_4x4x4_16b_bf16 v[224:227], v[228:229], v[208:209], v[224:227] cbsz:4 abid:0\n"
    "v_cvt_pk_bf16_f32 v3, v182, v183\n"
    "v_mfma_f32_16x16x16_bf16 v[176:179], v[216:217], v[104:105], v[48:51]\n"
    "v_exp_f32 v162, v162 clamp\n"
    "v_cvt_pk_bf16_f32 v4, v184, v185\n"
    "v_mfma_f32_16x16x16_bf16 v[180:183], v[218:219], v[106:107], v[52:55]\n"
    "v_cvt_pk_bf16_f32 v5, v186, v187\n"
    "v_cvt_pk_bf16_f32 v6, v188, v189\n"
    "v_cvt_pk_bf16_f32 v7, v190, v191\n"
    "v_mfma_f32_16x16x16_bf16 v[184:187], v[220:221], v[108:109], v[56:59]\n"
    "v_exp_f32 v163, v163 clamp\n"
    "v_cvt_pk_bf16_f32 v8, v176, v177\n"
    "v_mfma_f32_4x4x4_16b_bf16 v[224:227], v[228:229], v[210:211], v[224:227] cbsz:4 abid:1\n"
    "v_cvt_pk_bf16_f32 v9, v178, v179\n"
    "v_mfma_f32_16x16x16_bf16 v[188:191], v[222:223], v[110:111], v[60:63]\n"
    "v_exp_f32 v164, v164 clamp\n"
    "v_cvt_pk_bf16_f32 v10, v180, v181\n"
    "v_mfma_f32_16x16x16_bf16 v[176:179], v[216:217], v[112:113], v[64:67]\n"
    "v_cvt_pk_bf16_f32 v11, v182, v183\n"
    "v_cvt_pk_bf16_f32 v12, v184, v185\n"
    "v_cvt_pk_bf16_f32 v13, v186, v187\n"
    "v_mfma_f32_16x16x16_bf16 v[180:183], v[218:219], v[114:115], v[68:71]\n"
    "v_exp_f32 v165, v165 clamp\n"
    "v_cvt_pk_bf16_f32 v14, v188, v189\n"
    "v_mfma_f32_4x4x4_16b_bf16 v[224:227], v[228:229], v[212:213], v[224:227] cbsz:4 abid:2\n"
    "v_cvt_pk_bf16_f32 v15, v190, v191\n"
    "v_mfma_f32_16x16x16_bf16 v[184:187], v[220:221], v[116:117], v[72:75]\n"
    "v_exp_f32 v166, v166 clamp\n"
    "v_cvt_pk_bf16_f32 v16, v176, v177\n"
    "v_mfma_f32_16x16x16_bf16 v[188:191], v[222:223], v[118:119], v[76:79]\n"
    "v_cvt_pk_bf16_f32 v17, v178, v179\n"
    "v_cvt_pk_bf16_f32 v18, v180, v181\n"
    "v_cvt_pk_bf16_f32 v19, v182, v183\n"
    "v_mfma_f32_16x16x16_bf16 v[176:179], v[216:217], v[120:121], v[80:83]\n"
    "v_exp_f32 v167, v167 clamp\n"
    "v_cvt_pk_bf16_f32 v20, v184, v185\n"
    "v_mfma_f32_4x4x4_16b_bf16 v[224:227], v[228:229], v[214:215], v[224:227] cbsz:4 abid:3\n"
    "v_cvt_pk_bf16_f32 v21, v186, v187\n"
    "v_mfma_f32_16x16x16_bf16 v[180:183], v[218:219], v[122:123], v[84:87]\n"
    "v_exp_f32 v168, v168 clamp\n"
    "v_cvt_pk_bf16_f32 v22, v188, v189\n"
    "v_mfma_f32_16x16x16_bf16 v[184:187], v[220:221], v[124:125], v[88:91]\n"
    "v_cvt_pk_bf16_f32 v23, v190, v191\n"
    "v_cvt_pk_bf16_f32 v24, v176, v177\n"
    "v_cvt_pk_bf16_f32 v25, v178, v179\n"
    "v_mfma_f32_16x16x16_bf16 v[188:191], v[222:223], v[126:127], v[92:95]\n"
    "v_exp_f32 v169, v169 clamp\n"
    "v_cvt_pk_bf16_f32 v26, v180, v181\n"
    "s_waitcnt lgkmcnt(0)\n"
    "v_mfma_f32_16x16x32_bf16 v[176:179], v[0:3], v[192:195], v[144:147]\n"
    "v_cvt_pk_bf16_f32 v27, v182, v183\n"
    "v_cvt_pk_bf16_f32 v28, v184, v185\n"
    "v_mfma_f32_16x16x32_bf16 v[176:179], v[4:7], v[196:199], v[176:179]\n"
    "v_cvt_pk_bf16_f32 v29, v186, v187\n"
    "v_cvt_pk_bf16_f32 v30, v188, v189\n"
    "v_mfma_f32_16x16x32_bf16 v[180:183], v[8:11], v[192:195], v[148:151]\n"
    "v_exp_f32 v170, v170 clamp\n"
    "v_mfma_f32_16x16x32_bf16 v[180:183], v[12:15], v[196:199], v[180:183]\n"
    "v_exp_f32 v171, v171 clamp\n"
    "v_mfma_f32_16x16x32_bf16 v[184:187], v[16:19], v[192:195], v[152:155]\n"
    "v_exp_f32 v172, v172 clamp\n"
    "v_mfma_f32_16x16x32_bf16 v[184:187], v[20:23], v[196:199], v[184:187]\n"
    "v_exp_f32 v173, v173 clamp\n"
    "v_cvt_pk_bf16_f32 v31, v190, v191\n"
    "v_mfma_f32_16x16x32_bf16 v[188:191], v[24:27], v[192:195], v[156:159]\n"
    "v_exp_f32 v174, v174 clamp\n"
    "v_mfma_f32_16x16x32_bf16 v[188:191], v[28:31], v[196:199], v[188:191]\n"
    "v_exp_f32 v175, v175 clamp\n"
    "s_waitcnt vmcnt(4)\n"
    "ds_read_b64_tr_b16 v[208:209], v240 offset:6144\n"
    "ds_read_b64_tr_b16 v[210:211], v241 offset:6144\n"
    "ds_read_b64_tr_b16 v[212:213], v242 offset:6144\n"
    "ds_read_b64_tr_b16 v[214:215], v243 offset:6144\n"
    "ds_read_u8 v236, v251 offset:0\n"
    "ds_read_u8 v238, v251 offset:1\n"
    "ds_read_b128 v[192:195], v234 offset:0\n"
    "ds_read_b128 v[196:199], v235 offset:0\n"
    "v_pk_fma_f32 v[160:161], v[160:161], s[70:71], v[128:129]\n"
    "v_pk_fma_f32 v[162:163], v[162:163], s[70:71], v[130:131]\n"
    "v_pk_add_f32 v[230:231], v[160:161], v[162:163]\n"
    "v_pk_mul_f32 v[232:233], v[160:161], v[162:163]\n"
    "v_pk_fma_f32 v[164:165], v[164:165], s[70:71], v[132:133]\n"
    "v_pk_fma_f32 v[230:231], v[230:231], v[164:165], v[232:233]\n"
    "v_pk_mul_f32 v[232:233], v[232:233], v[164:165]\n"
    "v_pk_fma_f32 v[166:167], v[166:167], s[70:71], v[134:135]\n"
    "v_pk_fma_f32 v[230:231], v[230:231], v[166:167], v[232:233]\n"
    "v_pk_mul_f32 v[232:233], v[232:233], v[166:167]\n"
    "v_pk_fma_f32 v[168:169], v[168:169], s[70:71], v[136:137]\n"
    "v_pk_fma_f32 v[230:231], v[230:231], v[168:169], v[232:233]\n"
    "v_pk_mul_f32 v[232:233], v[232:233], v[168:169]\n"
    "v_pk_fma_f32 v[170:171], v[170:171], s[70:71], v[138:139]\n"
    "v_pk_fma_f32 v[230:231], v[230:231], v[170:171], v[232:233]\n"
    "v_pk_mul_f32 v[232:233], v[232:233], v[170:171]\n"
    "v_pk_fma_f32 v[172:173], v[172:173], s[70:71], v[140:141]\n"
    "v_pk_fma_f32 v[230:231], v[230:231], v[172:173], v[232:233]\n"
    "v_pk_mul_f32 v[232:233], v[232:233], v[172:173]\n"
    "v_pk_fma_f32 v[174:175], v[174:175], s[70:71], v[142:143]\n"
    "v_pk_fma_f32 v[230:231], v[230:231], v[174:175], v[232:233]\n"
    "v_pk_mul_f32 v[232:233], v[232:233], v[174:175]\n"
    "v_rcp_f32 v232, v232\n"
    "v_rcp_f32 v233, v233\n"
    "v_fma_f32 v230, v230, v232, v247\n"
    "v_fmac_f32 v230, v231, v233\n"
    "v_mbcnt_lo_u32_b32 v232, -1, 0\n"
    "v_mbcnt_hi_u32_b32 v232, -1, v232\n"
    "v_bfe_u32 v233, v232, 3, 1\n"
    "v_and_b32 v232, 7, v232\n"
    "v_lshl_add_u32 v232, v232, 1, v233\n"
    "v_add_u32 v232, s81, v232\n"
    "ds_read_u8 v232, v232 offset:49664\n"
    "s_add_u32 s81, s81, 16\n"
    "s_waitcnt lgkmcnt(0)\n"
    "buffer_load_ushort v232, v232, s[44:47], s63 idxen\n"
    "s_waitcnt vmcnt(0)\n"
    "v_lshlrev_b32 v232, 16, v232\n"
    "v_mul_f32 v230, v230, v232\n"
    "s_add_u32 s66, s66, 16\n"
    "s_waitcnt lgkmcnt(0)\n"
    "ds_write_b16_d16_hi v245, v230\n"
    "ds_read_b64 v[228:229], v246\n"
    "s_lshl1_add_u32 s76, s72, s76\n"
    "buffer_load_dwordx4 v[236:237], s[40:43], s67 idxen offen offset:0 nt lds\n"
    "buffer_load_dwordx4 v[238:239], s[40:43], s67 idxen offen offset:1024 nt lds\n"
    "s_mov_b32 s81, s64\n"
    "s_lshr_b32 s63, s65, 6\n"
    "s_mov_b32 s80, s83\n"
    "s_add_u32 s62, s62, 256\n"
    "s_bitcmp1_b32 s60, 30\n"
    "s_cbranch_scc0 .Ldin_A3\n"
    ".Ldin_A3s:\n"
    "s_cbranch_vccz .Ldin_A3s_np\n"
    "s_setprio 1\n"
    ".Ldin_A3s_np:\n"
    "v_mfma_f32_16x16x32_bf16 v[160:163], v[0:3], v[192:195], v[144:147]\n"
    "v_exp_f32 v176, v176 clamp\n"
    "v_mfma_f32_16x16x32_bf16 v[160:163], v[4:7], v[196:199], v[160:163]\n"
    "v_exp_f32 v177, v177 clamp\n"
    "v_mfma_f32_16x16x32_bf16 v[164:167], v[8:11], v[192:195], v[148:151]\n"
    "v_exp_f32 v178, v178 clamp\n"
    "v_mfma_f32_16x16x32_bf16 v[164:167], v[12:15], v[196:199], v[164:167]\n"
    "v_exp_f32 v179, v179 clamp\n"
    "v_mfma_f32_16x16x32_bf16 v[168:171], v[16:19], v[192:195], v[152:155]\n"
    "v_exp_f32 v180, v180 clamp\n"
    "v_mfma_f32_16x16x32_bf16 v[168:171], v[20:23], v[196:199], v[168:171]\n"
    "v_exp_f32 v181, v181 clamp\n"
    "v_mfma_f32_16x16x32_bf16 v[172:175], v[24:27], v[192:195], v[156:159]\n"
    "v_exp_f32 v182, v182 clamp\n"
    "v_mfma_f32_16x16x32_bf16 v[172:175], v[28:31], v[196:199], v[172:175]\n"
    "v_exp_f32 v183, v183 clamp\n"
    "v_exp_f32 v184, v184 clamp\n"
    "s_mov_b32 m0, s62\n"
    "ds_read_b64 v[216:217], v246 offset:128\n"
    "v_exp_f32 v185, v185 clamp\n"
    "v_add_u32_e64 v251, s66, v244\n"
    "s_waitcnt lgkmcnt(1)\n"
    "v_mfma_f32_4x4x4_16b_bf16 v[224:227], v[228:229], v[200:201], v[224:227] cbsz:4 abid:0\n"
    "v_exp_f32 v186, v186 clamp\n"
    "v_exp_f32 v187, v187 clamp\n"
    "v_mfma_f32_4x4x4_16b_bf16 v[224:227], v[228:229], v[202:203], v[224:227] cbsz:4 abid:1\n"
    "v_exp_f32 v188, v188 clamp\n"
    "v_exp_f32 v189, v189 clamp\n"
    "v_mfma_f32_4x4x4_16b_bf16 v[224:227], v[228:229], v[204:205], v[224:227] cbsz:4 abid:2\n"
    "v_exp_f32 v190, v190 clamp\n"
    "v_exp_f32 v191, v191 clamp\n"
    "v_mfma_f32_4x4x4_16b_bf16 v[224:227], v[228:229], v[206:207], v[224:227] cbsz:4 abid:3\n"
    "s_waitcnt vmcnt(4)\n"
    "s_waitcnt lgkmcnt(0)\n"
    "v_readlane_b32 s60, v216, s59\n"
    "v_readlane_b32 s61, v217, s59\n"
    "ds_read_b64_tr_b16 v[200:201], v240 offset:0\n"
    "ds_read_b64_tr_b16 v[202:203], v241 offset:0\n"
    "ds_read_b64_tr_b16 v[204:205], v242 offset:0\n"
    "ds_read_b64_tr_b16 v[206:207], v243 offset:0\n"
    "ds_read_u8 v236, v251 offset:0\n"
    "ds_read_u8 v238, v251 offset:1\n"
    "ds_read_b128 v[192:195], v234 offset:2048\n"
    "ds_read_b128 v[196:199], v235 offset:2048\n"
    "s_cmp_eq_u32 s60, -1\n"
    "s_cbranch_scc1 .Ldin_A3s_poll\n"
    ".Ldin_A3s_pr:\n"
    "s_lshr_b32 s94, s60, 12\n"
    "s_and_b32 s94, s94, 0x1ff0\n"
    "v_xor_b32 v252, s94, v250\n"
    "v_pk_add_f32 v[230:231], v[224:225], v[226:227]\n"
    "v_add_f32 v230, v230, v231\n"
    "ds_write_addtid_b32 v230 offset:49152\n"
    "ds_read_b128 v[216:219], v252 offset:0\n"
    "ds_read_b128 v[220:223], v252 offset:64\n"
    "v_mov_b64 v[224:225], 0\n"
    "v_mov_b64_e64 v[226:227], 0\n"
    "v_pk_fma_f32 v[176:177], v[176:177], s[70:71], v[128:129]\n"
    "v_pk_fma_f32 v[178:179], v[178:179], s[70:71], v[130:131]\n"
    "v_pk_add_f32 v[230:231], v[176:177], v[178:179]\n"
    "v_pk_mul_f32 v[232:233], v[176:177], v[178:179]\n"
    "v_pk_fma_f32 v[180:181], v[180:181], s[70:71], v[132:133]\n"
    "v_pk_fma_f32 v[230:231], v[230:231], v[180:181], v[232:233]\n"
    "v_pk_mul_f32 v[232:233], v[232:233], v[180:181]\n"
    "v_pk_fma_f32 v[182:183], v[182:183], s[70:71], v[134:135]\n"
    "v_pk_fma_f32 v[230:231], v[230:231], v[182:183], v[232:233]\n"
    "v_pk_mul_f32 v[232:233], v[232:233], v[182:183]\n"
    "v_pk_fma_f32 v[184:185], v[184:185], s[70:71], v[136:137]\n"
    "v_pk_fma_f32 v[230:231], v[230:231], v[184:185], v[232:233]\n"
    "v_pk_mul_f32 v[232:233], v[232:233], v[184:185]\n"
    "v_pk_fma_f32 v[186:187], v[186:187], s[70:71], v[138:139]\n"
    "v_pk_fma_f32 v[230:231], v[230:231], v[186:187], v[232:233]\n"
    "v_pk_mul_f32 v[232:233], v[232:233], v[186:187]\n"
    "v_pk_fma_f32 v[188:189], v[188:189], s[70:71], v[140:141]\n"
    "v_pk_fma_f32 v[230:231], v[230:231], v[188:189], v[232:233]\n"
    "v_pk_mul_f32 v[232:233], v[232:233], v[188:189]\n"
    "v_pk_fma_f32 v[190:191], v[190:191], s[70:71], v[142:143]\n"
    "v_pk_fma_f32 v[230:231], v[230:231], v[190:191], v[232:233]\n"
    "v_pk_mul_f32 v[232:233], v[232:233], v[190:191]\n"
    "s_add_u32 m0, s68, 0\n"
    "s_and_b32 s64, s60, 0xffff\n"
    "s_bfe_u32 s94, s60, 0x30013\n"
    "s_bfe_u32 s95, s60, 0x50019\n"
    "s_mul_i32 s94, s94, s56\n"
    "s_lshl4_add_u32 s83, s95, s64\n"
    "s_add_u32 s65, s58, s94\n"
    "s_add_u32 s82, s64, 64\n"
    "s_add_u32 s58, s58, s57\n"
    "s_add_u32 s59, s59, 1\n"
    "s_sub_u32 s95, s61, s76\n"
    "s_lshr_b32 s94, s61, 16\n"
    "s_sext_i32_i16 s95, s95\n"
    "s_cmp_lt_i32 s95, -400\n"
    "s_cselect_b32 s94, 0, s94\n"
    "s_mul_i32 s95, s95, s94\n"
    "s_ashr_i32 s95, s95, 16\n"
    "s_max_i32 s95, s95, -10\n"
    "s_min_i32 s95, s95, 10\n"
    "s_add_u32 s72, s77, s95\n"
    "s_cmp_lt_i32 s60, 0\n"
    "s_cselect_b32 s72, 0, s72\n"
    "v_rcp_f32 v232, v232\n"
    "v_rcp_f32 v233, v233\n"
    "v_fma_f32 v230, v230, v232, v247\n"
    "v_fmac_f32 v230, v231, v233\n"
    "v_mbcnt_lo_u32_b32 v232, -1, 0\n"
    "v_mbcnt_hi_u32_b32 v232, -1, v232\n"
    "v_bfe_u32 v233, v232, 3, 1\n"
    "v_and_b32 v232, 7, v232\n"
    "v_lshl_add_u32 v232, v232, 1, v233\n"
    "v_add_u32 v232, s81, v232\n"
    "ds_read_u8 v232, v232 offset:49664\n"
    "s_add_u32 s81, s81, 16\n"
    "s_waitcnt lgkmcnt(0)\n"
    "buffer_load_ushort v232, v232, s[44:47], s63 idxen\n"
    "s_waitcnt vmcnt(0)\n"
    "v_lshlrev_b32 v232, 16, v232\n"
    "v_mul_f32 v230, v230, v232\n"
    "s_add_u32 s66, s66, 16\n"
    "s_waitcnt lgkmcnt(0)\n"
    "ds_write_b16_d16_hi v245, v230\n"
    "ds_read_b64 v[228:229], v246\n"
    "buffer_load_dwordx4 v[236:237], s[40:43], s67 idxen offen offset:0 nt lds\n"
    "buffer_load_dwordx4 v[238:239], s[40:43], s67 idxen offen offset:1024 nt lds\n"
    "s_branch .Ldin_G0s\n"
    ".Ldin_A3s_poll:\n"
    "s_sleep 1\n"
    "ds_read_b64 v[216:217], v246 offset:128\n"
    "s_waitcnt lgkmcnt(0)\n"
    "v_readlane_b32 s60, v216, s59\n"
    "v_readlane_b32 s61, v217, s59\n"
    "s_cmp_eq_u32 s60, -1\n"
    "s_cbranch_scc1 .Ldin_A3s_poll\n"
    "s_branch .Ldin_A3s_pr\n"
    ".p2align 5\n"
    ".Ldin_L1s:\n"
    "s_setprio 0\n"
    "s_cmp_lt_i32 s60, 0\n"
    "s_cbranch_scc1 .Ldin_E1s\n"
    "ds_read_b128 v[144:147], v252 offset:8192\n"
    "ds_read_b128 v[148:151], v252 offset:8256\n"
    "ds_read_b128 v[152:155], v252 offset:16384\n"
    "ds_read_b128 v[156:159], v252 offset:16448\n"
    "s_add_u32 s76, s76, s73\n"
    "s_add_u32 m0, s68, 4096\n"
    "v_and_b32 v216, v248, v216\n"
    "v_and_b32 v217, v249, v217\n"
    "v_and_b32 v218, v248, v218\n"
    "v_and_b32 v219, v249, v219\n"
    "v_mfma_f32_16x16x16_bf16 v[160:163], v[216:217], v[96:97], v[32:35]\n"
    "v_and_b32 v220, v248, v220\n"
    "v_and_b32 v221, v249, v221\n"
    "v_and_b32 v222, v248, v222\n"
    "v_mfma_f32_16x16x16_bf16 v[164:167], v[218:219], v[98:99], v[36:39]\n"
    "v_exp_f32 v176, v176 clamp\n"
    "v_and_b32 v223, v249, v223\n"
    "v_mfma_f32_16x16x16_bf16 v[168:171], v[220:221], v[100:101], v[40:43]\n"
    "v_add_u32 v251, s66, v244\n"
    "v_cvt_pk_bf16_f32 v0, v160, v161\n"
    "v_cvt_pk_bf16_f32 v1, v162, v163\n"
    "s_waitcnt lgkmcnt(4)\n"
    "v_mfma_f32_16x16x16_bf16 v[172:175], v[222:223], v[102:103], v[44:47]\n"
    "v_exp_f32 v177, v177 clamp\n"
    "v_cvt_pk_bf16_f32 v2, v164, v165\n"
    "v_mfma_f32_4x4x4_16b_bf16 v[224:227], v[228:229], v[200:201], v[224:227] cbsz:4 abid:0\n"
    "v_cvt_pk_bf16_f32 v3, v166, v167\n"
    "v_mfma_f32_16x16x16_bf16 v[160:163], v[216:217], v[104:105], v[48:51]\n"
    "v_exp_f32 v178, v178 clamp\n"
    "v_cvt_pk_bf16_f32 v4, v168, v169\n"
    "v_mfma_f32_16x16x16_bf16 v[164:167], v[218:219], v[106:107], v[52:55]\n"
    "v_cvt_pk_bf16_f32 v5, v170, v171\n"
    "v_cvt_pk_bf16_f32 v6, v172, v173\n"
    "v_cvt_pk_bf16_f32 v7, v174, v175\n"
    "v_mfma_f32_16x16x16_bf16 v[168:171], v[220:221], v[108:109], v[56:59]\n"
    "v_exp_f32 v179, v179 clamp\n"
    "v_cvt_pk_bf16_f32 v8, v160, v161\n"
    "v_mfma_f32_4x4x4_16b_bf16 v[224:227], v[228:229], v[202:203], v[224:227] cbsz:4 abid:1\n"
    "v_cvt_pk_bf16_f32 v9, v162, v163\n"
    "v_mfma_f32_16x16x16_bf16 v[172:175], v[222:223], v[110:111], v[60:63]\n"
    "v_exp_f32 v180, v180 clamp\n"
    "v_cvt_pk_bf16_f32 v10, v164, v165\n"
    "v_mfma_f32_16x16x16_bf16 v[160:163], v[216:217], v[112:113], v[64:67]\n"
    "v_cvt_pk_bf16_f32 v11, v166, v167\n"
    "v_cvt_pk_bf16_f32 v12, v168, v169\n"
    "v_cvt_pk_bf16_f32 v13, v170, v171\n"
    "v_mfma_f32_16x16x16_bf16 v[164:167], v[218:219], v[114:115], v[68:71]\n"
    "v_exp_f32 v181, v181 clamp\n"
    "v_cvt_pk_bf16_f32 v14, v172, v173\n"
    "v_mfma_f32_4x4x4_16b_bf16 v[224:227], v[228:229], v[204:205], v[224:227] cbsz:4 abid:2\n"
    "v_cvt_pk_bf16_f32 v15, v174, v175\n"
    "v_mfma_f32_16x16x16_bf16 v[168:171], v[220:221], v[116:117], v[72:75]\n"
    "v_exp_f32 v182, v182 clamp\n"
    "v_cvt_pk_bf16_f32 v16, v160, v161\n"
    "v_mfma_f32_16x16x16_bf16 v[172:175], v[222:223], v[118:119], v[76:79]\n"
    "v_cvt_pk_bf16_f32 v17, v162, v163\n"
    "v_cvt_pk_bf16_f32 v18, v164, v165\n"
    "v_cvt_pk_bf16_f32 v19, v166, v167\n"
    "v_mfma_f32_16x16x16_bf16 v[160:163], v[216:217], v[120:121], v[80:83]\n"
    "v_exp_f32 v183, v183 clamp\n"
    "v_cvt_pk_bf16_f32 v20, v168, v169\n"
    "v_mfma_f32_4x4x4_16b_bf16 v[224:227], v[228:229], v[206:207], v[224:227] cbsz:4 abid:3\n"
    "v_cvt_pk_bf16_f32 v21, v170, v171\n"
    "v_mfma_f32_16x16x16_bf16 v[164:167], v[218:219], v[122:123], v[84:87]\n"
    "v_exp_f32 v184, v184 clamp\n"
    "v_cvt_pk_bf16_f32 v22, v172, v173\n"
    "v_mfma_f32_16x16x16_bf16 v[168:171], v[220:221], v[124:125], v[88:91]\n"
    "v_cvt_pk_bf16_f32 v23, v174, v175\n"
    "v_cvt_pk_bf16_f32 v24, v160, v161\n"
    "v_cvt_pk_bf16_f32 v25, v162, v163\n"
    "v_mfma_f32_16x16x16_bf16 v[172:175], v[222:223], v[126:127], v[92:95]\n"
    "v_exp_f32 v185, v185 clamp\n"
    "v_cvt_pk_bf16_f32 v26, v164, v165\n"
    "s_waitcnt lgkmcnt(0)\n"
    "v_mfma_f32_16x16x32_bf16 v[160:163], v[0:3], v[192:195], v[144:147]\n"
    "v_cvt_pk_bf16_f32 v27, v166, v167\n"
    "v_cvt_pk_bf16_f32 v28, v168, v169\n"
    "v_mfma_f32_16x16x32_bf16 v[160:163], v[4:7], v[196:199], v[160:163]\n"
    "v_cvt_pk_bf16_f32 v29, v170, v171\n"
    "v_cvt_pk_bf16_f32 v30, v172, v173\n"
    "v_mfma_f32_16x16x32_bf16 v[164:167], v[8:11], v[192:195], v[148:151]\n"
    "v_exp_f32 v186, v186 clamp\n"
    "v_mfma_f32_16x16x32_bf16 v[164:167], v[12:15], v[196:199], v[164:167]\n"
    "v_exp_f32 v187, v187 clamp\n"
    "v_mfma_f32_16x16x32_bf16 v[168:171], v[16:19], v[192:195], v[152:155]\n"
    "v_exp_f32 v188, v188 clamp\n"
    "v_mfma_f32_16x16x32_bf16 v[168:171], v[20:23], v[196:199], v[168:171]\n"
    "v_exp_f32 v189, v189 clamp\n"
    "v_cvt_pk_bf16_f32 v31, v174, v175\n"
    "v_mfma_f32_16x16x32_bf16 v[172:175], v[24:27], v[192:195], v[156:159]\n"
    "v_exp_f32 v190, v190 clamp\n"
    "v_mfma_f32_16x16x32_bf16 v[172:175], v[28:31], v[196:199], v[172:175]\n"
    "v_exp_f32 v191, v191 clamp\n"
    "s_waitcnt vmcnt(4)\n"
    "ds_read_b64_tr_b16 v[200:201], v240 offset:4096\n"
    "ds_read_b64_tr_b16 v[202:203], v241 offset:4096\n"
    "ds_read_b64_tr_b16 v[204:205], v242 offset:4096\n"
    "ds_read_b64_tr_b16 v[206:207], v243 offset:4096\n"
    "ds_read_u8 v236, v251 offset:0\n"
    "ds_read_u8 v238, v251 offset:1\n"
    "ds_read_b128 v[192:195], v234 offset:6144\n"
    "ds_read_b128 v[196:199], v235 offset:6144\n"
    "v_pk_fma_f32 v[176:177], v[176:177], s[70:71], v[128:129]\n"
    "v_pk_fma_f32 v[178:179], v[178:179], s[70:71], v[130:131]\n"
    "v_pk_add_f32 v[230:231], v[176:177], v[178:179]\n"
    "v_pk_mul_f32 v[232:233], v[176:177], v[178:179]\n"
    "v_pk_fma_f32 v[180:181], v[180:181], s[70:71], v[132:133]\n"
    "v_pk_fma_f32 v[230:231], v[230:231], v[180:181], v[232:233]\n"
    "v_pk_mul_f32 v[232:233], v[232:233], v[180:181]\n"
    "v_pk_fma_f32 v[182:183], v[182:183], s[70:71], v[134:135]\n"
    "v_pk_fma_f32 v[230:231], v[230:231], v[182:183], v[232:233]\n"
    "v_pk_mul_f32 v[232:233], v[232:233], v[182:183]\n"
    "v_pk_fma_f32 v[184:185], v[184:185], s[70:71], v[136:137]\n"
    "v_pk_fma_f32 v[230:231], v[230:231], v[184:185], v[232:233]\n"
    "v_pk_mul_f32 v[232:233], v[232:233], v[184:185]\n"
    "v_pk_fma_f32 v[186:187], v[186:187], s[70:71], v[138:139]\n"
    "v_pk_fma_f32 v[230:231], v[230:231], v[186:187], v[232:233]\n"
    "v_pk_mul_f32 v[232:233], v[232:233], v[186:187]\n"
    "v_pk_fma_f32 v[188:189], v[188:189], s[70:71], v[140:141]\n"
    "v_pk_fma_f32 v[230:231], v[230:231], v[188:189], v[232:233]\n"
    "v_pk_mul_f32 v[232:233], v[232:233], v[188:189]\n"
    "v_pk_fma_f32 v[190:191], v[190:191], s[70:71], v[142:143]\n"
    "v_pk_fma_f32 v[230:231], v[230:231], v[190:191], v[232:233]\n"
    "v_pk_mul_f32 v[232:233], v[232:233], v[190:191]\n"
    "v_rcp_f32 v232, v232\n"
    "v_rcp_f32 v233, v233\n"
    "v_fma_f32 v230, v230, v232, v247\n"
    "v_fmac_f32 v230, v231, v233\n"
    "v_mbcnt_lo_u32_b32 v232, -1, 0\n"
    "v_mbcnt_hi_u32_b32 v232, -1, v232\n"
    "v_bfe_u32 v233, v232, 3, 1\n"
    "v_and_b32 v232, 7, v232\n"
    "v_lshl_add_u32 v232, v232, 1, v233\n"
    "v_add_u32 v232, s81, v232\n"
    "ds_read_u8 v232, v232 offset:49664\n"
    "s_add_u32 s81, s81, 16\n"
    "s_waitcnt lgkmcnt(0)\n"
    "buffer_load_ushort v232, v232, s[44:47], s63 idxen\n"
    "s_waitcnt vmcnt(0)\n"
    "v_lshlrev_b32 v232, 16, v232\n"
    "v_mul_f32 v230, v230, v232\n"
    "s_add_u32 s66, s66, 16\n"
    "s_waitcnt lgkmcnt(0)\n"
    "ds_write_b16_d16_hi v245, v230\n"
    "ds_read_b64 v[228:229], v246\n"
    "s_lshl1_add_u32 s76, s72, s76\n"
    "buffer_load_dwordx4 v[236:237], s[40:43], s67 idxen offen offset:0 nt lds\n"
    "buffer_load_dwordx4 v[238:239], s[40:43], s67 idxen offen offset:1024 nt lds\n"
    "s_mov_b32 s81, s64\n"
    "s_lshr_b32 s63, s65, 6\n"
    "s_mov_b32 s80, s83\n"
    "s_add_u32 s62, s62, 256\n"
    "s_bitcmp1_b32 s60, 30\n"
    "s_cbranch_scc0 .Ldin_A2\n"
    ".Ldin_A2s:\n"
    "s_cbranch_vccz .Ldin_A2s_np\n"
    "s_setprio 1\n"
    ".Ldin_A2s_np:\n"
    "v_mfma_f32_16x16x32_bf16 v[176:179], v[0:3], v[192:195], v[144:147]\n"
    "v_exp_f32 v160, v160 clamp\n"
    "v_mfma_f32_16x16x32_bf16 v[176:179], v[4:7], v[196:199], v[176:179]\n"
    "v_exp_f32 v161, v161 clamp\n"
    "v_mfma_f32_16x16x32_bf16 v[180:183], v[8:11], v[192:195], v[148:151]\n"
    "v_exp_f32 v162, v162 clamp\n"
    "v_mfma_f32_16x16x32_bf16 v[180:183], v[12:15], v[196:199], v[180:183]\n"
    "v_exp_f32 v163, v163 clamp\n"
    "v_mfma_f32_16x16x32_bf16 v[184:187], v[16:19], v[192:195], v[152:155]\n"
    "v_exp_f32 v164, v164 clamp\n"
    "v_mfma_f32_16x16x32_bf16 v[184:187], v[20:23], v[196:199], v[184:187]\n"
    "v_exp_f32 v165, v165 clamp\n"
    "v_mfma_f32_16x16x32_bf16 v[188:191], v[24:27], v[192:195], v[156:159]\n"
    "v_exp_f32 v166, v166 clamp\n"
    "v_mfma_f32_16x16x32_bf16 v[188:191], v[28:31], v[196:199], v[188:191]\n"
    "v_exp_f32 v167, v167 clamp\n"
    "v_exp_f32 v168, v168 clamp\n"
    "s_mov_b32 m0, s62\n"
    "ds_read_b64 v[216:217], v246 offset:128\n"
    "v_exp_f32 v169, v169 clamp\n"
    "v_add_u32_e64 v251, s66, v244\n"
    "s_waitcnt lgkmcnt(1)\n"
    "v_mfma_f32_4x4x4_16b_bf16 v[224:227], v[228:229], v[208:209], v[224:227] cbsz:4 abid:0\n"
    "v_exp_f32 v170, v170 clamp\n"
    "v_exp_f32 v171, v171 clamp\n"
    "v_mfma_f32_4x4x4_16b_bf16 v[224:227], v[228:229], v[210:211], v[224:227] cbsz:4 abid:1\n"
    "v_exp_f32 v172, v172 clamp\n"
    "v_exp_f32 v173, v173 clamp\n"
    "v_mfma_f32_4x4x4_16b_bf16 v[224:227], v[228:229], v[212:213], v[224:227] cbsz:4 abid:2\n"
    "v_exp_f32 v174, v174 clamp\n"
    "v_exp_f32 v175, v175 clamp\n"
    "v_mfma_f32_4x4x4_16b_bf16 v[224:227], v[228:229], v[214:215], v[224:227] cbsz:4 abid:3\n"
    "s_waitcnt vmcnt(4)\n"
    "s_waitcnt lgkmcnt(0)\n"
    "v_readlane_b32 s60, v216, s59\n"
    "v_readlane_b32 s61, v217, s59\n"
    "ds_read_b64_tr_b16 v[208:209], v240 offset:6144\n"
    "ds_read_b64_tr_b16 v[210:211], v241 offset:6144\n"
    "ds_read_b64_tr_b16 v[212:213], v242 offset:6144\n"
    "ds_read_b64_tr_b16 v[214:215], v243 offset:6144\n"
    "ds_read_u8 v236, v251 offset:0\n"
    "ds_read_u8 v238, v251 offset:1\n"
    "ds_read_b128 v[192:195], v234 offset:0\n"
    "ds_read_b128 v[196:199], v235 offset:0\n"
    "s_cmp_eq_u32 s60, -1\n"
    "s_cbranch_scc1 .Ldin_A2s_poll\n"
    ".Ldin_A2s_pr:\n"
    "s_lshr_b32 s94, s60, 12\n"
    "s_and_b32 s94, s94, 0x1ff0\n"
    "v_xor_b32 v252, s94, v250\n"
    "v_pk_add_f32 v[230:231], v[224:225], v[226:227]\n"
    "v_add_f32 v230, v230, v231\n"
    "ds_write_addtid_b32 v230 offset:49152\n"
    "ds_read_b128 v[216:219], v252 offset:0\n"
    "ds_read_b128 v[220:223], v252 offset:64\n"
    "v_mov_b64 v[224:225], 0\n"
    "v_mov_b64_e64 v[226:227], 0\n"
    "v_pk_fma_f32 v[160:161], v[160:161], s[70:71], v[128:129]\n"
    "v_pk_fma_f32 v[162:163], v[162:163], s[70:71], v[130:131]\n"
    "v_pk_add_f32 v[230:231], v[160:161], v[162:163]\n"
    "v_pk_mul_f32 v[232:233], v[160:161], v[162:163]\n"
    "v_pk_fma_f32 v[164:165], v[164:165], s[70:71], v[132:133]\n"
    "v_pk_fma_f32 v[230:231], v[230:231], v[164:165], v[232:233]\n"
    "v_pk_mul_f32 v[232:233], v[232:233], v[164:165]\n"
    "v_pk_fma_f32 v[166:167], v[166:167], s[70:71], v[134:135]\n"
    "v_pk_fma_f32 v[230:231], v[230:231], v[166:167], v[232:233]\n"
    "v_pk_mul_f32 v[232:233], v[232:233], v[166:167]\n"
    "v_pk_fma_f32 v[168:169], v[168:169], s[70:71], v[136:137]\n"
    "v_pk_fma_f32 v[230:231], v[230:231], v[168:169], v[232:233]\n"
    "v_pk_mul_f32 v[232:233], v[232:233], v[168:169]\n"
    "v_pk_fma_f32 v[170:171], v[170:171], s[70:71], v[138:139]\n"
    "v_pk_fma_f32 v[230:231], v[230:231], v[170:171], v[232:233]\n"
    "v_pk_mul_f32 v[232:233], v[232:233], v[170:171]\n"
    "v_pk_fma_f32 v[172:173], v[172:173], s[70:71], v[140:141]\n"
    "v_pk_fma_f32 v[230:231], v[230:231], v[172:173], v[232:233]\n"
    "v_pk_mul_f32 v[232:233], v[232:233], v[172:173]\n"
    "v_pk_fma_f32 v[174:175], v[174:175], s[70:71], v[142:143]\n"
    "v_pk_fma_f32 v[230:231], v[230:231], v[174:175], v[232:233]\n"
    "v_pk_mul_f32 v[232:233], v[232:233], v[174:175]\n"
    "s_add_u32 m0, s68, 6144\n"
    "s_and_b32 s64, s60, 0xffff\n"
    "s_bfe_u32 s94, s60, 0x30013\n"
    "s_bfe_u32 s95, s60, 0x50019\n"
    "s_mul_i32 s94, s94, s56\n"
    "s_lshl4_add_u32 s83, s95, s64\n"
    "s_add_u32 s65, s58, s94\n"
    "s_add_u32 s82, s64, 64\n"
    "s_add_u32 s58, s58, s57\n"
    "s_add_u32 s59, s59, 1\n"
    "s_sub_u32 s95, s61, s76\n"
    "s_lshr_b32 s94, s61, 16\n"
    "s_sext_i32_i16 s95, s95\n"
    "s_cmp_lt_i32 s95, -400\n"
    "s_cselect_b32 s94, 0, s94\n"
    "s_mul_i32 s95, s95, s94\n"
    "s_ashr_i32 s95, s95, 16\n"
    "s_max_i32 s95, s95, -10\n"
    "s_min_i32 s95, s95, 10\n"
    "s_add_u32 s72, s77, s95\n"
    "s_cmp_lt_i32 s60, 0\n"
    "s_cselect_b32 s72, 0, s72\n"
    "v_rcp_f32 v232, v232\n"
    "v_rcp_f32 v233, v233\n"
    "v_fma_f32 v230, v230, v232, v247\n"
    "v_fmac_f32 v230, v231, v233\n"
    "v_mbcnt_lo_u32_b32 v232, -1, 0\n"
    "v_mbcnt_hi_u32_b32 v232, -1, v232\n"
    "v_bfe_u32 v233, v232, 3, 1\n"
    "v_and_b32 v232, 7, v232\n"
    "v_lshl_add_u32 v232, v232, 1, v233\n"
    "v_add_u32 v232, s81, v232\n"
    "ds_read_u8 v232, v232 offset:49664\n"
    "s_add_u32 s81, s81, 16\n"
    "s_waitcnt lgkmcnt(0)\n"
    "buffer_load_ushort v232, v232, s[44:47], s63 idxen\n"
    "s_waitcnt vmcnt(0)\n"
    "v_lshlrev_b32 v232, 16, v232\n"
    "v_mul_f32 v230, v230, v232\n"
    "s_add_u32 s66, s66, 16\n"
    "s_waitcnt lgkmcnt(0)\n"
    "ds_write_b16_d16_hi v245, v230\n"
    "ds_read_b64 v[228:229], v246\n"
    "buffer_load_dwordx4 v[236:237], s[40:43], s67 idxen offen offset:0 nt lds\n"
    "buffer_load_dwordx4 v[238:239], s[40:43], s67 idxen offen offset:1024 nt lds\n"
    "s_branch .Ldin_G3s\n"
    ".Ldin_A2s_poll:\n"
    "s_sleep 1\n"
    "ds_read_b64 v[216:217], v246 offset:128\n"
    "s_waitcnt lgkmcnt(0)\n"
    "v_readlane_b32 s60, v216, s59\n"
    "v_readlane_b32 s61, v217, s59\n"
    "s_cmp_eq_u32 s60, -1\n"
    "s_cbranch_scc1 .Ldin_A2s_poll\n"
    "s_branch .Ldin_A2s_pr\n"
    ".p2align 5\n"
    ".Ldin_L0s:\n"
    "s_setprio 0\n"
    "s_cmp_lt_i32 s60, 0\n"
    "s_cbranch_scc1 .Ldin_E0s\n"
    "ds_read_b128 v[144:147], v252 offset:8192\n"
    "ds_read_b128 v[148:151], v252 offset:8256\n"
    "ds_read_b128 v[152:155], v252 offset:16384\n"
    "ds_read_b128 v[156:159], v252 offset:16448\n"
    "s_add_u32 s76, s76, s73\n"
    "s_add_u32 m0, s68, 2048\n"
    "v_and_b32 v216, v248, v216\n"
    "v_and_b32 v217, v249, v217\n"
    "v_and_b32 v218, v248, v218\n"
    "v_and_b32 v219, v249, v219\n"
    "v_mfma_f32_16x16x16_bf16 v[176:179], v[216:217], v[96:97], v[32:35]\n"
    "v_and_b32 v220, v248, v220\n"
    "v_and_b32 v221, v249, v221\n"
    "v_and_b32 v222, v248, v222\n"
    "v_mfma_f32_16x16x16_bf16 v[180:183], v[218:219], v[98:99], v[36:39]\n"
    "v_exp_f32 v160, v160 clamp\n"
    "v_and_b32 v223, v249, v223\n"
    "v_mfma_f32_16x16x16_bf16 v[184:187], v[220:221], v[100:101], v[40:43]\n"
    "v_add_u32 v251, s66, v244\n"
    "v_cvt_pk_bf16_f32 v0, v176, v177\n"
    "v_cvt_pk_bf16_f32 v1, v178, v179\n"
    "s_waitcnt lgkmcnt(4)\n"
    "v_mfma_f32_16x16x16_bf16 v[188:191], v[222:223], v[102:103], v[44:47]\n"
    "v_exp_f32 v161, v161 clamp\n"
    "v_cvt_pk_bf16_f32 v2, v180, v181\n"
    "v_mfma_f32_4x4x4_16b_bf16 v[224:227], v[228:229], v[208:209], v[224:227] cbsz:4 abid:0\n"
    "v_cvt_pk_bf16_f32 v3, v182, v183\n"
    "v_mfma_f32_16x16x16_bf16 v[176:179], v[216:217], v[104:105], v[48:51]\n"
    "v_exp_f32 v162, v162 clamp\n"
    "v_cvt_pk_bf16_f32 v4, v184, v185\n"
    "v_mfma_f32_16x16x16_bf16 v[180:183], v[218:219], v[106:107], v[52:55]\n"
    "v_cvt_pk_bf16_f32 v5, v186, v187\n"
    "v_cvt_pk_bf16_f32 v6, v188, v189\n"
    "v_cvt_pk_bf16_f32 v7, v190, v191\n"
    "v_mfma_f32_16x16x16_bf16 v[184:187], v[220:221], v[108:109], v[56:59]\n"
    "v_exp_f32 v163, v163 clamp\n"
    "v_cvt_pk_bf16_f32 v8, v176, v177\n"
    "v_mfma_f32_4x4x4_16b_bf16 v[224:227], v[228:229], v[210:211], v[224:227] cbsz:4 abid:1\n"
    "v_cvt_pk_bf16_f32 v9, v178, v179\n"
    "v_mfma_f32_16x16x16_bf16 v[188:191], v[222:223], v[110:111], v[60:63]\n"
    "v_exp_f32 v164, v164 clamp\n"
    "v_cvt_pk_bf16_f32 v10, v180, v181\n"
    "v_mfma_f32_16x16x16_bf16 v[176:179], v[216:217], v[112:113], v[64:67]\n"
    "v_cvt_pk_bf16_f32 v11, v182, v183\n"
    "v_cvt_pk_bf16_f32 v12, v184, v185\n"
    "v_cvt_pk_bf16_f32 v13, v186, v187\n"
    "v_mfma_f32_16x16x16_bf16 v[180:183], v[218:219], v[114:115], v[68:71]\n"
    "v_exp_f32 v165, v165 clamp\n"
    "v_cvt_pk_bf16_f32 v14, v188, v189\n"
    "v_mfma_f32_4x4x4_16b_bf16 v[224:227], v[228:229], v[212:213], v[224:227] cbsz:4 abid:2\n"
    "v_cvt_pk_bf16_f32 v15, v190, v191\n"
    "v_mfma_f32_16x16x16_bf16 v[184:187], v[220:221], v[116:117], v[72:75]\n"
    "v_exp_f32 v166, v166 clamp\n"
    "v_cvt_pk_bf16_f32 v16, v176, v177\n"
    "v_mfma_f32_16x16x16_bf16 v[188:191], v[222:223], v[118:119], v[76:79]\n"
    "v_cvt_pk_bf16_f32 v17, v178, v179\n"
    "v_cvt_pk_bf16_f32 v18, v180, v181\n"
    "v_cvt_pk_bf16_f32 v19, v182, v183\n"
    "v_mfma_f32_16x16x16_bf16 v[176:179], v[216:217], v[120:121], v[80:83]\n"
    "v_exp_f32 v167, v167 clamp\n"
    "v_cvt_pk_bf16_f32 v20, v184, v185\n"
    "v_mfma_f32_4x4x4_16b_bf16 v[224:227], v[228:229], v[214:215], v[224:227] cbsz:4 abid:3\n"
    "v_cvt_pk_bf16_f32 v21, v186, v187\n"
    "v_mfma_f32_16x16x16_bf16 v[180:183], v[218:219], v[122:123], v[84:87]\n"
    "v_exp_f32 v168, v168 clamp\n"
    "v_cvt_pk_bf16_f32 v22, v188, v189\n"
    "v_mfma_f32_16x16x16_bf16 v[184:187], v[220:221], v[124:125], v[88:91]\n"
    "v_cvt_pk_bf16_f32 v23, v190, v191\n"
    "v_cvt_pk_bf16_f32 v24, v176, v177\n"
    "v_cvt_pk_bf16_f32 v25, v178, v179\n"
    "v_mfma_f32_16x16x16_bf16 v[188:191], v[222:223], v[126:127], v[92:95]\n"
    "v_exp_f32 v169, v169 clamp\n"
    "v_cvt_pk_bf16_f32 v26, v180, v181\n"
    "s_waitcnt lgkmcnt(0)\n"
    "v_mfma_f32_16x16x32_bf16 v[176:179], v[0:3], v[192:195], v[144:147]\n"
    "v_cvt_pk_bf16_f32 v27, v182, v183\n"
    "v_cvt_pk_bf16_f32 v28, v184, v185\n"
    "v_mfma_f32_16x16x32_bf16 v[176:179], v[4:7], v[196:199], v[176:179]\n"
    "v_cvt_pk_bf16_f32 v29, v186, v187\n"
    "v_cvt_pk_bf16_f32 v30, v188, v189\n"
    "v_mfma_f32_16x16x32_bf16 v[180:183], v[8:11], v[192:195], v[148:151]\n"
    "v_exp_f32 v170, v170 clamp\n"
    "v_mfma_f32_16x16x32_bf16 v[180:183], v[12:15], v[196:199], v[180:183]\n"
    "v_exp_f32 v171, v171 clamp\n"
    "v_mfma_f32_16x16x32_bf16 v[184:187], v[16:19], v[192:195], v[152:155]\n"
    "v_exp_f32 v172, v172 clamp\n"
    "v_mfma_f32_16x16x32_bf16 v[184:187], v[20:23], v[196:199], v[184:187]\n"
    "v_exp_f32 v173, v173 clamp\n"
    "v_cvt_pk_bf16_f32 v31, v190, v191\n"
    "v_mfma_f32_16x16x32_bf16 v[188:191], v[24:27], v[192:195], v[156:159]\n"
    "v_exp_f32 v174, v174 clamp\n"
    "v_mfma_f32_16x16x32_bf16 v[188:191], v[28:31], v[196:199], v[188:191]\n"
    "v_exp_f32 v175, v175 clamp\n"
    "s_waitcnt vmcnt(4)\n"
    "ds_read_b64_tr_b16 v[208:209], v240 offset:2048\n"
    "ds_read_b64_tr_b16 v[210:211], v241 offset:2048\n"
    "ds_read_b64_tr_b16 v[212:213], v242 offset:2048\n"
    "ds_read_b64_tr_b16 v[214:215], v243 offset:2048\n"
    "ds_read_u8 v236, v251 offset:0\n"
    "ds_read_u8 v238, v251 offset:1\n"
    "ds_read_b128 v[192:195], v234 offset:4096\n"
    "ds_read_b128 v[196:199], v235 offset:4096\n"
    "v_pk_fma_f32 v[160:161], v[160:161], s[70:71], v[128:129]\n"
    "v_pk_fma_f32 v[162:163], v[162:163], s[70:71], v[130:131]\n"
    "v_pk_add_f32 v[230:231], v[160:161], v[162:163]\n"
    "v_pk_mul_f32 v[232:233], v[160:161], v[162:163]\n"
    "v_pk_fma_f32 v[164:165], v[164:165], s[70:71], v[132:133]\n"
    "v_pk_fma_f32 v[230:231], v[230:231], v[164:165], v[232:233]\n"
    "v_pk_mul_f32 v[232:233], v[232:233], v[164:165]\n"
    "v_pk_fma_f32 v[166:167], v[166:167], s[70:71], v[134:135]\n"
    "v_pk_fma_f32 v[230:231], v[230:231], v[166:167], v[232:233]\n"
    "v_pk_mul_f32 v[232:233], v[232:233], v[166:167]\n"
    "v_pk_fma_f32 v[168:169], v[168:169], s[70:71], v[136:137]\n"
    "v_pk_fma_f32 v[230:231], v[230:231], v[168:169], v[232:233]\n"
    "v_pk_mul_f32 v[232:233], v[232:233], v[168:169]\n"
    "v_pk_fma_f32 v[170:171], v[170:171], s[70:71], v[138:139]\n"
    "v_pk_fma_f32 v[230:231], v[230:231], v[170:171], v[232:233]\n"
    "v_pk_mul_f32 v[232:233], v[232:233], v[170:171]\n"
    "v_pk_fma_f32 v[172:173], v[172:173], s[70:71], v[140:141]\n"
    "v_pk_fma_f32 v[230:231], v[230:231], v[172:173], v[232:233]\n"
    "v_pk_mul_f32 v[232:233], v[232:233], v[172:173]\n"
    "v_pk_fma_f32 v[174:175], v[174:175], s[70:71], v[142:143]\n"
    "v_pk_fma_f32 v[230:231], v[230:231], v[174:175], v[232:233]\n"
    "v_pk_mul_f32 v[232:233], v[232:233], v[174:175]\n"
    "v_rcp_f32 v232, v232\n"
    "v_rcp_f32 v233, v233\n"
    "v_fma_f32 v230, v230, v232, v247\n"
    "v_fmac_f32 v230, v231, v233\n"
    "v_mbcnt_lo_u32_b32 v232, -1, 0\n"
    "v_mbcnt_hi_u32_b32 v232, -1, v232\n"
    "v_bfe_u32 v233, v232, 3, 1\n"
    "v_and_b32 v232, 7, v232\n"
    "v_lshl_add_u32 v232, v232, 1, v233\n"
    "v_add_u32 v232, s81, v232\n"
    "ds_read_u8 v232, v232 offset:49664\n"
    "s_add_u32 s81, s81, 16\n"
    "s_waitcnt lgkmcnt(0)\n"
    "buffer_load_ushort v232, v232, s[44:47], s63 idxen\n"
    "s_waitcnt vmcnt(0)\n"
    "v_lshlrev_b32 v232, 16, v232\n"
    "v_mul_f32 v230, v230, v232\n"
    "s_add_u32 s66, s66, 16\n"
    "s_waitcnt lgkmcnt(0)\n"
    "ds_write_b16_d16_hi v245, v230\n"
    "ds_read_b64 v[228:229], v246\n"
    "s_lshl1_add_u32 s76, s72, s76\n"
    "buffer_load_dwordx4 v[236:237], s[40:43], s67 idxen offen offset:0 nt lds\n"
    "buffer_load_dwordx4 v[238:239], s[40:43], s67 idxen offen offset:1024 nt lds\n"
    "s_mov_b32 s81, s64\n"
    "s_lshr_b32 s63, s65, 6\n"
    "s_mov_b32 s80, s83\n"
    "s_add_u32 s62, s62, 256\n"
    "s_bitcmp1_b32 s60, 30\n"
    "s_cbranch_scc0 .Ldin_A1\n"
    ".Ldin_A1s:\n"
    "s_cbranch_vccz .Ldin_A1s_np\n"
    "s_setprio 1\n"
    ".Ldin_A1s_np:\n"
    "v_mfma_f32_16x16x32_bf16 v[160:163], v[0:3], v[192:195], v[144:147]\n"
    "v_exp_f32 v176, v176 clamp\n"
    "v_mfma_f32_16x16x32_bf16 v[160:163], v[4:7], v[196:199], v[160:163]\n"
    "v_exp_f32 v177, v177 clamp\n"
    "v_mfma_f32_16x16x32_bf16 v[164:167], v[8:11], v[192:195], v[148:151]\n"
    "v_exp_f32 v178, v178 clamp\n"
    "v_mfma_f32_16x16x32_bf16 v[164:167], v[12:15], v[196:199], v[164:167]\n"
    "v_exp_f32 v179, v179 clamp\n"
    "v_mfma_f32_16x16x32_bf16 v[168:171], v[16:19], v[192:195], v[152:155]\n"
    "v_exp_f32 v180, v180 clamp\n"
    "v_mfma_f32_16x16x32_bf16 v[168:171], v[20:23], v[196:199], v[168:171]\n"
    "v_exp_f32 v181, v181 clamp\n"
    "v_mfma_f32_16x16x32_bf16 v[172:175], v[24:27], v[192:195], v[156:159]\n"
    "v_exp_f32 v182, v182 clamp\n"
    "v_mfma_f32_16x16x32_bf16 v[172:175], v[28:31], v[196:199], v[172:175]\n"
    "v_exp_f32 v183, v183 clamp\n"
    "v_exp_f32 v184, v184 clamp\n"
    "s_mov_b32 m0, s62\n"
    "ds_read_b64 v[216:217], v246 offset:128\n"
    "v_exp_f32 v185, v185 clamp\n"
    "v_add_u32_e64 v251, s66, v244\n"
    "s_waitcnt lgkmcnt(1)\n"
    "v_mfma_f32_4x4x4_16b_bf16 v[224:227], v[228:229], v[200:201], v[224:227] cbsz:4 abid:0\n"
    "v_exp_f32 v186, v186 clamp\n"
    "v_exp_f32 v187, v187 clamp\n"
    "v_mfma_f32_4x4x4_16b_bf16 v[224:227], v[228:229], v[202:203], v[224:227] cbsz:4 abid:1\n"
    "v_exp_f32 v188, v188 clamp\n"
    "v_exp_f32 v189, v189 clamp\n"
    "v_mfma_f32_4x4x4_16b_bf16 v[224:227], v[228:229], v[204:205], v[224:227] cbsz:4 abid:2\n"
    "v_exp_f32 v190, v190 clamp\n"
    "v_exp_f32 v191, v191 clamp\n"
    "v_mfma_f32_4x4x4_16b_bf16 v[224:227], v[228:229], v[206:207], v[224:227] cbsz:4 abid:3\n"
    "s_waitcnt vmcnt(4)\n"
    "s_waitcnt lgkmcnt(0)\n"
    "v_readlane_b32 s60, v216, s59\n"
    "v_readlane_b32 s61, v217, s59\n"
    "ds_read_b64_tr_b16 v[200:201], v240 offset:4096\n"
    "ds_read_b64_tr_b16 v[202:203], v241 offset:4096\n"
    "ds_read_b64_tr_b16 v[204:205], v242 offset:4096\n"
    "ds_read_b64_tr_b16 v[206:207], v243 offset:4096\n"
    "ds_read_u8 v236, v251 offset:0\n"
    "ds_read_u8 v238, v251 offset:1\n"
    "ds_read_b128 v[192:195], v234 offset:6144\n"
    "ds_read_b128 v[196:199], v235 offset:6144\n"
    "s_cmp_eq_u32 s60, -1\n"
    "s_cbranch_scc1 .Ldin_A1s_poll\n"
    ".Ldin_A1s_pr:\n"
    "s_lshr_b32 s94, s60, 12\n"
    "s_and_b32 s94, s94, 0x1ff0\n"
    "v_xor_b32 v252, s94, v250\n"
    "v_pk_add_f32 v[230:231], v[224:225], v[226:227]\n"
    "v_add_f32 v230, v230, v231\n"
    "ds_write_addtid_b32 v230 offset:49152\n"
    "ds_read_b128 v[216:219], v252 offset:0\n"
    "ds_read_b128 v[220:223], v252 offset:64\n"
    "v_mov_b64 v[224:225], 0\n"
    "v_mov_b64_e64 v[226:227], 0\n"
    "v_pk_fma_f32 v[176:177], v[176:177], s[70:71], v[128:129]\n"
    "v_pk_fma_f32 v[178:179], v[178:179], s[70:71], v[130:131]\n"
    "v_pk_add_f32 v[230:231], v[176:177], v[178:179]\n"
    "v_pk_mul_f32 v[232:233], v[176:177], v[178:179]\n"
    "v_pk_fma_f32 v[180:181], v[180:181], s[70:71], v[132:133]\n"
    "v_pk_fma_f32 v[230:231], v[230:231], v[180:181], v[232:233]\n"
    "v_pk_mul_f32 v[232:233], v[232:233], v[180:181]\n"
    "v_pk_fma_f32 v[182:183], v[182:183], s[70:71], v[134:135]\n"
    "v_pk_fma_f32 v[230:231], v[230:231], v[182:183], v[232:233]\n"
    "v_pk_mul_f32 v[232:233], v[232:233], v[182:183]\n"
    "v_pk_fma_f32 v[184:185], v[184:185], s[70:71], v[136:137]\n"
    "v_pk_fma_f32 v[230:231], v[230:231], v[184:185], v[232:233]\n"
    "v_pk_mul_f32 v[232:233], v[232:233], v[184:185]\n"
    "v_pk_fma_f32 v[186:187], v[186:187], s[70:71], v[138:139]\n"
    "v_pk_fma_f32 v[230:231], v[230:231], v[186:187], v[232:233]\n"
    "v_pk_mul_f32 v[232:233], v[232:233], v[186:187]\n"
    "v_pk_fma_f32 v[188:189], v[188:189], s[70:71], v[140:141]\n"
    "v_pk_fma_f32 v[230:231], v[230:231], v[188:189], v[232:233]\n"
    "v_pk_mul_f32 v[232:233], v[232:233], v[188:189]\n"
    "v_pk_fma_f32 v[190:191], v[190:191], s[70:71], v[142:143]\n"
    "v_pk_fma_f32 v[230:231], v[230:231], v[190:191], v[232:233]\n"
    "v_pk_mul_f32 v[232:233], v[232:233], v[190:191]\n"
    "s_add_u32 m0, s68, 4096\n"
    "s_and_b32 s64, s60, 0xffff\n"
    "s_bfe_u32 s94, s60, 0x30013\n"
    "s_bfe_u32 s95, s60, 0x50019\n"
    "s_mul_i32 s94, s94, s56\n"
    "s_lshl4_add_u32 s83, s95, s64\n"
    "s_add_u32 s65, s58, s94\n"
    "s_add_u32 s82, s64, 64\n"
    "s_add_u32 s58, s58, s57\n"
    "s_add_u32 s59, s59, 1\n"
    "s_sub_u32 s95, s61, s76\n"
    "s_lshr_b32 s94, s61, 16\n"
    "s_sext_i32_i16 s95, s95\n"
    "s_cmp_lt_i32 s95, -400\n"
    "s_cselect_b32 s94, 0, s94\n"
    "s_mul_i32 s95, s95, s94\n"
    "s_ashr_i32 s95, s95, 16\n"
    "s_max_i32 s95, s95, -10\n"
    "s_min_i32 s95, s95, 10\n"
    "s_add_u32 s72, s77, s95\n"
    "s_cmp_lt_i32 s60, 0\n"
    "s_cselect_b32 s72, 0, s72\n"
    "v_rcp_f32 v232, v232\n"
    "v_rcp_f32 v233, v233\n"
    "v_fma_f32 v230, v230, v232, v247\n"
    "v_fmac_f32 v230, v231, v233\n"
    "v_mbcnt_lo_u32_b32 v232, -1, 0\n"
    "v_mbcnt_hi_u32_b32 v232, -1, v232\n"
    "v_bfe_u32 v233, v232, 3, 1\n"
    "v_and_b32 v232, 7, v232\n"
    "v_lshl_add_u32 v232, v232, 1, v233\n"
    "v_add_u32 v232, s81, v232\n"
    "ds_read_u8 v232, v232 offset:49664\n"
    "s_add_u32 s81, s81, 16\n"
    "s_waitcnt lgkmcnt(0)\n"
    "buffer_load_ushort v232, v232, s[44:47], s63 idxen\n"
    "s_waitcnt vmcnt(0)\n"
    "v_lshlrev_b32 v232, 16, v232\n"
    "v_mul_f32 v230, v230, v232\n"
    "s_add_u32 s66, s66, 16\n"
    "s_waitcnt lgkmcnt(0)\n"
    "ds_write_b16_d16_hi v245, v230\n"
    "ds_read_b64 v[228:229], v246\n"
    "buffer_load_dwordx4 v[236:237], s[40:43], s67 idxen offen offset:0 nt lds\n"
    "buffer_load_dwordx4 v[238:239], s[40:43], s67 idxen offen offset:1024 nt lds\n"
    "s_branch .Ldin_G2s\n"
    ".Ldin_A1s_poll:\n"
    "s_sleep 1\n"
    "ds_read_b64 v[216:217], v246 offset:128\n"
    "s_waitcnt lgkmcnt(0)\n"
    "v_readlane_b32 s60, v216, s59\n"
    "v_readlane_b32 s61, v217, s59\n"
    "s_cmp_eq_u32 s60, -1\n"
    "s_cbranch_scc1 .Ldin_A1s_poll\n"
    "s_branch .Ldin_A1s_pr\n"
    ".p2align 5\n"
    ".Ldin_L3s:\n"
    "s_setprio 0\n"
    "s_cmp_lt_i32 s60, 0\n"
    "s_cbranch_scc1 .Ldin_E1s\n"
    "ds_read_b128 v[144:147], v252 offset:8192\n"
    "ds_read_b128 v[148:151], v252 offset:8256\n"
    "ds_read_b128 v[152:155], v252 offset:16384\n"
    "ds_read_b128 v[156:159], v252 offset:16448\n"
    "s_add_u32 s76, s76, s73\n"
    "s_add_u32 m0, s68, 0\n"
    "v_and_b32 v216, v248, v216\n"
    "v_and_b32 v217, v249, v217\n"
    "v_and_b32 v218, v248, v218\n"
    "v_and_b32 v219, v249, v219\n"
    "v_mfma_f32_16x16x16_bf16 v[160:163], v[216:217], v[96:97], v[32:35]\n"
    "v_and_b32 v220, v248, v220\n"
    "v_and_b32 v221, v249, v221\n"
    "v_and_b32 v222, v248, v222\n"
    "v_mfma_f32_16x16x16_bf16 v[164:167], v[218:219], v[98:99], v[36:39]\n"
    "v_exp_f32 v176, v176 clamp\n"
    "v_and_b32 v223, v249, v223\n"
    "v_mfma_f32_16x16x16_bf16 v[168:171], v[220:221], v[100:101], v[40:43]\n"
    "v_add_u32_e64 v251, s66, v244\n"
    "v_cvt_pk_bf16_f32 v0, v160, v161\n"
    "v_cvt_pk_bf16_f32 v1, v162, v163\n"
    "s_waitcnt lgkmcnt(4)\n"
    "v_mfma_f32_16x16x16_bf16 v[172:175], v[222:223], v[102:103], v[44:47]\n"
    "v_exp_f32 v177, v177 clamp\n"
    "v_cvt_pk_bf16_f32 v2, v164, v165\n"
    "v_mfma_f32_4x4x4_16b_bf16 v[224:227], v[228:229], v[200:201], v[224:227] cbsz:4 abid:0\n"
    "v_cvt_pk_bf16_f32 v3, v166, v167\n"
    "v_mfma_f32_16x16x16_bf16 v[160:163], v[216:217], v[104:105], v[48:51]\n"
    "v_exp_f32 v178, v178 clamp\n"
    "v_cvt_pk_bf16_f32 v4, v168, v169\n"
    "v_mfma_f32_16x16x16_bf16 v[164:167], v[218:219], v[106:107], v[52:55]\n"
    "v_cvt_pk_bf16_f32 v5, v170, v171\n"
    "v_cvt_pk_bf16_f32 v6, v172, v173\n"
    "v_cvt_pk_bf16_f32 v7, v174, v175\n"
    "v_mfma_f32_16x16x16_bf16 v[168:171], v[220:221], v[108:109], v[56:59]\n"
    "v_exp_f32 v179, v179 clamp\n"
    "v_cvt_pk_bf16_f32 v8, v160, v161\n"
    "v_mfma_f32_4x4x4_16b_bf16 v[224:227], v[228:229], v[202:203], v[224:227] cbsz:4 abid:1\n"
    "v_cvt_pk_bf16_f32 v9, v162, v163\n"
    "v_mfma_f32_16x16x16_bf16 v[172:175], v[222:223], v[110:111], v[60:63]\n"
    "v_exp_f32 v180, v180 clamp\n"
    "v_cvt_pk_bf16_f32 v10, v164, v165\n"
    "v_mfma_f32_16x16x16_bf16 v[160:163], v[216:217], v[112:113], v[64:67]\n"
    "v_cvt_pk_bf16_f32 v11, v166, v167\n"
    "v_cvt_pk_bf16_f32 v12, v168, v169\n"
    "v_cvt_pk_bf16_f32 v13, v170, v171\n"
    "v_mfma_f32_16x16x16_bf16 v[164:167], v[218:219], v[114:115], v[68:71]\n"
    "v_exp_f32 v181, v181 clamp\n"
    "v_cvt_pk_bf16_f32 v14, v172, v173\n"
    "v_mfma_f32_4x4x4_16b_bf16 v[224:227], v[228:229], v[204:205], v[224:227] cbsz:4 abid:2\n"
    "v_cvt_pk_bf16_f32 v15, v174, v175\n"
    "v_mfma_f32_16x16x16_bf16 v[168:171], v[220:221], v[116:117], v[72:75]\n"
    "v_exp_f32 v182, v182 clamp\n"
    "v_cvt_pk_bf16_f32 v16, v160, v161\n"
    "v_mfma_f32_16x16x16_bf16 v[172:175], v[222:223], v[118:119], v[76:79]\n"
    "v_cvt_pk_bf16_f32 v17, v162, v163\n"
    "v_cvt_pk_bf16_f32 v18, v164, v165\n"
    "v_cvt_pk_bf16_f32 v19, v166, v167\n"
    "v_mfma_f32_16x16x16_bf16 v[160:163], v[216:217], v[120:121], v[80:83]\n"
    "v_exp_f32 v183, v183 clamp\n"
    "v_cvt_pk_bf16_f32 v20, v168, v169\n"
    "v_mfma_f32_4x4x4_16b_bf16 v[224:227], v[228:229], v[206:207], v[224:227] cbsz:4 abid:3\n"
    "v_cvt_pk_bf16_f32 v21, v170, v171\n"
    "v_mfma_f32_16x16x16_bf16 v[164:167], v[218:219], v[122:123], v[84:87]\n"
    "v_exp_f32 v184, v184 clamp\n"
    "v_cvt_pk_bf16_f32 v22, v172, v173\n"
    "v_mfma_f32_16x16x16_bf16 v[168:171], v[220:221], v[124:125], v[88:91]\n"
    "v_cvt_pk_bf16_f32 v23, v174, v175\n"
    "v_cvt_pk_bf16_f32 v24, v160, v161\n"
    "v_cvt_pk_bf16_f32 v25, v162, v163\n"
    "v_mfma_f32_16x16x16_bf16 v[172:175], v[222:223], v[126:127], v[92:95]\n"
    "v_exp_f32 v185, v185 clamp\n"
    "v_cvt_pk_bf16_f32 v26, v164, v165\n"
    "s_waitcnt lgkmcnt(0)\n"
    "v_mfma_f32_16x16x32_bf16 v[160:163], v[0:3], v[192:195], v[144:147]\n"
    "v_cvt_pk_bf16_f32 v27, v166, v167\n"
    "v_cvt_pk_bf16_f32 v28, v168, v169\n"
    "v_mfma_f32_16x16x32_bf16 v[160:163], v[4:7], v[196:199], v[160:163]\n"
    "v_cvt_pk_bf16_f32 v29, v170, v171\n"
    "v_cvt_pk_bf16_f32 v30, v172, v173\n"
    "v_mfma_f32_16x16x32_bf16 v[164:167], v[8:11], v[192:195], v[148:151]\n"
    "v_exp_f32 v186, v186 clamp\n"
    "v_mfma_f32_16x16x32_bf16 v[164:167], v[12:15], v[196:199], v[164:167]\n"
    "v_exp_f32 v187, v187 clamp\n"
    "v_mfma_f32_16x16x32_bf16 v[168:171], v[16:19], v[192:195], v[152:155]\n"
    "v_exp_f32 v188, v188 clamp\n"
    "v_mfma_f32_16x16x32_bf16 v[168:171], v[20:23], v[196:199], v[168:171]\n"
    "v_exp_f32 v189, v189 clamp\n"
    "v_cvt_pk_bf16_f32 v31, v174, v175\n"
    "v_mfma_f32_16x16x32_bf16 v[172:175], v[24:27], v[192:195], v[156:159]\n"
    "v_exp_f32 v190, v190 clamp\n"
    "v_mfma_f32_16x16x32_bf16 v[172:175], v[28:31], v[196:199], v[172:175]\n"
    "v_exp_f32 v191, v191 clamp\n"
    "s_waitcnt vmcnt(4)\n"
    "ds_read_b64_tr_b16 v[200:201], v240 offset:0\n"
    "ds_read_b64_tr_b16 v[202:203], v241 offset:0\n"
    "ds_read_b64_tr_b16 v[204:205], v242 offset:0\n"
    "ds_read_b64_tr_b16 v[206:207], v243 offset:0\n"
    "ds_read_u8 v236, v251 offset:0\n"
    "ds_read_u8 v238, v251 offset:1\n"
    "ds_read_b128 v[192:195], v234 offset:2048\n"
    "ds_read_b128 v[196:199], v235 offset:2048\n"
    "v_pk_fma_f32 v[176:177], v[176:177], s[70:71], v[128:129]\n"
    "v_pk_fma_f32 v[178:179], v[178:179], s[70:71], v[130:131]\n"
    "v_pk_add_f32 v[230:231], v[176:177], v[178:179]\n"
    "v_pk_mul_f32 v[232:233], v[176:177], v[178:179]\n"
    "v_pk_fma_f32 v[180:181], v[180:181], s[70:71], v[132:133]\n"
    "v_pk_fma_f32 v[230:231], v[230:231], v[180:181], v[232:233]\n"
    "v_pk_mul_f32 v[232:233], v[232:233], v[180:181]\n"
    "v_pk_fma_f32 v[182:183], v[182:183], s[70:71], v[134:135]\n"
    "v_pk_fma_f32 v[230:231], v[230:231], v[182:183], v[232:233]\n"
    "v_pk_mul_f32 v[232:233], v[232:233], v[182:183]\n"
    "v_pk_fma_f32 v[184:185], v[184:185], s[70:71], v[136:137]\n"
    "v_pk_fma_f32 v[230:231], v[230:231], v[184:185], v[232:233]\n"
    "v_pk_mul_f32 v[232:233], v[232:233], v[184:185]\n"
    "v_pk_fma_f32 v[186:187], v[186:187], s[70:71], v[138:139]\n"
    "v_pk_fma_f32 v[230:231], v[230:231], v[186:187], v[232:233]\n"
    "v_pk_mul_f32 v[232:233], v[232:233], v[186:187]\n"
    "v_pk_fma_f32 v[188:189], v[188:189], s[70:71], v[140:141]\n"
    "v_pk_fma_f32 v[230:231], v[230:231], v[188:189], v[232:233]\n"
    "v_pk_mul_f32 v[232:233], v[232:233], v[188:189]\n"
    "v_pk_fma_f32 v[190:191], v[190:191], s[70:71], v[142:143]\n"
    "v_pk_fma_f32 v[230:231], v[230:231], v[190:191], v[232:233]\n"
    "v_pk_mul_f32 v[232:233], v[232:233], v[190:191]\n"
    "v_rcp_f32 v232, v232\n"
    "v_rcp_f32 v233, v233\n"
    "v_fma_f32 v230, v230, v232, v247\n"
    "v_fmac_f32 v230, v231, v233\n"
    "v_mbcnt_lo_u32_b32 v232, -1, 0\n"
    "v_mbcnt_hi_u32_b32 v232, -1, v232\n"
    "v_bfe_u32 v233, v232, 3, 1\n"
    "v_and_b32 v232, 7, v232\n"
    "v_lshl_add_u32 v232, v232, 1, v233\n"
    "v_add_u32 v232, s81, v232\n"
    "ds_read_u8 v232, v232 offset:49664\n"
    "s_add_u32 s81, s81, 16\n"
    "s_waitcnt lgkmcnt(0)\n"
    "buffer_load_ushort v232, v232, s[44:47], s63 idxen\n"
    "s_waitcnt vmcnt(0)\n"
    "v_lshlrev_b32 v232, 16, v232\n"
    "v_mul_f32 v230, v230, v232\n"
    "s_add_u32 s66, s66, 16\n"
    "s_waitcnt lgkmcnt(0)\n"
    "ds_write_b16_d16_hi v245, v230\n"
    "ds_read_b64 v[228:229], v246\n"
    "s_lshl1_add_u32 s76, s72, s76\n"
    "buffer_load_dwordx4 v[236:237], s[40:43], s67 idxen offen offset:0 nt lds\n"
    "buffer_load_dwordx4 v[238:239], s[40:43], s67 idxen offen offset:1024 nt lds\n"
    "s_mov_b32 s81, s64\n"
    "s_lshr_b32 s63, s65, 6\n"
    "s_mov_b32 s80, s83\n"
    "s_add_u32 s62, s62, 256\n"
    "s_bitcmp1_b32 s60, 30\n"
    "s_cbranch_scc0 .Ldin_A0\n"
    "s_branch .Ldin_A0s\n"
    ".Ldin_E0s:\n"
    "v_exp_f32 v160, v160 clamp\n"
    "v_exp_f32 v161, v161 clamp\n"
    "v_exp_f32 v162, v162 clamp\n"
    "v_exp_f32 v163, v163 clamp\n"
    "v_exp_f32 v164, v164 clamp\n"
    "v_exp_f32 v165, v165 clamp\n"
    "v_exp_f32 v166, v166 clamp\n"
    "v_exp_f32 v167, v167 clamp\n"
    "s_add_u32 m0, s62, 256\n"
    "s_waitcnt lgkmcnt(0)\n"
    "v_mfma_f32_4x4x4_16b_bf16 v[224:227], v[228:229], v[208:209], v[224:227] cbsz:4 abid:0\n"
    "v_exp_f32 v168, v168 clamp\n"
    "v_exp_f32 v169, v169 clamp\n"
    "v_mfma_f32_4x4x4_16b_bf16 v[224:227], v[228:229], v[210:211], v[224:227] cbsz:4 abid:1\n"
    "v_exp_f32 v170, v170 clamp\n"
    "v_exp_f32 v171, v171 clamp\n"
    "v_mfma_f32_4x4x4_16b_bf16 v[224:227], v[228:229], v[212:213], v[224:227] cbsz:4 abid:2\n"
    "v_exp_f32 v172, v172 clamp\n"
    "v_exp_f32 v173, v173 clamp\n"
    "v_mfma_f32_4x4x4_16b_bf16 v[224:227], v[228:229], v[214:215], v[224:227] cbsz:4 abid:3\n"
    "v_exp_f32 v174, v174 clamp\n"
    "v_exp_f32 v175, v175 clamp\n"
    "v_pk_fma_f32 v[160:161], v[160:161], s[70:71], v[128:129]\n"
    "v_pk_fma_f32 v[162:163], v[162:163], s[70:71], v[130:131]\n"
    "v_pk_add_f32 v[230:231], v[160:161], v[162:163]\n"
    "v_pk_mul_f32 v[232:233], v[160:161], v[162:163]\n"
    "v_pk_fma_f32 v[164:165], v[164:165], s[70:71], v[132:133]\n"
    "v_pk_fma_f32 v[230:231], v[230:231], v[164:165], v[232:233]\n"
    "v_pk_mul_f32 v[232:233], v[232:233], v[164:165]\n"
    "v_pk_fma_f32 v[166:167], v[166:167], s[70:71], v[134:135]\n"
    "v_pk_fma_f32 v[230:231], v[230:231], v[166:167], v[232:233]\n"
    "v_pk_mul_f32 v[232:233], v[232:233], v[166:167]\n"
    "v_pk_fma_f32 v[168:169], v[168:169], s[70:71], v[136:137]\n"
    "v_pk_fma_f32 v[230:231], v[230:231], v[168:169], v[232:233]\n"
    "v_pk_mul_f32 v[232:233], v[232:233], v[168:169]\n"
    "v_pk_fma_f32 v[170:171], v[170:171], s[70:71], v[138:139]\n"
    "v_pk_fma_f32 v[230:231], v[230:231], v[170:171], v[232:233]\n"
    "v_pk_mul_f32 v[232:233], v[232:233], v[170:171]\n"
    "v_pk_fma_f32 v[172:173], v[172:173], s[70:71], v[140:141]\n"
    "v_pk_fma_f32 v[230:231], v[230:231], v[172:173], v[232:233]\n"
    "v_pk_mul_f32 v[232:233], v[232:233], v[172:173]\n"
    "v_pk_fma_f32 v[174:175], v[174:175], s[70:71], v[142:143]\n"
    "v_pk_fma_f32 v[230:231], v[230:231], v[174:175], v[232:233]\n"
    "v_pk_mul_f32 v[232:233], v[232:233], v[174:175]\n"
    "v_rcp_f32 v232, v232\n"
    "v_rcp_f32 v233, v233\n"
    "v_fma_f32 v230, v230, v232, v247\n"
    "v_fmac_f32 v230, v231, v233\n"
    "v_mbcnt_lo_u32_b32 v232, -1, 0\n"
    "v_mbcnt_hi_u32_b32 v232, -1, v232\n"
    "v_bfe_u32 v233, v232, 3, 1\n"
    "v_and_b32 v232, 7, v232\n"
    "v_lshl_add_u32 v232, v232, 1, v233\n"
    "v_add_u32 v232, s81, v232\n"
    "ds_read_u8 v232, v232 offset:49664\n"
    "s_add_u32 s81, s81, 16\n"
    "s_waitcnt lgkmcnt(0)\n"
    "buffer_load_ushort v232, v232, s[44:47], s63 idxen\n"
    "s_waitcnt vmcnt(0)\n"
    "v_lshlrev_b32 v232, 16, v232\n"
    "v_mul_f32 v230, v230, v232\n"
    "ds_write_b16_d16_hi v245, v230\n"
    "ds_read_b64 v[228:229], v246\n"
    "s_branch .Ldin_drain0\n"
    ".Ldin_E1s:\n"
    "v_exp_f32 v176, v176 clamp\n"
    "v_exp_f32 v177, v177 clamp\n"
    "v_exp_f32 v178, v178 clamp\n"
    "v_exp_f32 v179, v179 clamp\n"
    "v_exp_f32 v180, v180 clamp\n"
    "v_exp_f32 v181, v181 clamp\n"
    "v_exp_f32 v182, v182 clamp\n"
    "v_exp_f32 v183, v183 clamp\n"
    "s_add_u32 m0, s62, 256\n"
    "s_waitcnt lgkmcnt(0)\n"
    "v_mfma_f32_4x4x4_16b_bf16 v[224:227], v[228:229], v[200:201], v[224:227] cbsz:4 abid:0\n"
    "v_exp_f32 v184, v184 clamp\n"
    "v_exp_f32 v185, v185 clamp\n"
    "v_mfma_f32_4x4x4_16b_bf16 v[224:227], v[228:229], v[202:203], v[224:227] cbsz:4 abid:1\n"
    "v_exp_f32 v186, v186 clamp\n"
    "v_exp_f32 v187, v187 clamp\n"
    "v_mfma_f32_4x4x4_16b_bf16 v[224:227], v[228:229], v[204:205], v[224:227] cbsz:4 abid:2\n"
    "v_exp_f32 v188, v188 clamp\n"
    "v_exp_f32 v189, v189 clamp\n"
    "v_mfma_f32_4x4x4_16b_bf16 v[224:227], v[228:229], v[206:207], v[224:227] cbsz:4 abid:3\n"
    "v_exp_f32 v190, v190 clamp\n"
    "v_exp_f32 v191, v191 clamp\n"
    "v_pk_fma_f32 v[176:177], v[176:177], s[70:71], v[128:129]\n"
    "v_pk_fma_f32 v[178:179], v[178:179], s[70:71], v[130:131]\n"
    "v_pk_add_f32 v[230:231], v[176:177], v[178:179]\n"
    "v_pk_mul_f32 v[232:233], v[176:177], v[178:179]\n"
    "v_pk_fma_f32 v[180:181], v[180:181], s[70:71], v[132:133]\n"
    "v_pk_fma_f32 v[230:231], v[230:231], v[180:181], v[232:233]\n"
    "v_pk_mul_f32 v[232:233], v[232:233], v[180:181]\n"
    "v_pk_fma_f32 v[182:183], v[182:183], s[70:71], v[134:135]\n"
    "v_pk_fma_f32 v[230:231], v[230:231], v[182:183], v[232:233]\n"
    "v_pk_mul_f32 v[232:233], v[232:233], v[182:183]\n"
    "v_pk_fma_f32 v[184:185], v[184:185], s[70:71], v[136:137]\n"
    "v_pk_fma_f32 v[230:231], v[230:231], v[184:185], v[232:233]\n"
    "v_pk_mul_f32 v[232:233], v[232:233], v[184:185]\n"
    "v_pk_fma_f32 v[186:187], v[186:187], s[70:71], v[138:139]\n"
    "v_pk_fma_f32 v[230:231], v[230:231], v[186:187], v[232:233]\n"
    "v_pk_mul_f32 v[232:233], v[232:233], v[186:187]\n"
    "v_pk_fma_f32 v[188:189], v[188:189], s[70:71], v[140:141]\n"
    "v_pk_fma_f32 v[230:231], v[230:231], v[188:189], v[232:233]\n"
    "v_pk_mul_f32 v[232:233], v[232:233], v[188:189]\n"
    "v_pk_fma_f32 v[190:191], v[190:191], s[70:71], v[142:143]\n"
    "v_pk_fma_f32 v[230:231], v[230:231], v[190:191], v[232:233]\n"
    "v_pk_mul_f32 v[232:233], v[232:233], v[190:191]\n"
    "v_rcp_f32 v232, v232\n"
    "v_rcp_f32 v233, v233\n"
    "v_fma_f32 v230, v230, v232, v247\n"
    "v_fmac_f32 v230, v231, v233\n"
    "v_mbcnt_lo_u32_b32 v232, -1, 0\n"
    "v_mbcnt_hi_u32_b32 v232, -1, v232\n"
    "v_bfe_u32 v233, v232, 3, 1\n"
    "v_and_b32 v232, 7, v232\n"
    "v_lshl_add_u32 v232, v232, 1, v233\n"
    "v_add_u32 v232, s81, v232\n"
    "ds_read_u8 v232, v232 offset:49664\n"
    "s_add_u32 s81, s81, 16\n"
    "s_waitcnt lgkmcnt(0)\n"
    "buffer_load_ushort v232, v232, s[44:47], s63 idxen\n"
    "s_waitcnt vmcnt(0)\n"
    "v_lshlrev_b32 v232, 16, v232\n"
    "v_mul_f32 v230, v230, v232\n"
    "ds_write_b16_d16_hi v245, v230\n"
    "ds_read_b64 v[228:229], v246\n"
    "s_branch .Ldin_drain1\n"
    ".Ldin_idle:\n"
    "s_waitcnt vmcnt(0)\n"
    ".Ldin_exit:\n"

      :
      : "{s[0:1]}"(__builtin_amdgcn_kernarg_segment_ptr()), "{s2}"(blockIdx.x), "{v0}"(threadIdx.x), "{s97}"((uint32_t)(uintptr_t)smem)
      : "memory", "vcc", "scc", "v1", "v2", "v3", "v4", "v5", "v6", "v7", "v8", "v9", "v10", "v11", "v12", "v13", "v14", "v15", "v16", "v17", "v18", "v19", "v20", "v21", "v22", "v23", "v24", "v25", "v26", "v27", "v28", "v29", "v30", "v31", "v32", "v33", "v34", "v35", "v36", "v37", "v38", "v39", "v40", "v41", "v42", "v43", "v44", "v45", "v46", "v47", "v48", "v49", "v50", "v51", "v52", "v53", "v54", "v55", "v56", "v57", "v58", "v59", "v60", "v61", "v62", "v63", "v64", "v65", "v66", "v67", "v68", "v69", "v70", "v71", "v72", "v73", "v74", "v75", "v76", "v77", "v78", "v79", "v80", "v81", "v82", "v83", "v84", "v85", "v86", "v87", "v88", "v89", "v90", "v91", "v92", "v93", "v94", "v95", "v96", "v97", "v98", "v99", "v100", "v101", "v102", "v103", "v104", "v105", "v106", "v107", "v108", "v109", "v110", "v111", "v112", "v113", "v114", "v115", "v116", "v117", "v118", "v119", "v120", "v121", "v122", "v123", "v124", "v125", "v126", "v127", "v128", "v129", "v130", "v131", "v132", "v133", "v134", "v135", "v136", "v137", "v138", "v139", "v140", "v141", "v142", "v143", "v144", "v145", "v146", "v147", "v148", "v149", "v150", "v151", "v152", "v153", "v154", "v155", "v156", "v157", "v158", "v159", "v160", "v161", "v162", "v163", "v164", "v165", "v166", "v167", "v168", "v169", "v170", "v171", "v172", "v173", "v174", "v175", "v176", "v177", "v178", "v179", "v180", "v181", "v182", "v183", "v184", "v185", "v186", "v187", "v188", "v189", "v190", "v191", "v192", "v193", "v194", "v195", "v196", "v197", "v198", "v199", "v200", "v201", "v202", "v203", "v204", "v205", "v206", "v207", "v208", "v209", "v210", "v211", "v212", "v213", "v214", "v215", "v216", "v217", "v218", "v219", "v220", "v221", "v222", "v223", "v224", "v225", "v226", "v227", "v228", "v229", "v230", "v231", "v232", "v233", "v234", "v235", "v236", "v237", "v238", "v239", "v240", "v241", "v242", "v243", "v244", "v245", "v246", "v247", "v248", "v249", "v250", "v251", "v252", "v253", "s3", "s4", "s5", "s6", "s7", "s8", "s9", "s10", "s11", "s12", "s13", "s14", "s15", "s16", "s17", "s18", "s19", "s20", "s21", "s22", "s23", "s24", "s25", "s26", "s27", "s28", "s29", "s30", "s31", "s33", "s34", "s35", "s36", "s37", "s38", "s39", "s40", "s41", "s42", "s43", "s44", "s45", "s46", "s47", "s48", "s49", "s50", "s51", "s52", "s53", "s54", "s55", "s56", "s57", "s58", "s59", "s60", "s61", "s62", "s63", "s64", "s65", "s66", "s67", "s68", "s69", "s70", "s71", "s72", "s73", "s74", "s75", "s76", "s77", "s78", "s79", "s80", "s81", "s82", "s83", "s84", "s85", "s86", "s87", "s88", "s89", "s90", "s91", "s92", "s93", "s94", "s95", "s96");
}

__global__ __launch_bounds__(64) void din_fallback_kernel(
    const unsigned short* __restrict__ target, const unsigned short* __restrict__ hist,
    const unsigned short* __restrict__ mask, const unsigned short* __restrict__ w1,
    const unsigned short* __restrict__ b1, const unsigned short* __restrict__ w2,
    const unsigned short* __restrict__ b2, unsigned short* __restrict__ out, int B, int L) {
  __shared__ float s_w[64 * 64];
  __shared__ float s_x[64];
  const int b = blockIdx.x, h = threadIdx.x;
  float bias = bf2f(b1[h]);
  for (int d = 0; d < 64; ++d) {
    const float t = bf2f(target[(size_t)b * 64 + d]);
    const float wc = bf2f(w1[(128 + d) * 64 + h]);
    bias += t * (bf2f(w1[(64 + d) * 64 + h]) - wc);
    s_w[d * 64 + h] = bf2f(w1[d * 64 + h]) + wc + t * bf2f(w1[(192 + d) * 64 + h]);
  }
  const float w2h = bf2f(w2[h]), b2v = bf2f(b2[0]);
  float acc = 0.f;
  for (int l = 0; l < L; ++l) {
    const float m = bf2f(mask[(size_t)b * L + l]);
    if (m == 0.f) continue;
    const float x = bf2f(hist[((size_t)b * L + l) * 64 + h]);
    __syncthreads();
    s_x[h] = x;
    __syncthreads();
    float z = bias;
    for (int d = 0; d < 64; ++d) z += s_x[d] * s_w[d * 64 + h];
    float s = w2h / (1.0f + __expf(-z));
    for (int o = 32; o > 0; o >>= 1) s += __shfl_xor(s, o);
    acc += (s + b2v) * m * x;
  }
  const uint32_t u = __float_as_uint(acc);
  out[(size_t)b * 64 + h] = (unsigned short)((u + 0x7fffu + ((u >> 16) & 1u)) >> 16);
}

extern "C" void kernel_launch(void* const* d_in, const int* in_sizes, int n_in, void* d_out,
                              int out_size, void* d_ws, size_t ws_size, hipStream_t stream) {
  const unsigned short* target = (const unsigned short*)d_in[0];
  const unsigned short* hist = (const unsigned short*)d_in[1];
  const unsigned short* mask = (const unsigned short*)d_in[2];
  const unsigned short* w1 = (const unsigned short*)d_in[3];
  const unsigned short* b1 = (const unsigned short*)d_in[4];
  const unsigned short* w2 = (const unsigned short*)d_in[5];
  const unsigned short* b2 = (const unsigned short*)d_in[6];
  int B = in_sizes[0] / 64;
  int L = in_sizes[2] / B;
  if ((L & 1) || L > 254 || B >= (1 << 24) - 16) {
    din_fallback_kernel<<<B, 64, 0, stream>>>(target, hist, mask, w1, b1, w2, b2, (unsigned short*)d_out, B, L);
    return;
  }
  const uint32_t G = (B + 63) / 64, L2 = 2u * L, lanes = (L2 + 15) / 16, F = 0x20000u;
  din_kernel<<<G, 512, 0, stream>>>(hist, (uint32_t)L, F, mask, (uint32_t)B * L2, F, w1, 32768u, F, (unsigned short*)d_out, (uint32_t)B * 128u, F,
                                    target, (uint32_t)B * 128u, F, b1, w2, b2, L2, G * 8u * L2, lanes >= 32 ? 0xffffffffu : (1u << lanes) - 1u,
                                    G * 8u * 128u, G >= 224 ? 0xffffffffu : 0u, L2 + 31u);
}
```

```cpp
#include <hip/hip_runtime.h>
#include <stdint.h>

__device__ __forceinline__ float bf2f(unsigned short u) { return __uint_as_float(((uint32_t)u) << 16); }

__global__ __launch_bounds__(512, 2) void din_kernel(
    const unsigned short* hist, uint32_t hn, uint32_t hf, const unsigned short* mask, uint32_t mn, uint32_t mf,
    const unsigned short* w1, uint32_t wn, uint32_t wf, unsigned short* out, uint32_t on, uint32_t of,
    const unsigned short* target, uint32_t tn, uint32_t tf, const unsigned short* b1, const unsigned short* w2, const unsigned short* b2,
    uint32_t row_bytes, uint32_t round_bytes, uint32_t row_lanes, uint32_t round_out, uint32_t paced, uint32_t usual_units) {
  __shared__ __attribute__((aligned(1024))) char smem[154624];
  asm volatile(
    ".p2align 3\n"
    "s_load_dwordx16 s[40:55], s[0:1], 0x0\n"
    "s_load_dwordx16 s[4:19], s[0:1], 0x40\n"
    "s_memrealtime s[76:77]\n"
    "v_readfirstlane_b32 s3, v0\n"
    "v_and_b32 v1, 63, v0\n"
    "v_and_b32 v2, 48, v0\n"
    "v_lshlrev_b32 v251, 4, v1\n"
    "s_lshr_b32 s20, s3, 6\n"
    "s_lshl_b32 s78, s2, 3\n"
    "s_and_b32 s21, s20, 3\n"
    "s_lshr_b32 s75, s3, 8\n"
    "s_cselect_b64 vcc, -1, 0\n"
    "s_cbranch_scc0 .Ldin_p0\n"
    "s_setprio 1\n"
    ".Ldin_p0:\n"
    "s_cmp_lt_u32 s2, 32\n"
    "s_cbranch_scc0 .Ldin_ncp\n"
    "s_getpc_b64 s[34:35]\n"
    "v_lshlrev_b32 v5, 7, v1\n"
    "s_mul_i32 s23, s20, 3072\n"
    "s_add_u32 s34, s34, s23\n"
    "s_addc_u32 s35, s35, 0\n"
    "s_mov_b64 exec, 0xffffff\n"
    "global_load_dword v253, v5, s[34:35] offset:1024\n"
    "s_mov_b64 exec, -1\n"
    ".Ldin_ncp:\n"
    "v_lshlrev_b32 v6, 2, v0\n"
    "v_and_b32 v7, 32, v0\n"
    "v_and_b32 v6, 64, v6\n"
    "v_xor_b32 v5, v251, v2\n"
    "v_bitop3_b32 v6, v6, v7, v251 bitop3:0x96\n"
    "v_and_b32 v3, 0x1f0, v251\n"
    "v_xor_b32 v7, 32, v6\n"
    "v_mov_b64 v[40:41], 0\n"
    "v_mov_b64 v[42:43], 0\n"
    "v_mov_b64 v[44:45], 0\n"
    "v_mov_b64 v[46:47], 0\n"
    "v_bfe_u32 v50, v251, 3, 5\n"
    "s_lshl_b32 s23, s21, 5\n"
    "v_lshrrev_b32_e64 v51, 1, v2\n"
    "s_lshl_b32 s24, s20, 12\n"
    "v_or_b32 v51, s23, v51\n"
    "s_add_u32 m0, s24, 54144\n"
    "s_add_u32 s22, s78, s20\n"
    "s_lshl_b32 s25, s20, 10\n"
    "s_lshl_b32 s26, s2, 10\n"
    "s_waitcnt lgkmcnt(0)\n"
    "s_mul_i32 s67, s22, s14\n"
    "s_cbranch_vccz .Ldin_pm\n"
    "global_load_short_d16_hi v40, v50, s[10:11] offset:0\n"
    "global_load_short_d16_hi v41, v50, s[10:11] offset:32\n"
    "global_load_short_d16_hi v42, v50, s[10:11] offset:64\n"
    "global_load_short_d16_hi v43, v50, s[10:11] offset:96\n"
    "global_load_short_d16_hi v44, v45, s[12:13]\n"
    "global_load_dwordx2 v[46:47], v51, s[10:11]\n"
    "global_load_dwordx2 v[48:49], v51, s[8:9]\n"
    "s_branch .Ldin_p1\n"
    ".Ldin_pm:\n"
    "s_mov_b32 exec_lo, s16\n"
    "s_mov_b32 exec_hi, 0\n"
    "s_lshl2_add_u32 s28, s14, s67\n"
    "buffer_load_dwordx4 v[40:43], v3, s[44:47], s67 offen nt\n"
    "s_add_u32 s29, s67, s15\n"
    "buffer_load_dwordx4 v[44:47], v3, s[44:47], s28 offen nt\n"
    "s_mov_b32 exec_hi, s16\n"
    "s_mov_b32 exec_lo, 0\n"
    "s_add_u32 s30, s28, s15\n"
    "buffer_load_dwordx4 v[40:43], v3, s[44:47], s29 offen nt\n"
    "buffer_load_dwordx4 v[44:47], v3, s[44:47], s30 offen nt\n"
    "s_mov_b64 exec, -1\n"
    ".Ldin_p1:\n"
    "s_mul_i32 s23, s20, s17\n"
    "buffer_load_dwordx4 v6, s[48:51], s24 offen offset:0 lds\n"
    "buffer_load_dwordx4 v7, s[48:51], s24 offen offset:1024 lds\n"
    "buffer_load_dwordx4 v6, s[48:51], s24 offen offset:2048 lds\n"
    "buffer_load_dwordx4 v7, s[48:51], s24 offen offset:3072 lds\n"
    "s_add_u32 m0, s25, 24576\n"
    "s_add_u32 s23, s23, s26\n"
    "buffer_load_dwordx4 v5, s[4:7], s23 offen nt lds\n"
    ".Ldin_p2:\n"
    "s_lshl_b32 s67, s67, 6\n"
    "s_lshl_b32 s68, s20, 13\n"
    "s_lshl_b32 s23, s20, 8\n"
    "s_add_u32 s68, s68, 89088\n"
    "s_add_u32 s23, s23, 86912\n"
    "v_and_b32 v32, 0x70, v5\n"
    "v_lshrrev_b32 v4, 1, v3\n"
    "v_or_b32 v237, 0x400, v32\n"
    "v_xor_b32 v239, 64, v32\n"
    "v_and_b32 v32, 0x70, v4\n"
    "v_lshlrev_b32 v33, 6, v50\n"
    "v_bitop3_b32 v36, v32, v2, v33 bitop3:0x96\n"
    "v_and_b32 v32, 8, v0\n"
    "v_add_u32 v234, s68, v36\n"
    "v_mul_u32_u24 v33, 0x88, v32\n"
    "v_xor_b32 v235, 64, v234\n"
    "v_lshrrev_b32 v245, 1, v32\n"
    "v_lshlrev_b32 v32, 1, v2\n"
    "v_and_b32 v34, 0x18, v4\n"
    "v_bitop3_b32 v33, v33, v32, v34 bitop3:0x96\n"
    "v_or3_b32 v245, v245, v32, s23\n"
    "v_and_b32 v32, 0x40, v251\n"
    "v_lshl_or_b32 v246, v34, 2, s23\n"
    "v_lshl_or_b32 v33, v32, 1, v33\n"
    "v_and_b32 v32, 24, v50\n"
    "v_add_u32 v240, s68, v33\n"
    "v_or_b32 v246, v246, v32\n"
    "v_xor_b32 v241, 0x110, v240\n"
    "v_xor_b32 v242, 0x220, v240\n"
    "v_xor_b32 v243, 0x330, v240\n"
    "v_and_b32 v32, 12, v50\n"
    "v_and_b32 v33, 2, v50\n"
    "v_lshl_or_b32 v32, v32, 1, v33\n"
    "v_bfe_u32 v244, v0, 3, 3\n"
    "v_or_b32 v245, v245, v32\n"
    "s_mov_b32 s24, 49664\n"
    "v_lshl_add_u32 v244, v244, 1, s24\n"
    "v_or_b32 v250, 0x6000, v2\n"
    "s_mov_b64 s[26:27], 0xffff\n"
    "v_lshlrev_b64 v[248:249], v251, s[26:27]\n"
    "v_add_u32 v13, 1, v4\n"
    "v_mov_b64 v[14:15], -1\n"
    "v_lshl_add_u32 v8, v13, 16, v4\n"
    "v_add_u32 v9, 0x20002, v8\n"
    "v_add_u32 v10, 0x40004, v8\n"
    "v_add_u32 v11, 0x60006, v8\n"
    "v_mov_b32 v17, v237\n"
    "v_mov_b32 v19, v239\n"
    "v_mov_b32 v21, v237\n"
    "v_mov_b32 v23, v239\n"
    "v_mov_b32 v25, v237\n"
    "v_mov_b32 v27, v239\n"
    "v_mov_b32 v29, v237\n"
    "v_mov_b32 v31, v239\n"
    "v_mov_b32 v32, -1\n"
    "v_mov_b32 v33, 0\n"
    "s_mov_b64 exec, 0xff00\n"
    "v_mov_b32 v32, 0x8c000000\n"
    "s_mov_b64 exec, 0xfffc\n"
    "ds_write_b64 v246, v[32:33] offset:128\n"
    "s_mov_b64 exec, 0xfff\n"
    "ds_write_b64 v4, v[14:15] offset:49664\n"
    "ds_write_b32 v33, v33 offset:49416\n"
    "s_mov_b64 exec, -1\n"
    "s_mov_b32 s28, 0xc080c080\n"
    "s_mov_b32 s29, 0xffff0001\n"
    "s_mov_b32 s30, 0x01000100\n"
    "s_mov_b32 s31, 0x00010001\n"
    "s_add_u32 s40, s40, 0xfffffc00\n"
    "s_addc_u32 s41, s41, 0x7fffff\n"
    "s_lshl_b32 s56, s14, 6\n"
    "s_lshl_b32 s57, s15, 6\n"
    "s_lshr_b32 s79, s17, 7\n"
    "s_mul_i32 s58, s78, s56\n"
    "s_mov_b32 s59, 1\n"
    "s_mul_i32 s62, s20, 2304\n"
    "s_mov_b32 s70, 0x47000000\n"
    "s_mov_b32 s71, 0x47000000\n"
    "s_add_u32 s62, s62, 18016\n"
    "s_and_b32 s77, s18, 53\n"
    "s_and_b32 s73, s18, 8\n"
    "s_lshr_b32 s74, s6, 7\n"
    "s_cbranch_vccnz .Ldin_py\n"
    "v_lshrrev_b32 v32, 5, v1\n"
    "s_mul_i32 s24, s20, 272\n"
    "v_mul_u32_u24 v12, 2176, v32\n"
    "v_and_b32 v60, 32, v0\n"
    "v_add3_u32 v12, v12, s24, 15\n"
    "v_add_u32 v60, s23, v60\n"
    "v_add_u32 v13, v13, v12\n"
    "v_mov_b32 v57, 0\n"
    "v_mov_b32 v59, 0\n"
    "ds_write_b64 v13, v[14:15] offset:49760\n"
    "ds_write_b64 v13, v[14:15] offset:50848\n"
    "s_mul_i32 s23, s20, 0x80110\n"
    "s_lshr_b32 s24, s20, 1\n"
    "s_lshl_b32 s24, s24, 16\n"
    "s_add_u32 s23, s23, s24\n"
    "s_sub_u32 s24, s74, s22\n"
    "s_sub_u32 s25, s24, s79\n"
    "s_add_u32 s26, s23, 0x70\n"
    "s_cmp_gt_i32 s24, 0\n"
    "s_cselect_b32 s26, s26, 0x80000000\n"
    "v_writelane_b32 v56, s26, 31\n"
    "s_add_u32 s26, s23, 0x4008f0\n"
    "s_cmp_gt_i32 s25, 0\n"
    "s_cselect_b32 s26, s26, 0x80000000\n"
    "v_writelane_b32 v56, s26, 63\n"
    "s_add_u32 s26, s23, 0x2204b0\n"
    "s_cmp_gt_i32 s24, 4\n"
    "s_cselect_b32 s26, s26, 0x80000000\n"
    "v_writelane_b32 v58, s26, 31\n"
    "s_add_u32 s26, s23, 0x620d30\n"
    "s_cmp_gt_i32 s25, 4\n"
    "s_cselect_b32 s26, s26, 0x80000000\n"
    "v_writelane_b32 v58, s26, 63\n"
    "s_cmp_lg_u32 s21, 0\n"
    "s_cbranch_scc1 .Ldin_tw0\n"
    "s_call_b64 s[34:35], .Ldin_stub_G1\n"
    "s_call_b64 s[34:35], .Ldin_stub_A0_poll\n"
    "s_call_b64 s[34:35], .Ldin_stub_L2\n"
    "s_branch .Ldin_twe\n"
    ".Ldin_tw0:\n"
    "s_cmp_lg_u32 s21, 1\n"
    "s_cbranch_scc1 .Ldin_tw1\n"
    "s_call_b64 s[34:35], .Ldin_stub_L1\n"
    "s_call_b64 s[34:35], .Ldin_stub_L0\n"
    "s_branch .Ldin_twe\n"
    ".Ldin_tw1:\n"
    "s_cmp_lg_u32 s21, 2\n"
    "s_cbranch_scc1 .Ldin_tw2\n"
    "s_call_b64 s[34:35], .Ldin_stub_L3\n"
    "s_call_b64 s[34:35], .Ldin_stub_E0\n"
    "s_branch .Ldin_twe\n"
    ".Ldin_tw2:\n"
    "s_call_b64 s[34:35], .Ldin_stub_E1\n"
    "s_call_b64 s[34:35], .Ldin_stub_ps\n"
    ".Ldin_twe:\n"
    "s_and_b32 s23, s14, 14\n"
    "s_waitcnt vmcnt(5)\n"
    "s_cbranch_scc1 .Ldin_trimo\n"
    ".Ldin_trimo_r:\n"
    "v_pk_min_u16 v48, v40, 1 op_sel_hi:[1,0]\n"
    "v_pk_min_u16 v49, v41, 1 op_sel_hi:[1,0]\n"
    "v_pk_min_u16 v50, v42, 1 op_sel_hi:[1,0]\n"
    "v_pk_min_u16 v51, v43, 1 op_sel_hi:[1,0]\n"
    "v_add3_u32 v64, v48, v49, v50\n"
    "v_add_u32_e64 v64, v64, v51\n"
    "v_dot2_u32_u16 v62, v64, s31, 0\n"
    "v_pk_min_u16 v52, v44, 1 op_sel_hi:[1,0]\n"
    "v_pk_min_u16 v53, v45, 1 op_sel_hi:[1,0]\n"
    "v_pk_min_u16 v54, v46, 1 op_sel_hi:[1,0]\n"
    "v_pk_min_u16 v55, v47, 1 op_sel_hi:[1,0]\n"
    "v_add3_u32 v64, v52, v53, v54\n"
    "v_add_u32_e64 v64, v64, v55\n"
    "v_dot2_u32_u16 v62, v64, s30, v62\n"
    "v_pk_mad_u16 v40, v48, s28, v40\n"
    "v_pk_mad_u16 v41, v49, s28, v41\n"
    "v_pk_mad_u16 v42, v50, s28, v42\n"
    "v_add_u32_dpp v63, v62, v62 row_shr:1 row_mask:0xf bank_mask:0xf bound_ctrl:1\n"
    "v_sub_u32 v64, 0x10101, v62\n"
    "v_pk_mad_u16 v43, v51, s28, v43\n"
    "v_add_u32_dpp v63, v63, v63 row_shr:2 row_mask:0xf bank_mask:0xf bound_ctrl:1\n"
    "v_or3_b32 v65, v40, v41, 0\n"
    "v_or3_b32 v65, v42, v43, v65\n"
    "v_add_u32_dpp v63, v63, v63 row_shr:4 row_mask:0xf bank_mask:0xf bound_ctrl:1\n"
    "v_pk_mad_u16 v44, v52, s28, v44\n"
    "v_pk_mad_u16 v45, v53, s28, v45\n"
    "v_add_u32_dpp v63, v63, v63 row_shr:8 row_mask:0xf bank_mask:0xf bound_ctrl:1\n"
    "v_pk_mad_u16 v46, v54, s28, v46\n"
    "v_pk_mad_u16 v47, v55, s28, v47\n"
    "v_add_u32_dpp v63, v63, v63 row_bcast:15 row_mask:0xa bank_mask:0xf\n"
    "v_or3_b32 v65, v44, v45, v65\n"
    "v_or3_b32 v65, v46, v47, v65\n"
    "v_add_u32_e64 v64, v64, v63\n"
    "s_mov_b32 s26, 0xc040c00\n"
    "v_perm_b32 v66, v63, v64, s26\n"
    "v_mad_u32_u16 v68, v48, v66, v12\n"
    "v_mad_u32_u16 v69, v48, v66, v12 op_sel:[1,1,0,0]\n"
    "v_pk_mad_u16 v66, v48, s29, v66\n"
    "ds_write_b8 v68, v8 offset:49760\n"
    "ds_write_b8_d16_hi v69, v8 offset:49760\n"
    "v_mad_u32_u16 v70, v49, v66, v12\n"
    "v_mad_u32_u16 v71, v49, v66, v12 op_sel:[1,1,0,0]\n"
    "v_pk_mad_u16 v66, v49, s29, v66\n"
    "ds_write_b8 v70, v9 offset:49760\n"
    "ds_write_b8_d16_hi v71, v9 offset:49760\n"
    "v_mad_u32_u16 v68, v50, v66, v12\n"
    "v_mad_u32_u16 v69, v50, v66, v12 op_sel:[1,1,0,0]\n"
    "v_pk_mad_u16 v66, v50, s29, v66\n"
    "ds_write_b8 v68, v10 offset:49760\n"
    "ds_write_b8_d16_hi v69, v10 offset:49760\n"
    "v_mad_u32_u16 v70, v51, v66, v12\n"
    "v_mad_u32_u16 v71, v51, v66, v12 op_sel:[1,1,0,0]\n"
    "ds_write_b8 v70, v11 offset:49760\n"
    "ds_write_b8_d16_hi v71, v11 offset:49760\n"
    "s_waitcnt vmcnt(0)\n"
    "s_barrier\n"
    "s_mul_i32 s23, s20, 272\n"
    "v_add_u32_e64 v66, s23, v244\n"
    "ds_read_u8 v16, v66 offset:112\n"
    "ds_read_u8 v18, v66 offset:113\n"
    "ds_read_u8 v20, v66 offset:128\n"
    "ds_read_u8 v22, v66 offset:129\n"
    "ds_read_u8 v24, v66 offset:144\n"
    "ds_read_u8 v26, v66 offset:145\n"
    "ds_read_u8 v28, v66 offset:160\n"
    "ds_read_u8 v30, v66 offset:161\n"
    "v_cmp_ne_u32 s[26:27], 0, v65\n"
    "s_mov_b32 s24, 0x0c010c00\n"
    "v_perm_b32 v62, v63, v63, s24\n"
    "s_cmp_lg_u64 s[26:27], 0\n"
    "v_pk_add_u16 v62, v62, 15 op_sel_hi:[1,0]\n"
    "s_cselect_b32 s24, 0x200020, 0\n"
    "v_pk_lshrrev_b16 v62, 4, v62 op_sel_hi:[0,1]\n"
    "v_pk_max_u16 v62, v62, 6 op_sel_hi:[1,0]\n"
    "v_or_b32 v62, s24, v62\n"
    "v_lshl_or_b32 v56, v62, 25, v56\n"
    "v_lshrrev_b32 v62, 16, v62\n"
    "s_add_u32 m0, s68, 0\n"
    "s_waitcnt lgkmcnt(6)\n"
    "buffer_load_dwordx4 v[16:17], s[40:43], s67 idxen offen offset:0 nt lds\n"
    "buffer_load_dwordx4 v[18:19], s[40:43], s67 idxen offen offset:1024 nt lds\n"
    "s_add_u32 m0, s68, 2048\n"
    "s_waitcnt lgkmcnt(4)\n"
    "buffer_load_dwordx4 v[20:21], s[40:43], s67 idxen offen offset:0 nt lds\n"
    "buffer_load_dwordx4 v[22:23], s[40:43], s67 idxen offen offset:1024 nt lds\n"
    "s_add_u32 m0, s68, 4096\n"
    "s_waitcnt lgkmcnt(2)\n"
    "buffer_load_dwordx4 v[24:25], s[40:43], s67 idxen offen offset:0 nt lds\n"
    "buffer_load_dwordx4 v[26:27], s[40:43], s67 idxen offen offset:1024 nt lds\n"
    "s_add_u32 m0, s68, 6144\n"
    "s_waitcnt lgkmcnt(0)\n"
    "buffer_load_dwordx4 v[28:29], s[40:43], s67 idxen offen offset:0 nt lds\n"
    "buffer_load_dwordx4 v[30:31], s[40:43], s67 idxen offen offset:1024 nt lds\n"
    "v_lshl_or_b32 v58, v62, 25, v58\n"
    "v_readlane_b32 s60, v56, 31\n"
    "s_mov_b32 s26, 0xc050c01\n"
    "v_perm_b32 v66, v63, v64, s26\n"
    "v_mad_u32_u16 v68, v52, v66, v12\n"
    "v_mad_u32_u16 v69, v52, v66, v12 op_sel:[1,1,0,0]\n"
    "v_pk_mad_u16 v66, v52, s29, v66\n"
    "ds_write_b8 v68, v8 offset:50848\n"
    "ds_write_b8_d16_hi v69, v8 offset:50848\n"
    "v_mad_u32_u16 v70, v53, v66, v12\n"
    "v_mad_u32_u16 v71, v53, v66, v12 op_sel:[1,1,0,0]\n"
    "v_pk_mad_u16 v66, v53, s29, v66\n"
    "ds_write_b8 v70, v9 offset:50848\n"
    "ds_write_b8_d16_hi v71, v9 offset:50848\n"
    "v_mad_u32_u16 v68, v54, v66, v12\n"
    "v_mad_u32_u16 v69, v54, v66, v12 op_sel:[1,1,0,0]\n"
    "v_pk_mad_u16 v66, v54, s29, v66\n"
    "ds_write_b8 v68, v10 offset:50848\n"
    "ds_write_b8_d16_hi v69, v10 offset:50848\n"
    "v_mad_u32_u16 v70, v55, v66, v12\n"
    "v_mad_u32_u16 v71, v55, v66, v12 op_sel:[1,1,0,0]\n"
    "ds_write_b8 v70, v11 offset:50848\n"
    "ds_write_b8_d16_hi v71, v11 offset:50848\n"
    "s_brev_b32 exec_lo, 1\n"
    "s_brev_b32 exec_hi, 1\n"
    "ds_write_b64 v60, v[56:57] offset:128\n"
    "ds_write_b64 v60, v[58:59] offset:1152\n"
    "s_mov_b64 exec, -1\n"
    "s_waitcnt lgkmcnt(0)\n"
    "s_barrier\n"
    ".Ldin_go:\n"
    "s_or_b32 s45, s45, 0x20000\n"
    "s_mov_b32 s46, s42\n"
    "s_cmp_lt_i32 s60, 0\n"
    "s_cbranch_scc1 .Ldin_idle\n"
    "s_lshr_b32 s94, s60, 12\n"
    "s_and_b32 s94, s94, 0x1ff0\n"
    "v_xor_b32 v252, s94, v250\n"
    "ds_read_b128 v[216:219], v252 offset:0\n"
    "ds_read_b128 v[220:223], v252 offset:64\n"
    "ds_read_b128 v[96:99], v251 offset:16384\n"
    "ds_read_b128 v[32:35], v251 offset:0\n"
    "ds_read_b128 v[36:39], v251 offset:1024\n"
    "ds_read_b128 v[100:103], v251 offset:17408\n"
    "ds_read_b128 v[40:43], v251 offset:2048\n"
    "ds_read_b128 v[44:47], v251 offset:3072\n"
    "ds_read_b128 v[104:107], v251 offset:18432\n"
    "ds_read_b128 v[48:51], v251 offset:4096\n"
    "ds_read_b128 v[52:55], v251 offset:5120\n"
    "ds_read_b128 v[108:111], v251 offset:19456\n"
    "ds_read_b128 v[56:59], v251 offset:6144\n"
    "ds_read_b128 v[60:63], v251 offset:7168\n"
    "s_and_b32 s66, s60, 0xffff\n"
    "s_bfe_u32 s95, s60, 0x50019\n"
    "s_mov_b32 s81, s66\n"
    "s_lshr_b32 s63, s67, 6\n"
    "s_lshl4_add_u32 s80, s95, s66\n"
    "v_add_u32 v230, s66, v244\n"
    "s_add_u32 s66, s66, 80\n"
    "s_mov_b32 s8, s68\n"
    "s_mov_b32 s9, 64\n"
    "s_lshl_b32 s94, s20, 2\n"
    "s_lshr_b32 s10, 0x43437654, s94\n"
    "s_and_b32 s10, s10, 15\n"
    "s_add_u32 m0, s8, 0\n"
    "s_waitcnt lgkmcnt(12)\n"
    "v_and_b32 v216, v248, v216\n"
    "v_and_b32 v217, v249, v217\n"
    "v_and_b32 v218, v248, v218\n"
    "v_and_b32 v219, v249, v219\n"
    "s_waitcnt lgkmcnt(9)\n"
    "ds_read_b128 v[112:115], v251 offset:20480\n"
    "ds_read_b128 v[64:67], v251 offset:8192\n"
    "ds_read_b128 v[68:71], v251 offset:9216\n"
    "v_mfma_f32_4x4x4_16b_bf16 v[176:179], v[216:217], v[96:97], v[32:35]\n"
    "v_and_b32 v220, v248, v220\n"
    "v_and_b32 v221, v249, v221\n"
    "v_mfma_f32_4x4x4_16b_bf16 v[180:183], v[218:219], v[98:99], v[36:39]\n"
    "v_and_b32 v222, v248, v222\n"
    "v_and_b32 v223, v249, v223\n"
    "s_waitcnt lgkmcnt(9)\n"
    "ds_read_b128 v[116:119], v251 offset:21504\n"
    "ds_read_b128 v[72:75], v251 offset:10240\n"
    "ds_read_b128 v[76:79], v251 offset:11264\n"
    "v_mfma_f32_4x4x4_16b_bf16 v[184:187], v[220:221], v[100:101], v[40:43]\n"
    "v_cvt_pk_bf16_f32 v0, v176, v177\n"
    "v_cvt_pk_bf16_f32 v1, v178, v179\n"
    "v_mfma_f32_4x4x4_16b_bf16 v[188:191], v[222:223], v[102:103], v[44:47]\n"
    "v_cvt_pk_bf16_f32 v2, v180, v181\n"
    "v_cvt_pk_bf16_f32 v3, v182, v183\n"
    "s_waitcnt lgkmcnt(9)\n"
    "s_nop 0\n"
    "ds_read_b128 v[120:123], v251 offset:22528\n"
    "ds_read_b128 v[80:83], v251 offset:12288\n"
    "ds_read_b128 v[84:87], v251 offset:13312\n"
    "v_mfma_f32_4x4x4_16b_bf16 v[176:179], v[216:217], v[104:105], v[48:51]\n"
    "v_cvt_pk_bf16_f32 v4, v184, v185\n"
    "v_cvt_pk_bf16_f32 v5, v186, v187\n"
    "v_mfma_f32_4x4x4_16b_bf16 v[180:183], v[218:219], v[106:107], v[52:55]\n"
    "v_cvt_pk_bf16_f32 v6, v188, v189\n"
    "v_cvt_pk_bf16_f32 v7, v190, v191\n"
    "s_waitcnt lgkmcnt(9)\n"
    "s_nop 0\n"
    "ds_read_b128 v[124:127], v251 offset:23552\n"
    "ds_read_b128 v[88:91], v251 offset:14336\n"
    "ds_read_b128 v[92:95], v251 offset:15360\n"
    "v_mfma_f32_4x4x4_16b_bf16 v[184:187], v[220:221], v[108:109], v[56:59]\n"
    "v_cvt_pk_bf16_f32 v8, v176, v177\n"
    "v_cvt_pk_bf16_f32 v9, v178, v179\n"
    "v_mfma_f32_4x4x4_16b_bf16 v[188:191], v[222:223], v[110:111], v[60:63]\n"
    "v_cvt_pk_bf16_f32 v10, v180, v181\n"
    "v_cvt_pk_bf16_f32 v11, v182, v183\n"
    "s_waitcnt lgkmcnt(9)\n"
    "s_nop 0\n"
    "ds_read_b128 v[144:147], v252 offset:8192\n"
    "ds_read_b128 v[148:151], v252 offset:8256\n"
    "ds_read_b128 v[152:155], v252 offset:16384\n"
    "ds_read_b128 v[156:159], v252 offset:16448\n"
    "ds_read_u8 v236, v230 offset:64\n"
    "ds_read_u8 v238, v230 offset:65\n"
    "v_mfma_f32_4x4x4_16b_bf16 v[176:179], v[216:217], v[112:113], v[64:67]\n"
    "v_cvt_pk_bf16_f32 v12, v184, v185\n"
    "v_cvt_pk_bf16_f32 v13, v186, v187\n"
    "v_mfma_f32_4x4x4_16b_bf16 v[180:183], v[218:219], v[114:115], v[68:71]\n"
    "v_cvt_pk_bf16_f32 v14, v188, v189\n"
    "v_cvt_pk_bf16_f32 v15, v190, v191\n"
    "s_waitcnt lgkmcnt(12)\n"
    "s_waitcnt vmcnt(4)\n"
    "ds_read_b128 v[192:195], v234 offset:0\n"
    "ds_read_b128 v[196:199], v235 offset:0\n"
    "ds_read_b64_tr_b16 v[200:201], v240 offset:0\n"
    "ds_read_b64_tr_b16 v[202:203], v241 offset:0\n"
    "ds_read_b64_tr_b16 v[204:205], v242 offset:0\n"
    "ds_read_b64_tr_b16 v[206:207], v243 offset:0\n"
    "v_mfma_f32_4x4x4_16b_bf16 v[184:187], v[220:221], v[116:117], v[72:75]\n"
    "v_cvt_pk_bf16_f32 v16, v176, v177\n"
    "v_cvt_pk_bf16_f32 v17, v178, v179\n"
    "v_mfma_f32_4x4x4_16b_bf16 v[188:191], v[222:223], v[118:119], v[76:79]\n"
    "v_cvt_pk_bf16_f32 v18, v180, v181\n"
    "v_cvt_pk_bf16_f32 v19, v182, v183\n"
    "s_waitcnt lgkmcnt(15)\n"
    "s_nop 0\n"
    "ds_read_b128 v[128:131], v250 offset:24576\n"
    "ds_read_b128 v[132:135], v250 offset:24640\n"
    "ds_read_b128 v[136:139], v250 offset:24704\n"
    "ds_read_b128 v[140:143], v250 offset:24768\n"
    "ds_read_b64 v[232:233], v250 offset:24832\n"
    "v_mfma_f32_4x4x4_16b_bf16 v[176:179], v[216:217], v[120:121], v[80:83]\n"
    "v_cvt_pk_bf16_f32 v20, v184, v185\n"
    "v_cvt_pk_bf16_f32 v21, v186, v187\n"
    "v_mfma_f32_4x4x4_16b_bf16 v[180:183], v[218:219], v[122:123], v[84:87]\n"
    "v_cvt_pk_bf16_f32 v22, v188, v189\n"
    "v_cvt_pk_bf16_f32 v23, v190, v191\n"
    "s_waitcnt lgkmcnt(15)\n"
    "v_mfma_f32_4x4x4_16b_bf16 v[184:187], v[220:221], v[124:125], v[88:91]\n"
    "v_cvt_pk_bf16_f32 v24, v176, v177\n"
    "v_cvt_pk_bf16_f32 v25, v178, v179\n"
    "v_mfma_f32_4x4x4_16b_bf16 v[188:191], v[222:223], v[126:127], v[92:95]\n"
    "v_cvt_pk_bf16_f32 v26, v180, v181\n"
    "v_cvt_pk_bf16_f32 v27, v182, v183\n"
    "s_waitcnt lgkmcnt(5)\n"
    "buffer_load_dwordx4 v[236:237], s[40:43], s67 idxen offen offset:0 nt lds\n"
    "buffer_load_dwordx4 v[238:239], s[40:43], s67 idxen offen offset:1024 nt lds\n"
    "v_cvt_pk_bf16_f32 v28, v184, v185\n"
    "v_cvt_pk_bf16_f32 v29, v186, v187\n"
    "v_cvt_pk_bf16_f32 v30, v188, v189\n"
    "v_cvt_pk_bf16_f32 v31, v190, v191\n"
    "v_mfma_f32_16x16x32_bf16 v[160:163], v[0:3], v[192:195], v[144:147]\n"
    "v_mfma_f32_16x16x32_bf16 v[160:163], v[4:7], v[196:199], v[160:163]\n"
    "v_mfma_f32_16x16x32_bf16 v[164:167], v[8:11], v[192:195], v[148:151]\n"
    "v_mfma_f32_16x16x32_bf16 v[164:167], v[12:15], v[196:199], v[164:167]\n"
    "v_mfma_f32_16x16x32_bf16 v[168:171], v[16:19], v[192:195], v[152:155]\n"
    "v_mfma_f32_16x16x32_bf16 v[168:171], v[20:23], v[196:199], v[168:171]\n"
    "v_mfma_f32_16x16x32_bf16 v[172:175], v[24:27], v[192:195], v[156:159]\n"
    "v_mfma_f32_16x16x32_bf16 v[172:175], v[28:31], v[196:199], v[172:175]\n"
    "ds_read_b128 v[192:195], v234 offset:2048\n"
    "ds_read_b128 v[196:199], v235 offset:2048\n"
    "ds_read_b64 v[216:217], v246 offset:128\n"
    "s_memrealtime s[34:35]\n"
    "s_waitcnt lgkmcnt(0)\n"
    "s_mul_i32 s94, s75, 80\n"
    "s_add_u32 s94, s94, 0\n"
    "s_and_b32 s94, s18, s94\n"
    "s_add_u32 s76, s34, s94\n"
    "v_mov_b32 v247, v232\n"
    "v_readfirstlane_b32 s69, v233\n"
    "s_bitcmp1_b32 s60, 30\n"
    "s_nop 0\n"
    "s_cbranch_scc1 .Ldin_A0s\n"
    ".Ldin_A0:\n"
    "v_add_u32 v251, s66, v244\n"
    "v_readlane_b32 s60, v216, s59\n"
    "v_readlane_b32 s61, v217, s59\n"
    "v_mfma_f32_4x4x4_16b_bf16 v[224:227], v[228:229], v[208:209], v[224:227] cbsz:4 abid:0\n"
    "v_exp_f32 v160, v160 clamp\n"
    "v_exp_f32 v161, v161 clamp\n"
    "v_mfma_f32_4x4x4_16b_bf16 v[224:227], v[228:229], v[210:211], v[224:227] cbsz:4 abid:1\n"
    "v_exp_f32 v162, v162 clamp\n"
    "v_exp_f32 v163, v163 clamp\n"
    "v_mfma_f32_4x4x4_16b_bf16 v[224:227], v[228:229], v[212:213], v[224:227] cbsz:4 abid:2\n"
    "v_exp_f32 v164, v164 clamp\n"
    "v_exp_f32 v165, v165 clamp\n"
    "v_mfma_f32_4x4x4_16b_bf16 v[224:227], v[228:229], v[214:215], v[224:227] cbsz:4 abid:3\n"
    "s_waitcnt lgkmcnt(0)\n"
    "v_mfma_f32_16x16x32_bf16 v[176:179], v[0:3], v[192:195], v[144:147]\n"
    "s_setprio 1\n"
    "v_exp_f32 v166, v166 clamp\n"
    "v_mfma_f32_16x16x32_bf16 v[176:179], v[4:7], v[196:199], v[176:179]\n"
    "s_cmp_eq_u32 s60, -1\n"
    "v_exp_f32 v167, v167 clamp\n"
    "v_mfma_f32_16x16x32_bf16 v[180:183], v[8:11], v[192:195], v[148:151]\n"
    "s_cbranch_scc1 .Ldin_A0_poll\n"
    ".Ldin_A0_pr:\n"
    "v_exp_f32 v168, v168 clamp\n"
    "v_mfma_f32_16x16x32_bf16 v[180:183], v[12:15], v[196:199], v[180:183]\n"
    "s_lshr_b32 s94, s60, 12\n"
    "v_exp_f32 v169, v169 clamp\n"
    "v_mfma_f32_16x16x32_bf16 v[184:187], v[16:19], v[192:195], v[152:155]\n"
    "s_and_b32 s94, s94, 0x1ff0\n"
    "v_exp_f32 v170, v170 clamp\n"
    "v_mfma_f32_16x16x32_bf16 v[184:187], v[20:23], v[196:199], v[184:187]\n"
    "s_mov_b32 m0, s62\n"
    "v_exp_f32 v171, v171 clamp\n"
    "v_mfma_f32_16x16x32_bf16 v[188:191], v[24:27], v[192:195], v[156:159]\n"
    "s_add_u32 s66, s66, 16\n"
    "v_exp_f32 v172, v172 clamp\n"
    "v_mfma_f32_16x16x32_bf16 v[188:191], v[28:31], v[196:199], v[188:191]\n"
    "s_and_b32 s64, s60, 0xffff\n"
    "v_xor_b32 v252, s94, v250\n"
    "v_exp_f32 v173, v173 clamp\n"
    "v_exp_f32 v174, v174 clamp\n"
    "v_exp_f32 v175, v175 clamp\n"
    "v_pk_fma_f32 v[160:161], v[160:161], s[70:71], v[128:129]\n"
    "v_pk_fma_f32 v[162:163], v[162:163], s[70:71], v[130:131]\n"
    "v_pk_add_f32 v[230:231], v[160:161], v[162:163]\n"
    "v_pk_mul_f32 v[232:233], v[160:161], v[162:163]\n"
    "v_pk_add_f32 v[218:219], v[224:225], v[226:227]\n"
    "v_mov_b64 v[224:225], 0\n"
    "v_mov_b64 v[226:227], 0\n"
    "v_pk_fma_f32 v[164:165], v[164:165], s[70:71], v[132:133]\n"
    "v_pk_fma_f32 v[230:231], v[230:231], v[164:165], v[232:233]\n"
    "v_pk_mul_f32 v[232:233], v[232:233], v[164:165]\n"
    "v_pk_fma_f32 v[166:167], v[166:167], s[70:71], v[134:135]\n"
    "v_pk_fma_f32 v[230:231], v[230:231], v[166:167], v[232:233]\n"
    "v_pk_mul_f32 v[232:233], v[232:233], v[166:167]\n"
    "v_pk_fma_f32 v[168:169], v[168:169], s[70:71], v[136:137]\n"
    "v_pk_fma_f32 v[230:231], v[230:231], v[168:169], v[232:233]\n"
    "v_pk_mul_f32 v[232:233], v[232:233], v[168:169]\n"
    "v_pk_fma_f32 v[170:171], v[170:171], s[70:71], v[138:139]\n"
    "v_pk_fma_f32 v[230:231], v[230:231], v[170:171], v[232:233]\n"
    "v_pk_mul_f32 v[232:233], v[232:233], v[170:171]\n"
    "v_pk_fma_f32 v[172:173], v[172:173], s[70:71], v[140:141]\n"
    "v_pk_fma_f32 v[230:231], v[230:231], v[172:173], v[232:233]\n"
    "v_pk_mul_f32 v[232:233], v[232:233], v[172:173]\n"
    "v_pk_fma_f32 v[174:175], v[174:175], s[70:71], v[142:143]\n"
    "v_pk_fma_f32 v[230:231], v[230:231], v[174:175], v[232:233]\n"
    "v_pk_mul_f32 v[232:233], v[232:233], v[174:175]\n"
    "v_rcp_f32 v232, v232\n"
    "v_rcp_f32 v233, v233\n"
    "v_fma_f32 v230, v230, v232, v247\n"
    "v_fmac_f32 v230, v231, v233\n"
    "v_add_f32_e64 v218, v218, v219\n"
    "s_setprio 0\n"
    "ds_read_b64_tr_b16 v[208:209], v240 offset:2048\n"
    "ds_read_b64_tr_b16 v[210:211], v241 offset:2048\n"
    "ds_read_b64_tr_b16 v[212:213], v242 offset:2048\n"
    "ds_read_b64_tr_b16 v[214:215], v243 offset:2048\n"
    "ds_read_u8 v236, v251 offset:0\n"
    "ds_read_u8 v238, v251 offset:1\n"
    "ds_write_b16_d16_hi v245, v230\n"
    "ds_read_b64 v[228:229], v246\n"
    "ds_write_addtid_b32 v218 offset:49152\n"
    "s_add_u32 m0, s68, 2048\n"
    "s_bfe_u32 s94, s60, 0x30013\n"
    "s_add_u32 s58, s58, s57\n"
    "s_mul_i32 s94, s94, s56\n"
    "s_add_u32 s82, s64, s9\n"
    "s_add_u32 s65, s58, s94\n"
    "s_waitcnt vmcnt(2)\n"
    "ds_read_b128 v[192:195], v234 offset:4096\n"
    "ds_read_b128 v[196:199], v235 offset:4096\n"
    "ds_read_b128 v[216:219], v252 offset:0\n"
    "ds_read_b128 v[220:223], v252 offset:64\n"
    "s_sub_u32 s95, s61, s76\n"
    "s_lshr_b32 s94, s61, 16\n"
    "s_sext_i32_i16 s95, s95\n"
    "s_cmp_eq_u32 s59, s10\n"
    "s_cbranch_scc1 .Ldin_A0_skip\n"
    "s_waitcnt lgkmcnt(7)\n"
    "buffer_load_dwordx4 v[236:237], s[40:43], s67 idxen offen offset:0 nt lds\n"
    "buffer_load_dwordx4 v[238:239], s[40:43], s67 idxen offen offset:1024 nt lds\n"
    ".Ldin_A0_skr:\n"
    "s_cmp_lt_i32 s95, -550\n"
    "s_cselect_b32 s94, 0, s94\n"
    "s_mul_i32 s95, s95, s94\n"
    "s_bfe_u32 s94, s60, 0x50019\n"
    "s_ashr_i32 s95, s95, 16\n"
    "s_lshl4_add_u32 s83, s94, s64\n"
    "s_max_i32 s95, s95, -14\n"
    "s_add_u32 s59, s59, 1\n"
    "s_min_i32 s95, s95, 10\n"
    "s_add_u32 s72, s77, s95\n"
    "s_cmp_lt_i32 s60, 0\n"
    "s_cselect_b32 s72, 0, s72\n"
    "s_cmp_eq_u32 s66, s80\n"
    "s_cselect_b64 s[66:67], s[64:65], s[66:67]\n"
    "s_waitcnt lgkmcnt(5)\n"
    "s_branch .Ldin_G1\n"
    ".p2align 12\n"
    ".Ldin_stub_G1:\n"
    "s_setpc_b64 s[34:35]\n"
    ".p2align 5\n"
    ".Ldin_G1:\n"
    "v_add_u32 v251, s66, v244\n"
    "v_mfma_f32_4x4x4_16b_bf16 v[224:227], v[228:229], v[200:201], v[224:227] cbsz:4 abid:0\n"
    "v_exp_f32 v176, v176 clamp\n"
    "v_exp_f32 v177, v177 clamp\n"
    "v_mfma_f32_4x4x4_16b_bf16 v[224:227], v[228:229], v[202:203], v[224:227] cbsz:4 abid:1\n"
    "v_exp_f32 v178, v178 clamp\n"
    "v_exp_f32 v179, v179 clamp\n"
    "v_mfma_f32_4x4x4_16b_bf16 v[224:227], v[228:229], v[204:205], v[224:227] cbsz:4 abid:2\n"
    "v_exp_f32 v180, v180 clamp\n"
    "v_exp_f32 v181, v181 clamp\n"
    "v_mfma_f32_4x4x4_16b_bf16 v[224:227], v[228:229], v[206:207], v[224:227] cbsz:4 abid:3\n"
    "s_waitcnt lgkmcnt(0)\n"
    "v_mfma_f32_16x16x32_bf16 v[160:163], v[0:3], v[192:195], v[144:147]\n"
    "s_setprio 1\n"
    "v_exp_f32 v182, v182 clamp\n"
    "v_mfma_f32_16x16x32_bf16 v[160:163], v[4:7], v[196:199], v[160:163]\n"
    "s_add_u32 m0, s68, 4096\n"
    "v_exp_f32 v183, v183 clamp\n"
    "v_mfma_f32_16x16x32_bf16 v[164:167], v[8:11], v[192:195], v[148:151]\n"
    "s_add_u32 s66, s66, 16\n"
    "v_exp_f32 v184, v184 clamp\n"
    "v_mfma_f32_16x16x32_bf16 v[164:167], v[12:15], v[196:199], v[164:167]\n"
    "v_exp_f32 v185, v185 clamp\n"
    "v_mfma_f32_16x16x32_bf16 v[168:171], v[16:19], v[192:195], v[152:155]\n"
    "v_exp_f32 v186, v186 clamp\n"
    "v_mfma_f32_16x16x32_bf16 v[168:171], v[20:23], v[196:199], v[168:171]\n"
    "v_exp_f32 v187, v187 clamp\n"
    "v_mfma_f32_16x16x32_bf16 v[172:175], v[24:27], v[192:195], v[156:159]\n"
    "v_exp_f32 v188, v188 clamp\n"
    "v_mfma_f32_16x16x32_bf16 v[172:175], v[28:31], v[196:199], v[172:175]\n"
    "s_memrealtime s[34:35]\n"
    "v_exp_f32 v189, v189 clamp\n"
    "v_exp_f32 v190, v190 clamp\n"
    "v_exp_f32 v191, v191 clamp\n"
    "v_pk_fma_f32 v[176:177], v[176:177], s[70:71], v[128:129]\n"
    "v_pk_fma_f32 v[178:179], v[178:179], s[70:71], v[130:131]\n"
    "v_pk_add_f32 v[230:231], v[176:177], v[178:179]\n"
    "v_pk_mul_f32 v[232:233], v[176:177], v[178:179]\n"
    "v_pk_fma_f32 v[180:181], v[180:181], s[70:71], v[132:133]\n"
    "v_pk_fma_f32 v[230:231], v[230:231], v[180:181], v[232:233]\n"
    "v_pk_mul_f32 v[232:233], v[232:233], v[180:181]\n"
    "v_pk_fma_f32 v[182:183], v[182:183], s[70:71], v[134:135]\n"
    "v_pk_fma_f32 v[230:231], v[230:231], v[182:183], v[232:233]\n"
    "v_pk_mul_f32 v[232:233], v[232:233], v[182:183]\n"
    "v_pk_fma_f32 v[184:185], v[184:185], s[70:71], v[136:137]\n"
    "v_pk_fma_f32 v[230:231], v[230:231], v[184:185], v[232:233]\n"
    "v_pk_mul_f32 v[232:233], v[232:233], v[184:185]\n"
    "v_pk_fma_f32 v[186:187], v[186:187], s[70:71], v[138:139]\n"
    "v_pk_fma_f32 v[230:231], v[230:231], v[186:187], v[232:233]\n"
    "v_pk_mul_f32 v[232:233], v[232:233], v[186:187]\n"
    "v_pk_fma_f32 v[188:189], v[188:189], s[70:71], v[140:141]\n"
    "v_pk_fma_f32 v[230:231], v[230:231], v[188:189], v[232:233]\n"
    "v_pk_mul_f32 v[232:233], v[232:233], v[188:189]\n"
    "v_pk_fma_f32 v[190:191], v[190:191], s[70:71], v[142:143]\n"
    "v_pk_fma_f32 v[230:231], v[230:231], v[190:191], v[232:233]\n"
    "v_pk_mul_f32 v[232:233], v[232:233], v[190:191]\n"
    "v_rcp_f32 v232, v232\n"
    "v_rcp_f32 v233, v233\n"
    "v_fma_f32 v230, v230, v232, v247\n"
    "v_fmac_f32_e64 v230, v231, v233\n"
    "s_setprio 0\n"
    "s_waitcnt lgkmcnt(0)\n"
    "ds_read_b64_tr_b16 v[200:201], v240 offset:4096\n"
    "ds_read_b64_tr_b16 v[202:203], v241 offset:4096\n"
    "ds_read_b64_tr_b16 v[204:205], v242 offset:4096\n"
    "ds_read_b64_tr_b16 v[206:207], v243 offset:4096\n"
    "ds_read_u8 v236, v251 offset:0\n"
    "ds_read_u8 v238, v251 offset:1\n"
    "ds_write_b16_d16_hi v245, v230\n"
    "ds_read_b64 v[228:229], v246\n"
    "s_cmp_lt_u32 s34, s76\n"
    "s_cbranch_scc1 .Ldin_G1_wait\n"
    ".Ldin_G1_go:\n"
    "s_waitcnt vmcnt(2)\n"
    "ds_read_b128 v[192:195], v234 offset:6144\n"
    "ds_read_b128 v[196:199], v235 offset:6144\n"
    "s_waitcnt lgkmcnt(4)\n"
    "buffer_load_dwordx4 v[236:237], s[40:43], s67 idxen offen offset:0 nt lds\n"
    "buffer_load_dwordx4 v[238:239], s[40:43], s67 idxen offen offset:1024 nt lds\n"
    "s_add_u32 s76, s76, s72\n"
    "s_cmp_eq_u32 s66, s80\n"
    "s_cselect_b64 s[66:67], s[64:65], s[66:67]\n"
    "s_waitcnt lgkmcnt(2)\n"
    "s_cmp_eq_u32 s66, s82\n"
    "s_cbranch_scc1 .Ldin_L2\n"
    ".Ldin_G2:\n"
    "v_add_u32 v251, s66, v244\n"
    "v_mfma_f32_4x4x4_16b_bf16 v[224:227], v[228:229], v[208:209], v[224:227] cbsz:4 abid:0\n"
    "v_exp_f32 v160, v160 clamp\n"
    "v_exp_f32 v161, v161 clamp\n"
    "v_mfma_f32_4x4x4_16b_bf16 v[224:227], v[228:229], v[210:211], v[224:227] cbsz:4 abid:1\n"
    "v_exp_f32 v162, v162 clamp\n"
    "v_exp_f32 v163, v163 clamp\n"
    "v_mfma_f32_4x4x4_16b_bf16 v[224:227], v[228:229], v[212:213], v[224:227] cbsz:4 abid:2\n"
    "v_exp_f32 v164, v164 clamp\n"
    "v_exp_f32 v165, v165 clamp\n"
    "v_mfma_f32_4x4x4_16b_bf16 v[224:227], v[228:229], v[214:215], v[224:227] cbsz:4 abid:3\n"
    "s_waitcnt lgkmcnt(0)\n"
    "v_mfma_f32_16x16x32_bf16 v[176:179], v[0:3], v[192:195], v[144:147]\n"
    "s_setprio 1\n"
    "v_exp_f32 v166, v166 clamp\n"
    "v_mfma_f32_16x16x32_bf16 v[176:179], v[4:7], v[196:199], v[176:179]\n"
    "s_add_u32 m0, s68, 6144\n"
    "v_exp_f32 v167, v167 clamp\n"
    "v_mfma_f32_16x16x32_bf16 v[180:183], v[8:11], v[192:195], v[148:151]\n"
    "s_add_u32 s66, s66, 16\n"
    "v_exp_f32 v168, v168 clamp\n"
    "v_mfma_f32_16x16x32_bf16 v[180:183], v[12:15], v[196:199], v[180:183]\n"
    "v_exp_f32 v169, v169 clamp\n"
    "v_mfma_f32_16x16x32_bf16 v[184:187], v[16:19], v[192:195], v[152:155]\n"
    "v_exp_f32 v170, v170 clamp\n"
    "v_mfma_f32_16x16x32_bf16 v[184:187], v[20:23], v[196:199], v[184:187]\n"
    "v_exp_f32 v171, v171 clamp\n"
    "v_mfma_f32_16x16x32_bf16 v[188:191], v[24:27], v[192:195], v[156:159]\n"
    "v_exp_f32 v172, v172 clamp\n"
    "v_mfma_f32_16x16x32_bf16 v[188:191], v[28:31], v[196:199], v[188:191]\n"
    "s_memrealtime s[34:35]\n"
    "v_exp_f32 v173, v173 clamp\n"
    "v_exp_f32 v174, v174 clamp\n"
    "v_exp_f32 v175, v175 clamp\n"
    "v_pk_fma_f32 v[160:161], v[160:161], s[70:71], v[128:129]\n"
    "v_pk_fma_f32 v[162:163], v[162:163], s[70:71], v[130:131]\n"
    "v_pk_add_f32 v[230:231], v[160:161], v[162:163]\n"
    "v_pk_mul_f32 v[232:233], v[160:161], v[162:163]\n"
    "v_pk_fma_f32 v[164:165], v[164:165], s[70:71], v[132:133]\n"
    "v_pk_fma_f32 v[230:231], v[230:231], v[164:165], v[232:233]\n"
    "v_pk_mul_f32 v[232:233], v[232:233], v[164:165]\n"
    "v_pk_fma_f32 v[166:167], v[166:167], s[70:71], v[134:135]\n"
    "v_pk_fma_f32 v[230:231], v[230:231], v[166:167], v[232:233]\n"
    "v_pk_mul_f32 v[232:233], v[232:233], v[166:167]\n"
    "v_pk_fma_f32 v[168:169], v[168:169], s[70:71], v[136:137]\n"
    "v_pk_fma_f32 v[230:231], v[230:231], v[168:169], v[232:233]\n"
    "v_pk_mul_f32 v[232:233], v[232:233], v[168:169]\n"
    "v_pk_fma_f32 v[170:171], v[170:171], s[70:71], v[138:139]\n"
    "v_pk_fma_f32 v[230:231], v[230:231], v[170:171], v[232:233]\n"
    "v_pk_mul_f32 v[232:233], v[232:233], v[170:171]\n"
    "v_pk_fma_f32 v[172:173], v[172:173], s[70:71], v[140:141]\n"
    "v_pk_fma_f32 v[230:231], v[230:231], v[172:173], v[232:233]\n"
    "v_pk_mul_f32 v[232:233], v[232:233], v[172:173]\n"
    "v_pk_fma_f32 v[174:175], v[174:175], s[70:71], v[142:143]\n"
    "v_pk_fma_f32 v[230:231], v[230:231], v[174:175], v[232:233]\n"
    "v_pk_mul_f32 v[232:233], v[232:233], v[174:175]\n"
    "v_rcp_f32 v232, v232\n"
    "v_rcp_f32 v233, v233\n"
    "v_fma_f32 v230, v230, v232, v247\n"
    "v_fmac_f32_e64 v230, v231, v233\n"
    "s_setprio 0\n"
    "s_waitcnt lgkmcnt(0)\n"
    "ds_read_b64_tr_b16 v[208:209], v240 offset:6144\n"
    "ds_read_b64_tr_b16 v[210:211], v241 offset:6144\n"
    "ds_read_b64_tr_b16 v[212:213], v242 offset:6144\n"
    "ds_read_b64_tr_b16 v[214:215], v243 offset:6144\n"
    "ds_read_u8 v236, v251 offset:0\n"
    "ds_read_u8 v238, v251 offset:1\n"
    "ds_write_b16_d16_hi v245, v230\n"
    "ds_read_b64 v[228:229], v246\n"
    "s_cmp_lt_u32 s34, s76\n"
    "s_cbranch_scc1 .Ldin_G2_wait\n"
    ".Ldin_G2_go:\n"
    "s_waitcnt vmcnt(2)\n"
    "ds_read_b128 v[192:195], v234 offset:0\n"
    "ds_read_b128 v[196:199], v235 offset:0\n"
    "s_waitcnt lgkmcnt(4)\n"
    "buffer_load_dwordx4 v[236:237], s[40:43], s67 idxen offen offset:0 nt lds\n"
    "buffer_load_dwordx4 v[238:239], s[40:43], s67 idxen offen offset:1024 nt lds\n"
    "s_sub_u32 s94, s34, 30\n"
    "s_max_u32 s76, s76, s94\n"
    "s_add_u32 s76, s76, s72\n"
    "s_cmp_eq_u32 s66, s80\n"
    "s_cselect_b64 s[66:67], s[64:65], s[66:67]\n"
    "s_waitcnt lgkmcnt(2)\n"
    "s_cmp_eq_u32 s66, s82\n"
    "s_cbranch_scc1 .Ldin_L3\n"
    ".Ldin_G3:\n"
    "v_add_u32_e64 v251, s66, v244\n"
    "v_mfma_f32_4x4x4_16b_bf16 v[224:227], v[228:229], v[200:201], v[224:227] cbsz:4 abid:0\n"
    "v_exp_f32 v176, v176 clamp\n"
    "v_exp_f32 v177, v177 clamp\n"
    "v_mfma_f32_4x4x4_16b_bf16 v[224:227], v[228:229], v[202:203], v[224:227] cbsz:4 abid:1\n"
    "v_exp_f32 v178, v178 clamp\n"
    "v_exp_f32 v179, v179 clamp\n"
    "v_mfma_f32_4x4x4_16b_bf16 v[224:227], v[228:229], v[204:205], v[224:227] cbsz:4 abid:2\n"
    "v_exp_f32 v180, v180 clamp\n"
    "v_exp_f32 v181, v181 clamp\n"
    "v_mfma_f32_4x4x4_16b_bf16 v[224:227], v[228:229], v[206:207], v[224:227] cbsz:4 abid:3\n"
    "s_waitcnt lgkmcnt(0)\n"
    "v_mfma_f32_16x16x32_bf16 v[160:163], v[0:3], v[192:195], v[144:147]\n"
    "s_setprio 1\n"
    "v_exp_f32 v182, v182 clamp\n"
    "v_mfma_f32_16x16x32_bf16 v[160:163], v[4:7], v[196:199], v[160:163]\n"
    "s_add_u32 m0, s8, 0\n"
    "v_exp_f32 v183, v183 clamp\n"
    "v_mfma_f32_16x16x32_bf16 v[164:167], v[8:11], v[192:195], v[148:151]\n"
    "s_add_u32 s66, s66, 16\n"
    "v_exp_f32 v184, v184 clamp\n"
    "v_mfma_f32_16x16x32_bf16 v[164:167], v[12:15], v[196:199], v[164:167]\n"
    "v_exp_f32 v185, v185 clamp\n"
    "v_mfma_f32_16x16x32_bf16 v[168:171], v[16:19], v[192:195], v[152:155]\n"
    "v_exp_f32 v186, v186 clamp\n"
    "v_mfma_f32_16x16x32_bf16 v[168:171], v[20:23], v[196:199], v[168:171]\n"
    "v_exp_f32 v187, v187 clamp\n"
    "v_mfma_f32_16x16x32_bf16 v[172:175], v[24:27], v[192:195], v[156:159]\n"
    "v_exp_f32 v188, v188 clamp\n"
    "v_mfma_f32_16x16x32_bf16 v[172:175], v[28:31], v[196:199], v[172:175]\n"
    "s_memrealtime s[34:35]\n"
    "v_exp_f32 v189, v189 clamp\n"
    "v_exp_f32 v190, v190 clamp\n"
    "v_exp_f32 v191, v191 clamp\n"
    "v_pk_fma_f32 v[176:177], v[176:177], s[70:71], v[128:129]\n"
    "v_pk_fma_f32 v[178:179], v[178:179], s[70:71], v[130:131]\n"
    "v_pk_add_f32 v[230:231], v[176:177], v[178:179]\n"
    "v_pk_mul_f32 v[232:233], v[176:177], v[178:179]\n"
    "v_pk_fma_f32 v[180:181], v[180:181], s[70:71], v[132:133]\n"
    "v_pk_fma_f32 v[230:231], v[230:231], v[180:181], v[232:233]\n"
    "v_pk_mul_f32 v[232:233], v[232:233], v[180:181]\n"
    "v_pk_fma_f32 v[182:183], v[182:183], s[70:71], v[134:135]\n"
    "v_pk_fma_f32 v[230:231], v[230:231], v[182:183], v[232:233]\n"
    "v_pk_mul_f32 v[232:233], v[232:233], v[182:183]\n"
    "v_pk_fma_f32 v[184:185], v[184:185], s[70:71], v[136:137]\n"
    "v_pk_fma_f32 v[230:231], v[230:231], v[184:185], v[232:233]\n"
    "v_pk_mul_f32 v[232:233], v[232:233], v[184:185]\n"
    "v_pk_fma_f32 v[186:187], v[186:187], s[70:71], v[138:139]\n"
    "v_pk_fma_f32 v[230:231], v[230:231], v[186:187], v[232:233]\n"
    "v_pk_mul_f32 v[232:233], v[232:233], v[186:187]\n"
    "v_pk_fma_f32 v[188:189], v[188:189], s[70:71], v[140:141]\n"
    "v_pk_fma_f32 v[230:231], v[230:231], v[188:189], v[232:233]\n"
    "v_pk_mul_f32 v[232:233], v[232:233], v[188:189]\n"
    "v_pk_fma_f32 v[190:191], v[190:191], s[70:71], v[142:143]\n"
    "v_pk_fma_f32 v[230:231], v[230:231], v[190:191], v[232:233]\n"
    "v_pk_mul_f32 v[232:233], v[232:233], v[190:191]\n"
    "v_rcp_f32 v232, v232\n"
    "v_rcp_f32 v233, v233\n"
    "v_fma_f32 v230, v230, v232, v247\n"
    "v_fmac_f32_e64 v230, v231, v233\n"
    "s_setprio 0\n"
    "s_waitcnt lgkmcnt(0)\n"
    "ds_read_b64_tr_b16 v[200:201], v240 offset:0\n"
    "ds_read_b64_tr_b16 v[202:203], v241 offset:0\n"
    "ds_read_b64_tr_b16 v[204:205], v242 offset:0\n"
    "ds_read_b64_tr_b16 v[206:207], v243 offset:0\n"
    "ds_read_u8 v236, v251 offset:0\n"
    "ds_read_u8 v238, v251 offset:1\n"
    "ds_write_b16_d16_hi v245, v230\n"
    "ds_read_b64 v[228:229], v246\n"
    "s_cmp_lt_u32 s34, s76\n"
    "s_cbranch_scc1 .Ldin_G3_wait\n"
    ".Ldin_G3_go:\n"
    "s_waitcnt vmcnt(2)\n"
    "ds_read_b128 v[192:195], v234 offset:2048\n"
    "ds_read_b128 v[196:199], v235 offset:2048\n"
    "s_waitcnt lgkmcnt(4)\n"
    "buffer_load_dwordx4 v[236:237], s[40:43], s67 idxen offen offset:0 nt lds\n"
    "buffer_load_dwordx4 v[238:239], s[40:43], s67 idxen offen offset:1024 nt lds\n"
    "s_add_u32 s76, s76, s72\n"
    "s_cmp_eq_u32 s66, s80\n"
    "s_cselect_b64 s[66:67], s[64:65], s[66:67]\n"
    "s_waitcnt lgkmcnt(2)\n"
    "s_cmp_eq_u32 s66, s82\n"
    "s_cbranch_scc1 .Ldin_L0\n"
    ".Ldin_G0:\n"
    "v_add_u32 v251, s66, v244\n"
    "v_mfma_f32_4x4x4_16b_bf16 v[224:227], v[228:229], v[208:209], v[224:227] cbsz:4 abid:0\n"
    "v_exp_f32 v160, v160 clamp\n"
    "v_exp_f32 v161, v161 clamp\n"
    "v_mfma_f32_4x4x4_16b_bf16 v[224:227], v[228:229], v[210:211], v[224:227] cbsz:4 abid:1\n"
    "v_exp_f32 v162, v162 clamp\n"
    "v_exp_f32 v163, v163 clamp\n"
    "v_mfma_f32_4x4x4_16b_bf16 v[224:227], v[228:229], v[212:213], v[224:227] cbsz:4 abid:2\n"
    "v_exp_f32 v164, v164 clamp\n"
    "v_exp_f32 v165, v165 clamp\n"
    "v_mfma_f32_4x4x4_16b_bf16 v[224:227], v[228:229], v[214:215], v[224:227] cbsz:4 abid:3\n"
    "s_waitcnt lgkmcnt(0)\n"
    "v_mfma_f32_16x16x32_bf16 v[176:179], v[0:3], v[192:195], v[144:147]\n"
    "s_setprio 1\n"
    "v_exp_f32 v166, v166 clamp\n"
    "v_mfma_f32_16x16x32_bf16 v[176:179], v[4:7], v[196:199], v[176:179]\n"
    "s_add_u32 m0, s68, 2048\n"
    "v_exp_f32 v167, v167 clamp\n"
    "v_mfma_f32_16x16x32_bf16 v[180:183], v[8:11], v[192:195], v[148:151]\n"
    "s_add_u32 s66, s66, 16\n"
    "v_exp_f32 v168, v168 clamp\n"
    "v_mfma_f32_16x16x32_bf16 v[180:183], v[12:15], v[196:199], v[180:183]\n"
    "v_exp_f32 v169, v169 clamp\n"
    "v_mfma_f32_16x16x32_bf16 v[184:187], v[16:19], v[192:195], v[152:155]\n"
    "v_exp_f32 v170, v170 clamp\n"
    "v_mfma_f32_16x16x32_bf16 v[184:187], v[20:23], v[196:199], v[184:187]\n"
    "v_exp_f32 v171, v171 clamp\n"
    "v_mfma_f32_16x16x32_bf16 v[188:191], v[24:27], v[192:195], v[156:159]\n"
    "v_exp_f32 v172, v172 clamp\n"
    "v_mfma_f32_16x16x32_bf16 v[188:191], v[28:31], v[196:199], v[188:191]\n"
    "s_memrealtime s[34:35]\n"
    "v_exp_f32 v173, v173 clamp\n"
    "v_exp_f32 v174, v174 clamp\n"
    "v_exp_f32 v175, v175 clamp\n"
    "v_pk_fma_f32 v[160:161], v[160:161], s[70:71], v[128:129]\n"
    "v_pk_fma_f32 v[162:163], v[162:163], s[70:71], v[130:131]\n"
    "v_pk_add_f32 v[230:231], v[160:161], v[162:163]\n"
    "v_pk_mul_f32 v[232:233], v[160:161], v[162:163]\n"
    "v_pk_fma_f32 v[164:165], v[164:165], s[70:71], v[132:133]\n"
    "v_pk_fma_f32 v[230:231], v[230:231], v[164:165], v[232:233]\n"
    "v_pk_mul_f32 v[232:233], v[232:233], v[164:165]\n"
    "v_pk_fma_f32 v[166:167], v[166:167], s[70:71], v[134:135]\n"
    "v_pk_fma_f32 v[230:231], v[230:231], v[166:167], v[232:233]\n"
    "v_pk_mul_f32 v[232:233], v[232:233], v[166:167]\n"
    "v_pk_fma_f32 v[168:169], v[168:169], s[70:71], v[136:137]\n"
    "v_pk_fma_f32 v[230:231], v[230:231], v[168:169], v[232:233]\n"
    "v_pk_mul_f32 v[232:233], v[232:233], v[168:169]\n"
    "v_pk_fma_f32 v[170:171], v[170:171], s[70:71], v[138:139]\n"
    "v_pk_fma_f32 v[230:231], v[230:231], v[170:171], v[232:233]\n"
    "v_pk_mul_f32 v[232:233], v[232:233], v[170:171]\n"
    "v_pk_fma_f32 v[172:173], v[172:173], s[70:71], v[140:141]\n"
    "v_pk_fma_f32 v[230:231], v[230:231], v[172:173], v[232:233]\n"
    "v_pk_mul_f32 v[232:233], v[232:233], v[172:173]\n"
    "v_pk_fma_f32 v[174:175], v[174:175], s[70:71], v[142:143]\n"
    "v_pk_fma_f32 v[230:231], v[230:231], v[174:175], v[232:233]\n"
    "v_pk_mul_f32 v[232:233], v[232:233], v[174:175]\n"
    "v_rcp_f32 v232, v232\n"
    "v_rcp_f32 v233, v233\n"
    "v_fma_f32 v230, v230, v232, v247\n"
    "v_fmac_f32_e64 v230, v231, v233\n"
    "s_setprio 0\n"
    "s_waitcnt lgkmcnt(0)\n"
    "ds_read_b64_tr_b16 v[208:209], v240 offset:2048\n"
    "ds_read_b64_tr_b16 v[210:211], v241 offset:2048\n"
    "ds_read_b64_tr_b16 v[212:213], v242 offset:2048\n"
    "ds_read_b64_tr_b16 v[214:215], v243 offset:2048\n"
    "ds_read_u8 v236, v251 offset:0\n"
    "ds_read_u8 v238, v251 offset:1\n"
    "ds_write_b16_d16_hi v245, v230\n"
    "ds_read_b64 v[228:229], v246\n"
    "s_cmp_lt_u32 s34, s76\n"
    "s_cbranch_scc1 .Ldin_G0_wait\n"
    ".Ldin_G0_go:\n"
    "s_waitcnt vmcnt(2)\n"
    "ds_read_b128 v[192:195], v234 offset:4096\n"
    "ds_read_b128 v[196:199], v235 offset:4096\n"
    "s_waitcnt lgkmcnt(4)\n"
    "buffer_load_dwordx4 v[236:237], s[40:43], s67 idxen offen offset:0 nt lds\n"
    "buffer_load_dwordx4 v[238:239], s[40:43], s67 idxen offen offset:1024 nt lds\n"
    "s_sub_u32 s94, s34, 30\n"
    "s_max_u32 s76, s76, s94\n"
    "s_add_u32 s76, s76, s72\n"
    "s_cmp_eq_u32 s66, s80\n"
    "s_cselect_b64 s[66:67], s[64:65], s[66:67]\n"
    "s_waitcnt lgkmcnt(2)\n"
    "s_cmp_eq_u32 s66, s82\n"
    "s_cbranch_scc1 .Ldin_L1\n"
    "s_branch .Ldin_G1\n"
    ".Ldin_stub_A0_poll:\n"
    "s_setpc_b64 s[34:35]\n"
    ".Ldin_A0_poll:\n"
    "s_sleep 1\n"
    "ds_read_b64 v[216:217], v246 offset:128\n"
    "s_waitcnt lgkmcnt(0)\n"
    "v_readlane_b32 s60, v216, s59\n"
    "v_readlane_b32 s61, v217, s59\n"
    "s_cmp_eq_u32 s60, -1\n"
    "s_cbranch_scc1 .Ldin_A0_poll\n"
    "s_branch .Ldin_A0_pr\n"
    ".Ldin_A0_skip:\n"
    "s_add_u32 s8, s8, 6144\n"
    "s_sub_u32 s68, s68, 2048\n"
    "s_mov_b32 s9, 48\n"
    "s_sub_u32 s82, s82, 16\n"
    "s_sub_u32 s66, s66, 16\n"
    "s_sub_u32 s76, s76, s72\n"
    "s_waitcnt lgkmcnt(0)\n"
    "s_branch .Ldin_A0_skr\n"
    ".Ldin_G1_slp:\n"
    "s_sleep 1\n"
    ".Ldin_G1_wait:\n"
    "s_memrealtime s[34:35]\n"
    "s_waitcnt lgkmcnt(0)\n"
    "s_sub_u32 s94, s34, s76\n"
    "s_cmp_lt_i32 s94, 0\n"
    "s_cbranch_scc1 .Ldin_G1_slp\n"
    "s_branch .Ldin_G1_go\n"
    ".Ldin_G2_slp:\n"
    "s_sleep 1\n"
    ".Ldin_G2_wait:\n"
    "s_memrealtime s[34:35]\n"
    "s_waitcnt lgkmcnt(0)\n"
    "s_sub_u32 s94, s34, s76\n"
    "s_cmp_lt_i32 s94, 0\n"
    "s_cbranch_scc1 .Ldin_G2_slp\n"
    "s_branch .Ldin_G2_go\n"
    ".Ldin_G3_slp:\n"
    "s_sleep 1\n"
    ".Ldin_G3_wait:\n"
    "s_memrealtime s[34:35]\n"
    "s_waitcnt lgkmcnt(0)\n"
    "s_sub_u32 s94, s34, s76\n"
    "s_cmp_lt_i32 s94, 0\n"
    "s_cbranch_scc1 .Ldin_G3_slp\n"
    "s_branch .Ldin_G3_go\n"
    ".Ldin_G0_slp:\n"
    "s_sleep 1\n"
    ".Ldin_G0_wait:\n"
    "s_memrealtime s[34:35]\n"
    "s_waitcnt lgkmcnt(0)\n"
    "s_sub_u32 s94, s34, s76\n"
    "s_cmp_lt_i32 s94, 0\n"
    "s_cbranch_scc1 .Ldin_G0_slp\n"
    "s_branch .Ldin_G0_go\n"
    ".Ldin_stub_E0:\n"
    "s_setpc_b64 s[34:35]\n"
    ".p2align 5\n"
    ".Ldin_E0:\n"
    "s_add_u32 m0, s62, 256\n"
    "v_exp_f32 v160, v160 clamp\n"
    "s_setprio 1\n"
    "v_exp_f32 v161, v161 clamp\n"
    "v_exp_f32 v162, v162 clamp\n"
    "v_exp_f32 v163, v163 clamp\n"
    "v_exp_f32 v164, v164 clamp\n"
    "v_exp_f32 v165, v165 clamp\n"
    "v_exp_f32 v166, v166 clamp\n"
    "v_exp_f32 v167, v167 clamp\n"
    "v_mfma_f32_4x4x4_16b_bf16 v[224:227], v[228:229], v[208:209], v[224:227] cbsz:4 abid:0\n"
    "v_exp_f32 v168, v168 clamp\n"
    "v_exp_f32 v169, v169 clamp\n"
    "v_mfma_f32_4x4x4_16b_bf16 v[224:227], v[228:229], v[210:211], v[224:227] cbsz:4 abid:1\n"
    "v_exp_f32 v170, v170 clamp\n"
    "v_exp_f32 v171, v171 clamp\n"
    "v_mfma_f32_4x4x4_16b_bf16 v[224:227], v[228:229], v[212:213], v[224:227] cbsz:4 abid:2\n"
    "v_exp_f32 v172, v172 clamp\n"
    "v_exp_f32 v173, v173 clamp\n"
    "v_mfma_f32_4x4x4_16b_bf16 v[224:227], v[228:229], v[214:215], v[224:227] cbsz:4 abid:3\n"
    "v_exp_f32 v174, v174 clamp\n"
    "v_exp_f32 v175, v175 clamp\n"
    "s_nop 0\n"
    "v_pk_fma_f32 v[160:161], v[160:161], s[70:71], v[128:129]\n"
    "v_pk_fma_f32 v[162:163], v[162:163], s[70:71], v[130:131]\n"
    "v_pk_add_f32 v[230:231], v[160:161], v[162:163]\n"
    "v_pk_mul_f32 v[232:233], v[160:161], v[162:163]\n"
    "v_pk_fma_f32 v[164:165], v[164:165], s[70:71], v[132:133]\n"
    "v_pk_fma_f32 v[230:231], v[230:231], v[164:165], v[232:233]\n"
    "v_pk_mul_f32 v[232:233], v[232:233], v[164:165]\n"
    "v_pk_fma_f32 v[166:167], v[166:167], s[70:71], v[134:135]\n"
    "v_pk_fma_f32 v[230:231], v[230:231], v[166:167], v[232:233]\n"
    "v_pk_mul_f32 v[232:233], v[232:233], v[166:167]\n"
    "v_pk_fma_f32 v[168:169], v[168:169], s[70:71], v[136:137]\n"
    "v_pk_fma_f32 v[230:231], v[230:231], v[168:169], v[232:233]\n"
    "v_pk_mul_f32 v[232:233], v[232:233], v[168:169]\n"
    "v_pk_fma_f32 v[170:171], v[170:171], s[70:71], v[138:139]\n"
    "v_pk_fma_f32 v[230:231], v[230:231], v[170:171], v[232:233]\n"
    "v_pk_mul_f32 v[232:233], v[232:233], v[170:171]\n"
    "v_pk_fma_f32 v[172:173], v[172:173], s[70:71], v[140:141]\n"
    "v_pk_fma_f32 v[230:231], v[230:231], v[172:173], v[232:233]\n"
    "v_pk_mul_f32 v[232:233], v[232:233], v[172:173]\n"
    "v_pk_fma_f32 v[174:175], v[174:175], s[70:71], v[142:143]\n"
    "v_pk_fma_f32 v[230:231], v[230:231], v[174:175], v[232:233]\n"
    "v_pk_mul_f32 v[232:233], v[232:233], v[174:175]\n"
    "v_rcp_f32 v232, v232\n"
    "v_rcp_f32 v233, v233\n"
    "v_fma_f32 v230, v230, v232, v247\n"
    "v_fmac_f32 v230, v231, v233\n"
    "ds_write_b16_d16_hi v245, v230\n"
    "ds_read_b64 v[228:229], v246\n"
    ".Ldin_drain0:\n"
    "v_mbcnt_lo_u32_b32 v0, -1, 0\n"
    "v_mbcnt_hi_u32_b32 v0, -1, v0\n"
    "v_lshrrev_b32 v1, 3, v0\n"
    "v_lshlrev_b32 v2, 2, v1\n"
    "ds_read_b32 v3, v246 offset:128\n"
    "s_lshl_b32 s94, s59, 8\n"
    "s_sub_u32 s94, s62, s94\n"
    "s_waitcnt lgkmcnt(0)\n"
    "ds_bpermute_b32 v3, v2, v3\n"
    "v_mfma_f32_4x4x4_16b_bf16 v[224:227], v[228:229], v[200:201], v[224:227] cbsz:4 abid:0\n"
    "v_lshlrev_b32 v2, 5, v0\n"
    "v_mul_lo_u32 v1, v1, s79\n"
    "v_mfma_f32_4x4x4_16b_bf16 v[224:227], v[228:229], v[202:203], v[224:227] cbsz:4 abid:1\n"
    "v_add_u32 v2, s94, v2\n"
    "v_and_b32 v0, 7, v0\n"
    "v_mfma_f32_4x4x4_16b_bf16 v[224:227], v[228:229], v[204:205], v[224:227] cbsz:4 abid:2\n"
    "v_add_u32 v1, s78, v1\n"
    "v_lshlrev_b32 v0, 4, v0\n"
    "v_mfma_f32_4x4x4_16b_bf16 v[224:227], v[228:229], v[206:207], v[224:227] cbsz:4 abid:3\n"
    "s_waitcnt lgkmcnt(0)\n"
    "v_bfe_u32 v4, v3, 19, 3\n"
    "v_cmp_le_i32 vcc, 0, v3\n"
    "v_add_u32 v1, v1, v4\n"
    "s_mov_b32 s70, s69\n"
    "s_mov_b32 s71, s69\n"
    "v_pk_add_f32 v[232:233], v[224:225], v[226:227]\n"
    "v_add_f32 v232, v232, v233\n"
    "ds_write_addtid_b32 v232 offset:49152\n"
    "ds_read_b128 v[8:11], v2 offset:49920\n"
    "ds_read_b128 v[12:15], v2 offset:49936\n"
    "v_lshl_add_u32 v1, v1, 7, v0\n"
    "v_bfrev_b32 v4, 1\n"
    "v_cndmask_b32 v1, v4, v1, vcc\n"
    "s_waitcnt vmcnt(0) lgkmcnt(0)\n"
    "v_pk_mul_f32 v[8:9], v[8:9], s[70:71]\n"
    "v_pk_mul_f32 v[10:11], v[10:11], s[70:71]\n"
    "v_pk_mul_f32 v[12:13], v[12:13], s[70:71]\n"
    "v_pk_mul_f32 v[14:15], v[14:15], s[70:71]\n"
    "v_cvt_pk_bf16_f32 v16, v8, v9\n"
    "v_cvt_pk_bf16_f32 v17, v10, v11\n"
    "v_cvt_pk_bf16_f32 v18, v12, v13\n"
    "v_cvt_pk_bf16_f32 v19, v14, v15\n"
    "buffer_store_dwordx4 v[16:19], v1, s[52:55], 0 offen sc1\n"
    "s_endpgm\n"
    ".Ldin_stub_E1:\n"
    "s_setpc_b64 s[34:35]\n"
    ".p2align 5\n"
    ".Ldin_E1:\n"
    "s_add_u32 m0, s62, 256\n"
    "v_exp_f32 v176, v176 clamp\n"
    "s_setprio 1\n"
    "v_exp_f32 v177, v177 clamp\n"
    "v_exp_f32 v178, v178 clamp\n"
    "v_exp_f32 v179, v179 clamp\n"
    "v_exp_f32 v180, v180 clamp\n"
    "v_exp_f32 v181, v181 clamp\n"
    "v_exp_f32 v182, v182 clamp\n"
    "v_exp_f32 v183, v183 clamp\n"
    "v_mfma_f32_4x4x4_16b_bf16 v[224:227], v[228:229], v[200:201], v[224:227] cbsz:4 abid:0\n"
    "v_exp_f32 v184, v184 clamp\n"
    "v_exp_f32 v185, v185 clamp\n"
    "v_mfma_f32_4x4x4_16b_bf16 v[224:227], v[228:229], v[202:203], v[224:227] cbsz:4 abid:1\n"
    "v_exp_f32 v186, v186 clamp\n"
    "v_exp_f32 v187, v187 clamp\n"
    "v_mfma_f32_4x4x4_16b_bf16 v[224:227], v[228:229], v[204:205], v[224:227] cbsz:4 abid:2\n"
    "v_exp_f32 v188, v188 clamp\n"
    "v_exp_f32 v189, v189 clamp\n"
    "v_mfma_f32_4x4x4_16b_bf16 v[224:227], v[228:229], v[206:207], v[224:227] cbsz:4 abid:3\n"
    "v_exp_f32 v190, v190 clamp\n"
    "v_exp_f32 v191, v191 clamp\n"
    "s_nop 0\n"
    "v_pk_fma_f32 v[176:177], v[176:177], s[70:71], v[128:129]\n"
    "v_pk_fma_f32 v[178:179], v[178:179], s[70:71], v[130:131]\n"
    "v_pk_add_f32 v[230:231], v[176:177], v[178:179]\n"
    "v_pk_mul_f32 v[232:233], v[176:177], v[178:179]\n"
    "v_pk_fma_f32 v[180:181], v[180:181], s[70:71], v[132:133]\n"
    "v_pk_fma_f32 v[230:231], v[230:231], v[180:181], v[232:233]\n"
    "v_pk_mul_f32 v[232:233], v[232:233], v[180:181]\n"
    "v_pk_fma_f32 v[182:183], v[182:183], s[70:71], v[134:135]\n"
    "v_pk_fma_f32 v[230:231], v[230:231], v[182:183], v[232:233]\n"
    "v_pk_mul_f32 v[232:233], v[232:233], v[182:183]\n"
    "v_pk_fma_f32 v[184:185], v[184:185], s[70:71], v[136:137]\n"
    "v_pk_fma_f32 v[230:231], v[230:231], v[184:185], v[232:233]\n"
    "v_pk_mul_f32 v[232:233], v[232:233], v[184:185]\n"
    "v_pk_fma_f32 v[186:187], v[186:187], s[70:71], v[138:139]\n"
    "v_pk_fma_f32 v[230:231], v[230:231], v[186:187], v[232:233]\n"
    "v_pk_mul_f32 v[232:233], v[232:233], v[186:187]\n"
    "v_pk_fma_f32 v[188:189], v[188:189], s[70:71], v[140:141]\n"
    "v_pk_fma_f32 v[230:231], v[230:231], v[188:189], v[232:233]\n"
    "v_pk_mul_f32 v[232:233], v[232:233], v[188:189]\n"
    "v_pk_fma_f32 v[190:191], v[190:191], s[70:71], v[142:143]\n"
    "v_pk_fma_f32 v[230:231], v[230:231], v[190:191], v[232:233]\n"
    "v_pk_mul_f32 v[232:233], v[232:233], v[190:191]\n"
    "v_rcp_f32 v232, v232\n"
    "v_rcp_f32 v233, v233\n"
    "v_fma_f32 v230, v230, v232, v247\n"
    "v_fmac_f32 v230, v231, v233\n"
    "ds_write_b16_d16_hi v245, v230\n"
    "ds_read_b64 v[228:229], v246\n"
    ".Ldin_drain1:\n"
    "v_mbcnt_lo_u32_b32 v0, -1, 0\n"
    "v_mbcnt_hi_u32_b32 v0, -1, v0\n"
    "v_lshrrev_b32 v1, 3, v0\n"
    "v_lshlrev_b32 v2, 2, v1\n"
    "ds_read_b32 v3, v246 offset:128\n"
    "s_lshl_b32 s94, s59, 8\n"
    "s_sub_u32 s94, s62, s94\n"
    "s_waitcnt lgkmcnt(0)\n"
    "ds_bpermute_b32 v3, v2, v3\n"
    "v_mfma_f32_4x4x4_16b_bf16 v[224:227], v[228:229], v[208:209], v[224:227] cbsz:4 abid:0\n"
    "v_lshlrev_b32 v2, 5, v0\n"
    "v_mul_lo_u32 v1, v1, s79\n"
    "v_mfma_f32_4x4x4_16b_bf16 v[224:227], v[228:229], v[210:211], v[224:227] cbsz:4 abid:1\n"
    "v_add_u32 v2, s94, v2\n"
    "v_and_b32 v0, 7, v0\n"
    "v_mfma_f32_4x4x4_16b_bf16 v[224:227], v[228:229], v[212:213], v[224:227] cbsz:4 abid:2\n"
    "v_add_u32 v1, s78, v1\n"
    "v_lshlrev_b32 v0, 4, v0\n"
    "v_mfma_f32_4x4x4_16b_bf16 v[224:227], v[228:229], v[214:215], v[224:227] cbsz:4 abid:3\n"
    "s_waitcnt lgkmcnt(0)\n"
    "v_bfe_u32 v4, v3, 19, 3\n"
    "v_cmp_le_i32 vcc, 0, v3\n"
    "v_add_u32 v1, v1, v4\n"
    "s_mov_b32 s70, s69\n"
    "s_mov_b32 s71, s69\n"
    "v_pk_add_f32 v[232:233], v[224:225], v[226:227]\n"
    "v_add_f32 v232, v232, v233\n"
    "ds_write_addtid_b32 v232 offset:49152\n"
    "ds_read_b128 v[8:11], v2 offset:49920\n"
    "ds_read_b128 v[12:15], v2 offset:49936\n"
    "v_lshl_add_u32 v1, v1, 7, v0\n"
    "v_bfrev_b32 v4, 1\n"
    "v_cndmask_b32 v1, v4, v1, vcc\n"
    "s_waitcnt vmcnt(0) lgkmcnt(0)\n"
    "v_pk_mul_f32 v[8:9], v[8:9], s[70:71]\n"
    "v_pk_mul_f32 v[10:11], v[10:11], s[70:71]\n"
    "v_pk_mul_f32 v[12:13], v[12:13], s[70:71]\n"
    "v_pk_mul_f32 v[14:15], v[14:15], s[70:71]\n"
    "v_cvt_pk_bf16_f32 v16, v8, v9\n"
    "v_cvt_pk_bf16_f32 v17, v10, v11\n"
    "v_cvt_pk_bf16_f32 v18, v12, v13\n"
    "v_cvt_pk_bf16_f32 v19, v14, v15\n"
    "buffer_store_dwordx4 v[16:19], v1, s[52:55], 0 offen sc1\n"
    "s_endpgm\n"
    ".p2align 12\n"
    ".Ldin_stub_L2:\n"
    "s_setpc_b64 s[34:35]\n"
    ".p2align 5\n"
    ".Ldin_L2:\n"
    "s_cmp_lt_i32 s60, 0\n"
    "s_cbranch_scc1 .Ldin_E0\n"
    "ds_read_b128 v[144:147], v252 offset:8192\n"
    "ds_read_b128 v[148:151], v252 offset:8256\n"
    "ds_read_b128 v[152:155], v252 offset:16384\n"
    "ds_read_b128 v[156:159], v252 offset:16448\n"
    "v_add_u32 v251, s66, v244\n"
    "v_and_b32 v216, v248, v216\n"
    "v_and_b32 v217, v249, v217\n"
    "v_and_b32 v218, v248, v218\n"
    "v_and_b32 v219, v249, v219\n"
    "v_mfma_f32_4x4x4_16b_bf16 v[176:179], v[216:217], v[96:97], v[32:35]\n"
    "v_and_b32 v220, v248, v220\n"
    "v_and_b32 v221, v249, v221\n"
    "v_mfma_f32_4x4x4_16b_bf16 v[180:183], v[218:219], v[98:99], v[36:39]\n"
    "v_and_b32 v222, v248, v222\n"
    "v_and_b32_e64 v223, v249, v223\n"
    "v_mfma_f32_4x4x4_16b_bf16 v[184:187], v[220:221], v[100:101], v[40:43]\n"
    "v_cvt_pk_bf16_f32 v0, v176, v177\n"
    "v_mfma_f32_4x4x4_16b_bf16 v[224:227], v[228:229], v[208:209], v[224:227] cbsz:4 abid:0\n"
    "v_cvt_pk_bf16_f32 v1, v178, v179\n"
    "v_mfma_f32_4x4x4_16b_bf16 v[188:191], v[222:223], v[102:103], v[44:47]\n"
    "v_cvt_pk_bf16_f32 v2, v180, v181\n"
    "v_cvt_pk_bf16_f32 v3, v182, v183\n"
    "v_mfma_f32_4x4x4_16b_bf16 v[176:179], v[216:217], v[104:105], v[48:51]\n"
    "v_cvt_pk_bf16_f32 v4, v184, v185\n"
    "v_cvt_pk_bf16_f32 v5, v186, v187\n"
    "v_mfma_f32_4x4x4_16b_bf16 v[180:183], v[218:219], v[106:107], v[52:55]\n"
    "v_cvt_pk_bf16_f32 v6, v188, v189\n"
    "v_mfma_f32_4x4x4_16b_bf16 v[224:227], v[228:229], v[210:211], v[224:227] cbsz:4 abid:1\n"
    "v_cvt_pk_bf16_f32 v7, v190, v191\n"
    "v_mfma_f32_4x4x4_16b_bf16 v[184:187], v[220:221], v[108:109], v[56:59]\n"
    "v_cvt_pk_bf16_f32 v8, v176, v177\n"
    "v_cvt_pk_bf16_f32 v9, v178, v179\n"
    "v_mfma_f32_4x4x4_16b_bf16 v[188:191], v[222:223], v[110:111], v[60:63]\n"
    "v_cvt_pk_bf16_f32 v10, v180, v181\n"
    "v_cvt_pk_bf16_f32 v11, v182, v183\n"
    "v_mfma_f32_4x4x4_16b_bf16 v[176:179], v[216:217], v[112:113], v[64:67]\n"
    "v_cvt_pk_bf16_f32 v12, v184, v185\n"
    "v_mfma_f32_4x4x4_16b_bf16 v[224:227], v[228:229], v[212:213], v[224:227] cbsz:4 abid:2\n"
    "v_cvt_pk_bf16_f32 v13, v186, v187\n"
    "s_setprio 1\n"
    "s_waitcnt lgkmcnt(0)\n"
    "v_mfma_f32_4x4x4_16b_bf16 v[180:183], v[218:219], v[114:115], v[68:71]\n"
    "v_cvt_pk_bf16_f32 v14, v188, v189\n"
    "v_cvt_pk_bf16_f32 v15, v190, v191\n"
    "v_mfma_f32_4x4x4_16b_bf16 v[184:187], v[220:221], v[116:117], v[72:75]\n"
    "v_cvt_pk_bf16_f32 v16, v176, v177\n"
    "v_cvt_pk_bf16_f32 v17, v178, v179\n"
    "v_mfma_f32_4x4x4_16b_bf16 v[188:191], v[222:223], v[118:119], v[76:79]\n"
    "v_cvt_pk_bf16_f32 v18, v180, v181\n"
    "v_mfma_f32_4x4x4_16b_bf16 v[224:227], v[228:229], v[214:215], v[224:227] cbsz:4 abid:3\n"
    "v_cvt_pk_bf16_f32 v19, v182, v183\n"
    "v_mfma_f32_4x4x4_16b_bf16 v[176:179], v[216:217], v[120:121], v[80:83]\n"
    "v_cvt_pk_bf16_f32 v20, v184, v185\n"
    "v_cvt_pk_bf16_f32 v21, v186, v187\n"
    "v_mfma_f32_4x4x4_16b_bf16 v[180:183], v[218:219], v[122:123], v[84:87]\n"
    "v_cvt_pk_bf16_f32 v22, v188, v189\n"
    "v_cvt_pk_bf16_f32 v23, v190, v191\n"
    "v_mfma_f32_4x4x4_16b_bf16 v[184:187], v[220:221], v[124:125], v[88:91]\n"
    "v_cvt_pk_bf16_f32 v24, v176, v177\n"
    "v_cvt_pk_bf16_f32 v25, v178, v179\n"
    "v_mfma_f32_4x4x4_16b_bf16 v[188:191], v[222:223], v[126:127], v[92:95]\n"
    "v_cvt_pk_bf16_f32 v26, v180, v181\n"
    "v_cvt_pk_bf16_f32 v27, v182, v183\n"
    "v_cvt_pk_bf16_f32 v28, v184, v185\n"
    "v_cvt_pk_bf16_f32 v29, v186, v187\n"
    "v_mfma_f32_16x16x32_bf16 v[176:179], v[0:3], v[192:195], v[144:147]\n"
    "s_add_u32 m0, s68, 6144\n"
    "v_cvt_pk_bf16_f32 v30, v188, v189\n"
    "v_cvt_pk_bf16_f32 v31, v190, v191\n"
    "v_exp_f32 v160, v160 clamp\n"
    "v_mfma_f32_16x16x32_bf16 v[176:179], v[4:7], v[196:199], v[176:179]\n"
    "s_add_u32 s66, s66, 16\n"
    "v_exp_f32 v161, v161 clamp\n"
    "v_exp_f32 v162, v162 clamp\n"
    "v_mfma_f32_16x16x32_bf16 v[180:183], v[8:11], v[192:195], v[148:151]\n"
    "s_add_u32 s76, s76, s73\n"
    "v_exp_f32 v163, v163 clamp\n"
    "v_exp_f32 v164, v164 clamp\n"
    "v_mfma_f32_16x16x32_bf16 v[180:183], v[12:15], v[196:199], v[180:183]\n"
    "s_lshl1_add_u32 s76, s72, s76\n"
    "v_exp_f32 v165, v165 clamp\n"
    "v_exp_f32 v166, v166 clamp\n"
    "v_mfma_f32_16x16x32_bf16 v[184:187], v[16:19], v[192:195], v[152:155]\n"
    "s_mov_b32 s80, s83\n"
    "v_exp_f32 v167, v167 clamp\n"
    "v_exp_f32 v168, v168 clamp\n"
    "v_mfma_f32_16x16x32_bf16 v[184:187], v[20:23], v[196:199], v[184:187]\n"
    "s_add_u32 s62, s62, 256\n"
    "v_exp_f32 v169, v169 clamp\n"
    "v_exp_f32 v170, v170 clamp\n"
    "v_mfma_f32_16x16x32_bf16 v[188:191], v[24:27], v[192:195], v[156:159]\n"
    "s_mov_b32 s81, s64\n"
    "v_exp_f32 v171, v171 clamp\n"
    "v_exp_f32 v172, v172 clamp\n"
    "v_mfma_f32_16x16x32_bf16 v[188:191], v[28:31], v[196:199], v[188:191]\n"
    "s_lshr_b32 s63, s65, 6\n"
    "v_exp_f32 v173, v173 clamp\n"
    "v_exp_f32 v174, v174 clamp\n"
    "v_exp_f32 v175, v175 clamp\n"
    "v_pk_fma_f32 v[160:161], v[160:161], s[70:71], v[128:129]\n"
    "v_pk_fma_f32 v[162:163], v[162:163], s[70:71], v[130:131]\n"
    "v_pk_add_f32 v[230:231], v[160:161], v[162:163]\n"
    "v_pk_mul_f32 v[232:233], v[160:161], v[162:163]\n"
    "v_pk_fma_f32 v[164:165], v[164:165], s[70:71], v[132:133]\n"
    "v_pk_fma_f32 v[230:231], v[230:231], v[164:165], v[232:233]\n"
    "v_pk_mul_f32 v[232:233], v[232:233], v[164:165]\n"
    "v_pk_fma_f32 v[166:167], v[166:167], s[70:71], v[134:135]\n"
    "v_pk_fma_f32 v[230:231], v[230:231], v[166:167], v[232:233]\n"
    "v_pk_mul_f32 v[232:233], v[232:233], v[166:167]\n"
    "v_pk_fma_f32 v[168:169], v[168:169], s[70:71], v[136:137]\n"
    "v_pk_fma_f32 v[230:231], v[230:231], v[168:169], v[232:233]\n"
    "v_pk_mul_f32 v[232:233], v[232:233], v[168:169]\n"
    "v_pk_fma_f32 v[170:171], v[170:171], s[70:71], v[138:139]\n"
    "v_pk_fma_f32 v[230:231], v[230:231], v[170:171], v[232:233]\n"
    "v_pk_mul_f32 v[232:233], v[232:233], v[170:171]\n"
    "v_pk_fma_f32 v[172:173], v[172:173], s[70:71], v[140:141]\n"
    "v_pk_fma_f32 v[230:231], v[230:231], v[172:173], v[232:233]\n"
    "v_pk_mul_f32 v[232:233], v[232:233], v[172:173]\n"
    "v_pk_fma_f32 v[174:175], v[174:175], s[70:71], v[142:143]\n"
    "v_pk_fma_f32 v[230:231], v[230:231], v[174:175], v[232:233]\n"
    "v_pk_mul_f32 v[232:233], v[232:233], v[174:175]\n"
    "v_rcp_f32 v232, v232\n"
    "v_rcp_f32 v233, v233\n"
    "v_fma_f32 v230, v230, v232, v247\n"
    "v_fmac_f32 v230, v231, v233\n"
    "s_setprio 0\n"
    "ds_read_b64_tr_b16 v[208:209], v240 offset:6144\n"
    "ds_read_b64_tr_b16 v[210:211], v241 offset:6144\n"
    "ds_read_b64_tr_b16 v[212:213], v242 offset:6144\n"
    "ds_read_b64_tr_b16 v[214:215], v243 offset:6144\n"
    "ds_read_u8 v236, v251 offset:0\n"
    "ds_read_u8 v238, v251 offset:1\n"
    "ds_write_b16_d16_hi v245, v230\n"
    "ds_read_b64 v[228:229], v246\n"
    "ds_read_b64 v[216:217], v246 offset:128\n"
    "s_waitcnt vmcnt(2)\n"
    "ds_read_b128 v[192:195], v234 offset:0\n"
    "ds_read_b128 v[196:199], v235 offset:0\n"
    "s_waitcnt lgkmcnt(5)\n"
    "buffer_load_dwordx4 v[236:237], s[40:43], s67 idxen offen offset:0 nt lds\n"
    "buffer_load_dwordx4 v[238:239], s[40:43], s67 idxen offen offset:1024 nt lds\n"
    "s_waitcnt lgkmcnt(2)\n"
    "s_bitcmp1_b32 s60, 30\n"
    "s_cbranch_scc1 .Ldin_A3s\n"
    ".Ldin_A3:\n"
    "v_add_u32 v251, s66, v244\n"
    "v_readlane_b32 s60, v216, s59\n"
    "v_readlane_b32 s61, v217, s59\n"
    "v_mfma_f32_4x4x4_16b_bf16 v[224:227], v[228:229], v[200:201], v[224:227] cbsz:4 abid:0\n"
    "v_exp_f32 v176, v176 clamp\n"
    "v_exp_f32 v177, v177 clamp\n"
    "v_mfma_f32_4x4x4_16b_bf16 v[224:227], v[228:229], v[202:203], v[224:227] cbsz:4 abid:1\n"
    "v_exp_f32 v178, v178 clamp\n"
    "v_exp_f32 v179, v179 clamp\n"
    "v_mfma_f32_4x4x4_16b_bf16 v[224:227], v[228:229], v[204:205], v[224:227] cbsz:4 abid:2\n"
    "v_exp_f32 v180, v180 clamp\n"
    "v_exp_f32 v181, v181 clamp\n"
    "v_mfma_f32_4x4x4_16b_bf16 v[224:227], v[228:229], v[206:207], v[224:227] cbsz:4 abid:3\n"
    "s_waitcnt lgkmcnt(0)\n"
    "v_mfma_f32_16x16x32_bf16 v[160:163], v[0:3], v[192:195], v[144:147]\n"
    "s_setprio 1\n"
    "v_exp_f32 v182, v182 clamp\n"
    "v_mfma_f32_16x16x32_bf16 v[160:163], v[4:7], v[196:199], v[160:163]\n"
    "s_cmp_eq_u32 s60, -1\n"
    "v_exp_f32 v183, v183 clamp\n"
    "v_mfma_f32_16x16x32_bf16 v[164:167], v[8:11], v[192:195], v[148:151]\n"
    "s_cbranch_scc1 .Ldin_A3_poll\n"
    ".Ldin_A3_pr:\n"
    "v_exp_f32 v184, v184 clamp\n"
    "v_mfma_f32_16x16x32_bf16 v[164:167], v[12:15], v[196:199], v[164:167]\n"
    "s_lshr_b32 s94, s60, 12\n"
    "v_exp_f32 v185, v185 clamp\n"
    "v_mfma_f32_16x16x32_bf16 v[168:171], v[16:19], v[192:195], v[152:155]\n"
    "s_and_b32 s94, s94, 0x1ff0\n"
    "v_exp_f32 v186, v186 clamp\n"
    "v_mfma_f32_16x16x32_bf16 v[168:171], v[20:23], v[196:199], v[168:171]\n"
    "s_mov_b32 m0, s62\n"
    "v_exp_f32 v187, v187 clamp\n"
    "v_mfma_f32_16x16x32_bf16 v[172:175], v[24:27], v[192:195], v[156:159]\n"
    "s_add_u32 s66, s66, 16\n"
    "v_exp_f32 v188, v188 clamp\n"
    "v_mfma_f32_16x16x32_bf16 v[172:175], v[28:31], v[196:199], v[172:175]\n"
    "s_and_b32 s64, s60, 0xffff\n"
    "v_xor_b32 v252, s94, v250\n"
    "v_exp_f32 v189, v189 clamp\n"
    "v_exp_f32 v190, v190 clamp\n"
    "v_exp_f32 v191, v191 clamp\n"
    "v_pk_fma_f32 v[176:177], v[176:177], s[70:71], v[128:129]\n"
    "v_pk_fma_f32 v[178:179], v[178:179], s[70:71], v[130:131]\n"
    "v_pk_add_f32 v[230:231], v[176:177], v[178:179]\n"
    "v_pk_mul_f32 v[232:233], v[176:177], v[178:179]\n"
    "v_pk_add_f32 v[218:219], v[224:225], v[226:227]\n"
    "v_mov_b64 v[224:225], 0\n"
    "v_mov_b64 v[226:227], 0\n"
    "v_pk_fma_f32 v[180:181], v[180:181], s[70:71], v[132:133]\n"
    "v_pk_fma_f32 v[230:231], v[230:231], v[180:181], v[232:233]\n"
    "v_pk_mul_f32 v[232:233], v[232:233], v[180:181]\n"
    "v_pk_fma_f32 v[182:183], v[182:183], s[70:71], v[134:135]\n"
    "v_pk_fma_f32 v[230:231], v[230:231], v[182:183], v[232:233]\n"
    "v_pk_mul_f32 v[232:233], v[232:233], v[182:183]\n"
    "v_pk_fma_f32 v[184:185], v[184:185], s[70:71], v[136:137]\n"
    "v_pk_fma_f32 v[230:231], v[230:231], v[184:185], v[232:233]\n"
    "v_pk_mul_f32 v[232:233], v[232:233], v[184:185]\n"
    "v_pk_fma_f32 v[186:187], v[186:187], s[70:71], v[138:139]\n"
    "v_pk_fma_f32 v[230:231], v[230:231], v[186:187], v[232:233]\n"
    "v_pk_mul_f32 v[232:233], v[232:233], v[186:187]\n"
    "v_pk_fma_f32 v[188:189], v[188:189], s[70:71], v[140:141]\n"
    "v_pk_fma_f32 v[230:231], v[230:231], v[188:189], v[232:233]\n"
    "v_pk_mul_f32 v[232:233], v[232:233], v[188:189]\n"
    "v_pk_fma_f32 v[190:191], v[190:191], s[70:71], v[142:143]\n"
    "v_pk_fma_f32 v[230:231], v[230:231], v[190:191], v[232:233]\n"
    "v_pk_mul_f32 v[232:233], v[232:233], v[190:191]\n"
    "v_rcp_f32 v232, v232\n"
    "v_rcp_f32 v233, v233\n"
    "v_fma_f32 v230, v230, v232, v247\n"
    "v_fmac_f32 v230, v231, v233\n"
    "v_add_f32_e64 v218, v218, v219\n"
    "s_setprio 0\n"
    "ds_read_b64_tr_b16 v[200:201], v240 offset:0\n"
    "ds_read_b64_tr_b16 v[202:203], v241 offset:0\n"
    "ds_read_b64_tr_b16 v[204:205], v242 offset:0\n"
    "ds_read_b64_tr_b16 v[206:207], v243 offset:0\n"
    "ds_read_u8 v236, v251 offset:0\n"
    "ds_read_u8 v238, v251 offset:1\n"
    "ds_write_b16_d16_hi v245, v230\n"
    "ds_read_b64 v[228:229], v246\n"
    "ds_write_addtid_b32 v218 offset:49152\n"
    "s_add_u32 m0, s8, 0\n"
    "s_bfe_u32 s94, s60, 0x30013\n"
    "s_add_u32 s58, s58, s57\n"
    "s_mul_i32 s94, s94, s56\n"
    "s_add_u32 s82, s64, s9\n"
    "s_add_u32 s65, s58, s94\n"
    "s_waitcnt vmcnt(2)\n"
    "ds_read_b128 v[192:195], v234 offset:2048\n"
    "ds_read_b128 v[196:199], v235 offset:2048\n"
    "ds_read_b128 v[216:219], v252 offset:0\n"
    "ds_read_b128 v[220:223], v252 offset:64\n"
    "s_sub_u32 s95, s61, s76\n"
    "s_lshr_b32 s94, s61, 16\n"
    "s_sext_i32_i16 s95, s95\n"
    "s_cmp_eq_u32 s59, s10\n"
    "s_cbranch_scc1 .Ldin_A3_skip\n"
    "s_waitcnt lgkmcnt(7)\n"
    "buffer_load_dwordx4 v[236:237], s[40:43], s67 idxen offen offset:0 nt lds\n"
    "buffer_load_dwordx4 v[238:239], s[40:43], s67 idxen offen offset:1024 nt lds\n"
    ".Ldin_A3_skr:\n"
    "s_cmp_lt_i32 s95, -550\n"
    "s_cselect_b32 s94, 0, s94\n"
    "s_mul_i32 s95, s95, s94\n"
    "s_bfe_u32 s94, s60, 0x50019\n"
    "s_ashr_i32 s95, s95, 16\n"
    "s_lshl4_add_u32 s83, s94, s64\n"
    "s_max_i32 s95, s95, -14\n"
    "s_add_u32 s59, s59, 1\n"
    "s_min_i32 s95, s95, 10\n"
    "s_add_u32 s72, s77, s95\n"
    "s_cmp_lt_i32 s60, 0\n"
    "s_cselect_b32 s72, 0, s72\n"
    "s_cmp_eq_u32 s66, s80\n"
    "s_cselect_b64 s[66:67], s[64:65], s[66:67]\n"
    "s_waitcnt lgkmcnt(5)\n"
    "s_branch .Ldin_G0\n"
    ".Ldin_A3_poll:\n"
    "s_sleep 1\n"
    "ds_read_b64 v[216:217], v246 offset:128\n"
    "s_waitcnt lgkmcnt(0)\n"
    "v_readlane_b32 s60, v216, s59\n"
    "v_readlane_b32 s61, v217, s59\n"
    "s_cmp_eq_u32 s60, -1\n"
    "s_cbranch_scc1 .Ldin_A3_poll\n"
    "s_branch .Ldin_A3_pr\n"
    ".Ldin_A3_skip:\n"
    "s_add_u32 s8, s8, 6144\n"
    "s_sub_u32 s68, s68, 2048\n"
    "s_mov_b32 s9, 48\n"
    "s_sub_u32 s82, s82, 16\n"
    "s_sub_u32 s66, s66, 16\n"
    "s_sub_u32 s76, s76, s72\n"
    "s_waitcnt lgkmcnt(0)\n"
    "s_branch .Ldin_A3_skr\n"
    ".Ldin_stub_L1:\n"
    "s_setpc_b64 s[34:35]\n"
    ".p2align 5\n"
    ".Ldin_L1:\n"
    "s_cmp_lt_i32 s60, 0\n"
    "s_cbranch_scc1 .Ldin_E1\n"
    "ds_read_b128 v[144:147], v252 offset:8192\n"
    "ds_read_b128 v[148:151], v252 offset:8256\n"
    "ds_read_b128 v[152:155], v252 offset:16384\n"
    "ds_read_b128 v[156:159], v252 offset:16448\n"
    "v_add_u32 v251, s66, v244\n"
    "v_and_b32 v216, v248, v216\n"
    "v_and_b32 v217, v249, v217\n"
    "v_and_b32 v218, v248, v218\n"
    "v_and_b32 v219, v249, v219\n"
    "v_mfma_f32_4x4x4_16b_bf16 v[160:163], v[216:217], v[96:97], v[32:35]\n"
    "v_and_b32 v220, v248, v220\n"
    "v_and_b32 v221, v249, v221\n"
    "v_mfma_f32_4x4x4_16b_bf16 v[164:167], v[218:219], v[98:99], v[36:39]\n"
    "v_and_b32 v222, v248, v222\n"
    "v_and_b32_e64 v223, v249, v223\n"
    "v_mfma_f32_4x4x4_16b_bf16 v[168:171], v[220:221], v[100:101], v[40:43]\n"
    "v_cvt_pk_bf16_f32 v0, v160, v161\n"
    "v_mfma_f32_4x4x4_16b_bf16 v[224:227], v[228:229], v[200:201], v[224:227] cbsz:4 abid:0\n"
    "v_cvt_pk_bf16_f32 v1, v162, v163\n"
    "v_mfma_f32_4x4x4_16b_bf16 v[172:175], v[222:223], v[102:103], v[44:47]\n"
    "v_cvt_pk_bf16_f32 v2, v164, v165\n"
    "v_cvt_pk_bf16_f32 v3, v166, v167\n"
    "v_mfma_f32_4x4x4_16b_bf16 v[160:163], v[216:217], v[104:105], v[48:51]\n"
    "v_cvt_pk_bf16_f32 v4, v168, v169\n"
    "v_cvt_pk_bf16_f32 v5, v170, v171\n"
    "v_mfma_f32_4x4x4_16b_bf16 v[164:167], v[218:219], v[106:107], v[52:55]\n"
    "v_cvt_pk_bf16_f32 v6, v172, v173\n"
    "v_mfma_f32_4x4x4_16b_bf16 v[224:227], v[228:229], v[202:203], v[224:227] cbsz:4 abid:1\n"
    "v_cvt_pk_bf16_f32 v7, v174, v175\n"
    "v_mfma_f32_4x4x4_16b_bf16 v[168:171], v[220:221], v[108:109], v[56:59]\n"
    "v_cvt_pk_bf16_f32 v8, v160, v161\n"
    "v_cvt_pk_bf16_f32 v9, v162, v163\n"
    "v_mfma_f32_4x4x4_16b_bf16 v[172:175], v[222:223], v[110:111], v[60:63]\n"
    "v_cvt_pk_bf16_f32 v10, v164, v165\n"
    "v_cvt_pk_bf16_f32 v11, v166, v167\n"
    "v_mfma_f32_4x4x4_16b_bf16 v[160:163], v[216:217], v[112:113], v[64:67]\n"
    "v_cvt_pk_bf16_f32 v12, v168, v169\n"
    "v_mfma_f32_4x4x4_16b_bf16 v[224:227], v[228:229], v[204:205], v[224:227] cbsz:4 abid:2\n"
    "v_cvt_pk_bf16_f32 v13, v170, v171\n"
    "s_setprio 1\n"
    "s_waitcnt lgkmcnt(0)\n"
    "v_mfma_f32_4x4x4_16b_bf16 v[164:167], v[218:219], v[114:115], v[68:71]\n"
    "v_cvt_pk_bf16_f32 v14, v172, v173\n"
    "v_cvt_pk_bf16_f32 v15, v174, v175\n"
    "v_mfma_f32_4x4x4_16b_bf16 v[168:171], v[220:221], v[116:117], v[72:75]\n"
    "v_cvt_pk_bf16_f32 v16, v160, v161\n"
    "v_cvt_pk_bf16_f32 v17, v162, v163\n"
    "v_mfma_f32_4x4x4_16b_bf16 v[172:175], v[222:223], v[118:119], v[76:79]\n"
    "v_cvt_pk_bf16_f32 v18, v164, v165\n"
    "v_mfma_f32_4x4x4_16b_bf16 v[224:227], v[228:229], v[206:207], v[224:227] cbsz:4 abid:3\n"
    "v_cvt_pk_bf16_f32 v19, v166, v167\n"
    "v_mfma_f32_4x4x4_16b_bf16 v[160:163], v[216:217], v[120:121], v[80:83]\n"
    "v_cvt_pk_bf16_f32 v20, v168, v169\n"
    "v_cvt_pk_bf16_f32 v21, v170, v171\n"
    "v_mfma_f32_4x4x4_16b_bf16 v[164:167], v[218:219], v[122:123], v[84:87]\n"
    "v_cvt_pk_bf16_f32 v22, v172, v173\n"
    "v_cvt_pk_bf16_f32 v23, v174, v175\n"
    "v_mfma_f32_4x4x4_16b_bf16 v[168:171], v[220:221], v[124:125], v[88:91]\n"
    "v_cvt_pk_bf16_f32 v24, v160, v161\n"
    "v_cvt_pk_bf16_f32 v25, v162, v163\n"
    "v_mfma_f32_4x4x4_16b_bf16 v[172:175], v[222:223], v[126:127], v[92:95]\n"
    "v_cvt_pk_bf16_f32 v26, v164, v165\n"
    "v_cvt_pk_bf16_f32 v27, v166, v167\n"
    "v_cvt_pk_bf16_f32 v28, v168, v169\n"
    "v_cvt_pk_bf16_f32 v29, v170, v171\n"
    "v_mfma_f32_16x16x32_bf16 v[160:163], v[0:3], v[192:195], v[144:147]\n"
    "s_add_u32 m0, s68, 4096\n"
    "v_cvt_pk_bf16_f32 v30, v172, v173\n"
    "v_cvt_pk_bf16_f32 v31, v174, v175\n"
    "v_exp_f32 v176, v176 clamp\n"
    "v_mfma_f32_16x16x32_bf16 v[160:163], v[4:7], v[196:199], v[160:163]\n"
    "s_add_u32 s66, s66, 16\n"
    "v_exp_f32 v177, v177 clamp\n"
    "v_exp_f32 v178, v178 clamp\n"
    "v_mfma_f32_16x16x32_bf16 v[164:167], v[8:11], v[192:195], v[148:151]\n"
    "s_add_u32 s76, s76, s73\n"
    "v_exp_f32 v179, v179 clamp\n"
    "v_exp_f32 v180, v180 clamp\n"
    "v_mfma_f32_16x16x32_bf16 v[164:167], v[12:15], v[196:199], v[164:167]\n"
    "s_lshl1_add_u32 s76, s72, s76\n"
    "v_exp_f32 v181, v181 clamp\n"
    "v_exp_f32 v182, v182 clamp\n"
    "v_mfma_f32_16x16x32_bf16 v[168:171], v[16:19], v[192:195], v[152:155]\n"
    "s_mov_b32 s80, s83\n"
    "v_exp_f32 v183, v183 clamp\n"
    "v_exp_f32 v184, v184 clamp\n"
    "v_mfma_f32_16x16x32_bf16 v[168:171], v[20:23], v[196:199], v[168:171]\n"
    "s_add_u32 s62, s62, 256\n"
    "v_exp_f32 v185, v185 clamp\n"
    "v_exp_f32 v186, v186 clamp\n"
    "v_mfma_f32_16x16x32_bf16 v[172:175], v[24:27], v[192:195], v[156:159]\n"
    "s_mov_b32 s81, s64\n"
    "v_exp_f32 v187, v187 clamp\n"
    "v_exp_f32 v188, v188 clamp\n"
    "v_mfma_f32_16x16x32_bf16 v[172:175], v[28:31], v[196:199], v[172:175]\n"
    "s_lshr_b32 s63, s65, 6\n"
    "v_exp_f32 v189, v189 clamp\n"
    "v_exp_f32 v190, v190 clamp\n"
    "v_exp_f32 v191, v191 clamp\n"
    "v_pk_fma_f32 v[176:177], v[176:177], s[70:71], v[128:129]\n"
    "v_pk_fma_f32 v[178:179], v[178:179], s[70:71], v[130:131]\n"
    "v_pk_add_f32 v[230:231], v[176:177], v[178:179]\n"
    "v_pk_mul_f32 v[232:233], v[176:177], v[178:179]\n"
    "v_pk_fma_f32 v[180:181], v[180:181], s[70:71], v[132:133]\n"
    "v_pk_fma_f32 v[230:231], v[230:231], v[180:181], v[232:233]\n"
    "v_pk_mul_f32 v[232:233], v[232:233], v[180:181]\n"
    "v_pk_fma_f32 v[182:183], v[182:183], s[70:71], v[134:135]\n"
    "v_pk_fma_f32 v[230:231], v[230:231], v[182:183], v[232:233]\n"
    "v_pk_mul_f32 v[232:233], v[232:233], v[182:183]\n"
    "v_pk_fma_f32 v[184:185], v[184:185], s[70:71], v[136:137]\n"
    "v_pk_fma_f32 v[230:231], v[230:231], v[184:185], v[232:233]\n"
    "v_pk_mul_f32 v[232:233], v[232:233], v[184:185]\n"
    "v_pk_fma_f32 v[186:187], v[186:187], s[70:71], v[138:139]\n"
    "v_pk_fma_f32 v[230:231], v[230:231], v[186:187], v[232:233]\n"
    "v_pk_mul_f32 v[232:233], v[232:233], v[186:187]\n"
    "v_pk_fma_f32 v[188:189], v[188:189], s[70:71], v[140:141]\n"
    "v_pk_fma_f32 v[230:231], v[230:231], v[188:189], v[232:233]\n"
    "v_pk_mul_f32 v[232:233], v[232:233], v[188:189]\n"
    "v_pk_fma_f32 v[190:191], v[190:191], s[70:71], v[142:143]\n"
    "v_pk_fma_f32 v[230:231], v[230:231], v[190:191], v[232:233]\n"
    "v_pk_mul_f32 v[232:233], v[232:233], v[190:191]\n"
    "v_rcp_f32 v232, v232\n"
    "v_rcp_f32 v233, v233\n"
    "v_fma_f32 v230, v230, v232, v247\n"
    "v_fmac_f32 v230, v231, v233\n"
    "s_setprio 0\n"
    "ds_read_b64_tr_b16 v[200:201], v240 offset:4096\n"
    "ds_read_b64_tr_b16 v[202:203], v241 offset:4096\n"
    "ds_read_b64_tr_b16 v[204:205], v242 offset:4096\n"
    "ds_read_b64_tr_b16 v[206:207], v243 offset:4096\n"
    "ds_read_u8 v236, v251 offset:0\n"
    "ds_read_u8 v238, v251 offset:1\n"
    "ds_write_b16_d16_hi v245, v230\n"
    "ds_read_b64 v[228:229], v246\n"
    "ds_read_b64 v[216:217], v246 offset:128\n"
    "s_waitcnt vmcnt(2)\n"
    "ds_read_b128 v[192:195], v234 offset:6144\n"
    "ds_read_b128 v[196:199], v235 offset:6144\n"
    "s_waitcnt lgkmcnt(5)\n"
    "buffer_load_dwordx4 v[236:237], s[40:43], s67 idxen offen offset:0 nt lds\n"
    "buffer_load_dwordx4 v[238:239], s[40:43], s67 idxen offen offset:1024 nt lds\n"
    "s_waitcnt lgkmcnt(2)\n"
    "s_bitcmp1_b32 s60, 30\n"
    "s_cbranch_scc1 .Ldin_A2s\n"
    ".Ldin_A2:\n"
    "v_add_u32 v251, s66, v244\n"
    "v_readlane_b32 s60, v216, s59\n"
    "v_readlane_b32 s61, v217, s59\n"
    "v_mfma_f32_4x4x4_16b_bf16 v[224:227], v[228:229], v[208:209], v[224:227] cbsz:4 abid:0\n"
    "v_exp_f32 v160, v160 clamp\n"
    "v_exp_f32 v161, v161 clamp\n"
    "v_mfma_f32_4x4x4_16b_bf16 v[224:227], v[228:229], v[210:211], v[224:227] cbsz:4 abid:1\n"
    "v_exp_f32 v162, v162 clamp\n"
    "v_exp_f32 v163, v163 clamp\n"
    "v_mfma_f32_4x4x4_16b_bf16 v[224:227], v[228:229], v[212:213], v[224:227] cbsz:4 abid:2\n"
    "v_exp_f32 v164, v164 clamp\n"
    "v_exp_f32 v165, v165 clamp\n"
    "v_mfma_f32_4x4x4_16b_bf16 v[224:227], v[228:229], v[214:215], v[224:227] cbsz:4 abid:3\n"
    "s_waitcnt lgkmcnt(0)\n"
    "v_mfma_f32_16x16x32_bf16 v[176:179], v[0:3], v[192:195], v[144:147]\n"
    "s_setprio 1\n"
    "v_exp_f32 v166, v166 clamp\n"
    "v_mfma_f32_16x16x32_bf16 v[176:179], v[4:7], v[196:199], v[176:179]\n"
    "s_cmp_eq_u32 s60, -1\n"
    "v_exp_f32 v167, v167 clamp\n"
    "v_mfma_f32_16x16x32_bf16 v[180:183], v[8:11], v[192:195], v[148:151]\n"
    "s_cbranch_scc1 .Ldin_A2_poll\n"
    ".Ldin_A2_pr:\n"
    "v_exp_f32 v168, v168 clamp\n"
    "v_mfma_f32_16x16x32_bf16 v[180:183], v[12:15], v[196:199], v[180:183]\n"
    "s_lshr_b32 s94, s60, 12\n"
    "v_exp_f32 v169, v169 clamp\n"
    "v_mfma_f32_16x16x32_bf16 v[184:187], v[16:19], v[192:195], v[152:155]\n"
    "s_and_b32 s94, s94, 0x1ff0\n"
    "v_exp_f32 v170, v170 clamp\n"
    "v_mfma_f32_16x16x32_bf16 v[184:187], v[20:23], v[196:199], v[184:187]\n"
    "s_mov_b32 m0, s62\n"
    "v_exp_f32 v171, v171 clamp\n"
    "v_mfma_f32_16x16x32_bf16 v[188:191], v[24:27], v[192:195], v[156:159]\n"
    "s_add_u32 s66, s66, 16\n"
    "v_exp_f32 v172, v172 clamp\n"
    "v_mfma_f32_16x16x32_bf16 v[188:191], v[28:31], v[196:199], v[188:191]\n"
    "s_and_b32 s64, s60, 0xffff\n"
    "v_xor_b32 v252, s94, v250\n"
    "v_exp_f32 v173, v173 clamp\n"
    "v_exp_f32 v174, v174 clamp\n"
    "v_exp_f32 v175, v175 clamp\n"
    "v_pk_fma_f32 v[160:161], v[160:161], s[70:71], v[128:129]\n"
    "v_pk_fma_f32 v[162:163], v[162:163], s[70:71], v[130:131]\n"
    "v_pk_add_f32 v[230:231], v[160:161], v[162:163]\n"
    "v_pk_mul_f32 v[232:233], v[160:161], v[162:163]\n"
    "v_pk_add_f32 v[218:219], v[224:225], v[226:227]\n"
    "v_mov_b64 v[224:225], 0\n"
    "v_mov_b64 v[226:227], 0\n"
    "v_pk_fma_f32 v[164:165], v[164:165], s[70:71], v[132:133]\n"
    "v_pk_fma_f32 v[230:231], v[230:231], v[164:165], v[232:233]\n"
    "v_pk_mul_f32 v[232:233], v[232:233], v[164:165]\n"
    "v_pk_fma_f32 v[166:167], v[166:167], s[70:71], v[134:135]\n"
    "v_pk_fma_f32 v[230:231], v[230:231], v[166:167], v[232:233]\n"
    "v_pk_mul_f32 v[232:233], v[232:233], v[166:167]\n"
    "v_pk_fma_f32 v[168:169], v[168:169], s[70:71], v[136:137]\n"
    "v_pk_fma_f32 v[230:231], v[230:231], v[168:169], v[232:233]\n"
    "v_pk_mul_f32 v[232:233], v[232:233], v[168:169]\n"
    "v_pk_fma_f32 v[170:171], v[170:171], s[70:71], v[138:139]\n"
    "v_pk_fma_f32 v[230:231], v[230:231], v[170:171], v[232:233]\n"
    "v_pk_mul_f32 v[232:233], v[232:233], v[170:171]\n"
    "v_pk_fma_f32 v[172:173], v[172:173], s[70:71], v[140:141]\n"
    "v_pk_fma_f32 v[230:231], v[230:231], v[172:173], v[232:233]\n"
    "v_pk_mul_f32 v[232:233], v[232:233], v[172:173]\n"
    "v_pk_fma_f32 v[174:175], v[174:175], s[70:71], v[142:143]\n"
    "v_pk_fma_f32 v[230:231], v[230:231], v[174:175], v[232:233]\n"
    "v_pk_mul_f32 v[232:233], v[232:233], v[174:175]\n"
    "v_rcp_f32 v232, v232\n"
    "v_rcp_f32 v233, v233\n"
    "v_fma_f32 v230, v230, v232, v247\n"
    "v_fmac_f32 v230, v231, v233\n"
    "v_add_f32_e64 v218, v218, v219\n"
    "s_setprio 0\n"
    "ds_read_b64_tr_b16 v[208:209], v240 offset:6144\n"
    "ds_read_b64_tr_b16 v[210:211], v241 offset:6144\n"
    "ds_read_b64_tr_b16 v[212:213], v242 offset:6144\n"
    "ds_read_b64_tr_b16 v[214:215], v243 offset:6144\n"
    "ds_read_u8 v236, v251 offset:0\n"
    "ds_read_u8 v238, v251 offset:1\n"
    "ds_write_b16_d16_hi v245, v230\n"
    "ds_read_b64 v[228:229], v246\n"
    "ds_write_addtid_b32 v218 offset:49152\n"
    "s_add_u32 m0, s68, 6144\n"
    "s_bfe_u32 s94, s60, 0x30013\n"
    "s_add_u32 s58, s58, s57\n"
    "s_mul_i32 s94, s94, s56\n"
    "s_add_u32 s82, s64, s9\n"
    "s_add_u32 s65, s58, s94\n"
    "s_waitcnt vmcnt(2)\n"
    "ds_read_b128 v[192:195], v234 offset:0\n"
    "ds_read_b128 v[196:199], v235 offset:0\n"
    "ds_read_b128 v[216:219], v252 offset:0\n"
    "ds_read_b128 v[220:223], v252 offset:64\n"
    "s_sub_u32 s95, s61, s76\n"
    "s_lshr_b32 s94, s61, 16\n"
    "s_sext_i32_i16 s95, s95\n"
    "s_cmp_eq_u32 s59, s10\n"
    "s_cbranch_scc1 .Ldin_A2_skip\n"
    "s_waitcnt lgkmcnt(7)\n"
    "buffer_load_dwordx4 v[236:237], s[40:43], s67 idxen offen offset:0 nt lds\n"
    "buffer_load_dwordx4 v[238:239], s[40:43], s67 idxen offen offset:1024 nt lds\n"
    ".Ldin_A2_skr:\n"
    "s_cmp_lt_i32 s95, -550\n"
    "s_cselect_b32 s94, 0, s94\n"
    "s_mul_i32 s95, s95, s94\n"
    "s_bfe_u32 s94, s60, 0x50019\n"
    "s_ashr_i32 s95, s95, 16\n"
    "s_lshl4_add_u32 s83, s94, s64\n"
    "s_max_i32 s95, s95, -14\n"
    "s_add_u32 s59, s59, 1\n"
    "s_min_i32 s95, s95, 10\n"
    "s_add_u32 s72, s77, s95\n"
    "s_cmp_lt_i32 s60, 0\n"
    "s_cselect_b32 s72, 0, s72\n"
    "s_cmp_eq_u32 s66, s80\n"
    "s_cselect_b64 s[66:67], s[64:65], s[66:67]\n"
    "s_waitcnt lgkmcnt(5)\n"
    "s_branch .Ldin_G3\n"
    ".Ldin_A2_poll:\n"
    "s_sleep 1\n"
    "ds_read_b64 v[216:217], v246 offset:128\n"
    "s_waitcnt lgkmcnt(0)\n"
    "v_readlane_b32 s60, v216, s59\n"
    "v_readlane_b32 s61, v217, s59\n"
    "s_cmp_eq_u32 s60, -1\n"
    "s_cbranch_scc1 .Ldin_A2_poll\n"
    "s_branch .Ldin_A2_pr\n"
    ".Ldin_A2_skip:\n"
    "s_add_u32 s8, s8, 6144\n"
    "s_sub_u32 s68, s68, 2048\n"
    "s_mov_b32 s9, 48\n"
    "s_sub_u32 s82, s82, 16\n"
    "s_sub_u32 s66, s66, 16\n"
    "s_sub_u32 s76, s76, s72\n"
    "s_waitcnt lgkmcnt(0)\n"
    "s_branch .Ldin_A2_skr\n"
    ".p2align 12\n"
    ".Ldin_stub_L0:\n"
    "s_setpc_b64 s[34:35]\n"
    ".p2align 5\n"
    ".Ldin_L0:\n"
    "s_cmp_lt_i32 s60, 0\n"
    "s_cbranch_scc1 .Ldin_E0\n"
    "ds_read_b128 v[144:147], v252 offset:8192\n"
    "ds_read_b128 v[148:151], v252 offset:8256\n"
    "ds_read_b128 v[152:155], v252 offset:16384\n"
    "ds_read_b128 v[156:159], v252 offset:16448\n"
    "v_add_u32 v251, s66, v244\n"
    "v_and_b32 v216, v248, v216\n"
    "v_and_b32 v217, v249, v217\n"
    "v_and_b32 v218, v248, v218\n"
    "v_and_b32 v219, v249, v219\n"
    "v_mfma_f32_4x4x4_16b_bf16 v[176:179], v[216:217], v[96:97], v[32:35]\n"
    "v_and_b32 v220, v248, v220\n"
    "v_and_b32 v221, v249, v221\n"
    "v_mfma_f32_4x4x4_16b_bf16 v[180:183], v[218:219], v[98:99], v[36:39]\n"
    "v_and_b32 v222, v248, v222\n"
    "v_and_b32_e64 v223, v249, v223\n"
    "v_mfma_f32_4x4x4_16b_bf16 v[184:187], v[220:221], v[100:101], v[40:43]\n"
    "v_cvt_pk_bf16_f32 v0, v176, v177\n"
    "v_mfma_f32_4x4x4_16b_bf16 v[224:227], v[228:229], v[208:209], v[224:227] cbsz:4 abid:0\n"
    "v_cvt_pk_bf16_f32 v1, v178, v179\n"
    "v_mfma_f32_4x4x4_16b_bf16 v[188:191], v[222:223], v[102:103], v[44:47]\n"
    "v_cvt_pk_bf16_f32 v2, v180, v181\n"
    "v_cvt_pk_bf16_f32 v3, v182, v183\n"
    "v_mfma_f32_4x4x4_16b_bf16 v[176:179], v[216:217], v[104:105], v[48:51]\n"
    "v_cvt_pk_bf16_f32 v4, v184, v185\n"
    "v_cvt_pk_bf16_f32 v5, v186, v187\n"
    "v_mfma_f32_4x4x4_16b_bf16 v[180:183], v[218:219], v[106:107], v[52:55]\n"
    "v_cvt_pk_bf16_f32 v6, v188, v189\n"
    "v_mfma_f32_4x4x4_16b_bf16 v[224:227], v[228:229], v[210:211], v[224:227] cbsz:4 abid:1\n"
    "v_cvt_pk_bf16_f32 v7, v190, v191\n"
    "v_mfma_f32_4x4x4_16b_bf16 v[184:187], v[220:221], v[108:109], v[56:59]\n"
    "v_cvt_pk_bf16_f32 v8, v176, v177\n"
    "v_cvt_pk_bf16_f32 v9, v178, v179\n"
    "v_mfma_f32_4x4x4_16b_bf16 v[188:191], v[222:223], v[110:111], v[60:63]\n"
    "v_cvt_pk_bf16_f32 v10, v180, v181\n"
    "v_cvt_pk_bf16_f32 v11, v182, v183\n"
    "v_mfma_f32_4x4x4_16b_bf16 v[176:179], v[216:217], v[112:113], v[64:67]\n"
    "v_cvt_pk_bf16_f32 v12, v184, v185\n"
    "v_mfma_f32_4x4x4_16b_bf16 v[224:227], v[228:229], v[212:213], v[224:227] cbsz:4 abid:2\n"
    "v_cvt_pk_bf16_f32 v13, v186, v187\n"
    "s_setprio 1\n"
    "s_waitcnt lgkmcnt(0)\n"
    "v_mfma_f32_4x4x4_16b_bf16 v[180:183], v[218:219], v[114:115], v[68:71]\n"
    "v_cvt_pk_bf16_f32 v14, v188, v189\n"
    "v_cvt_pk_bf16_f32 v15, v190, v191\n"
    "v_mfma_f32_4x4x4_16b_bf16 v[184:187], v[220:221], v[116:117], v[72:75]\n"
    "v_cvt_pk_bf16_f32 v16, v176, v177\n"
    "v_cvt_pk_bf16_f32 v17, v178, v179\n"
    "v_mfma_f32_4x4x4_16b_bf16 v[188:191], v[222:223], v[118:119], v[76:79]\n"
    "v_cvt_pk_bf16_f32 v18, v180, v181\n"
    "v_mfma_f32_4x4x4_16b_bf16 v[224:227], v[228:229], v[214:215], v[224:227] cbsz:4 abid:3\n"
    "v_cvt_pk_bf16_f32 v19, v182, v183\n"
    "v_mfma_f32_4x4x4_16b_bf16 v[176:179], v[216:217], v[120:121], v[80:83]\n"
    "v_cvt_pk_bf16_f32 v20, v184, v185\n"
    "v_cvt_pk_bf16_f32 v21, v186, v187\n"
    "v_mfma_f32_4x4x4_16b_bf16 v[180:183], v[218:219], v[122:123], v[84:87]\n"
    "v_cvt_pk_bf16_f32 v22, v188, v189\n"
    "v_cvt_pk_bf16_f32 v23, v190, v191\n"
    "v_mfma_f32_4x4x4_16b_bf16 v[184:187], v[220:221], v[124:125], v[88:91]\n"
    "v_cvt_pk_bf16_f32 v24, v176, v177\n"
    "v_cvt_pk_bf16_f32 v25, v178, v179\n"
    "v_mfma_f32_4x4x4_16b_bf16 v[188:191], v[222:223], v[126:127], v[92:95]\n"
    "v_cvt_pk_bf16_f32 v26, v180, v181\n"
    "v_cvt_pk_bf16_f32 v27, v182, v183\n"
    "v_cvt_pk_bf16_f32 v28, v184, v185\n"
    "v_cvt_pk_bf16_f32 v29, v186, v187\n"
    "v_mfma_f32_16x16x32_bf16 v[176:179], v[0:3], v[192:195], v[144:147]\n"
    "s_add_u32 m0, s68, 2048\n"
    "v_cvt_pk_bf16_f32 v30, v188, v189\n"
    "v_cvt_pk_bf16_f32 v31, v190, v191\n"
    "v_exp_f32 v160, v160 clamp\n"
    "v_mfma_f32_16x16x32_bf16 v[176:179], v[4:7], v[196:199], v[176:179]\n"
    "s_add_u32 s66, s66, 16\n"
    "v_exp_f32 v161, v161 clamp\n"
    "v_exp_f32 v162, v162 clamp\n"
    "v_mfma_f32_16x16x32_bf16 v[180:183], v[8:11], v[192:195], v[148:151]\n"
    "s_add_u32 s76, s76, s73\n"
    "v_exp_f32 v163, v163 clamp\n"
    "v_exp_f32 v164, v164 clamp\n"
    "v_mfma_f32_16x16x32_bf16 v[180:183], v[12:15], v[196:199], v[180:183]\n"
    "s_lshl1_add_u32 s76, s72, s76\n"
    "v_exp_f32 v165, v165 clamp\n"
    "v_exp_f32 v166, v166 clamp\n"
    "v_mfma_f32_16x16x32_bf16 v[184:187], v[16:19], v[192:195], v[152:155]\n"
    "s_mov_b32 s80, s83\n"
    "v_exp_f32 v167, v167 clamp\n"
    "v_exp_f32 v168, v168 clamp\n"
    "v_mfma_f32_16x16x32_bf16 v[184:187], v[20:23], v[196:199], v[184:187]\n"
    "s_add_u32 s62, s62, 256\n"
    "v_exp_f32 v169, v169 clamp\n"
    "v_exp_f32 v170, v170 clamp\n"
    "v_mfma_f32_16x16x32_bf16 v[188:191], v[24:27], v[192:195], v[156:159]\n"
    "s_mov_b32 s81, s64\n"
    "v_exp_f32 v171, v171 clamp\n"
    "v_exp_f32 v172, v172 clamp\n"
    "v_mfma_f32_16x16x32_bf16 v[188:191], v[28:31], v[196:199], v[188:191]\n"
    "s_lshr_b32 s63, s65, 6\n"
    "v_exp_f32 v173, v173 clamp\n"
    "v_exp_f32 v174, v174 clamp\n"
    "v_exp_f32 v175, v175 clamp\n"
    "v_pk_fma_f32 v[160:161], v[160:161], s[70:71], v[128:129]\n"
    "v_pk_fma_f32 v[162:163], v[162:163], s[70:71], v[130:131]\n"
    "v_pk_add_f32 v[230:231], v[160:161], v[162:163]\n"
    "v_pk_mul_f32 v[232:233], v[160:161], v[162:163]\n"
    "v_pk_fma_f32 v[164:165], v[164:165], s[70:71], v[132:133]\n"
    "v_pk_fma_f32 v[230:231], v[230:231], v[164:165], v[232:233]\n"
    "v_pk_mul_f32 v[232:233], v[232:233], v[164:165]\n"
    "v_pk_fma_f32 v[166:167], v[166:167], s[70:71], v[134:135]\n"
    "v_pk_fma_f32 v[230:231], v[230:231], v[166:167], v[232:233]\n"
    "v_pk_mul_f32 v[232:233], v[232:233], v[166:167]\n"
    "v_pk_fma_f32 v[168:169], v[168:169], s[70:71], v[136:137]\n"
    "v_pk_fma_f32 v[230:231], v[230:231], v[168:169], v[232:233]\n"
    "v_pk_mul_f32 v[232:233], v[232:233], v[168:169]\n"
    "v_pk_fma_f32 v[170:171], v[170:171], s[70:71], v[138:139]\n"
    "v_pk_fma_f32 v[230:231], v[230:231], v[170:171], v[232:233]\n"
    "v_pk_mul_f32 v[232:233], v[232:233], v[170:171]\n"
    "v_pk_fma_f32 v[172:173], v[172:173], s[70:71], v[140:141]\n"
    "v_pk_fma_f32 v[230:231], v[230:231], v[172:173], v[232:233]\n"
    "v_pk_mul_f32 v[232:233], v[232:233], v[172:173]\n"
    "v_pk_fma_f32 v[174:175], v[174:175], s[70:71], v[142:143]\n"
    "v_pk_fma_f32 v[230:231], v[230:231], v[174:175], v[232:233]\n"
    "v_pk_mul_f32 v[232:233], v[232:233], v[174:175]\n"
    "v_rcp_f32 v232, v232\n"
    "v_rcp_f32 v233, v233\n"
    "v_fma_f32 v230, v230, v232, v247\n"
    "v_fmac_f32 v230, v231, v233\n"
    "s_setprio 0\n"
    "ds_read_b64_tr_b16 v[208:209], v240 offset:2048\n"
    "ds_read_b64_tr_b16 v[210:211], v241 offset:2048\n"
    "ds_read_b64_tr_b16 v[212:213], v242 offset:2048\n"
    "ds_read_b64_tr_b16 v[214:215], v243 offset:2048\n"
    "ds_read_u8 v236, v251 offset:0\n"
    "ds_read_u8 v238, v251 offset:1\n"
    "ds_write_b16_d16_hi v245, v230\n"
    "ds_read_b64 v[228:229], v246\n"
    "ds_read_b64 v[216:217], v246 offset:128\n"
    "s_waitcnt vmcnt(2)\n"
    "ds_read_b128 v[192:195], v234 offset:4096\n"
    "ds_read_b128 v[196:199], v235 offset:4096\n"
    "s_waitcnt lgkmcnt(5)\n"
    "buffer_load_dwordx4 v[236:237], s[40:43], s67 idxen offen offset:0 nt lds\n"
    "buffer_load_dwordx4 v[238:239], s[40:43], s67 idxen offen offset:1024 nt lds\n"
    "s_waitcnt lgkmcnt(2)\n"
    "s_bitcmp1_b32 s60, 30\n"
    "s_cbranch_scc1 .Ldin_A1s\n"
    ".Ldin_A1:\n"
    "v_add_u32 v251, s66, v244\n"
    "v_readlane_b32 s60, v216, s59\n"
    "v_readlane_b32 s61, v217, s59\n"
    "v_mfma_f32_4x4x4_16b_bf16 v[224:227], v[228:229], v[200:201], v[224:227] cbsz:4 abid:0\n"
    "v_exp_f32 v176, v176 clamp\n"
    "v_exp_f32 v177, v177 clamp\n"
    "v_mfma_f32_4x4x4_16b_bf16 v[224:227], v[228:229], v[202:203], v[224:227] cbsz:4 abid:1\n"
    "v_exp_f32 v178, v178 clamp\n"
    "v_exp_f32 v179, v179 clamp\n"
    "v_mfma_f32_4x4x4_16b_bf16 v[224:227], v[228:229], v[204:205], v[224:227] cbsz:4 abid:2\n"
    "v_exp_f32 v180, v180 clamp\n"
    "v_exp_f32 v181, v181 clamp\n"
    "v_mfma_f32_4x4x4_16b_bf16 v[224:227], v[228:229], v[206:207], v[224:227] cbsz:4 abid:3\n"
    "s_waitcnt lgkmcnt(0)\n"
    "v_mfma_f32_16x16x32_bf16 v[160:163], v[0:3], v[192:195], v[144:147]\n"
    "s_setprio 1\n"
    "v_exp_f32 v182, v182 clamp\n"
    "v_mfma_f32_16x16x32_bf16 v[160:163], v[4:7], v[196:199], v[160:163]\n"
    "s_cmp_eq_u32 s60, -1\n"
    "v_exp_f32 v183, v183 clamp\n"
    "v_mfma_f32_16x16x32_bf16 v[164:167], v[8:11], v[192:195], v[148:151]\n"
    "s_cbranch_scc1 .Ldin_A1_poll\n"
    ".Ldin_A1_pr:\n"
    "v_exp_f32 v184, v184 clamp\n"
    "v_mfma_f32_16x16x32_bf16 v[164:167], v[12:15], v[196:199], v[164:167]\n"
    "s_lshr_b32 s94, s60, 12\n"
    "v_exp_f32 v185, v185 clamp\n"
    "v_mfma_f32_16x16x32_bf16 v[168:171], v[16:19], v[192:195], v[152:155]\n"
    "s_and_b32 s94, s94, 0x1ff0\n"
    "v_exp_f32 v186, v186 clamp\n"
    "v_mfma_f32_16x16x32_bf16 v[168:171], v[20:23], v[196:199], v[168:171]\n"
    "s_mov_b32 m0, s62\n"
    "v_exp_f32 v187, v187 clamp\n"
    "v_mfma_f32_16x16x32_bf16 v[172:175], v[24:27], v[192:195], v[156:159]\n"
    "s_add_u32 s66, s66, 16\n"
    "v_exp_f32 v188, v188 clamp\n"
    "v_mfma_f32_16x16x32_bf16 v[172:175], v[28:31], v[196:199], v[172:175]\n"
    "s_and_b32 s64, s60, 0xffff\n"
    "v_xor_b32 v252, s94, v250\n"
    "v_exp_f32 v189, v189 clamp\n"
    "v_exp_f32 v190, v190 clamp\n"
    "v_exp_f32 v191, v191 clamp\n"
    "v_pk_fma_f32 v[176:177], v[176:177], s[70:71], v[128:129]\n"
    "v_pk_fma_f32 v[178:179], v[178:179], s[70:71], v[130:131]\n"
    "v_pk_add_f32 v[230:231], v[176:177], v[178:179]\n"
    "v_pk_mul_f32 v[232:233], v[176:177], v[178:179]\n"
    "v_pk_add_f32 v[218:219], v[224:225], v[226:227]\n"
    "v_mov_b64 v[224:225], 0\n"
    "v_mov_b64 v[226:227], 0\n"
    "v_pk_fma_f32 v[180:181], v[180:181], s[70:71], v[132:133]\n"
    "v_pk_fma_f32 v[230:231], v[230:231], v[180:181], v[232:233]\n"
    "v_pk_mul_f32 v[232:233], v[232:233], v[180:181]\n"
    "v_pk_fma_f32 v[182:183], v[182:183], s[70:71], v[134:135]\n"
    "v_pk_fma_f32 v[230:231], v[230:231], v[182:183], v[232:233]\n"
    "v_pk_mul_f32 v[232:233], v[232:233], v[182:183]\n"
    "v_pk_fma_f32 v[184:185], v[184:185], s[70:71], v[136:137]\n"
    "v_pk_fma_f32 v[230:231], v[230:231], v[184:185], v[232:233]\n"
    "v_pk_mul_f32 v[232:233], v[232:233], v[184:185]\n"
    "v_pk_fma_f32 v[186:187], v[186:187], s[70:71], v[138:139]\n"
    "v_pk_fma_f32 v[230:231], v[230:231], v[186:187], v[232:233]\n"
    "v_pk_mul_f32 v[232:233], v[232:233], v[186:187]\n"
    "v_pk_fma_f32 v[188:189], v[188:189], s[70:71], v[140:141]\n"
    "v_pk_fma_f32 v[230:231], v[230:231], v[188:189], v[232:233]\n"
    "v_pk_mul_f32 v[232:233], v[232:233], v[188:189]\n"
    "v_pk_fma_f32 v[190:191], v[190:191], s[70:71], v[142:143]\n"
    "v_pk_fma_f32 v[230:231], v[230:231], v[190:191], v[232:233]\n"
    "v_pk_mul_f32 v[232:233], v[232:233], v[190:191]\n"
    "v_rcp_f32 v232, v232\n"
    "v_rcp_f32 v233, v233\n"
    "v_fma_f32 v230, v230, v232, v247\n"
    "v_fmac_f32 v230, v231, v233\n"
    "v_add_f32_e64 v218, v218, v219\n"
    "s_setprio 0\n"
    "ds_read_b64_tr_b16 v[200:201], v240 offset:4096\n"
    "ds_read_b64_tr_b16 v[202:203], v241 offset:4096\n"
    "ds_read_b64_tr_b16 v[204:205], v242 offset:4096\n"
    "ds_read_b64_tr_b16 v[206:207], v243 offset:4096\n"
    "ds_read_u8 v236, v251 offset:0\n"
    "ds_read_u8 v238, v251 offset:1\n"
    "ds_write_b16_d16_hi v245, v230\n"
    "ds_read_b64 v[228:229], v246\n"
    "ds_write_addtid_b32 v218 offset:49152\n"
    "s_add_u32 m0, s68, 4096\n"
    "s_bfe_u32 s94, s60, 0x30013\n"
    "s_add_u32 s58, s58, s57\n"
    "s_mul_i32 s94, s94, s56\n"
    "s_add_u32 s82, s64, s9\n"
    "s_add_u32 s65, s58, s94\n"
    "s_waitcnt vmcnt(2)\n"
    "ds_read_b128 v[192:195], v234 offset:6144\n"
    "ds_read_b128 v[196:199], v235 offset:6144\n"
    "ds_read_b128 v[216:219], v252 offset:0\n"
    "ds_read_b128 v[220:223], v252 offset:64\n"
    "s_sub_u32 s95, s61, s76\n"
    "s_lshr_b32 s94, s61, 16\n"
    "s_sext_i32_i16 s95, s95\n"
    "s_cmp_eq_u32 s59, s10\n"
    "s_cbranch_scc1 .Ldin_A1_skip\n"
    "s_waitcnt lgkmcnt(7)\n"
    "buffer_load_dwordx4 v[236:237], s[40:43], s67 idxen offen offset:0 nt lds\n"
    "buffer_load_dwordx4 v[238:239], s[40:43], s67 idxen offen offset:1024 nt lds\n"
    ".Ldin_A1_skr:\n"
    "s_cmp_lt_i32 s95, -550\n"
    "s_cselect_b32 s94, 0, s94\n"
    "s_mul_i32 s95, s95, s94\n"
    "s_bfe_u32 s94, s60, 0x50019\n"
    "s_ashr_i32 s95, s95, 16\n"
    "s_lshl4_add_u32 s83, s94, s64\n"
    "s_max_i32 s95, s95, -14\n"
    "s_add_u32 s59, s59, 1\n"
    "s_min_i32 s95, s95, 10\n"
    "s_add_u32 s72, s77, s95\n"
    "s_cmp_lt_i32 s60, 0\n"
    "s_cselect_b32 s72, 0, s72\n"
    "s_cmp_eq_u32 s66, s80\n"
    "s_cselect_b64 s[66:67], s[64:65], s[66:67]\n"
    "s_waitcnt lgkmcnt(5)\n"
    "s_branch .Ldin_G2\n"
    ".Ldin_A1_poll:\n"
    "s_sleep 1\n"
    "ds_read_b64 v[216:217], v246 offset:128\n"
    "s_waitcnt lgkmcnt(0)\n"
    "v_readlane_b32 s60, v216, s59\n"
    "v_readlane_b32 s61, v217, s59\n"
    "s_cmp_eq_u32 s60, -1\n"
    "s_cbranch_scc1 .Ldin_A1_poll\n"
    "s_branch .Ldin_A1_pr\n"
    ".Ldin_A1_skip:\n"
    "s_add_u32 s8, s8, 6144\n"
    "s_sub_u32 s68, s68, 2048\n"
    "s_mov_b32 s9, 48\n"
    "s_sub_u32 s82, s82, 16\n"
    "s_sub_u32 s66, s66, 16\n"
    "s_sub_u32 s76, s76, s72\n"
    "s_waitcnt lgkmcnt(0)\n"
    "s_branch .Ldin_A1_skr\n"
    ".Ldin_stub_L3:\n"
    "s_setpc_b64 s[34:35]\n"
    ".p2align 5\n"
    ".Ldin_L3:\n"
    "s_cmp_lt_i32 s60, 0\n"
    "s_cbranch_scc1 .Ldin_E1\n"
    "ds_read_b128 v[144:147], v252 offset:8192\n"
    "ds_read_b128 v[148:151], v252 offset:8256\n"
    "ds_read_b128 v[152:155], v252 offset:16384\n"
    "ds_read_b128 v[156:159], v252 offset:16448\n"
    "v_add_u32 v251, s66, v244\n"
    "v_and_b32 v216, v248, v216\n"
    "v_and_b32 v217, v249, v217\n"
    "v_and_b32 v218, v248, v218\n"
    "v_and_b32 v219, v249, v219\n"
    "v_mfma_f32_4x4x4_16b_bf16 v[160:163], v[216:217], v[96:97], v[32:35]\n"
    "v_and_b32 v220, v248, v220\n"
    "v_and_b32 v221, v249, v221\n"
    "v_mfma_f32_4x4x4_16b_bf16 v[164:167], v[218:219], v[98:99], v[36:39]\n"
    "v_and_b32 v222, v248, v222\n"
    "v_and_b32_e64 v223, v249, v223\n"
    "v_mfma_f32_4x4x4_16b_bf16 v[168:171], v[220:221], v[100:101], v[40:43]\n"
    "v_cvt_pk_bf16_f32 v0, v160, v161\n"
    "v_mfma_f32_4x4x4_16b_bf16 v[224:227], v[228:229], v[200:201], v[224:227] cbsz:4 abid:0\n"
    "v_cvt_pk_bf16_f32 v1, v162, v163\n"
    "v_mfma_f32_4x4x4_16b_bf16 v[172:175], v[222:223], v[102:103], v[44:47]\n"
    "v_cvt_pk_bf16_f32 v2, v164, v165\n"
    "v_cvt_pk_bf16_f32 v3, v166, v167\n"
    "v_mfma_f32_4x4x4_16b_bf16 v[160:163], v[216:217], v[104:105], v[48:51]\n"
    "v_cvt_pk_bf16_f32 v4, v168, v169\n"
    "v_cvt_pk_bf16_f32 v5, v170, v171\n"
    "v_mfma_f32_4x4x4_16b_bf16 v[164:167], v[218:219], v[106:107], v[52:55]\n"
    "v_cvt_pk_bf16_f32 v6, v172, v173\n"
    "v_mfma_f32_4x4x4_16b_bf16 v[224:227], v[228:229], v[202:203], v[224:227] cbsz:4 abid:1\n"
    "v_cvt_pk_bf16_f32 v7, v174, v175\n"
    "v_mfma_f32_4x4x4_16b_bf16 v[168:171], v[220:221], v[108:109], v[56:59]\n"
    "v_cvt_pk_bf16_f32 v8, v160, v161\n"
    "v_cvt_pk_bf16_f32 v9, v162, v163\n"
    "v_mfma_f32_4x4x4_16b_bf16 v[172:175], v[222:223], v[110:111], v[60:63]\n"
    "v_cvt_pk_bf16_f32 v10, v164, v165\n"
    "v_cvt_pk_bf16_f32 v11, v166, v167\n"
    "v_mfma_f32_4x4x4_16b_bf16 v[160:163], v[216:217], v[112:113], v[64:67]\n"
    "v_cvt_pk_bf16_f32 v12, v168, v169\n"
    "v_mfma_f32_4x4x4_16b_bf16 v[224:227], v[228:229], v[204:205], v[224:227] cbsz:4 abid:2\n"
    "v_cvt_pk_bf16_f32 v13, v170, v171\n"
    "s_setprio 1\n"
    "s_waitcnt lgkmcnt(0)\n"
    "v_mfma_f32_4x4x4_16b_bf16 v[164:167], v[218:219], v[114:115], v[68:71]\n"
    "v_cvt_pk_bf16_f32 v14, v172, v173\n"
    "v_cvt_pk_bf16_f32 v15, v174, v175\n"
    "v_mfma_f32_4x4x4_16b_bf16 v[168:171], v[220:221], v[116:117], v[72:75]\n"
    "v_cvt_pk_bf16_f32 v16, v160, v161\n"
    "v_cvt_pk_bf16_f32 v17, v162, v163\n"
    "v_mfma_f32_4x4x4_16b_bf16 v[172:175], v[222:223], v[118:119], v[76:79]\n"
    "v_cvt_pk_bf16_f32 v18, v164, v165\n"
    "v_mfma_f32_4x4x4_16b_bf16 v[224:227], v[228:229], v[206:207], v[224:227] cbsz:4 abid:3\n"
    "v_cvt_pk_bf16_f32 v19, v166, v167\n"
    "v_mfma_f32_4x4x4_16b_bf16 v[160:163], v[216:217], v[120:121], v[80:83]\n"
    "v_cvt_pk_bf16_f32 v20, v168, v169\n"
    "v_cvt_pk_bf16_f32 v21, v170, v171\n"
    "v_mfma_f32_4x4x4_16b_bf16 v[164:167], v[218:219], v[122:123], v[84:87]\n"
    "v_cvt_pk_bf16_f32 v22, v172, v173\n"
    "v_cvt_pk_bf16_f32 v23, v174, v175\n"
    "v_mfma_f32_4x4x4_16b_bf16 v[168:171], v[220:221], v[124:125], v[88:91]\n"
    "v_cvt_pk_bf16_f32 v24, v160, v161\n"
    "v_cvt_pk_bf16_f32 v25, v162, v163\n"
    "v_mfma_f32_4x4x4_16b_bf16 v[172:175], v[222:223], v[126:127], v[92:95]\n"
    "v_cvt_pk_bf16_f32 v26, v164, v165\n"
    "v_cvt_pk_bf16_f32 v27, v166, v167\n"
    "v_cvt_pk_bf16_f32 v28, v168, v169\n"
    "v_cvt_pk_bf16_f32 v29, v170, v171\n"
    "v_mfma_f32_16x16x32_bf16 v[160:163], v[0:3], v[192:195], v[144:147]\n"
    "s_add_u32 m0, s8, 0\n"
    "v_cvt_pk_bf16_f32 v30, v172, v173\n"
    "v_cvt_pk_bf16_f32 v31, v174, v175\n"
    "v_exp_f32 v176, v176 clamp\n"
    "v_mfma_f32_16x16x32_bf16 v[160:163], v[4:7], v[196:199], v[160:163]\n"
    "s_add_u32 s66, s66, 16\n"
    "v_exp_f32 v177, v177 clamp\n"
    "v_exp_f32 v178, v178 clamp\n"
    "v_mfma_f32_16x16x32_bf16 v[164:167], v[8:11], v[192:195], v[148:151]\n"
    "s_add_u32 s76, s76, s73\n"
    "v_exp_f32 v179, v179 clamp\n"
    "v_exp_f32 v180, v180 clamp\n"
    "v_mfma_f32_16x16x32_bf16 v[164:167], v[12:15], v[196:199], v[164:167]\n"
    "s_lshl1_add_u32 s76, s72, s76\n"
    "v_exp_f32 v181, v181 clamp\n"
    "v_exp_f32 v182, v182 clamp\n"
    "v_mfma_f32_16x16x32_bf16 v[168:171], v[16:19], v[192:195], v[152:155]\n"
    "s_mov_b32 s80, s83\n"
    "v_exp_f32 v183, v183 clamp\n"
    "v_exp_f32 v184, v184 clamp\n"
    "v_mfma_f32_16x16x32_bf16 v[168:171], v[20:23], v[196:199], v[168:171]\n"
    "s_add_u32 s62, s62, 256\n"
    "v_exp_f32 v185, v185 clamp\n"
    "v_exp_f32 v186, v186 clamp\n"
    "v_mfma_f32_16x16x32_bf16 v[172:175], v[24:27], v[192:195], v[156:159]\n"
    "s_mov_b32 s81, s64\n"
    "v_exp_f32 v187, v187 clamp\n"
    "v_exp_f32 v188, v188 clamp\n"
    "v_mfma_f32_16x16x32_bf16 v[172:175], v[28:31], v[196:199], v[172:175]\n"
    "s_lshr_b32 s63, s65, 6\n"
    "v_exp_f32 v189, v189 clamp\n"
    "v_exp_f32 v190, v190 clamp\n"
    "v_exp_f32 v191, v191 clamp\n"
    "s_nop 0\n"
    "v_pk_fma_f32 v[176:177], v[176:177], s[70:71], v[128:129]\n"
    "v_pk_fma_f32 v[178:179], v[178:179], s[70:71], v[130:131]\n"
    "v_pk_add_f32 v[230:231], v[176:177], v[178:179]\n"
    "v_pk_mul_f32 v[232:233], v[176:177], v[178:179]\n"
    "v_pk_fma_f32 v[180:181], v[180:181], s[70:71], v[132:133]\n"
    "v_pk_fma_f32 v[230:231], v[230:231], v[180:181], v[232:233]\n"
    "v_pk_mul_f32 v[232:233], v[232:233], v[180:181]\n"
    "v_pk_fma_f32 v[182:183], v[182:183], s[70:71], v[134:135]\n"
    "v_pk_fma_f32 v[230:231], v[230:231], v[182:183], v[232:233]\n"
    "v_pk_mul_f32 v[232:233], v[232:233], v[182:183]\n"
    "v_pk_fma_f32 v[184:185], v[184:185], s[70:71], v[136:137]\n"
    "v_pk_fma_f32 v[230:231], v[230:231], v[184:185], v[232:233]\n"
    "v_pk_mul_f32 v[232:233], v[232:233], v[184:185]\n"
    "v_pk_fma_f32 v[186:187], v[186:187], s[70:71], v[138:139]\n"
    "v_pk_fma_f32 v[230:231], v[230:231], v[186:187], v[232:233]\n"
    "v_pk_mul_f32 v[232:233], v[232:233], v[186:187]\n"
    "v_pk_fma_f32 v[188:189], v[188:189], s[70:71], v[140:141]\n"
    "v_pk_fma_f32 v[230:231], v[230:231], v[188:189], v[232:233]\n"
    "v_pk_mul_f32 v[232:233], v[232:233], v[188:189]\n"
    "v_pk_fma_f32 v[190:191], v[190:191], s[70:71], v[142:143]\n"
    "v_pk_fma_f32 v[230:231], v[230:231], v[190:191], v[232:233]\n"
    "v_pk_mul_f32 v[232:233], v[232:233], v[190:191]\n"
    "v_rcp_f32 v232, v232\n"
    "v_rcp_f32 v233, v233\n"
    "v_fma_f32 v230, v230, v232, v247\n"
    "v_fmac_f32 v230, v231, v233\n"
    "s_setprio 0\n"
    "ds_read_b64_tr_b16 v[200:201], v240 offset:0\n"
    "ds_read_b64_tr_b16 v[202:203], v241 offset:0\n"
    "ds_read_b64_tr_b16 v[204:205], v242 offset:0\n"
    "ds_read_b64_tr_b16 v[206:207], v243 offset:0\n"
    "ds_read_u8 v236, v251 offset:0\n"
    "ds_read_u8 v238, v251 offset:1\n"
    "ds_write_b16_d16_hi v245, v230\n"
    "ds_read_b64 v[228:229], v246\n"
    "ds_read_b64 v[216:217], v246 offset:128\n"
    "s_waitcnt vmcnt(2)\n"
    "ds_read_b128 v[192:195], v234 offset:2048\n"
    "ds_read_b128 v[196:199], v235 offset:2048\n"
    "s_waitcnt lgkmcnt(5)\n"
    "buffer_load_dwordx4 v[236:237], s[40:43], s67 idxen offen offset:0 nt lds\n"
    "buffer_load_dwordx4 v[238:239], s[40:43], s67 idxen offen offset:1024 nt lds\n"
    "s_waitcnt lgkmcnt(2)\n"
    "s_bitcmp1_b32 s60, 30\n"
    "s_cbranch_scc1 .Ldin_A0s\n"
    "s_branch .Ldin_A0\n"
    ".p2align 12\n"
    ".Ldin_py:\n"
    "v_and_b32 v32, 0x58, v4\n"
    "v_lshlrev_b32 v33, 1, v2\n"
    "v_and_b32 v34, 0x3c0, v251\n"
    "v_bitop3_b32 v35, v33, v0, 32 bitop3:0x28\n"
    "v_lshl_or_b32 v34, v34, 1, v32\n"
    "s_lshl_b32 s23, s21, 11\n"
    "s_add_u32 s23, s23, 54144\n"
    "v_add3_u32 v52, v34, v35, s23\n"
    "v_and_or_b32 v32, v33, 32, v32\n"
    "v_xor_b32 v53, 32, v52\n"
    "v_xor_b32 v54, 64, v52\n"
    "v_xor_b32 v55, 96, v52\n"
    "v_and_b32 v34, 0xc0, v251\n"
    "v_lshl_or_b32 v32, v2, 6, v32\n"
    "s_lshl_b32 s23, s21, 5\n"
    "v_lshl_or_b32 v32, v34, 1, v32\n"
    "v_and_b32 v58, 0x7bf, v36\n"
    "v_xor_b32 v32, s23, v32\n"
    "s_mul_i32 s23, s21, 0x1040\n"
    "v_add_u32 v56, 54144, v32\n"
    "s_and_b32 s23, s23, 0x2040\n"
    "v_xor_b32 v57, 32, v56\n"
    "v_add_u32 v59, s23, v58\n"
    "v_and_b32 v32, 0xf0, v251\n"
    "v_and_b32 v33, 32, v0\n"
    "v_and_b32 v34, 16, v0\n"
    "v_lshl_or_b32 v32, v33, 3, v32\n"
    "s_lshl_b32 s24, s21, 9\n"
    "s_and_b32 s23, s24, 0x400\n"
    "v_lshl_or_b32 v60, v34, 6, v32\n"
    "s_add_u32 s23, s23, s24\n"
    "v_lshrrev_b32 v34, 1, v34\n"
    "v_or_b32 v60, s23, v60\n"
    "v_or3_b32 v61, v32, v34, s24\n"
    "v_and_b32 v62, 0x3f803f80, v248\n"
    "v_and_b32 v63, 0x3f803f80, v249\n"
    "v_lshrrev_b32 v32, 5, v1\n"
    "s_mul_i32 s23, s21, 544\n"
    "v_mul_u32_u24 v12, 272, v32\n"
    "v_mad_u32_u24 v64, v32, s14, v3\n"
    "v_add3_u32 v12, v12, s23, 15\n"
    "s_not_b32 exec_lo, s16\n"
    "s_not_b32 exec_hi, s16\n"
    "v_bfrev_b32 v64, 1\n"
    "s_mov_b64 exec, -1\n"
    "v_add_u32 v13, v13, v12\n"
    "s_lshl1_add_u32 s23, s21, s78\n"
    "s_mul_i32 s23, s23, s14\n"
    "s_lshl1_add_u32 s22, s15, s23\n"
    "s_mov_b32 s26, 0xbfb8aa3b\n"
    "v_bfrev_b32 v35, 1\n"
    "s_waitcnt vmcnt(5)\n"
    "v_mul_f32 v32, 0x35800000, v44\n"
    "v_max3_f32 v33, |v40|, |v41|, |v42|\n"
    "v_max3_f32 v33, v33, |v43|, |v32|\n"
    "v_lshlrev_b32 v76, 16, v46\n"
    "v_and_b32 v77, 0xffff0000, v46\n"
    "v_max_f32_dpp v33, v33, v33 quad_perm:[1,0,3,2] row_mask:0xf bank_mask:0xf bound_ctrl:1\n"
    "v_lshlrev_b32_e64 v78, 16, v47\n"
    "v_and_b32 v79, 0xffff0000, v47\n"
    "v_max_f32_dpp v33, v33, v33 quad_perm:[2,3,0,1] row_mask:0xf bank_mask:0xf bound_ctrl:1\n"
    "v_bitop3_b32 v68, v76, s26, v35 bitop3:0x6c\n"
    "v_bitop3_b32 v69, v77, s26, v35 bitop3:0x6c\n"
    "v_max_f32_dpp v33, v33, v33 row_half_mirror row_mask:0xf bank_mask:0xf bound_ctrl:1\n"
    "v_bitop3_b32 v70, v78, s26, v35 bitop3:0x6c\n"
    "v_bitop3_b32 v71, v79, s26, v35 bitop3:0x6c\n"
    "v_max_f32_dpp v33, v33, v33 row_mirror row_mask:0xf bank_mask:0xf bound_ctrl:1\n"
    "v_rcp_f32_e64 v76, |v76|\n"
    "v_rcp_f32_e64 v77, |v77|\n"
    "v_rcp_f32_e64 v78, |v78|\n"
    "v_rcp_f32_e64 v79, |v79|\n"
    "v_lshrrev_b32 v33, 23, v33\n"
    "s_movk_i32 s23, 253\n"
    "v_med3_u32 v33, v33, 15, s23\n"
    "v_add_lshl_u32 v33, v33, -14, 23\n"
    "s_lshl_b32 s23, s21, 6\n"
    "v_pk_mul_f32 v[76:77], v[76:77], v[32:33] op_sel:[0,1] op_sel_hi:[1,1]\n"
    "v_pk_mul_f32 v[78:79], v[78:79], v[32:33] op_sel:[0,1] op_sel_hi:[1,1]\n"
    "v_or_b32 v34, s23, v2\n"
    "v_min_f32 v76, 0x46800000, v76\n"
    "v_min_f32 v77, 0x46800000, v77\n"
    "v_min_f32 v78, 0x46800000, v78\n"
    "v_min_f32 v79, 0x46800000, v79\n"
    "ds_write_b128 v34, v[76:79] offset:49152\n"
    "v_log_f32 v72, v76\n"
    "v_log_f32 v73, v77\n"
    "v_log_f32 v74, v78\n"
    "v_log_f32 v75, v79\n"
    "v_bitop3_b32 v80, v40, s26, v35 bitop3:0x6c\n"
    "v_bitop3_b32 v81, v41, s26, v35 bitop3:0x6c\n"
    "v_bitop3_b32 v82, v42, s26, v35 bitop3:0x6c\n"
    "v_bitop3_b32 v83, v43, s26, v35 bitop3:0x6c\n"
    "s_cmp_eq_u32 s21, 0\n"
    "v_add_f32 v72, 0xc1700000, v72\n"
    "v_add_f32 v73, 0xc1700000, v73\n"
    "v_add_f32 v74, 0xc1700000, v74\n"
    "v_add_f32 v75, 0xc1700000, v75\n"
    "v_lshlrev_b32 v84, 16, v48\n"
    "v_and_b32 v85, 0xffff0000, v48\n"
    "v_lshlrev_b32 v86, 16, v49\n"
    "v_and_b32 v87, 0xffff0000, v49\n"
    "s_cbranch_scc0 .Ldin_cg_r\n"
    "v_min_f32 v40, 0, v40\n"
    "v_min_f32 v41, 0, v41\n"
    "v_min_f32 v42, 0, v42\n"
    "v_min_f32 v43, 0, v43\n"
    "v_pk_add_f32 v[40:41], v[40:41], v[42:43]\n"
    "v_sub_u32 v34, 0x7f000000, v33\n"
    "v_add_f32 v32, v40, v41\n"
    "v_and_b32 v42, 12, v0\n"
    "v_lshlrev_b32 v42, 2, v42\n"
    "v_add_f32_dpp v32, v32, v32 quad_perm:[1,0,3,2] row_mask:0xf bank_mask:0xf bound_ctrl:1\n"
    "v_or_b32 v33, 23600, v33\n"
    "s_nop 0\n"
    "v_add_f32_dpp v32, v32, v32 quad_perm:[2,3,0,1] row_mask:0xf bank_mask:0xf bound_ctrl:1\n"
    "s_mov_b64 exec, 15\n"
    "v_add_f32 v32, v32, v44\n"
    "s_mov_b64 exec, 0xffff\n"
    "v_mul_f32_e64 v32, v32, v34\n"
    "ds_write_b64 v42, v[32:33] offset:49408\n"
    "s_mov_b64 exec, -1\n"
    ".Ldin_cg_r:\n"
    "s_waitcnt vmcnt(0) lgkmcnt(0)\n"
    "buffer_load_dwordx4 v[96:99], v64, s[44:47], s22 offen nt\n"
    "s_add_u32 s22, s22, s15\n"
    "buffer_load_dwordx4 v[100:103], v64, s[44:47], s22 offen nt\n"
    "s_add_u32 s22, s22, s15\n"
    "buffer_load_dwordx4 v[104:107], v64, s[44:47], s22 offen nt\n"
    "s_add_u32 s22, s22, s15\n"
    "buffer_load_dwordx4 v[108:111], v64, s[44:47], s22 offen nt\n"
    "s_add_u32 s22, s22, s15\n"
    "buffer_load_dwordx4 v[112:115], v64, s[44:47], s22 offen nt\n"
    "s_add_u32 s22, s22, s15\n"
    "buffer_load_dwordx4 v[116:119], v64, s[44:47], s22 offen nt\n"
    "s_barrier\n"
    "ds_read_b64_tr_b16 v[120:121], v52 offset:0\n"
    "ds_read_b64_tr_b16 v[122:123], v52 offset:16384\n"
    "ds_read_b64_tr_b16 v[124:125], v52 offset:24576\n"
    "ds_read_b64_tr_b16 v[126:127], v53 offset:0\n"
    "ds_read_b64_tr_b16 v[128:129], v53 offset:16384\n"
    "ds_read_b64_tr_b16 v[130:131], v53 offset:24576\n"
    "ds_read_b64_tr_b16 v[132:133], v54 offset:0\n"
    "ds_read_b64_tr_b16 v[134:135], v54 offset:16384\n"
    "ds_read_b64_tr_b16 v[136:137], v54 offset:24576\n"
    "ds_read_b64_tr_b16 v[138:139], v55 offset:0\n"
    "ds_read_b64_tr_b16 v[140:141], v55 offset:16384\n"
    "ds_read_b64_tr_b16 v[142:143], v55 offset:24576\n"
    "s_waitcnt lgkmcnt(9)\n"
    "v_mfma_f32_4x4x4_16b_bf16 v[192:195], v[62:63], v[120:121], 0\n"
    "v_mfma_f32_4x4x4_16b_bf16 v[196:199], v[62:63], v[124:125], 0\n"
    "s_nop 0\n"
    "v_mfma_f32_4x4x4_16b_bf16 v[192:195], v[62:63], v[122:123], v[192:195]\n"
    "ds_read_b64_tr_b16 v[144:145], v56 offset:8192\n"
    "ds_read_b64_tr_b16 v[146:147], v57 offset:8704\n"
    "ds_read_b64_tr_b16 v[148:149], v56 offset:12288\n"
    "ds_read_b64_tr_b16 v[150:151], v57 offset:12800\n"
    "s_waitcnt lgkmcnt(10)\n"
    "v_mfma_f32_4x4x4_16b_bf16 v[200:203], v[62:63], v[126:127], 0\n"
    "v_mfma_f32_4x4x4_16b_bf16 v[204:207], v[62:63], v[130:131], 0\n"
    "s_nop 0\n"
    "v_mfma_f32_4x4x4_16b_bf16 v[200:203], v[62:63], v[128:129], v[200:203]\n"
    "ds_read_b128 v[160:163], v58 offset:24576\n"
    "ds_read_b128 v[164:167], v58 offset:24640\n"
    "s_waitcnt lgkmcnt(9)\n"
    "v_mfma_f32_4x4x4_16b_bf16 v[208:211], v[62:63], v[132:133], 0\n"
    "v_mfma_f32_4x4x4_16b_bf16 v[212:215], v[62:63], v[136:137], 0\n"
    "s_nop 0\n"
    "v_mfma_f32_4x4x4_16b_bf16 v[208:211], v[62:63], v[134:135], v[208:211]\n"
    "ds_read_b64_tr_b16 v[152:153], v56 offset:16384\n"
    "ds_read_b64_tr_b16 v[154:155], v57 offset:16896\n"
    "ds_read_b64_tr_b16 v[156:157], v56 offset:20480\n"
    "ds_read_b64_tr_b16 v[158:159], v57 offset:20992\n"
    "s_waitcnt lgkmcnt(10)\n"
    "v_mfma_f32_4x4x4_16b_bf16 v[216:219], v[62:63], v[138:139], 0\n"
    "v_mfma_f32_4x4x4_16b_bf16 v[220:223], v[62:63], v[142:143], 0\n"
    "s_nop 0\n"
    "v_mfma_f32_4x4x4_16b_bf16 v[216:219], v[62:63], v[140:141], v[216:219]\n"
    "s_waitcnt lgkmcnt(4)\n"
    "v_mfma_f32_16x16x32_bf16 v[224:227], v[144:147], v[160:163], v[84:87]\n"
    "v_mfma_f32_16x16x32_bf16 v[224:227], v[148:151], v[164:167], v[224:227]\n"
    "s_waitcnt lgkmcnt(0)\n"
    "v_mfma_f32_16x16x32_bf16 v[228:231], v[152:155], v[160:163], 0\n"
    "v_mfma_f32_16x16x32_bf16 v[228:231], v[156:159], v[164:167], v[228:231]\n"
    "v_pk_mul_f32 v[192:193], v[192:193], v[80:81] op_sel_hi:[1,0]\n"
    "v_pk_mul_f32 v[194:195], v[194:195], v[80:81] op_sel_hi:[1,0]\n"
    "ds_write_b128 v60, v[192:195] offset:0\n"
    "v_pk_mul_f32 v[196:197], v[196:197], v[80:81] op_sel_hi:[1,0]\n"
    "v_pk_mul_f32 v[198:199], v[198:199], v[80:81] op_sel_hi:[1,0]\n"
    "v_cvt_pk_bf16_f32 v196, v196, v197\n"
    "v_cvt_pk_bf16_f32 v197, v198, v199\n"
    "ds_write_b64 v61, v[196:197] offset:16384\n"
    "v_pk_mul_f32 v[200:201], v[200:201], v[80:81] op_sel:[0,1] op_sel_hi:[1,1]\n"
    "v_pk_mul_f32 v[202:203], v[202:203], v[80:81] op_sel:[0,1] op_sel_hi:[1,1]\n"
    "ds_write_b128 v60, v[200:203] offset:4096\n"
    "v_pk_mul_f32 v[204:205], v[204:205], v[80:81] op_sel:[0,1] op_sel_hi:[1,1]\n"
    "v_pk_mul_f32 v[206:207], v[206:207], v[80:81] op_sel:[0,1] op_sel_hi:[1,1]\n"
    "v_cvt_pk_bf16_f32 v204, v204, v205\n"
    "v_cvt_pk_bf16_f32 v205, v206, v207\n"
    "ds_write_b64 v61, v[204:205] offset:18432\n"
    "v_pk_mul_f32 v[208:209], v[208:209], v[82:83] op_sel_hi:[1,0]\n"
    "v_pk_mul_f32 v[210:211], v[210:211], v[82:83] op_sel_hi:[1,0]\n"
    "ds_write_b128 v60, v[208:211] offset:8192\n"
    "v_pk_mul_f32 v[212:213], v[212:213], v[82:83] op_sel_hi:[1,0]\n"
    "v_pk_mul_f32 v[214:215], v[214:215], v[82:83] op_sel_hi:[1,0]\n"
    "v_cvt_pk_bf16_f32 v212, v212, v213\n"
    "v_cvt_pk_bf16_f32 v213, v214, v215\n"
    "ds_write_b64 v61, v[212:213] offset:20480\n"
    "v_pk_mul_f32 v[216:217], v[216:217], v[82:83] op_sel:[0,1] op_sel_hi:[1,1]\n"
    "v_pk_mul_f32 v[218:219], v[218:219], v[82:83] op_sel:[0,1] op_sel_hi:[1,1]\n"
    "ds_write_b128 v60, v[216:219] offset:12288\n"
    "v_pk_mul_f32 v[220:221], v[220:221], v[82:83] op_sel:[0,1] op_sel_hi:[1,1]\n"
    "v_pk_mul_f32 v[222:223], v[222:223], v[82:83] op_sel:[0,1] op_sel_hi:[1,1]\n"
    "v_cvt_pk_bf16_f32 v220, v220, v221\n"
    "v_cvt_pk_bf16_f32 v221, v222, v223\n"
    "ds_write_b64 v61, v[220:221] offset:22528\n"
    "v_pk_add_f32 v[224:225], v[224:225], v[228:229] neg_lo:[0,1] neg_hi:[0,1]\n"
    "v_pk_add_f32 v[226:227], v[226:227], v[230:231] neg_lo:[0,1] neg_hi:[0,1]\n"
    "v_pk_fma_f32 v[224:225], v[224:225], v[68:69], v[72:73]\n"
    "v_pk_fma_f32 v[226:227], v[226:227], v[70:71], v[74:75]\n"
    "ds_write_b128 v59, v[224:227] offset:32768\n"
    "s_waitcnt lgkmcnt(0)\n"
    "s_barrier\n"
    "s_setprio 0\n"
    "s_mul_i32 s23, s20, 272\n"
    "v_add_u32 v32, s23, v244\n"
    "ds_read_u8 v16, v32 offset:112\n"
    "ds_read_u8 v18, v32 offset:113\n"
    "ds_read_u8 v20, v32 offset:128\n"
    "ds_read_u8 v22, v32 offset:129\n"
    "ds_read_u8 v24, v32 offset:144\n"
    "ds_read_u8 v26, v32 offset:145\n"
    "ds_read_u8 v28, v32 offset:160\n"
    "ds_read_u8 v30, v32 offset:161\n"
    "ds_read_b32 v33, v246 offset:128\n"
    "ds_read_b128 v[168:171], v58 offset:26624\n"
    "ds_read_b128 v[172:175], v58 offset:26688\n"
    "ds_read_b128 v[176:179], v58 offset:28672\n"
    "ds_read_b128 v[180:183], v58 offset:28736\n"
    "ds_read_b128 v[184:187], v58 offset:30720\n"
    "ds_read_b128 v[188:191], v58 offset:30784\n"
    "s_add_u32 m0, s68, 0\n"
    "s_waitcnt lgkmcnt(13)\n"
    "buffer_load_dwordx4 v[16:17], s[40:43], s67 idxen offen offset:0 nt lds\n"
    "buffer_load_dwordx4 v[18:19], s[40:43], s67 idxen offen offset:1024 nt lds\n"
    "s_add_u32 m0, s68, 2048\n"
    "s_waitcnt lgkmcnt(11)\n"
    "buffer_load_dwordx4 v[20:21], s[40:43], s67 idxen offen offset:0 nt lds\n"
    "buffer_load_dwordx4 v[22:23], s[40:43], s67 idxen offen offset:1024 nt lds\n"
    "s_add_u32 m0, s68, 4096\n"
    "s_waitcnt lgkmcnt(9)\n"
    "buffer_load_dwordx4 v[24:25], s[40:43], s67 idxen offen offset:0 nt lds\n"
    "buffer_load_dwordx4 v[26:27], s[40:43], s67 idxen offen offset:1024 nt lds\n"
    "s_add_u32 m0, s68, 6144\n"
    "s_waitcnt lgkmcnt(7)\n"
    "s_nop 0\n"
    "buffer_load_dwordx4 v[28:29], s[40:43], s67 idxen offen offset:0 nt lds\n"
    "buffer_load_dwordx4 v[30:31], s[40:43], s67 idxen offen offset:1024 nt lds\n"
    "ds_write_b64 v13, v[14:15] offset:54112\n"
    "ds_write_b64 v13, v[14:15] offset:56288\n"
    "ds_write_b64 v13, v[14:15] offset:58464\n"
    "ds_write_b64 v13, v[14:15] offset:60640\n"
    "ds_write_b64 v13, v[14:15] offset:62816\n"
    "ds_write_b64 v13, v[14:15] offset:64992\n"
    "s_waitcnt lgkmcnt(6)\n"
    "v_readfirstlane_b32 s60, v33\n"
    "v_mfma_f32_16x16x32_bf16 v[120:123], v[144:147], v[168:171], v[84:87]\n"
    "v_mfma_f32_16x16x32_bf16 v[120:123], v[148:151], v[172:175], v[120:123]\n"
    "v_mfma_f32_16x16x32_bf16 v[124:127], v[152:155], v[168:171], 0\n"
    "v_mfma_f32_16x16x32_bf16 v[124:127], v[156:159], v[172:175], v[124:127]\n"
    "v_mfma_f32_16x16x32_bf16 v[128:131], v[144:147], v[176:179], v[84:87]\n"
    "v_mfma_f32_16x16x32_bf16 v[128:131], v[148:151], v[180:183], v[128:131]\n"
    "v_mfma_f32_16x16x32_bf16 v[132:135], v[152:155], v[176:179], 0\n"
    "v_mfma_f32_16x16x32_bf16 v[132:135], v[156:159], v[180:183], v[132:135]\n"
    "v_mfma_f32_16x16x32_bf16 v[136:139], v[144:147], v[184:187], v[84:87]\n"
    "v_mfma_f32_16x16x32_bf16 v[136:139], v[148:151], v[188:191], v[136:139]\n"
    "v_mfma_f32_16x16x32_bf16 v[140:143], v[152:155], v[184:187], 0\n"
    "v_mfma_f32_16x16x32_bf16 v[140:143], v[156:159], v[188:191], v[140:143]\n"
    "s_and_b32 s23, s14, 14\n"
    "s_waitcnt vmcnt(8)\n"
    "s_cbranch_scc1 .Ldin_trimy\n"
    ".Ldin_trimy_r:\n"
    "v_pk_min_u16 v192, v96, 1 op_sel_hi:[1,0]\n"
    "v_pk_min_u16 v193, v97, 1 op_sel_hi:[1,0]\n"
    "v_pk_min_u16 v194, v98, 1 op_sel_hi:[1,0]\n"
    "v_pk_min_u16 v195, v99, 1 op_sel_hi:[1,0]\n"
    "v_add3_u32 v44, v192, v193, v194\n"
    "v_add_u32 v44, v44, v195\n"
    "v_dot2_u32_u16 v40, v44, s31, 0\n"
    "v_pk_min_u16 v196, v100, 1 op_sel_hi:[1,0]\n"
    "v_pk_min_u16 v197, v101, 1 op_sel_hi:[1,0]\n"
    "v_pk_min_u16 v198, v102, 1 op_sel_hi:[1,0]\n"
    "v_pk_min_u16 v199, v103, 1 op_sel_hi:[1,0]\n"
    "v_add3_u32 v44, v196, v197, v198\n"
    "v_add_u32_e64 v44, v44, v199\n"
    "v_dot2_u32_u16 v40, v44, s30, v40\n"
    "v_pk_min_u16 v200, v104, 1 op_sel_hi:[1,0]\n"
    "v_pk_min_u16 v201, v105, 1 op_sel_hi:[1,0]\n"
    "v_pk_min_u16 v202, v106, 1 op_sel_hi:[1,0]\n"
    "v_pk_min_u16 v203, v107, 1 op_sel_hi:[1,0]\n"
    "v_add3_u32 v42, v200, v201, v202\n"
    "v_add_u32 v42, v42, v203\n"
    "v_lshl_add_u32 v42, v42, 16, v42\n"
    "v_and_b32 v42, 0xffff0000, v42\n"
    "v_or_b32 v40, v40, v42\n"
    "v_pk_min_u16 v204, v108, 1 op_sel_hi:[1,0]\n"
    "v_pk_min_u16 v205, v109, 1 op_sel_hi:[1,0]\n"
    "v_pk_min_u16 v206, v110, 1 op_sel_hi:[1,0]\n"
    "v_pk_min_u16 v207, v111, 1 op_sel_hi:[1,0]\n"
    "v_add3_u32 v45, v204, v205, v206\n"
    "v_add_u32_e64 v45, v45, v207\n"
    "v_dot2_u32_u16 v41, v45, s31, 0\n"
    "v_pk_min_u16 v208, v112, 1 op_sel_hi:[1,0]\n"
    "v_pk_min_u16 v209, v113, 1 op_sel_hi:[1,0]\n"
    "v_pk_min_u16 v210, v114, 1 op_sel_hi:[1,0]\n"
    "v_pk_min_u16 v211, v115, 1 op_sel_hi:[1,0]\n"
    "v_add3_u32 v45, v208, v209, v210\n"
    "v_add_u32_e64 v45, v45, v211\n"
    "v_dot2_u32_u16 v41, v45, s30, v41\n"
    "v_pk_min_u16 v212, v116, 1 op_sel_hi:[1,0]\n"
    "v_pk_min_u16 v213, v117, 1 op_sel_hi:[1,0]\n"
    "v_pk_min_u16 v214, v118, 1 op_sel_hi:[1,0]\n"
    "v_pk_min_u16 v215, v119, 1 op_sel_hi:[1,0]\n"
    "v_add3_u32 v43, v212, v213, v214\n"
    "v_add_u32 v43, v43, v215\n"
    "v_lshl_add_u32 v43, v43, 16, v43\n"
    "v_and_b32 v43, 0xffff0000, v43\n"
    "v_or_b32 v41, v41, v43\n"
    "v_pk_add_f32 v[120:121], v[120:121], v[124:125] neg_lo:[0,1] neg_hi:[0,1]\n"
    "v_pk_add_f32 v[122:123], v[122:123], v[126:127] neg_lo:[0,1] neg_hi:[0,1]\n"
    "v_pk_fma_f32 v[120:121], v[120:121], v[68:69], v[72:73]\n"
    "v_pk_fma_f32 v[122:123], v[122:123], v[70:71], v[74:75]\n"
    "ds_write_b128 v59, v[120:123] offset:34816\n"
    "v_pk_add_f32 v[128:129], v[128:129], v[132:133] neg_lo:[0,1] neg_hi:[0,1]\n"
    "v_pk_add_f32 v[130:131], v[130:131], v[134:135] neg_lo:[0,1] neg_hi:[0,1]\n"
    "v_pk_fma_f32 v[128:129], v[128:129], v[68:69], v[72:73]\n"
    "v_pk_fma_f32 v[130:131], v[130:131], v[70:71], v[74:75]\n"
    "ds_write_b128 v59, v[128:131] offset:36864\n"
    "v_pk_add_f32 v[136:137], v[136:137], v[140:141] neg_lo:[0,1] neg_hi:[0,1]\n"
    "v_pk_add_f32 v[138:139], v[138:139], v[142:143] neg_lo:[0,1] neg_hi:[0,1]\n"
    "v_pk_fma_f32 v[136:137], v[136:137], v[68:69], v[72:73]\n"
    "v_pk_fma_f32 v[138:139], v[138:139], v[70:71], v[74:75]\n"
    "ds_write_b128 v59, v[136:139] offset:38912\n"
    "v_sub_u32 v44, 0x10101, v40\n"
    "v_sub_u32 v45, 0x10101, v41\n"
    "v_add_u32_dpp v42, v40, v40 row_shr:1 row_mask:0xf bank_mask:0xf bound_ctrl:1\n"
    "v_add_u32_dpp v43, v41, v41 row_shr:1 row_mask:0xf bank_mask:0xf bound_ctrl:1\n"
    "v_pk_mad_u16 v96, v192, s28, v96\n"
    "v_add_u32_dpp v42, v42, v42 row_shr:2 row_mask:0xf bank_mask:0xf bound_ctrl:1\n"
    "v_add_u32_dpp v43, v43, v43 row_shr:2 row_mask:0xf bank_mask:0xf bound_ctrl:1\n"
    "v_pk_mad_u16 v97, v193, s28, v97\n"
    "v_add_u32_dpp v42, v42, v42 row_shr:4 row_mask:0xf bank_mask:0xf bound_ctrl:1\n"
    "v_add_u32_dpp v43, v43, v43 row_shr:4 row_mask:0xf bank_mask:0xf bound_ctrl:1\n"
    "v_pk_mad_u16 v98, v194, s28, v98\n"
    "v_add_u32_dpp v42, v42, v42 row_shr:8 row_mask:0xf bank_mask:0xf bound_ctrl:1\n"
    "v_add_u32_dpp v43, v43, v43 row_shr:8 row_mask:0xf bank_mask:0xf bound_ctrl:1\n"
    "v_pk_mad_u16 v99, v195, s28, v99\n"
    "v_add_u32_dpp v42, v42, v42 row_bcast:15 row_mask:0xa bank_mask:0xf\n"
    "v_add_u32_dpp v43, v43, v43 row_bcast:15 row_mask:0xa bank_mask:0xf\n"
    "v_or3_b32 v46, v96, v97, 0\n"
    "v_add_u32 v44, v44, v42\n"
    "v_add_u32 v45, v45, v43\n"
    "v_lshrrev_b32 v32, 3, v1\n"
    "v_and_b32_e64 v32, 4, v32\n"
    "v_lshl_add_u32 v32, s21, 3, v32\n"
    "s_brev_b32 exec_lo, 1\n"
    "s_brev_b32 exec_hi, 1\n"
    "ds_write_b32 v32, v42 offset:49472\n"
    "ds_write_b32 v32, v43 offset:49504\n"
    "s_mov_b64 exec, -1\n"
    "s_mov_b32 s26, 0xc040c00\n"
    "v_perm_b32 v47, v42, v44, s26\n"
    "v_mad_u32_u16 v48, v192, v47, v12\n"
    "v_mad_u32_u16 v49, v192, v47, v12 op_sel:[1,1,0,0]\n"
    "v_pk_mad_u16 v47, v192, s29, v47\n"
    "ds_write_b8 v48, v8 offset:54112\n"
    "ds_write_b8_d16_hi v49, v8 offset:54112\n"
    "v_mad_u32_u16 v50, v193, v47, v12\n"
    "v_mad_u32_u16 v51, v193, v47, v12 op_sel:[1,1,0,0]\n"
    "v_pk_mad_u16 v47, v193, s29, v47\n"
    "ds_write_b8 v50, v9 offset:54112\n"
    "ds_write_b8_d16_hi v51, v9 offset:54112\n"
    "v_mad_u32_u16 v48, v194, v47, v12\n"
    "v_mad_u32_u16 v49, v194, v47, v12 op_sel:[1,1,0,0]\n"
    "v_pk_mad_u16 v47, v194, s29, v47\n"
    "ds_write_b8 v48, v10 offset:54112\n"
    "ds_write_b8_d16_hi v49, v10 offset:54112\n"
    "v_mad_u32_u16 v50, v195, v47, v12\n"
    "v_mad_u32_u16 v51, v195, v47, v12 op_sel:[1,1,0,0]\n"
    "ds_write_b8 v50, v11 offset:54112\n"
    "ds_write_b8_d16_hi v51, v11 offset:54112\n"
    "v_or3_b32 v46, v98, v99, v46\n"
    "v_pk_mad_u16 v100, v196, s28, v100\n"
    "v_pk_mad_u16 v101, v197, s28, v101\n"
    "v_pk_mad_u16 v102, v198, s28, v102\n"
    "v_pk_mad_u16 v103, v199, s28, v103\n"
    "v_or3_b32 v46, v100, v101, v46\n"
    "s_mov_b32 s26, 0xc050c01\n"
    "v_perm_b32 v47, v42, v44, s26\n"
    "v_mad_u32_u16 v48, v196, v47, v12\n"
    "v_mad_u32_u16 v49, v196, v47, v12 op_sel:[1,1,0,0]\n"
    "v_pk_mad_u16 v47, v196, s29, v47\n"
    "ds_write_b8 v48, v8 offset:56288\n"
    "ds_write_b8_d16_hi v49, v8 offset:56288\n"
    "v_mad_u32_u16 v50, v197, v47, v12\n"
    "v_mad_u32_u16 v51, v197, v47, v12 op_sel:[1,1,0,0]\n"
    "v_pk_mad_u16 v47, v197, s29, v47\n"
    "ds_write_b8 v50, v9 offset:56288\n"
    "ds_write_b8_d16_hi v51, v9 offset:56288\n"
    "v_mad_u32_u16 v48, v198, v47, v12\n"
    "v_mad_u32_u16 v49, v198, v47, v12 op_sel:[1,1,0,0]\n"
    "v_pk_mad_u16 v47, v198, s29, v47\n"
    "ds_write_b8 v48, v10 offset:56288\n"
    "ds_write_b8_d16_hi v49, v10 offset:56288\n"
    "v_mad_u32_u16 v50, v199, v47, v12\n"
    "v_mad_u32_u16 v51, v199, v47, v12 op_sel:[1,1,0,0]\n"
    "ds_write_b8 v50, v11 offset:56288\n"
    "ds_write_b8_d16_hi v51, v11 offset:56288\n"
    "v_or3_b32 v46, v102, v103, v46\n"
    "v_pk_mad_u16 v104, v200, s28, v104\n"
    "v_pk_mad_u16 v105, v201, s28, v105\n"
    "v_pk_mad_u16 v106, v202, s28, v106\n"
    "v_pk_mad_u16 v107, v203, s28, v107\n"
    "v_or3_b32 v46, v104, v105, v46\n"
    "s_mov_b32 s26, 0xc060c02\n"
    "v_perm_b32 v47, v42, v44, s26\n"
    "v_mad_u32_u16 v48, v200, v47, v12\n"
    "v_mad_u32_u16 v49, v200, v47, v12 op_sel:[1,1,0,0]\n"
    "v_pk_mad_u16 v47, v200, s29, v47\n"
    "ds_write_b8 v48, v8 offset:58464\n"
    "ds_write_b8_d16_hi v49, v8 offset:58464\n"
    "v_mad_u32_u16 v50, v201, v47, v12\n"
    "v_mad_u32_u16 v51, v201, v47, v12 op_sel:[1,1,0,0]\n"
    "v_pk_mad_u16 v47, v201, s29, v47\n"
    "ds_write_b8 v50, v9 offset:58464\n"
    "ds_write_b8_d16_hi v51, v9 offset:58464\n"
    "v_mad_u32_u16 v48, v202, v47, v12\n"
    "v_mad_u32_u16 v49, v202, v47, v12 op_sel:[1,1,0,0]\n"
    "v_pk_mad_u16 v47, v202, s29, v47\n"
    "ds_write_b8 v48, v10 offset:58464\n"
    "ds_write_b8_d16_hi v49, v10 offset:58464\n"
    "v_mad_u32_u16 v50, v203, v47, v12\n"
    "v_mad_u32_u16 v51, v203, v47, v12 op_sel:[1,1,0,0]\n"
    "ds_write_b8 v50, v11 offset:58464\n"
    "ds_write_b8_d16_hi v51, v11 offset:58464\n"
    "v_or3_b32 v46, v106, v107, v46\n"
    "v_pk_mad_u16 v108, v204, s28, v108\n"
    "v_pk_mad_u16 v109, v205, s28, v109\n"
    "v_pk_mad_u16 v110, v206, s28, v110\n"
    "v_pk_mad_u16 v111, v207, s28, v111\n"
    "v_or3_b32 v46, v108, v109, v46\n"
    "s_mov_b32 s26, 0xc040c00\n"
    "v_perm_b32 v47, v43, v45, s26\n"
    "v_mad_u32_u16 v48, v204, v47, v12\n"
    "v_mad_u32_u16 v49, v204, v47, v12 op_sel:[1,1,0,0]\n"
    "v_pk_mad_u16 v47, v204, s29, v47\n"
    "ds_write_b8 v48, v8 offset:60640\n"
    "ds_write_b8_d16_hi v49, v8 offset:60640\n"
    "v_mad_u32_u16 v50, v205, v47, v12\n"
    "v_mad_u32_u16 v51, v205, v47, v12 op_sel:[1,1,0,0]\n"
    "v_pk_mad_u16 v47, v205, s29, v47\n"
    "ds_write_b8 v50, v9 offset:60640\n"
    "ds_write_b8_d16_hi v51, v9 offset:60640\n"
    "v_mad_u32_u16 v48, v206, v47, v12\n"
    "v_mad_u32_u16 v49, v206, v47, v12 op_sel:[1,1,0,0]\n"
    "v_pk_mad_u16 v47, v206, s29, v47\n"
    "ds_write_b8 v48, v10 offset:60640\n"
    "ds_write_b8_d16_hi v49, v10 offset:60640\n"
    "v_mad_u32_u16 v50, v207, v47, v12\n"
    "v_mad_u32_u16 v51, v207, v47, v12 op_sel:[1,1,0,0]\n"
    "ds_write_b8 v50, v11 offset:60640\n"
    "ds_write_b8_d16_hi v51, v11 offset:60640\n"
    "v_or3_b32 v46, v110, v111, v46\n"
    "v_pk_mad_u16 v112, v208, s28, v112\n"
    "v_pk_mad_u16 v113, v209, s28, v113\n"
    "v_pk_mad_u16 v114, v210, s28, v114\n"
    "v_pk_mad_u16 v115, v211, s28, v115\n"
    "v_or3_b32 v46, v112, v113, v46\n"
    "s_mov_b32 s26, 0xc050c01\n"
    "v_perm_b32 v47, v43, v45, s26\n"
    "v_mad_u32_u16 v48, v208, v47, v12\n"
    "v_mad_u32_u16 v49, v208, v47, v12 op_sel:[1,1,0,0]\n"
    "v_pk_mad_u16 v47, v208, s29, v47\n"
    "ds_write_b8 v48, v8 offset:62816\n"
    "ds_write_b8_d16_hi v49, v8 offset:62816\n"
    "v_mad_u32_u16 v50, v209, v47, v12\n"
    "v_mad_u32_u16 v51, v209, v47, v12 op_sel:[1,1,0,0]\n"
    "v_pk_mad_u16 v47, v209, s29, v47\n"
    "ds_write_b8 v50, v9 offset:62816\n"
    "ds_write_b8_d16_hi v51, v9 offset:62816\n"
    "v_mad_u32_u16 v48, v210, v47, v12\n"
    "v_mad_u32_u16 v49, v210, v47, v12 op_sel:[1,1,0,0]\n"
    "v_pk_mad_u16 v47, v210, s29, v47\n"
    "ds_write_b8 v48, v10 offset:62816\n"
    "ds_write_b8_d16_hi v49, v10 offset:62816\n"
    "v_mad_u32_u16 v50, v211, v47, v12\n"
    "v_mad_u32_u16 v51, v211, v47, v12 op_sel:[1,1,0,0]\n"
    "ds_write_b8 v50, v11 offset:62816\n"
    "ds_write_b8_d16_hi v51, v11 offset:62816\n"
    "v_or3_b32 v46, v114, v115, v46\n"
    "v_pk_mad_u16 v116, v212, s28, v116\n"
    "v_pk_mad_u16 v117, v213, s28, v117\n"
    "v_pk_mad_u16 v118, v214, s28, v118\n"
    "v_pk_mad_u16 v119, v215, s28, v119\n"
    "v_or3_b32 v46, v116, v117, v46\n"
    "s_mov_b32 s26, 0xc060c02\n"
    "v_perm_b32 v47, v43, v45, s26\n"
    "v_mad_u32_u16 v48, v212, v47, v12\n"
    "v_mad_u32_u16 v49, v212, v47, v12 op_sel:[1,1,0,0]\n"
    "v_pk_mad_u16 v47, v212, s29, v47\n"
    "ds_write_b8 v48, v8 offset:64992\n"
    "ds_write_b8_d16_hi v49, v8 offset:64992\n"
    "v_mad_u32_u16 v50, v213, v47, v12\n"
    "v_mad_u32_u16 v51, v213, v47, v12 op_sel:[1,1,0,0]\n"
    "v_pk_mad_u16 v47, v213, s29, v47\n"
    "ds_write_b8 v50, v9 offset:64992\n"
    "ds_write_b8_d16_hi v51, v9 offset:64992\n"
    "v_mad_u32_u16 v48, v214, v47, v12\n"
    "v_mad_u32_u16 v49, v214, v47, v12 op_sel:[1,1,0,0]\n"
    "v_pk_mad_u16 v47, v214, s29, v47\n"
    "ds_write_b8 v48, v10 offset:64992\n"
    "ds_write_b8_d16_hi v49, v10 offset:64992\n"
    "v_mad_u32_u16 v50, v215, v47, v12\n"
    "v_mad_u32_u16 v51, v215, v47, v12 op_sel:[1,1,0,0]\n"
    "ds_write_b8 v50, v11 offset:64992\n"
    "ds_write_b8_d16_hi v51, v11 offset:64992\n"
    "v_or3_b32 v46, v118, v119, v46\n"
    "v_cmp_ne_u32 vcc, 0, v46\n"
    "v_mov_b32 v32, 0\n"
    "s_cmp_lg_u64 vcc, 0\n"
    "s_cselect_b32 s23, 0x101, 1\n"
    "s_mov_b64 exec, 1\n"
    "v_mov_b32 v33, s23\n"
    "ds_add_rtn_u32 v33, v32, v33 offset:49416\n"
    "s_mov_b64 exec, -1\n"
    "s_waitcnt lgkmcnt(0)\n"
    "v_readfirstlane_b32 s24, v33\n"
    "s_add_u32 s24, s24, s23\n"
    "s_bitcmp1_b32 s24, 2\n"
    "s_cbranch_scc0 .Ldin_go\n"
    "s_branch .Ldin_ps\n"
    ".p2align 12\n"
    ".Ldin_stub_ps:\n"
    "s_setpc_b64 s[34:35]\n"
    ".Ldin_ps:\n"
    "s_setprio 2\n"
    "v_lshrrev_b32 v40, 3, v1\n"
    "v_and_b32 v41, 7, v0\n"
    "v_lshl_add_u32 v60, v41, 2, v40\n"
    "s_mov_b32 s23, 86912\n"
    "s_mov_b32 exec_lo, 0xff000000\n"
    "v_add_u32 v60, 29, v60\n"
    "s_mov_b32 exec_lo, -1\n"
    "v_lshl_add_u32 v62, v40, 8, s23\n"
    "v_lshl_add_u32 v55, v41, 8, s23\n"
    "ds_read_u8 v44, v60 offset:49472\n"
    "ds_read2_b32 v[46:47], v62 offset0:32 offset1:40\n"
    "ds_read2_b32 v[48:49], v55 offset0:32 offset1:40\n"
    "s_cmp_gt_u32 s24, 255\n"
    "s_cselect_b32 s24, 2.0, 0\n"
    "s_add_u32 s24, s24, 0x801170\n"
    "v_mul_u32_u24 v52, 0x80110, v1\n"
    "v_bfe_u32 v60, v0, 1, 2\n"
    "v_mul_lo_u32 v62, v40, s79\n"
    "v_lshl_add_u32 v52, v60, 16, v52\n"
    "s_lshl1_add_u32 s23, s79, s78\n"
    "v_add_u32 v52, s24, v52\n"
    "v_add3_u32 v62, v62, v41, s23\n"
    "v_and_b32 v42, 56, v0\n"
    "v_cmp_gt_u32 vcc, s74, v62\n"
    "v_lshlrev_b32 v43, 3, v41\n"
    "s_waitcnt lgkmcnt(2)\n"
    "v_add_u32 v44, 15, v44\n"
    "v_bfrev_b32 v60, 1\n"
    "v_lshrrev_b32 v44, 4, v44\n"
    "v_cndmask_b32 v52, v60, v52, vcc\n"
    "v_max_u32 v44, 6, v44\n"
    "v_lshl_or_b32 v52, v44, 25, v52\n"
    "v_add_u32_e64 v44, -5, v44\n"
    "v_cndmask_b32 v44, 0, v44, vcc\n"
    "v_lshl_or_b32 v53, v44, 3, v41\n"
    "s_waitcnt lgkmcnt(0)\n"
    "v_bfe_u32 v46, v46, 25, 5\n"
    "v_bfe_u32 v47, v47, 25, 5\n"
    "v_mov_b32_dpp v54, v53 row_half_mirror row_mask:0xf bank_mask:0xf bound_ctrl:1\n"
    "v_sub_u32_dpp v64, v53, v53 quad_perm:[1,0,3,2] row_mask:0xf bank_mask:0xf bound_ctrl:1\n"
    "v_sub_u32_dpp v65, v53, v53 quad_perm:[2,3,0,1] row_mask:0xf bank_mask:0xf bound_ctrl:1\n"
    "v_sub_u32_dpp v66, v53, v53 quad_perm:[3,2,1,0] row_mask:0xf bank_mask:0xf bound_ctrl:1\n"
    "v_sub_u32 v67, v54, v53\n"
    "v_sub_u32_dpp v68, v54, v53 quad_perm:[1,0,3,2] row_mask:0xf bank_mask:0xf bound_ctrl:1\n"
    "v_sub_u32_dpp v69, v54, v53 quad_perm:[2,3,0,1] row_mask:0xf bank_mask:0xf bound_ctrl:1\n"
    "v_sub_u32_dpp v70, v54, v53 quad_perm:[3,2,1,0] row_mask:0xf bank_mask:0xf bound_ctrl:1\n"
    "v_lshrrev_b32 v56, 31, v64\n"
    "v_alignbit_b32 v56, v56, v65, 31\n"
    "v_alignbit_b32 v56, v56, v66, 31\n"
    "v_alignbit_b32 v56, v56, v67, 31\n"
    "v_alignbit_b32 v56, v56, v68, 31\n"
    "v_alignbit_b32 v56, v56, v69, 31\n"
    "v_alignbit_b32 v56, v56, v70, 31\n"
    "v_bcnt_u32_b32 v56, v56, -7\n"
    "v_mul_i32_i24 v56, -4, v56\n"
    "v_lshlrev_b32 v44, v56, v44\n"
    "v_lshl_add_u32 v56, v42, 2, v56\n"
    "ds_permute_b32 v52, v56, v52\n"
    "v_or_b32_dpp v44, v44, v44 quad_perm:[1,0,3,2] row_mask:0xf bank_mask:0xf bound_ctrl:1\n"
    "v_and_b32_e64 v50, 4, v40\n"
    "v_mad_u32_u24 v50, v50, 8, v40\n"
    "v_or_b32_dpp v44, v44, v44 quad_perm:[2,3,0,1] row_mask:0xf bank_mask:0xf bound_ctrl:1\n"
    "v_lshl_add_u32 v50, v46, 3, v50\n"
    "v_bfe_u32 v48, v48, 25, 5\n"
    "v_or_b32_dpp v44, v44, v44 row_half_mirror row_mask:0xf bank_mask:0xf bound_ctrl:1\n"
    "v_bfe_u32 v49, v49, 25, 5\n"
    "v_and_b32_e64 v51, 4, v41\n"
    "v_mad_u32_u24 v51, v51, 8, v41\n"
    "v_lshl_add_u32 v51, v48, 3, v51\n"
    "v_readlane_b32 s4, v44, 0\n"
    "v_readlane_b32 s5, v44, 8\n"
    "v_readlane_b32 s6, v44, 16\n"
    "v_readlane_b32 s7, v44, 24\n"
    "v_readlane_b32 s8, v44, 32\n"
    "v_readlane_b32 s9, v44, 40\n"
    "v_cmp_eq_u32 vcc, s20, v40\n"
    "s_nop 1\n"
    "v_cndmask_b32 v60, 0, 16, vcc\n"
    "v_add_u32 v50, v50, v60\n"
    "v_cmp_eq_u32 vcc, s20, v41\n"
    "s_nop 1\n"
    "v_cndmask_b32 v60, 0, 16, vcc\n"
    "v_add_u32 v51, v51, v60\n"
    "v_add_u32 v57, 40, v51\n"
    "v_lshl_add_u32 v50, v47, 3, v50\n"
    "v_lshl_add_u32 v51, v49, 3, v51\n"
    "v_cmp_lt_u32 vcc, v51, v50\n"
    "s_lshl_b64 exec, 0xff, 8\n"
    "v_mov_b32 v57, v51\n"
    "s_mov_b64 exec, -1\n"
    "v_lshrrev_b64 v[62:63], v43, vcc\n"
    "v_lshrrev_b64 v[60:61], v42, vcc\n"
    "v_and_b32 v62, 0xff, v62\n"
    "v_and_b32 v60, 0xff, v60\n"
    "v_bcnt_u32_b32 v62, v62, 0\n"
    "v_bcnt_u32_b32 v60, v60, 0\n"
    "v_lshlrev_b32 v62, 2, v62\n"
    "v_lshlrev_b32 v60, 2, v60\n"
    "v_bfe_u32 v63, s4, v62, 4\n"
    "v_bfe_u32 v61, s4, v60, 4\n"
    "s_lshl_b64 exec, 0xff, 0\n"
    "v_lshl_add_u32 v58, v42, 2, v62\n"
    "s_mov_b64 exec, -1\n"
    "v_lshl_add_u32 v51, v63, 3, v51\n"
    "v_lshl_add_u32 v50, v61, 3, v50\n"
    "v_cmp_lt_u32 vcc, v51, v50\n"
    "s_lshl_b64 exec, 0xff, 16\n"
    "v_mov_b32 v57, v51\n"
    "s_mov_b64 exec, -1\n"
    "v_lshrrev_b64 v[62:63], v43, vcc\n"
    "v_lshrrev_b64 v[60:61], v42, vcc\n"
    "v_and_b32 v62, 0xff, v62\n"
    "v_and_b32 v60, 0xff, v60\n"
    "v_bcnt_u32_b32 v62, v62, 0\n"
    "v_bcnt_u32_b32 v60, v60, 0\n"
    "v_lshlrev_b32 v62, 2, v62\n"
    "v_lshlrev_b32 v60, 2, v60\n"
    "v_bfe_u32 v63, s5, v62, 4\n"
    "v_bfe_u32 v61, s5, v60, 4\n"
    "s_lshl_b64 exec, 0xff, 8\n"
    "v_lshl_add_u32 v58, v42, 2, v62\n"
    "s_mov_b64 exec, -1\n"
    "v_lshl_add_u32 v51, v63, 3, v51\n"
    "v_lshl_add_u32 v50, v61, 3, v50\n"
    "v_cmp_lt_u32 vcc, v51, v50\n"
    "s_lshl_b64 exec, 0xff, 24\n"
    "v_mov_b32 v57, v51\n"
    "s_mov_b64 exec, -1\n"
    "v_lshrrev_b64 v[62:63], v43, vcc\n"
    "v_lshrrev_b64 v[60:61], v42, vcc\n"
    "v_and_b32 v62, 0xff, v62\n"
    "v_and_b32 v60, 0xff, v60\n"
    "v_bcnt_u32_b32 v62, v62, 0\n"
    "v_bcnt_u32_b32 v60, v60, 0\n"
    "v_lshlrev_b32 v62, 2, v62\n"
    "v_lshlrev_b32 v60, 2, v60\n"
    "v_bfe_u32 v63, s6, v62, 4\n"
    "v_bfe_u32 v61, s6, v60, 4\n"
    "s_lshl_b64 exec, 0xff, 16\n"
    "v_lshl_add_u32 v58, v42, 2, v62\n"
    "s_mov_b64 exec, -1\n"
    "v_lshl_add_u32 v51, v63, 3, v51\n"
    "v_lshl_add_u32 v50, v61, 3, v50\n"
    "v_cmp_lt_u32 vcc, v51, v50\n"
    "s_lshl_b64 exec, 0xff, 32\n"
    "v_mov_b32 v57, v51\n"
    "s_mov_b64 exec, -1\n"
    "v_lshrrev_b64 v[62:63], v43, vcc\n"
    "v_lshrrev_b64 v[60:61], v42, vcc\n"
    "v_and_b32 v62, 0xff, v62\n"
    "v_and_b32 v60, 0xff, v60\n"
    "v_bcnt_u32_b32 v62, v62, 0\n"
    "v_bcnt_u32_b32 v60, v60, 0\n"
    "v_lshlrev_b32 v62, 2, v62\n"
    "v_lshlrev_b32 v60, 2, v60\n"
    "v_bfe_u32 v63, s7, v62, 4\n"
    "v_bfe_u32 v61, s7, v60, 4\n"
    "s_lshl_b64 exec, 0xff, 24\n"
    "v_lshl_add_u32 v58, v42, 2, v62\n"
    "s_mov_b64 exec, -1\n"
    "v_lshl_add_u32 v51, v63, 3, v51\n"
    "v_lshl_add_u32 v50, v61, 3, v50\n"
    "v_cmp_lt_u32 vcc, v51, v50\n"
    "s_lshl_b64 exec, 0xff, 40\n"
    "v_mov_b32 v57, v51\n"
    "s_mov_b64 exec, -1\n"
    "v_lshrrev_b64 v[62:63], v43, vcc\n"
    "v_lshrrev_b64 v[60:61], v42, vcc\n"
    "v_and_b32 v62, 0xff, v62\n"
    "v_and_b32 v60, 0xff, v60\n"
    "v_bcnt_u32_b32 v62, v62, 0\n"
    "v_bcnt_u32_b32 v60, v60, 0\n"
    "v_lshlrev_b32 v62, 2, v62\n"
    "v_lshlrev_b32 v60, 2, v60\n"
    "v_bfe_u32 v63, s8, v62, 4\n"
    "v_bfe_u32 v61, s8, v60, 4\n"
    "s_lshl_b64 exec, 0xff, 32\n"
    "v_lshl_add_u32 v58, v42, 2, v62\n"
    "s_mov_b64 exec, -1\n"
    "v_lshl_add_u32 v51, v63, 3, v51\n"
    "v_lshl_add_u32 v50, v61, 3, v50\n"
    "v_cmp_lt_u32 vcc, v51, v50\n"
    "s_nop 1\n"
    "v_lshrrev_b64 v[62:63], v43, vcc\n"
    "v_and_b32 v62, 0xff, v62\n"
    "v_bcnt_u32_b32 v62, v62, 0\n"
    "v_lshlrev_b32 v62, 2, v62\n"
    "v_bfe_u32 v63, s9, v62, 4\n"
    "s_lshl_b64 exec, 0xff, 40\n"
    "v_lshl_add_u32 v58, v42, 2, v62\n"
    "s_mov_b64 exec, -1\n"
    "s_waitcnt lgkmcnt(0)\n"
    "ds_bpermute_b32 v52, v58, v52\n"
    "v_lshl_add_u32 v60, v63, 3, v51\n"
    "v_sub_u32 v57, v51, v57\n"
    "v_sub_u32 v50, 6, v40\n"
    "v_add_u32_dpp v60, v60, v60 quad_perm:[1,0,3,2] row_mask:0xf bank_mask:0xf bound_ctrl:1\n"
    "v_mad_u32_u24 v57, v50, 40, v57\n"
    "s_mov_b32 exec_lo, 0xeafffff\n"
    "s_mov_b32 exec_hi, 0x80c\n"
    "v_add_u32 v57, -8, v57\n"
    "s_mov_b64 exec, -1\n"
    "v_mul_i32_i24 v50, 8, v50\n"
    "v_add_u32_dpp v60, v60, v60 quad_perm:[2,3,0,1] row_mask:0xf bank_mask:0xf bound_ctrl:1\n"
    "v_cvt_f32_u32 v54, v57\n"
    "v_mul_u32_u24 v57, 105, v57\n"
    "v_add_u32_dpp v60, v60, v60 row_half_mirror row_mask:0xf bank_mask:0xf bound_ctrl:1\n"
    "v_rcp_f32 v54, v54\n"
    "v_mad_u32_u24 v50, v63, 30, v50\n"
    "v_lshrrev_b32 v60, 3, v60\n"
    "v_lshrrev_b32 v57, 4, v57\n"
    "v_add_u32 v60, 219, v60\n"
    "v_mul_f32 v54, 0x49000000, v54\n"
    "v_lshrrev_b32 v56, 5, v60\n"
    "v_cmp_lt_u32 vcc, 0, v63\n"
    "v_cvt_u32_f32 v54, v54\n"
    "v_sub_u32 v44, v60, v56\n"
    "v_add_u32 v60, v60, v56\n"
    "v_cndmask_b32 v54, 0, v54, vcc\n"
    "v_med3_u32 v60, s19, v44, v60\n"
    "v_and_b32 v54, s18, v54\n"
    "v_mul_u32_u24 v60, 401, v60\n"
    "s_add_u32 s23, s76, 360\n"
    "v_add_u32 v50, v50, v57\n"
    "s_mov_b32 s4, 0x4b4b4b00\n"
    "s_mov_b32 s5, 0x284b\n"
    "v_lshrrev_b64 v[62:63], v42, s[4:5]\n"
    "s_mov_b32 s6, 0xffff1111\n"
    "v_lshlrev_b32 v57, 2, v41\n"
    "v_and_b32 v62, 0xff, v62\n"
    "v_bfe_i32 v57, s6, v57, 4\n"
    "v_mad_i32_i24 v50, v62, v57, v50\n"
    "v_lshrrev_b32 v60, 6, v60\n"
    "v_add_u32 v56, 16, v42\n"
    "v_sub_u32 v60, v60, v50\n"
    "v_and_b32 v44, 24, v56\n"
    "v_add_u32 v60, s23, v60\n"
    "v_lshl_add_u32 v55, v44, 2, v55\n"
    "v_and_b32 v60, 0xffff, v60\n"
    "v_lshrrev_b32 v56, 5, v56\n"
    "v_lshl_or_b32 v53, v54, 16, v60\n"
    "v_lshl_add_u32 v55, v56, 3, v55\n"
    "s_mov_b32 exec_hi, 0xffff\n"
    "s_waitcnt lgkmcnt(0)\n"
    "ds_write_b64 v55, v[52:53] offset:128\n"
    "s_mov_b32 exec_hi, -1\n"
    "s_branch .Ldin_go\n"
    ".Ldin_trimo:\n"
    "v_add_u32 v66, 4, v3\n"
    "v_cmp_gt_u32 vcc, s14, v66\n"
    "s_nop 1\n"
    "v_cndmask_b32 v41, 0, v41, vcc\n"
    "v_cndmask_b32 v45, 0, v45, vcc\n"
    "v_add_u32 v66, 8, v3\n"
    "v_cmp_gt_u32 vcc, s14, v66\n"
    "s_nop 1\n"
    "v_cndmask_b32 v42, 0, v42, vcc\n"
    "v_cndmask_b32 v46, 0, v46, vcc\n"
    "v_add_u32 v66, 12, v3\n"
    "v_cmp_gt_u32 vcc, s14, v66\n"
    "s_nop 1\n"
    "v_cndmask_b32 v43, 0, v43, vcc\n"
    "v_cndmask_b32 v47, 0, v47, vcc\n"
    "s_branch .Ldin_trimo_r\n"
    ".Ldin_trimy:\n"
    "v_add_u32 v32, 4, v3\n"
    "v_cmp_gt_u32 vcc, s14, v32\n"
    "s_nop 1\n"
    "v_cndmask_b32 v97, 0, v97, vcc\n"
    "v_cndmask_b32 v101, 0, v101, vcc\n"
    "v_cndmask_b32 v105, 0, v105, vcc\n"
    "v_cndmask_b32 v109, 0, v109, vcc\n"
    "v_cndmask_b32 v113, 0, v113, vcc\n"
    "v_cndmask_b32 v117, 0, v117, vcc\n"
    "v_add_u32 v32, 8, v3\n"
    "v_cmp_gt_u32 vcc, s14, v32\n"
    "s_nop 1\n"
    "v_cndmask_b32 v98, 0, v98, vcc\n"
    "v_cndmask_b32 v102, 0, v102, vcc\n"
    "v_cndmask_b32 v106, 0, v106, vcc\n"
    "v_cndmask_b32 v110, 0, v110, vcc\n"
    "v_cndmask_b32 v114, 0, v114, vcc\n"
    "v_cndmask_b32 v118, 0, v118, vcc\n"
    "v_add_u32 v32, 12, v3\n"
    "v_cmp_gt_u32 vcc, s14, v32\n"
    "s_nop 1\n"
    "v_cndmask_b32 v99, 0, v99, vcc\n"
    "v_cndmask_b32 v103, 0, v103, vcc\n"
    "v_cndmask_b32 v107, 0, v107, vcc\n"
    "v_cndmask_b32 v111, 0, v111, vcc\n"
    "v_cndmask_b32 v115, 0, v115, vcc\n"
    "v_cndmask_b32 v119, 0, v119, vcc\n"
    "s_branch .Ldin_trimy_r\n"
    ".Ldin_A0s:\n"
    "v_add_u32 v251, s66, v244\n"
    "v_readlane_b32 s60, v216, s59\n"
    "v_readlane_b32 s61, v217, s59\n"
    "v_mfma_f32_4x4x4_16b_bf16 v[224:227], v[228:229], v[208:209], v[224:227] cbsz:4 abid:0\n"
    "v_exp_f32 v160, v160 clamp\n"
    "v_exp_f32 v161, v161 clamp\n"
    "v_mfma_f32_4x4x4_16b_bf16 v[224:227], v[228:229], v[210:211], v[224:227] cbsz:4 abid:1\n"
    "v_exp_f32 v162, v162 clamp\n"
    "v_exp_f32 v163, v163 clamp\n"
    "v_mfma_f32_4x4x4_16b_bf16 v[224:227], v[228:229], v[212:213], v[224:227] cbsz:4 abid:2\n"
    "v_exp_f32 v164, v164 clamp\n"
    "v_exp_f32 v165, v165 clamp\n"
    "v_mfma_f32_4x4x4_16b_bf16 v[224:227], v[228:229], v[214:215], v[224:227] cbsz:4 abid:3\n"
    "s_waitcnt lgkmcnt(0)\n"
    "v_mfma_f32_16x16x32_bf16 v[176:179], v[0:3], v[192:195], v[144:147]\n"
    "s_setprio 1\n"
    "v_exp_f32 v166, v166 clamp\n"
    "v_mfma_f32_16x16x32_bf16 v[176:179], v[4:7], v[196:199], v[176:179]\n"
    "s_cmp_eq_u32 s60, -1\n"
    "v_exp_f32 v167, v167 clamp\n"
    "v_mfma_f32_16x16x32_bf16 v[180:183], v[8:11], v[192:195], v[148:151]\n"
    "s_cbranch_scc1 .Ldin_A0s_poll\n"
    ".Ldin_A0s_pr:\n"
    "v_exp_f32 v168, v168 clamp\n"
    "v_mfma_f32_16x16x32_bf16 v[180:183], v[12:15], v[196:199], v[180:183]\n"
    "s_lshr_b32 s94, s60, 12\n"
    "v_exp_f32 v169, v169 clamp\n"
    "v_mfma_f32_16x16x32_bf16 v[184:187], v[16:19], v[192:195], v[152:155]\n"
    "s_and_b32 s94, s94, 0x1ff0\n"
    "v_exp_f32 v170, v170 clamp\n"
    "v_mfma_f32_16x16x32_bf16 v[184:187], v[20:23], v[196:199], v[184:187]\n"
    "s_mov_b32 m0, s62\n"
    "v_exp_f32 v171, v171 clamp\n"
    "v_mfma_f32_16x16x32_bf16 v[188:191], v[24:27], v[192:195], v[156:159]\n"
    "s_add_u32 s66, s66, 16\n"
    "v_exp_f32 v172, v172 clamp\n"
    "v_mfma_f32_16x16x32_bf16 v[188:191], v[28:31], v[196:199], v[188:191]\n"
    "s_and_b32 s64, s60, 0xffff\n"
    "v_xor_b32 v252, s94, v250\n"
    "v_exp_f32 v173, v173 clamp\n"
    "v_exp_f32 v174, v174 clamp\n"
    "v_exp_f32 v175, v175 clamp\n"
    "v_pk_fma_f32 v[160:161], v[160:161], s[70:71], v[128:129]\n"
    "v_pk_fma_f32 v[162:163], v[162:163], s[70:71], v[130:131]\n"
    "v_pk_add_f32 v[230:231], v[160:161], v[162:163]\n"
    "v_pk_mul_f32 v[232:233], v[160:161], v[162:163]\n"
    "v_pk_add_f32 v[218:219], v[224:225], v[226:227]\n"
    "v_mov_b64 v[224:225], 0\n"
    "v_mov_b64 v[226:227], 0\n"
    "v_pk_fma_f32 v[164:165], v[164:165], s[70:71], v[132:133]\n"
    "v_pk_fma_f32 v[230:231], v[230:231], v[164:165], v[232:233]\n"
    "v_pk_mul_f32 v[232:233], v[232:233], v[164:165]\n"
    "v_pk_fma_f32 v[166:167], v[166:167], s[70:71], v[134:135]\n"
    "v_pk_fma_f32 v[230:231], v[230:231], v[166:167], v[232:233]\n"
    "v_pk_mul_f32 v[232:233], v[232:233], v[166:167]\n"
    "v_pk_fma_f32 v[168:169], v[168:169], s[70:71], v[136:137]\n"
    "v_pk_fma_f32 v[230:231], v[230:231], v[168:169], v[232:233]\n"
    "v_pk_mul_f32 v[232:233], v[232:233], v[168:169]\n"
    "v_pk_fma_f32 v[170:171], v[170:171], s[70:71], v[138:139]\n"
    "v_pk_fma_f32 v[230:231], v[230:231], v[170:171], v[232:233]\n"
    "v_pk_mul_f32 v[232:233], v[232:233], v[170:171]\n"
    "v_pk_fma_f32 v[172:173], v[172:173], s[70:71], v[140:141]\n"
    "v_pk_fma_f32 v[230:231], v[230:231], v[172:173], v[232:233]\n"
    "v_pk_mul_f32 v[232:233], v[232:233], v[172:173]\n"
    "v_pk_fma_f32 v[174:175], v[174:175], s[70:71], v[142:143]\n"
    "v_pk_fma_f32 v[230:231], v[230:231], v[174:175], v[232:233]\n"
    "v_pk_mul_f32 v[232:233], v[232:233], v[174:175]\n"
    "v_rcp_f32 v232, v232\n"
    "v_rcp_f32 v233, v233\n"
    "v_fma_f32 v230, v230, v232, v247\n"
    "v_fmac_f32 v230, v231, v233\n"
    "v_mbcnt_lo_u32_b32 v232, -1, 0\n"
    "v_mbcnt_hi_u32_b32 v232, -1, v232\n"
    "v_bfe_u32 v233, v232, 3, 1\n"
    "v_and_b32 v232, 7, v232\n"
    "v_lshl_add_u32 v232, v232, 1, v233\n"
    "v_add_u32 v232, s81, v232\n"
    "ds_read_u8 v232, v232 offset:49664\n"
    "s_add_u32 s81, s81, 16\n"
    "s_waitcnt lgkmcnt(0)\n"
    "buffer_load_ushort v232, v232, s[44:47], s63 idxen\n"
    "s_waitcnt vmcnt(0)\n"
    "v_lshlrev_b32 v232, 16, v232\n"
    "v_mul_f32 v230, v230, v232\n"
    "v_add_f32 v218, v218, v219\n"
    "s_setprio 0\n"
    "ds_read_b64_tr_b16 v[208:209], v240 offset:2048\n"
    "ds_read_b64_tr_b16 v[210:211], v241 offset:2048\n"
    "ds_read_b64_tr_b16 v[212:213], v242 offset:2048\n"
    "ds_read_b64_tr_b16 v[214:215], v243 offset:2048\n"
    "ds_read_u8 v236, v251 offset:0\n"
    "ds_read_u8 v238, v251 offset:1\n"
    "ds_write_b16_d16_hi v245, v230\n"
    "ds_read_b64 v[228:229], v246\n"
    "ds_write_addtid_b32 v218 offset:49152\n"
    "s_add_u32 m0, s68, 2048\n"
    "s_bfe_u32 s94, s60, 0x30013\n"
    "s_add_u32 s58, s58, s57\n"
    "s_mul_i32 s94, s94, s56\n"
    "s_add_u32 s82, s64, s9\n"
    "s_add_u32 s65, s58, s94\n"
    "s_waitcnt vmcnt(2)\n"
    "ds_read_b128 v[192:195], v234 offset:4096\n"
    "ds_read_b128 v[196:199], v235 offset:4096\n"
    "ds_read_b128 v[216:219], v252 offset:0\n"
    "ds_read_b128 v[220:223], v252 offset:64\n"
    "s_sub_u32 s95, s61, s76\n"
    "s_lshr_b32 s94, s61, 16\n"
    "s_sext_i32_i16 s95, s95\n"
    "s_cmp_eq_u32 s59, s10\n"
    "s_cbranch_scc1 .Ldin_A0s_skip\n"
    "s_waitcnt lgkmcnt(7)\n"
    "buffer_load_dwordx4 v[236:237], s[40:43], s67 idxen offen offset:0 nt lds\n"
    "buffer_load_dwordx4 v[238:239], s[40:43], s67 idxen offen offset:1024 nt lds\n"
    ".Ldin_A0s_skr:\n"
    "s_cmp_lt_i32 s95, -550\n"
    "s_cselect_b32 s94, 0, s94\n"
    "s_mul_i32 s95, s95, s94\n"
    "s_bfe_u32 s94, s60, 0x50019\n"
    "s_ashr_i32 s95, s95, 16\n"
    "s_lshl4_add_u32 s83, s94, s64\n"
    "s_max_i32 s95, s95, -14\n"
    "s_add_u32 s59, s59, 1\n"
    "s_min_i32 s95, s95, 10\n"
    "s_add_u32 s72, s77, s95\n"
    "s_cmp_lt_i32 s60, 0\n"
    "s_cselect_b32 s72, 0, s72\n"
    "s_cmp_eq_u32 s66, s80\n"
    "s_cselect_b64 s[66:67], s[64:65], s[66:67]\n"
    "s_waitcnt lgkmcnt(5)\n"
    ".Ldin_G1s:\n"
    "v_add_u32 v251, s66, v244\n"
    "v_mfma_f32_4x4x4_16b_bf16 v[224:227], v[228:229], v[200:201], v[224:227] cbsz:4 abid:0\n"
    "v_exp_f32 v176, v176 clamp\n"
    "v_exp_f32 v177, v177 clamp\n"
    "v_mfma_f32_4x4x4_16b_bf16 v[224:227], v[228:229], v[202:203], v[224:227] cbsz:4 abid:1\n"
    "v_exp_f32 v178, v178 clamp\n"
    "v_exp_f32 v179, v179 clamp\n"
    "v_mfma_f32_4x4x4_16b_bf16 v[224:227], v[228:229], v[204:205], v[224:227] cbsz:4 abid:2\n"
    "v_exp_f32 v180, v180 clamp\n"
    "v_exp_f32 v181, v181 clamp\n"
    "v_mfma_f32_4x4x4_16b_bf16 v[224:227], v[228:229], v[206:207], v[224:227] cbsz:4 abid:3\n"
    "s_waitcnt lgkmcnt(0)\n"
    "v_mfma_f32_16x16x32_bf16 v[160:163], v[0:3], v[192:195], v[144:147]\n"
    "s_setprio 1\n"
    "v_exp_f32 v182, v182 clamp\n"
    "v_mfma_f32_16x16x32_bf16 v[160:163], v[4:7], v[196:199], v[160:163]\n"
    "s_add_u32 m0, s68, 4096\n"
    "v_exp_f32 v183, v183 clamp\n"
    "v_mfma_f32_16x16x32_bf16 v[164:167], v[8:11], v[192:195], v[148:151]\n"
    "s_add_u32 s66, s66, 16\n"
    "v_exp_f32 v184, v184 clamp\n"
    "v_mfma_f32_16x16x32_bf16 v[164:167], v[12:15], v[196:199], v[164:167]\n"
    "v_exp_f32 v185, v185 clamp\n"
    "v_mfma_f32_16x16x32_bf16 v[168:171], v[16:19], v[192:195], v[152:155]\n"
    "v_exp_f32 v186, v186 clamp\n"
    "v_mfma_f32_16x16x32_bf16 v[168:171], v[20:23], v[196:199], v[168:171]\n"
    "v_exp_f32 v187, v187 clamp\n"
    "v_mfma_f32_16x16x32_bf16 v[172:175], v[24:27], v[192:195], v[156:159]\n"
    "v_exp_f32 v188, v188 clamp\n"
    "v_mfma_f32_16x16x32_bf16 v[172:175], v[28:31], v[196:199], v[172:175]\n"
    "s_memrealtime s[34:35]\n"
    "v_exp_f32 v189, v189 clamp\n"
    "v_exp_f32 v190, v190 clamp\n"
    "v_exp_f32 v191, v191 clamp\n"
    "v_pk_fma_f32 v[176:177], v[176:177], s[70:71], v[128:129]\n"
    "v_pk_fma_f32 v[178:179], v[178:179], s[70:71], v[130:131]\n"
    "v_pk_add_f32 v[230:231], v[176:177], v[178:179]\n"
    "v_pk_mul_f32 v[232:233], v[176:177], v[178:179]\n"
    "v_pk_fma_f32 v[180:181], v[180:181], s[70:71], v[132:133]\n"
    "v_pk_fma_f32 v[230:231], v[230:231], v[180:181], v[232:233]\n"
    "v_pk_mul_f32 v[232:233], v[232:233], v[180:181]\n"
    "v_pk_fma_f32 v[182:183], v[182:183], s[70:71], v[134:135]\n"
    "v_pk_fma_f32 v[230:231], v[230:231], v[182:183], v[232:233]\n"
    "v_pk_mul_f32 v[232:233], v[232:233], v[182:183]\n"
    "v_pk_fma_f32 v[184:185], v[184:185], s[70:71], v[136:137]\n"
    "v_pk_fma_f32 v[230:231], v[230:231], v[184:185], v[232:233]\n"
    "v_pk_mul_f32 v[232:233], v[232:233], v[184:185]\n"
    "v_pk_fma_f32 v[186:187], v[186:187], s[70:71], v[138:139]\n"
    "v_pk_fma_f32 v[230:231], v[230:231], v[186:187], v[232:233]\n"
    "v_pk_mul_f32 v[232:233], v[232:233], v[186:187]\n"
    "v_pk_fma_f32 v[188:189], v[188:189], s[70:71], v[140:141]\n"
    "v_pk_fma_f32 v[230:231], v[230:231], v[188:189], v[232:233]\n"
    "v_pk_mul_f32 v[232:233], v[232:233], v[188:189]\n"
    "v_pk_fma_f32 v[190:191], v[190:191], s[70:71], v[142:143]\n"
    "v_pk_fma_f32 v[230:231], v[230:231], v[190:191], v[232:233]\n"
    "v_pk_mul_f32 v[232:233], v[232:233], v[190:191]\n"
    "v_rcp_f32 v232, v232\n"
    "v_rcp_f32 v233, v233\n"
    "v_fma_f32 v230, v230, v232, v247\n"
    "v_fmac_f32 v230, v231, v233\n"
    "v_mbcnt_lo_u32_b32 v232, -1, 0\n"
    "v_mbcnt_hi_u32_b32 v232, -1, v232\n"
    "v_bfe_u32 v233, v232, 3, 1\n"
    "v_and_b32 v232, 7, v232\n"
    "v_lshl_add_u32 v232, v232, 1, v233\n"
    "v_add_u32 v232, s81, v232\n"
    "ds_read_u8 v232, v232 offset:49664\n"
    "s_add_u32 s81, s81, 16\n"
    "s_waitcnt lgkmcnt(0)\n"
    "buffer_load_ushort v232, v232, s[44:47], s63 idxen\n"
    "s_waitcnt vmcnt(0)\n"
    "v_lshlrev_b32 v232, 16, v232\n"
    "v_mul_f32 v230, v230, v232\n"
    "s_setprio 0\n"
    "s_waitcnt lgkmcnt(0)\n"
    "ds_read_b64_tr_b16 v[200:201], v240 offset:4096\n"
    "ds_read_b64_tr_b16 v[202:203], v241 offset:4096\n"
    "ds_read_b64_tr_b16 v[204:205], v242 offset:4096\n"
    "ds_read_b64_tr_b16 v[206:207], v243 offset:4096\n"
    "ds_read_u8 v236, v251 offset:0\n"
    "ds_read_u8 v238, v251 offset:1\n"
    "ds_write_b16_d16_hi v245, v230\n"
    "ds_read_b64 v[228:229], v246\n"
    "s_cmp_lt_u32 s34, s76\n"
    "s_cbranch_scc1 .Ldin_G1s_wait\n"
    ".Ldin_G1s_go:\n"
    "s_waitcnt vmcnt(2)\n"
    "ds_read_b128 v[192:195], v234 offset:6144\n"
    "ds_read_b128 v[196:199], v235 offset:6144\n"
    "s_waitcnt lgkmcnt(4)\n"
    "buffer_load_dwordx4 v[236:237], s[40:43], s67 idxen offen offset:0 nt lds\n"
    "buffer_load_dwordx4 v[238:239], s[40:43], s67 idxen offen offset:1024 nt lds\n"
    "s_add_u32 s76, s76, s72\n"
    "s_cmp_eq_u32 s66, s80\n"
    "s_cselect_b64 s[66:67], s[64:65], s[66:67]\n"
    "s_waitcnt lgkmcnt(2)\n"
    "s_cmp_eq_u32 s66, s82\n"
    "s_cbranch_scc1 .Ldin_L2s\n"
    ".Ldin_G2s:\n"
    "v_add_u32 v251, s66, v244\n"
    "v_mfma_f32_4x4x4_16b_bf16 v[224:227], v[228:229], v[208:209], v[224:227] cbsz:4 abid:0\n"
    "v_exp_f32 v160, v160 clamp\n"
    "v_exp_f32 v161, v161 clamp\n"
    "v_mfma_f32_4x4x4_16b_bf16 v[224:227], v[228:229], v[210:211], v[224:227] cbsz:4 abid:1\n"
    "v_exp_f32 v162, v162 clamp\n"
    "v_exp_f32 v163, v163 clamp\n"
    "v_mfma_f32_4x4x4_16b_bf16 v[224:227], v[228:229], v[212:213], v[224:227] cbsz:4 abid:2\n"
    "v_exp_f32 v164, v164 clamp\n"
    "v_exp_f32 v165, v165 clamp\n"
    "v_mfma_f32_4x4x4_16b_bf16 v[224:227], v[228:229], v[214:215], v[224:227] cbsz:4 abid:3\n"
    "s_waitcnt lgkmcnt(0)\n"
    "v_mfma_f32_16x16x32_bf16 v[176:179], v[0:3], v[192:195], v[144:147]\n"
    "s_setprio 1\n"
    "v_exp_f32 v166, v166 clamp\n"
    "v_mfma_f32_16x16x32_bf16 v[176:179], v[4:7], v[196:199], v[176:179]\n"
    "s_add_u32 m0, s68, 6144\n"
    "v_exp_f32 v167, v167 clamp\n"
    "v_mfma_f32_16x16x32_bf16 v[180:183], v[8:11], v[192:195], v[148:151]\n"
    "s_add_u32 s66, s66, 16\n"
    "v_exp_f32 v168, v168 clamp\n"
    "v_mfma_f32_16x16x32_bf16 v[180:183], v[12:15], v[196:199], v[180:183]\n"
    "v_exp_f32 v169, v169 clamp\n"
    "v_mfma_f32_16x16x32_bf16 v[184:187], v[16:19], v[192:195], v[152:155]\n"
    "v_exp_f32 v170, v170 clamp\n"
    "v_mfma_f32_16x16x32_bf16 v[184:187], v[20:23], v[196:199], v[184:187]\n"
    "v_exp_f32 v171, v171 clamp\n"
    "v_mfma_f32_16x16x32_bf16 v[188:191], v[24:27], v[192:195], v[156:159]\n"
    "v_exp_f32 v172, v172 clamp\n"
    "v_mfma_f32_16x16x32_bf16 v[188:191], v[28:31], v[196:199], v[188:191]\n"
    "s_memrealtime s[34:35]\n"
    "v_exp_f32 v173, v173 clamp\n"
    "v_exp_f32 v174, v174 clamp\n"
    "v_exp_f32 v175, v175 clamp\n"
    "v_pk_fma_f32 v[160:161], v[160:161], s[70:71], v[128:129]\n"
    "v_pk_fma_f32 v[162:163], v[162:163], s[70:71], v[130:131]\n"
    "v_pk_add_f32 v[230:231], v[160:161], v[162:163]\n"
    "v_pk_mul_f32 v[232:233], v[160:161], v[162:163]\n"
    "v_pk_fma_f32 v[164:165], v[164:165], s[70:71], v[132:133]\n"
    "v_pk_fma_f32 v[230:231], v[230:231], v[164:165], v[232:233]\n"
    "v_pk_mul_f32 v[232:233], v[232:233], v[164:165]\n"
    "v_pk_fma_f32 v[166:167], v[166:167], s[70:71], v[134:135]\n"
    "v_pk_fma_f32 v[230:231], v[230:231], v[166:167], v[232:233]\n"
    "v_pk_mul_f32 v[232:233], v[232:233], v[166:167]\n"
    "v_pk_fma_f32 v[168:169], v[168:169], s[70:71], v[136:137]\n"
    "v_pk_fma_f32 v[230:231], v[230:231], v[168:169], v[232:233]\n"
    "v_pk_mul_f32 v[232:233], v[232:233], v[168:169]\n"
    "v_pk_fma_f32 v[170:171], v[170:171], s[70:71], v[138:139]\n"
    "v_pk_fma_f32 v[230:231], v[230:231], v[170:171], v[232:233]\n"
    "v_pk_mul_f32 v[232:233], v[232:233], v[170:171]\n"
    "v_pk_fma_f32 v[172:173], v[172:173], s[70:71], v[140:141]\n"
    "v_pk_fma_f32 v[230:231], v[230:231], v[172:173], v[232:233]\n"
    "v_pk_mul_f32 v[232:233], v[232:233], v[172:173]\n"
    "v_pk_fma_f32 v[174:175], v[174:175], s[70:71], v[142:143]\n"
    "v_pk_fma_f32 v[230:231], v[230:231], v[174:175], v[232:233]\n"
    "v_pk_mul_f32 v[232:233], v[232:233], v[174:175]\n"
    "v_rcp_f32 v232, v232\n"
    "v_rcp_f32 v233, v233\n"
    "v_fma_f32 v230, v230, v232, v247\n"
    "v_fmac_f32 v230, v231, v233\n"
    "v_mbcnt_lo_u32_b32 v232, -1, 0\n"
    "v_mbcnt_hi_u32_b32 v232, -1, v232\n"
    "v_bfe_u32 v233, v232, 3, 1\n"
    "v_and_b32 v232, 7, v232\n"
    "v_lshl_add_u32 v232, v232, 1, v233\n"
    "v_add_u32 v232, s81, v232\n"
    "ds_read_u8 v232, v232 offset:49664\n"
    "s_add_u32 s81, s81, 16\n"
    "s_waitcnt lgkmcnt(0)\n"
    "buffer_load_ushort v232, v232, s[44:47], s63 idxen\n"
    "s_waitcnt vmcnt(0)\n"
    "v_lshlrev_b32 v232, 16, v232\n"
    "v_mul_f32 v230, v230, v232\n"
    "s_setprio 0\n"
    "s_waitcnt lgkmcnt(0)\n"
    "ds_read_b64_tr_b16 v[208:209], v240 offset:6144\n"
    "ds_read_b64_tr_b16 v[210:211], v241 offset:6144\n"
    "ds_read_b64_tr_b16 v[212:213], v242 offset:6144\n"
    "ds_read_b64_tr_b16 v[214:215], v243 offset:6144\n"
    "ds_read_u8 v236, v251 offset:0\n"
    "ds_read_u8 v238, v251 offset:1\n"
    "ds_write_b16_d16_hi v245, v230\n"
    "ds_read_b64 v[228:229], v246\n"
    "s_cmp_lt_u32 s34, s76\n"
    "s_cbranch_scc1 .Ldin_G2s_wait\n"
    ".Ldin_G2s_go:\n"
    "s_waitcnt vmcnt(2)\n"
    "ds_read_b128 v[192:195], v234 offset:0\n"
    "ds_read_b128 v[196:199], v235 offset:0\n"
    "s_waitcnt lgkmcnt(4)\n"
    "buffer_load_dwordx4 v[236:237], s[40:43], s67 idxen offen offset:0 nt lds\n"
    "buffer_load_dwordx4 v[238:239], s[40:43], s67 idxen offen offset:1024 nt lds\n"
    "s_sub_u32 s94, s34, 30\n"
    "s_max_u32 s76, s76, s94\n"
    "s_add_u32 s76, s76, s72\n"
    "s_cmp_eq_u32 s66, s80\n"
    "s_cselect_b64 s[66:67], s[64:65], s[66:67]\n"
    "s_waitcnt lgkmcnt(2)\n"
    "s_cmp_eq_u32 s66, s82\n"
    "s_cbranch_scc1 .Ldin_L3s\n"
    ".Ldin_G3s:\n"
    "v_add_u32_e64 v251, s66, v244\n"
    "v_mfma_f32_4x4x4_16b_bf16 v[224:227], v[228:229], v[200:201], v[224:227] cbsz:4 abid:0\n"
    "v_exp_f32 v176, v176 clamp\n"
    "v_exp_f32 v177, v177 clamp\n"
    "v_mfma_f32_4x4x4_16b_bf16 v[224:227], v[228:229], v[202:203], v[224:227] cbsz:4 abid:1\n"
    "v_exp_f32 v178, v178 clamp\n"
    "v_exp_f32 v179, v179 clamp\n"
    "v_mfma_f32_4x4x4_16b_bf16 v[224:227], v[228:229], v[204:205], v[224:227] cbsz:4 abid:2\n"
    "v_exp_f32 v180, v180 clamp\n"
    "v_exp_f32 v181, v181 clamp\n"
    "v_mfma_f32_4x4x4_16b_bf16 v[224:227], v[228:229], v[206:207], v[224:227] cbsz:4 abid:3\n"
    "s_waitcnt lgkmcnt(0)\n"
    "v_mfma_f32_16x16x32_bf16 v[160:163], v[0:3], v[192:195], v[144:147]\n"
    "s_setprio 1\n"
    "v_exp_f32 v182, v182 clamp\n"
    "v_mfma_f32_16x16x32_bf16 v[160:163], v[4:7], v[196:199], v[160:163]\n"
    "s_add_u32 m0, s8, 0\n"
    "v_exp_f32 v183, v183 clamp\n"
    "v_mfma_f32_16x16x32_bf16 v[164:167], v[8:11], v[192:195], v[148:151]\n"
    "s_add_u32 s66, s66, 16\n"
    "v_exp_f32 v184, v184 clamp\n"
    "v_mfma_f32_16x16x32_bf16 v[164:167], v[12:15], v[196:199], v[164:167]\n"
    "v_exp_f32 v185, v185 clamp\n"
    "v_mfma_f32_16x16x32_bf16 v[168:171], v[16:19], v[192:195], v[152:155]\n"
    "v_exp_f32 v186, v186 clamp\n"
    "v_mfma_f32_16x16x32_bf16 v[168:171], v[20:23], v[196:199], v[168:171]\n"
    "v_exp_f32 v187, v187 clamp\n"
    "v_mfma_f32_16x16x32_bf16 v[172:175], v[24:27], v[192:195], v[156:159]\n"
    "v_exp_f32 v188, v188 clamp\n"
    "v_mfma_f32_16x16x32_bf16 v[172:175], v[28:31], v[196:199], v[172:175]\n"
    "s_memrealtime s[34:35]\n"
    "v_exp_f32 v189, v189 clamp\n"
    "v_exp_f32 v190, v190 clamp\n"
    "v_exp_f32 v191, v191 clamp\n"
    "v_pk_fma_f32 v[176:177], v[176:177], s[70:71], v[128:129]\n"
    "v_pk_fma_f32 v[178:179], v[178:179], s[70:71], v[130:131]\n"
    "v_pk_add_f32 v[230:231], v[176:177], v[178:179]\n"
    "v_pk_mul_f32 v[232:233], v[176:177], v[178:179]\n"
    "v_pk_fma_f32 v[180:181], v[180:181], s[70:71], v[132:133]\n"
    "v_pk_fma_f32 v[230:231], v[230:231], v[180:181], v[232:233]\n"
    "v_pk_mul_f32 v[232:233], v[232:233], v[180:181]\n"
    "v_pk_fma_f32 v[182:183], v[182:183], s[70:71], v[134:135]\n"
    "v_pk_fma_f32 v[230:231], v[230:231], v[182:183], v[232:233]\n"
    "v_pk_mul_f32 v[232:233], v[232:233], v[182:183]\n"
    "v_pk_fma_f32 v[184:185], v[184:185], s[70:71], v[136:137]\n"
    "v_pk_fma_f32 v[230:231], v[230:231], v[184:185], v[232:233]\n"
    "v_pk_mul_f32 v[232:233], v[232:233], v[184:185]\n"
    "v_pk_fma_f32 v[186:187], v[186:187], s[70:71], v[138:139]\n"
    "v_pk_fma_f32 v[230:231], v[230:231], v[186:187], v[232:233]\n"
    "v_pk_mul_f32 v[232:233], v[232:233], v[186:187]\n"
    "v_pk_fma_f32 v[188:189], v[188:189], s[70:71], v[140:141]\n"
    "v_pk_fma_f32 v[230:231], v[230:231], v[188:189], v[232:233]\n"
    "v_pk_mul_f32 v[232:233], v[232:233], v[188:189]\n"
    "v_pk_fma_f32 v[190:191], v[190:191], s[70:71], v[142:143]\n"
    "v_pk_fma_f32 v[230:231], v[230:231], v[190:191], v[232:233]\n"
    "v_pk_mul_f32 v[232:233], v[232:233], v[190:191]\n"
    "v_rcp_f32 v232, v232\n"
    "v_rcp_f32 v233, v233\n"
    "v_fma_f32 v230, v230, v232, v247\n"
    "v_fmac_f32 v230, v231, v233\n"
    "v_mbcnt_lo_u32_b32 v232, -1, 0\n"
    "v_mbcnt_hi_u32_b32 v232, -1, v232\n"
    "v_bfe_u32 v233, v232, 3, 1\n"
    "v_and_b32 v232, 7, v232\n"
    "v_lshl_add_u32 v232, v232, 1, v233\n"
    "v_add_u32 v232, s81, v232\n"
    "ds_read_u8 v232, v232 offset:49664\n"
    "s_add_u32 s81, s81, 16\n"
    "s_waitcnt lgkmcnt(0)\n"
    "buffer_load_ushort v232, v232, s[44:47], s63 idxen\n"
    "s_waitcnt vmcnt(0)\n"
    "v_lshlrev_b32 v232, 16, v232\n"
    "v_mul_f32 v230, v230, v232\n"
    "s_setprio 0\n"
    "s_waitcnt lgkmcnt(0)\n"
    "ds_read_b64_tr_b16 v[200:201], v240 offset:0\n"
    "ds_read_b64_tr_b16 v[202:203], v241 offset:0\n"
    "ds_read_b64_tr_b16 v[204:205], v242 offset:0\n"
    "ds_read_b64_tr_b16 v[206:207], v243 offset:0\n"
    "ds_read_u8 v236, v251 offset:0\n"
    "ds_read_u8 v238, v251 offset:1\n"
    "ds_write_b16_d16_hi v245, v230\n"
    "ds_read_b64 v[228:229], v246\n"
    "s_cmp_lt_u32 s34, s76\n"
    "s_cbranch_scc1 .Ldin_G3s_wait\n"
    ".Ldin_G3s_go:\n"
    "s_waitcnt vmcnt(2)\n"
    "ds_read_b128 v[192:195], v234 offset:2048\n"
    "ds_read_b128 v[196:199], v235 offset:2048\n"
    "s_waitcnt lgkmcnt(4)\n"
    "buffer_load_dwordx4 v[236:237], s[40:43], s67 idxen offen offset:0 nt lds\n"
    "buffer_load_dwordx4 v[238:239], s[40:43], s67 idxen offen offset:1024 nt lds\n"
    "s_add_u32 s76, s76, s72\n"
    "s_cmp_eq_u32 s66, s80\n"
    "s_cselect_b64 s[66:67], s[64:65], s[66:67]\n"
    "s_waitcnt lgkmcnt(2)\n"
    "s_cmp_eq_u32 s66, s82\n"
    "s_cbranch_scc1 .Ldin_L0s\n"
    ".Ldin_G0s:\n"
    "v_add_u32 v251, s66, v244\n"
    "v_mfma_f32_4x4x4_16b_bf16 v[224:227], v[228:229], v[208:209], v[224:227] cbsz:4 abid:0\n"
    "v_exp_f32 v160, v160 clamp\n"
    "v_exp_f32 v161, v161 clamp\n"
    "v_mfma_f32_4x4x4_16b_bf16 v[224:227], v[228:229], v[210:211], v[224:227] cbsz:4 abid:1\n"
    "v_exp_f32 v162, v162 clamp\n"
    "v_exp_f32 v163, v163 clamp\n"
    "v_mfma_f32_4x4x4_16b_bf16 v[224:227], v[228:229], v[212:213], v[224:227] cbsz:4 abid:2\n"
    "v_exp_f32 v164, v164 clamp\n"
    "v_exp_f32 v165, v165 clamp\n"
    "v_mfma_f32_4x4x4_16b_bf16 v[224:227], v[228:229], v[214:215], v[224:227] cbsz:4 abid:3\n"
    "s_waitcnt lgkmcnt(0)\n"
    "v_mfma_f32_16x16x32_bf16 v[176:179], v[0:3], v[192:195], v[144:147]\n"
    "s_setprio 1\n"
    "v_exp_f32 v166, v166 clamp\n"
    "v_mfma_f32_16x16x32_bf16 v[176:179], v[4:7], v[196:199], v[176:179]\n"
    "s_add_u32 m0, s68, 2048\n"
    "v_exp_f32 v167, v167 clamp\n"
    "v_mfma_f32_16x16x32_bf16 v[180:183], v[8:11], v[192:195], v[148:151]\n"
    "s_add_u32 s66, s66, 16\n"
    "v_exp_f32 v168, v168 clamp\n"
    "v_mfma_f32_16x16x32_bf16 v[180:183], v[12:15], v[196:199], v[180:183]\n"
    "v_exp_f32 v169, v169 clamp\n"
    "v_mfma_f32_16x16x32_bf16 v[184:187], v[16:19], v[192:195], v[152:155]\n"
    "v_exp_f32 v170, v170 clamp\n"
    "v_mfma_f32_16x16x32_bf16 v[184:187], v[20:23], v[196:199], v[184:187]\n"
    "v_exp_f32 v171, v171 clamp\n"
    "v_mfma_f32_16x16x32_bf16 v[188:191], v[24:27], v[192:195], v[156:159]\n"
    "v_exp_f32 v172, v172 clamp\n"
    "v_mfma_f32_16x16x32_bf16 v[188:191], v[28:31], v[196:199], v[188:191]\n"
    "s_memrealtime s[34:35]\n"
    "v_exp_f32 v173, v173 clamp\n"
    "v_exp_f32 v174, v174 clamp\n"
    "v_exp_f32 v175, v175 clamp\n"
    "v_pk_fma_f32 v[160:161], v[160:161], s[70:71], v[128:129]\n"
    "v_pk_fma_f32 v[162:163], v[162:163], s[70:71], v[130:131]\n"
    "v_pk_add_f32 v[230:231], v[160:161], v[162:163]\n"
    "v_pk_mul_f32 v[232:233], v[160:161], v[162:163]\n"
    "v_pk_fma_f32 v[164:165], v[164:165], s[70:71], v[132:133]\n"
    "v_pk_fma_f32 v[230:231], v[230:231], v[164:165], v[232:233]\n"
    "v_pk_mul_f32 v[232:233], v[232:233], v[164:165]\n"
    "v_pk_fma_f32 v[166:167], v[166:167], s[70:71], v[134:135]\n"
    "v_pk_fma_f32 v[230:231], v[230:231], v[166:167], v[232:233]\n"
    "v_pk_mul_f32 v[232:233], v[232:233], v[166:167]\n"
    "v_pk_fma_f32 v[168:169], v[168:169], s[70:71], v[136:137]\n"
    "v_pk_fma_f32 v[230:231], v[230:231], v[168:169], v[232:233]\n"
    "v_pk_mul_f32 v[232:233], v[232:233], v[168:169]\n"
    "v_pk_fma_f32 v[170:171], v[170:171], s[70:71], v[138:139]\n"
    "v_pk_fma_f32 v[230:231], v[230:231], v[170:171], v[232:233]\n"
    "v_pk_mul_f32 v[232:233], v[232:233], v[170:171]\n"
    "v_pk_fma_f32 v[172:173], v[172:173], s[70:71], v[140:141]\n"
    "v_pk_fma_f32 v[230:231], v[230:231], v[172:173], v[232:233]\n"
    "v_pk_mul_f32 v[232:233], v[232:233], v[172:173]\n"
    "v_pk_fma_f32 v[174:175], v[174:175], s[70:71], v[142:143]\n"
    "v_pk_fma_f32 v[230:231], v[230:231], v[174:175], v[232:233]\n"
    "v_pk_mul_f32 v[232:233], v[232:233], v[174:175]\n"
    "v_rcp_f32 v232, v232\n"
    "v_rcp_f32 v233, v233\n"
    "v_fma_f32 v230, v230, v232, v247\n"
    "v_fmac_f32 v230, v231, v233\n"
    "v_mbcnt_lo_u32_b32 v232, -1, 0\n"
    "v_mbcnt_hi_u32_b32 v232, -1, v232\n"
    "v_bfe_u32 v233, v232, 3, 1\n"
    "v_and_b32 v232, 7, v232\n"
    "v_lshl_add_u32 v232, v232, 1, v233\n"
    "v_add_u32 v232, s81, v232\n"
    "ds_read_u8 v232, v232 offset:49664\n"
    "s_add_u32 s81, s81, 16\n"
    "s_waitcnt lgkmcnt(0)\n"
    "buffer_load_ushort v232, v232, s[44:47], s63 idxen\n"
    "s_waitcnt vmcnt(0)\n"
    "v_lshlrev_b32 v232, 16, v232\n"
    "v_mul_f32 v230, v230, v232\n"
    "s_setprio 0\n"
    "s_waitcnt lgkmcnt(0)\n"
    "ds_read_b64_tr_b16 v[208:209], v240 offset:2048\n"
    "ds_read_b64_tr_b16 v[210:211], v241 offset:2048\n"
    "ds_read_b64_tr_b16 v[212:213], v242 offset:2048\n"
    "ds_read_b64_tr_b16 v[214:215], v243 offset:2048\n"
    "ds_read_u8 v236, v251 offset:0\n"
    "ds_read_u8 v238, v251 offset:1\n"
    "ds_write_b16_d16_hi v245, v230\n"
    "ds_read_b64 v[228:229], v246\n"
    "s_cmp_lt_u32 s34, s76\n"
    "s_cbranch_scc1 .Ldin_G0s_wait\n"
    ".Ldin_G0s_go:\n"
    "s_waitcnt vmcnt(2)\n"
    "ds_read_b128 v[192:195], v234 offset:4096\n"
    "ds_read_b128 v[196:199], v235 offset:4096\n"
    "s_waitcnt lgkmcnt(4)\n"
    "buffer_load_dwordx4 v[236:237], s[40:43], s67 idxen offen offset:0 nt lds\n"
    "buffer_load_dwordx4 v[238:239], s[40:43], s67 idxen offen offset:1024 nt lds\n"
    "s_sub_u32 s94, s34, 30\n"
    "s_max_u32 s76, s76, s94\n"
    "s_add_u32 s76, s76, s72\n"
    "s_cmp_eq_u32 s66, s80\n"
    "s_cselect_b64 s[66:67], s[64:65], s[66:67]\n"
    "s_waitcnt lgkmcnt(2)\n"
    "s_cmp_eq_u32 s66, s82\n"
    "s_cbranch_scc1 .Ldin_L1s\n"
    "s_branch .Ldin_G1s\n"
    ".Ldin_A0s_poll:\n"
    "s_sleep 1\n"
    "ds_read_b64 v[216:217], v246 offset:128\n"
    "s_waitcnt lgkmcnt(0)\n"
    "v_readlane_b32 s60, v216, s59\n"
    "v_readlane_b32 s61, v217, s59\n"
    "s_cmp_eq_u32 s60, -1\n"
    "s_cbranch_scc1 .Ldin_A0s_poll\n"
    "s_branch .Ldin_A0s_pr\n"
    ".Ldin_A0s_skip:\n"
    "s_add_u32 s8, s8, 6144\n"
    "s_sub_u32 s68, s68, 2048\n"
    "s_mov_b32 s9, 48\n"
    "s_sub_u32 s82, s82, 16\n"
    "s_sub_u32 s66, s66, 16\n"
    "s_sub_u32 s76, s76, s72\n"
    "s_waitcnt lgkmcnt(0)\n"
    "s_branch .Ldin_A0s_skr\n"
    ".Ldin_G1s_slp:\n"
    "s_sleep 1\n"
    ".Ldin_G1s_wait:\n"
    "s_memrealtime s[34:35]\n"
    "s_waitcnt lgkmcnt(0)\n"
    "s_sub_u32 s94, s34, s76\n"
    "s_cmp_lt_i32 s94, 0\n"
    "s_cbranch_scc1 .Ldin_G1s_slp\n"
    "s_branch .Ldin_G1s_go\n"
    ".Ldin_G2s_slp:\n"
    "s_sleep 1\n"
    ".Ldin_G2s_wait:\n"
    "s_memrealtime s[34:35]\n"
    "s_waitcnt lgkmcnt(0)\n"
    "s_sub_u32 s94, s34, s76\n"
    "s_cmp_lt_i32 s94, 0\n"
    "s_cbranch_scc1 .Ldin_G2s_slp\n"
    "s_branch .Ldin_G2s_go\n"
    ".Ldin_G3s_slp:\n"
    "s_sleep 1\n"
    ".Ldin_G3s_wait:\n"
    "s_memrealtime s[34:35]\n"
    "s_waitcnt lgkmcnt(0)\n"
    "s_sub_u32 s94, s34, s76\n"
    "s_cmp_lt_i32 s94, 0\n"
    "s_cbranch_scc1 .Ldin_G3s_slp\n"
    "s_branch .Ldin_G3s_go\n"
    ".Ldin_G0s_slp:\n"
    "s_sleep 1\n"
    ".Ldin_G0s_wait:\n"
    "s_memrealtime s[34:35]\n"
    "s_waitcnt lgkmcnt(0)\n"
    "s_sub_u32 s94, s34, s76\n"
    "s_cmp_lt_i32 s94, 0\n"
    "s_cbranch_scc1 .Ldin_G0s_slp\n"
    "s_branch .Ldin_G0s_go\n"
    ".p2align 5\n"
    ".Ldin_L2s:\n"
    "s_cmp_lt_i32 s60, 0\n"
    "s_cbranch_scc1 .Ldin_E0s\n"
    "ds_read_b128 v[144:147], v252 offset:8192\n"
    "ds_read_b128 v[148:151], v252 offset:8256\n"
    "ds_read_b128 v[152:155], v252 offset:16384\n"
    "ds_read_b128 v[156:159], v252 offset:16448\n"
    "v_add_u32 v251, s66, v244\n"
    "v_and_b32 v216, v248, v216\n"
    "v_and_b32 v217, v249, v217\n"
    "v_and_b32 v218, v248, v218\n"
    "v_and_b32 v219, v249, v219\n"
    "v_mfma_f32_4x4x4_16b_bf16 v[176:179], v[216:217], v[96:97], v[32:35]\n"
    "v_and_b32 v220, v248, v220\n"
    "v_and_b32 v221, v249, v221\n"
    "v_mfma_f32_4x4x4_16b_bf16 v[180:183], v[218:219], v[98:99], v[36:39]\n"
    "v_and_b32 v222, v248, v222\n"
    "v_and_b32_e64 v223, v249, v223\n"
    "v_mfma_f32_4x4x4_16b_bf16 v[184:187], v[220:221], v[100:101], v[40:43]\n"
    "v_cvt_pk_bf16_f32 v0, v176, v177\n"
    "v_mfma_f32_4x4x4_16b_bf16 v[224:227], v[228:229], v[208:209], v[224:227] cbsz:4 abid:0\n"
    "v_cvt_pk_bf16_f32 v1, v178, v179\n"
    "v_mfma_f32_4x4x4_16b_bf16 v[188:191], v[222:223], v[102:103], v[44:47]\n"
    "v_cvt_pk_bf16_f32 v2, v180, v181\n"
    "v_cvt_pk_bf16_f32 v3, v182, v183\n"
    "v_mfma_f32_4x4x4_16b_bf16 v[176:179], v[216:217], v[104:105], v[48:51]\n"
    "v_cvt_pk_bf16_f32 v4, v184, v185\n"
    "v_cvt_pk_bf16_f32 v5, v186, v187\n"
    "v_mfma_f32_4x4x4_16b_bf16 v[180:183], v[218:219], v[106:107], v[52:55]\n"
    "v_cvt_pk_bf16_f32 v6, v188, v189\n"
    "v_mfma_f32_4x4x4_16b_bf16 v[224:227], v[228:229], v[210:211], v[224:227] cbsz:4 abid:1\n"
    "v_cvt_pk_bf16_f32 v7, v190, v191\n"
    "v_mfma_f32_4x4x4_16b_bf16 v[184:187], v[220:221], v[108:109], v[56:59]\n"
    "v_cvt_pk_bf16_f32 v8, v176, v177\n"
    "v_cvt_pk_bf16_f32 v9, v178, v179\n"
    "v_mfma_f32_4x4x4_16b_bf16 v[188:191], v[222:223], v[110:111], v[60:63]\n"
    "v_cvt_pk_bf16_f32 v10, v180, v181\n"
    "v_cvt_pk_bf16_f32 v11, v182, v183\n"
    "v_mfma_f32_4x4x4_16b_bf16 v[176:179], v[216:217], v[112:113], v[64:67]\n"
    "v_cvt_pk_bf16_f32 v12, v184, v185\n"
    "v_mfma_f32_4x4x4_16b_bf16 v[224:227], v[228:229], v[212:213], v[224:227] cbsz:4 abid:2\n"
    "v_cvt_pk_bf16_f32 v13, v186, v187\n"
    "s_setprio 1\n"
    "s_waitcnt lgkmcnt(0)\n"
    "v_mfma_f32_4x4x4_16b_bf16 v[180:183], v[218:219], v[114:115], v[68:71]\n"
    "v_cvt_pk_bf16_f32 v14, v188, v189\n"
    "v_cvt_pk_bf16_f32 v15, v190, v191\n"
    "v_mfma_f32_4x4x4_16b_bf16 v[184:187], v[220:221], v[116:117], v[72:75]\n"
    "v_cvt_pk_bf16_f32 v16, v176, v177\n"
    "v_cvt_pk_bf16_f32 v17, v178, v179\n"
    "v_mfma_f32_4x4x4_16b_bf16 v[188:191], v[222:223], v[118:119], v[76:79]\n"
    "v_cvt_pk_bf16_f32 v18, v180, v181\n"
    "v_mfma_f32_4x4x4_16b_bf16 v[224:227], v[228:229], v[214:215], v[224:227] cbsz:4 abid:3\n"
    "v_cvt_pk_bf16_f32 v19, v182, v183\n"
    "v_mfma_f32_4x4x4_16b_bf16 v[176:179], v[216:217], v[120:121], v[80:83]\n"
    "v_cvt_pk_bf16_f32 v20, v184, v185\n"
    "v_cvt_pk_bf16_f32 v21, v186, v187\n"
    "v_mfma_f32_4x4x4_16b_bf16 v[180:183], v[218:219], v[122:123], v[84:87]\n"
    "v_cvt_pk_bf16_f32 v22, v188, v189\n"
    "v_cvt_pk_bf16_f32 v23, v190, v191\n"
    "v_mfma_f32_4x4x4_16b_bf16 v[184:187], v[220:221], v[124:125], v[88:91]\n"
    "v_cvt_pk_bf16_f32 v24, v176, v177\n"
    "v_cvt_pk_bf16_f32 v25, v178, v179\n"
    "v_mfma_f32_4x4x4_16b_bf16 v[188:191], v[222:223], v[126:127], v[92:95]\n"
    "v_cvt_pk_bf16_f32 v26, v180, v181\n"
    "v_cvt_pk_bf16_f32 v27, v182, v183\n"
    "v_cvt_pk_bf16_f32 v28, v184, v185\n"
    "v_cvt_pk_bf16_f32 v29, v186, v187\n"
    "v_mfma_f32_16x16x32_bf16 v[176:179], v[0:3], v[192:195], v[144:147]\n"
    "s_add_u32 m0, s68, 6144\n"
    "v_cvt_pk_bf16_f32 v30, v188, v189\n"
    "v_cvt_pk_bf16_f32 v31, v190, v191\n"
    "v_exp_f32 v160, v160 clamp\n"
    "v_mfma_f32_16x16x32_bf16 v[176:179], v[4:7], v[196:199], v[176:179]\n"
    "s_add_u32 s66, s66, 16\n"
    "v_exp_f32 v161, v161 clamp\n"
    "v_exp_f32 v162, v162 clamp\n"
    "v_mfma_f32_16x16x32_bf16 v[180:183], v[8:11], v[192:195], v[148:151]\n"
    "s_add_u32 s76, s76, s73\n"
    "v_exp_f32 v163, v163 clamp\n"
    "v_exp_f32 v164, v164 clamp\n"
    "v_mfma_f32_16x16x32_bf16 v[180:183], v[12:15], v[196:199], v[180:183]\n"
    "s_lshl1_add_u32 s76, s72, s76\n"
    "v_exp_f32 v165, v165 clamp\n"
    "v_exp_f32 v166, v166 clamp\n"
    "v_mfma_f32_16x16x32_bf16 v[184:187], v[16:19], v[192:195], v[152:155]\n"
    "s_mov_b32 s80, s83\n"
    "v_exp_f32 v167, v167 clamp\n"
    "v_exp_f32 v168, v168 clamp\n"
    "v_mfma_f32_16x16x32_bf16 v[184:187], v[20:23], v[196:199], v[184:187]\n"
    "s_add_u32 s62, s62, 256\n"
    "v_exp_f32 v169, v169 clamp\n"
    "v_exp_f32 v170, v170 clamp\n"
    "v_mfma_f32_16x16x32_bf16 v[188:191], v[24:27], v[192:195], v[156:159]\n"
    "v_exp_f32 v171, v171 clamp\n"
    "v_exp_f32 v172, v172 clamp\n"
    "v_mfma_f32_16x16x32_bf16 v[188:191], v[28:31], v[196:199], v[188:191]\n"
    "v_exp_f32 v173, v173 clamp\n"
    "v_exp_f32 v174, v174 clamp\n"
    "v_exp_f32 v175, v175 clamp\n"
    "v_pk_fma_f32 v[160:161], v[160:161], s[70:71], v[128:129]\n"
    "v_pk_fma_f32 v[162:163], v[162:163], s[70:71], v[130:131]\n"
    "v_pk_add_f32 v[230:231], v[160:161], v[162:163]\n"
    "v_pk_mul_f32 v[232:233], v[160:161], v[162:163]\n"
    "v_pk_fma_f32 v[164:165], v[164:165], s[70:71], v[132:133]\n"
    "v_pk_fma_f32 v[230:231], v[230:231], v[164:165], v[232:233]\n"
    "v_pk_mul_f32 v[232:233], v[232:233], v[164:165]\n"
    "v_pk_fma_f32 v[166:167], v[166:167], s[70:71], v[134:135]\n"
    "v_pk_fma_f32 v[230:231], v[230:231], v[166:167], v[232:233]\n"
    "v_pk_mul_f32 v[232:233], v[232:233], v[166:167]\n"
    "v_pk_fma_f32 v[168:169], v[168:169], s[70:71], v[136:137]\n"
    "v_pk_fma_f32 v[230:231], v[230:231], v[168:169], v[232:233]\n"
    "v_pk_mul_f32 v[232:233], v[232:233], v[168:169]\n"
    "v_pk_fma_f32 v[170:171], v[170:171], s[70:71], v[138:139]\n"
    "v_pk_fma_f32 v[230:231], v[230:231], v[170:171], v[232:233]\n"
    "v_pk_mul_f32 v[232:233], v[232:233], v[170:171]\n"
    "v_pk_fma_f32 v[172:173], v[172:173], s[70:71], v[140:141]\n"
    "v_pk_fma_f32 v[230:231], v[230:231], v[172:173], v[232:233]\n"
    "v_pk_mul_f32 v[232:233], v[232:233], v[172:173]\n"
    "v_pk_fma_f32 v[174:175], v[174:175], s[70:71], v[142:143]\n"
    "v_pk_fma_f32 v[230:231], v[230:231], v[174:175], v[232:233]\n"
    "v_pk_mul_f32 v[232:233], v[232:233], v[174:175]\n"
    "v_rcp_f32 v232, v232\n"
    "v_rcp_f32 v233, v233\n"
    "v_fma_f32 v230, v230, v232, v247\n"
    "v_fmac_f32 v230, v231, v233\n"
    "v_mbcnt_lo_u32_b32 v232, -1, 0\n"
    "v_mbcnt_hi_u32_b32 v232, -1, v232\n"
    "v_bfe_u32 v233, v232, 3, 1\n"
    "v_and_b32 v232, 7, v232\n"
    "v_lshl_add_u32 v232, v232, 1, v233\n"
    "v_add_u32 v232, s81, v232\n"
    "ds_read_u8 v232, v232 offset:49664\n"
    "s_add_u32 s81, s81, 16\n"
    "s_waitcnt lgkmcnt(0)\n"
    "buffer_load_ushort v232, v232, s[44:47], s63 idxen\n"
    "s_waitcnt vmcnt(0)\n"
    "v_lshlrev_b32 v232, 16, v232\n"
    "v_mul_f32_e64 v230, v230, v232\n"
    "s_setprio 0\n"
    "ds_read_b64_tr_b16 v[208:209], v240 offset:6144\n"
    "ds_read_b64_tr_b16 v[210:211], v241 offset:6144\n"
    "ds_read_b64_tr_b16 v[212:213], v242 offset:6144\n"
    "ds_read_b64_tr_b16 v[214:215], v243 offset:6144\n"
    "ds_read_u8 v236, v251 offset:0\n"
    "ds_read_u8 v238, v251 offset:1\n"
    "ds_write_b16_d16_hi v245, v230\n"
    "ds_read_b64 v[228:229], v246\n"
    "ds_read_b64 v[216:217], v246 offset:128\n"
    "s_mov_b32 s81, s64\n"
    "s_lshr_b32 s63, s65, 6\n"
    "s_waitcnt vmcnt(2)\n"
    "ds_read_b128 v[192:195], v234 offset:0\n"
    "ds_read_b128 v[196:199], v235 offset:0\n"
    "s_waitcnt lgkmcnt(5)\n"
    "buffer_load_dwordx4 v[236:237], s[40:43], s67 idxen offen offset:0 nt lds\n"
    "buffer_load_dwordx4 v[238:239], s[40:43], s67 idxen offen offset:1024 nt lds\n"
    "s_waitcnt lgkmcnt(2)\n"
    "s_bitcmp1_b32 s60, 30\n"
    "s_cbranch_scc0 .Ldin_A3\n"
    ".Ldin_A3s:\n"
    "v_add_u32 v251, s66, v244\n"
    "v_readlane_b32 s60, v216, s59\n"
    "v_readlane_b32 s61, v217, s59\n"
    "v_mfma_f32_4x4x4_16b_bf16 v[224:227], v[228:229], v[200:201], v[224:227] cbsz:4 abid:0\n"
    "v_exp_f32 v176, v176 clamp\n"
    "v_exp_f32 v177, v177 clamp\n"
    "v_mfma_f32_4x4x4_16b_bf16 v[224:227], v[228:229], v[202:203], v[224:227] cbsz:4 abid:1\n"
    "v_exp_f32 v178, v178 clamp\n"
    "v_exp_f32 v179, v179 clamp\n"
    "v_mfma_f32_4x4x4_16b_bf16 v[224:227], v[228:229], v[204:205], v[224:227] cbsz:4 abid:2\n"
    "v_exp_f32 v180, v180 clamp\n"
    "v_exp_f32 v181, v181 clamp\n"
    "v_mfma_f32_4x4x4_16b_bf16 v[224:227], v[228:229], v[206:207], v[224:227] cbsz:4 abid:3\n"
    "s_waitcnt lgkmcnt(0)\n"
    "v_mfma_f32_16x16x32_bf16 v[160:163], v[0:3], v[192:195], v[144:147]\n"
    "s_setprio 1\n"
    "v_exp_f32 v182, v182 clamp\n"
    "v_mfma_f32_16x16x32_bf16 v[160:163], v[4:7], v[196:199], v[160:163]\n"
    "s_cmp_eq_u32 s60, -1\n"
    "v_exp_f32 v183, v183 clamp\n"
    "v_mfma_f32_16x16x32_bf16 v[164:167], v[8:11], v[192:195], v[148:151]\n"
    "s_cbranch_scc1 .Ldin_A3s_poll\n"
    ".Ldin_A3s_pr:\n"
    "v_exp_f32 v184, v184 clamp\n"
    "v_mfma_f32_16x16x32_bf16 v[164:167], v[12:15], v[196:199], v[164:167]\n"
    "s_lshr_b32 s94, s60, 12\n"
    "v_exp_f32 v185, v185 clamp\n"
    "v_mfma_f32_16x16x32_bf16 v[168:171], v[16:19], v[192:195], v[152:155]\n"
    "s_and_b32 s94, s94, 0x1ff0\n"
    "v_exp_f32 v186, v186 clamp\n"
    "v_mfma_f32_16x16x32_bf16 v[168:171], v[20:23], v[196:199], v[168:171]\n"
    "s_mov_b32 m0, s62\n"
    "v_exp_f32 v187, v187 clamp\n"
    "v_mfma_f32_16x16x32_bf16 v[172:175], v[24:27], v[192:195], v[156:159]\n"
    "s_add_u32 s66, s66, 16\n"
    "v_exp_f32 v188, v188 clamp\n"
    "v_mfma_f32_16x16x32_bf16 v[172:175], v[28:31], v[196:199], v[172:175]\n"
    "s_and_b32 s64, s60, 0xffff\n"
    "v_xor_b32 v252, s94, v250\n"
    "v_exp_f32 v189, v189 clamp\n"
    "v_exp_f32 v190, v190 clamp\n"
    "v_exp_f32 v191, v191 clamp\n"
    "v_pk_fma_f32 v[176:177], v[176:177], s[70:71], v[128:129]\n"
    "v_pk_fma_f32 v[178:179], v[178:179], s[70:71], v[130:131]\n"
    "v_pk_add_f32 v[230:231], v[176:177], v[178:179]\n"
    "v_pk_mul_f32 v[232:233], v[176:177], v[178:179]\n"
    "v_pk_add_f32 v[218:219], v[224:225], v[226:227]\n"
    "v_mov_b64 v[224:225], 0\n"
    "v_mov_b64 v[226:227], 0\n"
    "v_pk_fma_f32 v[180:181], v[180:181], s[70:71], v[132:133]\n"
    "v_pk_fma_f32 v[230:231], v[230:231], v[180:181], v[232:233]\n"
    "v_pk_mul_f32 v[232:233], v[232:233], v[180:181]\n"
    "v_pk_fma_f32 v[182:183], v[182:183], s[70:71], v[134:135]\n"
    "v_pk_fma_f32 v[230:231], v[230:231], v[182:183], v[232:233]\n"
    "v_pk_mul_f32 v[232:233], v[232:233], v[182:183]\n"
    "v_pk_fma_f32 v[184:185], v[184:185], s[70:71], v[136:137]\n"
    "v_pk_fma_f32 v[230:231], v[230:231], v[184:185], v[232:233]\n"
    "v_pk_mul_f32 v[232:233], v[232:233], v[184:185]\n"
    "v_pk_fma_f32 v[186:187], v[186:187], s[70:71], v[138:139]\n"
    "v_pk_fma_f32 v[230:231], v[230:231], v[186:187], v[232:233]\n"
    "v_pk_mul_f32 v[232:233], v[232:233], v[186:187]\n"
    "v_pk_fma_f32 v[188:189], v[188:189], s[70:71], v[140:141]\n"
    "v_pk_fma_f32 v[230:231], v[230:231], v[188:189], v[232:233]\n"
    "v_pk_mul_f32 v[232:233], v[232:233], v[188:189]\n"
    "v_pk_fma_f32 v[190:191], v[190:191], s[70:71], v[142:143]\n"
    "v_pk_fma_f32 v[230:231], v[230:231], v[190:191], v[232:233]\n"
    "v_pk_mul_f32 v[232:233], v[232:233], v[190:191]\n"
    "v_rcp_f32 v232, v232\n"
    "v_rcp_f32 v233, v233\n"
    "v_fma_f32 v230, v230, v232, v247\n"
    "v_fmac_f32 v230, v231, v233\n"
    "v_mbcnt_lo_u32_b32 v232, -1, 0\n"
    "v_mbcnt_hi_u32_b32 v232, -1, v232\n"
    "v_bfe_u32 v233, v232, 3, 1\n"
    "v_and_b32 v232, 7, v232\n"
    "v_lshl_add_u32 v232, v232, 1, v233\n"
    "v_add_u32 v232, s81, v232\n"
    "ds_read_u8 v232, v232 offset:49664\n"
    "s_add_u32 s81, s81, 16\n"
    "s_waitcnt lgkmcnt(0)\n"
    "buffer_load_ushort v232, v232, s[44:47], s63 idxen\n"
    "s_waitcnt vmcnt(0)\n"
    "v_lshlrev_b32 v232, 16, v232\n"
    "v_mul_f32 v230, v230, v232\n"
    "v_add_f32 v218, v218, v219\n"
    "s_setprio 0\n"
    "ds_read_b64_tr_b16 v[200:201], v240 offset:0\n"
    "ds_read_b64_tr_b16 v[202:203], v241 offset:0\n"
    "ds_read_b64_tr_b16 v[204:205], v242 offset:0\n"
    "ds_read_b64_tr_b16 v[206:207], v243 offset:0\n"
    "ds_read_u8 v236, v251 offset:0\n"
    "ds_read_u8 v238, v251 offset:1\n"
    "ds_write_b16_d16_hi v245, v230\n"
    "ds_read_b64 v[228:229], v246\n"
    "ds_write_addtid_b32 v218 offset:49152\n"
    "s_add_u32 m0, s8, 0\n"
    "s_bfe_u32 s94, s60, 0x30013\n"
    "s_add_u32 s58, s58, s57\n"
    "s_mul_i32 s94, s94, s56\n"
    "s_add_u32 s82, s64, s9\n"
    "s_add_u32 s65, s58, s94\n"
    "s_waitcnt vmcnt(2)\n"
    "ds_read_b128 v[192:195], v234 offset:2048\n"
    "ds_read_b128 v[196:199], v235 offset:2048\n"
    "ds_read_b128 v[216:219], v252 offset:0\n"
    "ds_read_b128 v[220:223], v252 offset:64\n"
    "s_sub_u32 s95, s61, s76\n"
    "s_lshr_b32 s94, s61, 16\n"
    "s_sext_i32_i16 s95, s95\n"
    "s_cmp_eq_u32 s59, s10\n"
    "s_cbranch_scc1 .Ldin_A3s_skip\n"
    "s_waitcnt lgkmcnt(7)\n"
    "buffer_load_dwordx4 v[236:237], s[40:43], s67 idxen offen offset:0 nt lds\n"
    "buffer_load_dwordx4 v[238:239], s[40:43], s67 idxen offen offset:1024 nt lds\n"
    ".Ldin_A3s_skr:\n"
    "s_cmp_lt_i32 s95, -550\n"
    "s_cselect_b32 s94, 0, s94\n"
    "s_mul_i32 s95, s95, s94\n"
    "s_bfe_u32 s94, s60, 0x50019\n"
    "s_ashr_i32 s95, s95, 16\n"
    "s_lshl4_add_u32 s83, s94, s64\n"
    "s_max_i32 s95, s95, -14\n"
    "s_add_u32 s59, s59, 1\n"
    "s_min_i32 s95, s95, 10\n"
    "s_add_u32 s72, s77, s95\n"
    "s_cmp_lt_i32 s60, 0\n"
    "s_cselect_b32 s72, 0, s72\n"
    "s_cmp_eq_u32 s66, s80\n"
    "s_cselect_b64 s[66:67], s[64:65], s[66:67]\n"
    "s_waitcnt lgkmcnt(5)\n"
    "s_branch .Ldin_G0s\n"
    ".Ldin_A3s_poll:\n"
    "s_sleep 1\n"
    "ds_read_b64 v[216:217], v246 offset:128\n"
    "s_waitcnt lgkmcnt(0)\n"
    "v_readlane_b32 s60, v216, s59\n"
    "v_readlane_b32 s61, v217, s59\n"
    "s_cmp_eq_u32 s60, -1\n"
    "s_cbranch_scc1 .Ldin_A3s_poll\n"
    "s_branch .Ldin_A3s_pr\n"
    ".Ldin_A3s_skip:\n"
    "s_add_u32 s8, s8, 6144\n"
    "s_sub_u32 s68, s68, 2048\n"
    "s_mov_b32 s9, 48\n"
    "s_sub_u32 s82, s82, 16\n"
    "s_sub_u32 s66, s66, 16\n"
    "s_sub_u32 s76, s76, s72\n"
    "s_waitcnt lgkmcnt(0)\n"
    "s_branch .Ldin_A3s_skr\n"
    ".p2align 5\n"
    ".Ldin_L1s:\n"
    "s_cmp_lt_i32 s60, 0\n"
    "s_cbranch_scc1 .Ldin_E1s\n"
    "ds_read_b128 v[144:147], v252 offset:8192\n"
    "ds_read_b128 v[148:151], v252 offset:8256\n"
    "ds_read_b128 v[152:155], v252 offset:16384\n"
    "ds_read_b128 v[156:159], v252 offset:16448\n"
    "v_add_u32 v251, s66, v244\n"
    "v_and_b32 v216, v248, v216\n"
    "v_and_b32 v217, v249, v217\n"
    "v_and_b32 v218, v248, v218\n"
    "v_and_b32 v219, v249, v219\n"
    "v_mfma_f32_4x4x4_16b_bf16 v[160:163], v[216:217], v[96:97], v[32:35]\n"
    "v_and_b32 v220, v248, v220\n"
    "v_and_b32 v221, v249, v221\n"
    "v_mfma_f32_4x4x4_16b_bf16 v[164:167], v[218:219], v[98:99], v[36:39]\n"
    "v_and_b32 v222, v248, v222\n"
    "v_and_b32_e64 v223, v249, v223\n"
    "v_mfma_f32_4x4x4_16b_bf16 v[168:171], v[220:221], v[100:101], v[40:43]\n"
    "v_cvt_pk_bf16_f32 v0, v160, v161\n"
    "v_mfma_f32_4x4x4_16b_bf16 v[224:227], v[228:229], v[200:201], v[224:227] cbsz:4 abid:0\n"
    "v_cvt_pk_bf16_f32 v1, v162, v163\n"
    "v_mfma_f32_4x4x4_16b_bf16 v[172:175], v[222:223], v[102:103], v[44:47]\n"
    "v_cvt_pk_bf16_f32 v2, v164, v165\n"
    "v_cvt_pk_bf16_f32 v3, v166, v167\n"
    "v_mfma_f32_4x4x4_16b_bf16 v[160:163], v[216:217], v[104:105], v[48:51]\n"
    "v_cvt_pk_bf16_f32 v4, v168, v169\n"
    "v_cvt_pk_bf16_f32 v5, v170, v171\n"
    "v_mfma_f32_4x4x4_16b_bf16 v[164:167], v[218:219], v[106:107], v[52:55]\n"
    "v_cvt_pk_bf16_f32 v6, v172, v173\n"
    "v_mfma_f32_4x4x4_16b_bf16 v[224:227], v[228:229], v[202:203], v[224:227] cbsz:4 abid:1\n"
    "v_cvt_pk_bf16_f32 v7, v174, v175\n"
    "v_mfma_f32_4x4x4_16b_bf16 v[168:171], v[220:221], v[108:109], v[56:59]\n"
    "v_cvt_pk_bf16_f32 v8, v160, v161\n"
    "v_cvt_pk_bf16_f32 v9, v162, v163\n"
    "v_mfma_f32_4x4x4_16b_bf16 v[172:175], v[222:223], v[110:111], v[60:63]\n"
    "v_cvt_pk_bf16_f32 v10, v164, v165\n"
    "v_cvt_pk_bf16_f32 v11, v166, v167\n"
    "v_mfma_f32_4x4x4_16b_bf16 v[160:163], v[216:217], v[112:113], v[64:67]\n"
    "v_cvt_pk_bf16_f32 v12, v168, v169\n"
    "v_mfma_f32_4x4x4_16b_bf16 v[224:227], v[228:229], v[204:205], v[224:227] cbsz:4 abid:2\n"
    "v_cvt_pk_bf16_f32 v13, v170, v171\n"
    "s_setprio 1\n"
    "s_waitcnt lgkmcnt(0)\n"
    "v_mfma_f32_4x4x4_16b_bf16 v[164:167], v[218:219], v[114:115], v[68:71]\n"
    "v_cvt_pk_bf16_f32 v14, v172, v173\n"
    "v_cvt_pk_bf16_f32 v15, v174, v175\n"
    "v_mfma_f32_4x4x4_16b_bf16 v[168:171], v[220:221], v[116:117], v[72:75]\n"
    "v_cvt_pk_bf16_f32 v16, v160, v161\n"
    "v_cvt_pk_bf16_f32 v17, v162, v163\n"
    "v_mfma_f32_4x4x4_16b_bf16 v[172:175], v[222:223], v[118:119], v[76:79]\n"
    "v_cvt_pk_bf16_f32 v18, v164, v165\n"
    "v_mfma_f32_4x4x4_16b_bf16 v[224:227], v[228:229], v[206:207], v[224:227] cbsz:4 abid:3\n"
    "v_cvt_pk_bf16_f32 v19, v166, v167\n"
    "v_mfma_f32_4x4x4_16b_bf16 v[160:163], v[216:217], v[120:121], v[80:83]\n"
    "v_cvt_pk_bf16_f32 v20, v168, v169\n"
    "v_cvt_pk_bf16_f32 v21, v170, v171\n"
    "v_mfma_f32_4x4x4_16b_bf16 v[164:167], v[218:219], v[122:123], v[84:87]\n"
    "v_cvt_pk_bf16_f32 v22, v172, v173\n"
    "v_cvt_pk_bf16_f32 v23, v174, v175\n"
    "v_mfma_f32_4x4x4_16b_bf16 v[168:171], v[220:221], v[124:125], v[88:91]\n"
    "v_cvt_pk_bf16_f32 v24, v160, v161\n"
    "v_cvt_pk_bf16_f32 v25, v162, v163\n"
    "v_mfma_f32_4x4x4_16b_bf16 v[172:175], v[222:223], v[126:127], v[92:95]\n"
    "v_cvt_pk_bf16_f32 v26, v164, v165\n"
    "v_cvt_pk_bf16_f32 v27, v166, v167\n"
    "v_cvt_pk_bf16_f32 v28, v168, v169\n"
    "v_cvt_pk_bf16_f32 v29, v170, v171\n"
    "v_mfma_f32_16x16x32_bf16 v[160:163], v[0:3], v[192:195], v[144:147]\n"
    "s_add_u32 m0, s68, 4096\n"
    "v_cvt_pk_bf16_f32 v30, v172, v173\n"
    "v_cvt_pk_bf16_f32 v31, v174, v175\n"
    "v_exp_f32 v176, v176 clamp\n"
    "v_mfma_f32_16x16x32_bf16 v[160:163], v[4:7], v[196:199], v[160:163]\n"
    "s_add_u32 s66, s66, 16\n"
    "v_exp_f32 v177, v177 clamp\n"
    "v_exp_f32 v178, v178 clamp\n"
    "v_mfma_f32_16x16x32_bf16 v[164:167], v[8:11], v[192:195], v[148:151]\n"
    "s_add_u32 s76, s76, s73\n"
    "v_exp_f32 v179, v179 clamp\n"
    "v_exp_f32 v180, v180 clamp\n"
    "v_mfma_f32_16x16x32_bf16 v[164:167], v[12:15], v[196:199], v[164:167]\n"
    "s_lshl1_add_u32 s76, s72, s76\n"
    "v_exp_f32 v181, v181 clamp\n"
    "v_exp_f32 v182, v182 clamp\n"
    "v_mfma_f32_16x16x32_bf16 v[168:171], v[16:19], v[192:195], v[152:155]\n"
    "s_mov_b32 s80, s83\n"
    "v_exp_f32 v183, v183 clamp\n"
    "v_exp_f32 v184, v184 clamp\n"
    "v_mfma_f32_16x16x32_bf16 v[168:171], v[20:23], v[196:199], v[168:171]\n"
    "s_add_u32 s62, s62, 256\n"
    "v_exp_f32 v185, v185 clamp\n"
    "v_exp_f32 v186, v186 clamp\n"
    "v_mfma_f32_16x16x32_bf16 v[172:175], v[24:27], v[192:195], v[156:159]\n"
    "v_exp_f32 v187, v187 clamp\n"
    "v_exp_f32 v188, v188 clamp\n"
    "v_mfma_f32_16x16x32_bf16 v[172:175], v[28:31], v[196:199], v[172:175]\n"
    "v_exp_f32 v189, v189 clamp\n"
    "v_exp_f32 v190, v190 clamp\n"
    "v_exp_f32 v191, v191 clamp\n"
    "v_pk_fma_f32 v[176:177], v[176:177], s[70:71], v[128:129]\n"
    "v_pk_fma_f32 v[178:179], v[178:179], s[70:71], v[130:131]\n"
    "v_pk_add_f32 v[230:231], v[176:177], v[178:179]\n"
    "v_pk_mul_f32 v[232:233], v[176:177], v[178:179]\n"
    "v_pk_fma_f32 v[180:181], v[180:181], s[70:71], v[132:133]\n"
    "v_pk_fma_f32 v[230:231], v[230:231], v[180:181], v[232:233]\n"
    "v_pk_mul_f32 v[232:233], v[232:233], v[180:181]\n"
    "v_pk_fma_f32 v[182:183], v[182:183], s[70:71], v[134:135]\n"
    "v_pk_fma_f32 v[230:231], v[230:231], v[182:183], v[232:233]\n"
    "v_pk_mul_f32 v[232:233], v[232:233], v[182:183]\n"
    "v_pk_fma_f32 v[184:185], v[184:185], s[70:71], v[136:137]\n"
    "v_pk_fma_f32 v[230:231], v[230:231], v[184:185], v[232:233]\n"
    "v_pk_mul_f32 v[232:233], v[232:233], v[184:185]\n"
    "v_pk_fma_f32 v[186:187], v[186:187], s[70:71], v[138:139]\n"
    "v_pk_fma_f32 v[230:231], v[230:231], v[186:187], v[232:233]\n"
    "v_pk_mul_f32 v[232:233], v[232:233], v[186:187]\n"
    "v_pk_fma_f32 v[188:189], v[188:189], s[70:71], v[140:141]\n"
    "v_pk_fma_f32 v[230:231], v[230:231], v[188:189], v[232:233]\n"
    "v_pk_mul_f32 v[232:233], v[232:233], v[188:189]\n"
    "v_pk_fma_f32 v[190:191], v[190:191], s[70:71], v[142:143]\n"
    "v_pk_fma_f32 v[230:231], v[230:231], v[190:191], v[232:233]\n"
    "v_pk_mul_f32 v[232:233], v[232:233], v[190:191]\n"
    "v_rcp_f32 v232, v232\n"
    "v_rcp_f32 v233, v233\n"
    "v_fma_f32 v230, v230, v232, v247\n"
    "v_fmac_f32 v230, v231, v233\n"
    "v_mbcnt_lo_u32_b32 v232, -1, 0\n"
    "v_mbcnt_hi_u32_b32 v232, -1, v232\n"
    "v_bfe_u32 v233, v232, 3, 1\n"
    "v_and_b32 v232, 7, v232\n"
    "v_lshl_add_u32 v232, v232, 1, v233\n"
    "v_add_u32 v232, s81, v232\n"
    "ds_read_u8 v232, v232 offset:49664\n"
    "s_add_u32 s81, s81, 16\n"
    "s_waitcnt lgkmcnt(0)\n"
    "buffer_load_ushort v232, v232, s[44:47], s63 idxen\n"
    "s_waitcnt vmcnt(0)\n"
    "v_lshlrev_b32 v232, 16, v232\n"
    "v_mul_f32_e64 v230, v230, v232\n"
    "s_setprio 0\n"
    "ds_read_b64_tr_b16 v[200:201], v240 offset:4096\n"
    "ds_read_b64_tr_b16 v[202:203], v241 offset:4096\n"
    "ds_read_b64_tr_b16 v[204:205], v242 offset:4096\n"
    "ds_read_b64_tr_b16 v[206:207], v243 offset:4096\n"
    "ds_read_u8 v236, v251 offset:0\n"
    "ds_read_u8 v238, v251 offset:1\n"
    "ds_write_b16_d16_hi v245, v230\n"
    "ds_read_b64 v[228:229], v246\n"
    "ds_read_b64 v[216:217], v246 offset:128\n"
    "s_mov_b32 s81, s64\n"
    "s_lshr_b32 s63, s65, 6\n"
    "s_waitcnt vmcnt(2)\n"
    "ds_read_b128 v[192:195], v234 offset:6144\n"
    "ds_read_b128 v[196:199], v235 offset:6144\n"
    "s_waitcnt lgkmcnt(5)\n"
    "buffer_load_dwordx4 v[236:237], s[40:43], s67 idxen offen offset:0 nt lds\n"
    "buffer_load_dwordx4 v[238:239], s[40:43], s67 idxen offen offset:1024 nt lds\n"
    "s_waitcnt lgkmcnt(2)\n"
    "s_bitcmp1_b32 s60, 30\n"
    "s_cbranch_scc0 .Ldin_A2\n"
    ".Ldin_A2s:\n"
    "v_add_u32 v251, s66, v244\n"
    "v_readlane_b32 s60, v216, s59\n"
    "v_readlane_b32 s61, v217, s59\n"
    "v_mfma_f32_4x4x4_16b_bf16 v[224:227], v[228:229], v[208:209], v[224:227] cbsz:4 abid:0\n"
    "v_exp_f32 v160, v160 clamp\n"
    "v_exp_f32 v161, v161 clamp\n"
    "v_mfma_f32_4x4x4_16b_bf16 v[224:227], v[228:229], v[210:211], v[224:227] cbsz:4 abid:1\n"
    "v_exp_f32 v162, v162 clamp\n"
    "v_exp_f32 v163, v163 clamp\n"
    "v_mfma_f32_4x4x4_16b_bf16 v[224:227], v[228:229], v[212:213], v[224:227] cbsz:4 abid:2\n"
    "v_exp_f32 v164, v164 clamp\n"
    "v_exp_f32 v165, v165 clamp\n"
    "v_mfma_f32_4x4x4_16b_bf16 v[224:227], v[228:229], v[214:215], v[224:227] cbsz:4 abid:3\n"
    "s_waitcnt lgkmcnt(0)\n"
    "v_mfma_f32_16x16x32_bf16 v[176:179], v[0:3], v[192:195], v[144:147]\n"
    "s_setprio 1\n"
    "v_exp_f32 v166, v166 clamp\n"
    "v_mfma_f32_16x16x32_bf16 v[176:179], v[4:7], v[196:199], v[176:179]\n"
    "s_cmp_eq_u32 s60, -1\n"
    "v_exp_f32 v167, v167 clamp\n"
    "v_mfma_f32_16x16x32_bf16 v[180:183], v[8:11], v[192:195], v[148:151]\n"
    "s_cbranch_scc1 .Ldin_A2s_poll\n"
    ".Ldin_A2s_pr:\n"
    "v_exp_f32 v168, v168 clamp\n"
    "v_mfma_f32_16x16x32_bf16 v[180:183], v[12:15], v[196:199], v[180:183]\n"
    "s_lshr_b32 s94, s60, 12\n"
    "v_exp_f32 v169, v169 clamp\n"
    "v_mfma_f32_16x16x32_bf16 v[184:187], v[16:19], v[192:195], v[152:155]\n"
    "s_and_b32 s94, s94, 0x1ff0\n"
    "v_exp_f32 v170, v170 clamp\n"
    "v_mfma_f32_16x16x32_bf16 v[184:187], v[20:23], v[196:199], v[184:187]\n"
    "s_mov_b32 m0, s62\n"
    "v_exp_f32 v171, v171 clamp\n"
    "v_mfma_f32_16x16x32_bf16 v[188:191], v[24:27], v[192:195], v[156:159]\n"
    "s_add_u32 s66, s66, 16\n"
    "v_exp_f32 v172, v172 clamp\n"
    "v_mfma_f32_16x16x32_bf16 v[188:191], v[28:31], v[196:199], v[188:191]\n"
    "s_and_b32 s64, s60, 0xffff\n"
    "v_xor_b32 v252, s94, v250\n"
    "v_exp_f32 v173, v173 clamp\n"
    "v_exp_f32 v174, v174 clamp\n"
    "v_exp_f32 v175, v175 clamp\n"
    "v_pk_fma_f32 v[160:161], v[160:161], s[70:71], v[128:129]\n"
    "v_pk_fma_f32 v[162:163], v[162:163], s[70:71], v[130:131]\n"
    "v_pk_add_f32 v[230:231], v[160:161], v[162:163]\n"
    "v_pk_mul_f32 v[232:233], v[160:161], v[162:163]\n"
    "v_pk_add_f32 v[218:219], v[224:225], v[226:227]\n"
    "v_mov_b64 v[224:225], 0\n"
    "v_mov_b64 v[226:227], 0\n"
    "v_pk_fma_f32 v[164:165], v[164:165], s[70:71], v[132:133]\n"
    "v_pk_fma_f32 v[230:231], v[230:231], v[164:165], v[232:233]\n"
    "v_pk_mul_f32 v[232:233], v[232:233], v[164:165]\n"
    "v_pk_fma_f32 v[166:167], v[166:167], s[70:71], v[134:135]\n"
    "v_pk_fma_f32 v[230:231], v[230:231], v[166:167], v[232:233]\n"
    "v_pk_mul_f32 v[232:233], v[232:233], v[166:167]\n"
    "v_pk_fma_f32 v[168:169], v[168:169], s[70:71], v[136:137]\n"
    "v_pk_fma_f32 v[230:231], v[230:231], v[168:169], v[232:233]\n"
    "v_pk_mul_f32 v[232:233], v[232:233], v[168:169]\n"
    "v_pk_fma_f32 v[170:171], v[170:171], s[70:71], v[138:139]\n"
    "v_pk_fma_f32 v[230:231], v[230:231], v[170:171], v[232:233]\n"
    "v_pk_mul_f32 v[232:233], v[232:233], v[170:171]\n"
    "v_pk_fma_f32 v[172:173], v[172:173], s[70:71], v[140:141]\n"
    "v_pk_fma_f32 v[230:231], v[230:231], v[172:173], v[232:233]\n"
    "v_pk_mul_f32 v[232:233], v[232:233], v[172:173]\n"
    "v_pk_fma_f32 v[174:175], v[174:175], s[70:71], v[142:143]\n"
    "v_pk_fma_f32 v[230:231], v[230:231], v[174:175], v[232:233]\n"
    "v_pk_mul_f32 v[232:233], v[232:233], v[174:175]\n"
    "v_rcp_f32 v232, v232\n"
    "v_rcp_f32 v233, v233\n"
    "v_fma_f32 v230, v230, v232, v247\n"
    "v_fmac_f32 v230, v231, v233\n"
    "v_mbcnt_lo_u32_b32 v232, -1, 0\n"
    "v_mbcnt_hi_u32_b32 v232, -1, v232\n"
    "v_bfe_u32 v233, v232, 3, 1\n"
    "v_and_b32 v232, 7, v232\n"
    "v_lshl_add_u32 v232, v232, 1, v233\n"
    "v_add_u32 v232, s81, v232\n"
    "ds_read_u8 v232, v232 offset:49664\n"
    "s_add_u32 s81, s81, 16\n"
    "s_waitcnt lgkmcnt(0)\n"
    "buffer_load_ushort v232, v232, s[44:47], s63 idxen\n"
    "s_waitcnt vmcnt(0)\n"
    "v_lshlrev_b32 v232, 16, v232\n"
    "v_mul_f32 v230, v230, v232\n"
    "v_add_f32 v218, v218, v219\n"
    "s_setprio 0\n"
    "ds_read_b64_tr_b16 v[208:209], v240 offset:6144\n"
    "ds_read_b64_tr_b16 v[210:211], v241 offset:6144\n"
    "ds_read_b64_tr_b16 v[212:213], v242 offset:6144\n"
    "ds_read_b64_tr_b16 v[214:215], v243 offset:6144\n"
    "ds_read_u8 v236, v251 offset:0\n"
    "ds_read_u8 v238, v251 offset:1\n"
    "ds_write_b16_d16_hi v245, v230\n"
    "ds_read_b64 v[228:229], v246\n"
    "ds_write_addtid_b32 v218 offset:49152\n"
    "s_add_u32 m0, s68, 6144\n"
    "s_bfe_u32 s94, s60, 0x30013\n"
    "s_add_u32 s58, s58, s57\n"
    "s_mul_i32 s94, s94, s56\n"
    "s_add_u32 s82, s64, s9\n"
    "s_add_u32 s65, s58, s94\n"
    "s_waitcnt vmcnt(2)\n"
    "ds_read_b128 v[192:195], v234 offset:0\n"
    "ds_read_b128 v[196:199], v235 offset:0\n"
    "ds_read_b128 v[216:219], v252 offset:0\n"
    "ds_read_b128 v[220:223], v252 offset:64\n"
    "s_sub_u32 s95, s61, s76\n"
    "s_lshr_b32 s94, s61, 16\n"
    "s_sext_i32_i16 s95, s95\n"
    "s_cmp_eq_u32 s59, s10\n"
    "s_cbranch_scc1 .Ldin_A2s_skip\n"
    "s_waitcnt lgkmcnt(7)\n"
    "buffer_load_dwordx4 v[236:237], s[40:43], s67 idxen offen offset:0 nt lds\n"
    "buffer_load_dwordx4 v[238:239], s[40:43], s67 idxen offen offset:1024 nt lds\n"
    ".Ldin_A2s_skr:\n"
    "s_cmp_lt_i32 s95, -550\n"
    "s_cselect_b32 s94, 0, s94\n"
    "s_mul_i32 s95, s95, s94\n"
    "s_bfe_u32 s94, s60, 0x50019\n"
    "s_ashr_i32 s95, s95, 16\n"
    "s_lshl4_add_u32 s83, s94, s64\n"
    "s_max_i32 s95, s95, -14\n"
    "s_add_u32 s59, s59, 1\n"
    "s_min_i32 s95, s95, 10\n"
    "s_add_u32 s72, s77, s95\n"
    "s_cmp_lt_i32 s60, 0\n"
    "s_cselect_b32 s72, 0, s72\n"
    "s_cmp_eq_u32 s66, s80\n"
    "s_cselect_b64 s[66:67], s[64:65], s[66:67]\n"
    "s_waitcnt lgkmcnt(5)\n"
    "s_branch .Ldin_G3s\n"
    ".Ldin_A2s_poll:\n"
    "s_sleep 1\n"
    "ds_read_b64 v[216:217], v246 offset:128\n"
    "s_waitcnt lgkmcnt(0)\n"
    "v_readlane_b32 s60, v216, s59\n"
    "v_readlane_b32 s61, v217, s59\n"
    "s_cmp_eq_u32 s60, -1\n"
    "s_cbranch_scc1 .Ldin_A2s_poll\n"
    "s_branch .Ldin_A2s_pr\n"
    ".Ldin_A2s_skip:\n"
    "s_add_u32 s8, s8, 6144\n"
    "s_sub_u32 s68, s68, 2048\n"
    "s_mov_b32 s9, 48\n"
    "s_sub_u32 s82, s82, 16\n"
    "s_sub_u32 s66, s66, 16\n"
    "s_sub_u32 s76, s76, s72\n"
    "s_waitcnt lgkmcnt(0)\n"
    "s_branch .Ldin_A2s_skr\n"
    ".p2align 5\n"
    ".Ldin_L0s:\n"
    "s_cmp_lt_i32 s60, 0\n"
    "s_cbranch_scc1 .Ldin_E0s\n"
    "ds_read_b128 v[144:147], v252 offset:8192\n"
    "ds_read_b128 v[148:151], v252 offset:8256\n"
    "ds_read_b128 v[152:155], v252 offset:16384\n"
    "ds_read_b128 v[156:159], v252 offset:16448\n"
    "v_add_u32 v251, s66, v244\n"
    "v_and_b32 v216, v248, v216\n"
    "v_and_b32 v217, v249, v217\n"
    "v_and_b32 v218, v248, v218\n"
    "v_and_b32 v219, v249, v219\n"
    "v_mfma_f32_4x4x4_16b_bf16 v[176:179], v[216:217], v[96:97], v[32:35]\n"
    "v_and_b32 v220, v248, v220\n"
    "v_and_b32 v221, v249, v221\n"
    "v_mfma_f32_4x4x4_16b_bf16 v[180:183], v[218:219], v[98:99], v[36:39]\n"
    "v_and_b32 v222, v248, v222\n"
    "v_and_b32_e64 v223, v249, v223\n"
    "v_mfma_f32_4x4x4_16b_bf16 v[184:187], v[220:221], v[100:101], v[40:43]\n"
    "v_cvt_pk_bf16_f32 v0, v176, v177\n"
    "v_mfma_f32_4x4x4_16b_bf16 v[224:227], v[228:229], v[208:209], v[224:227] cbsz:4 abid:0\n"
    "v_cvt_pk_bf16_f32 v1, v178, v179\n"
    "v_mfma_f32_4x4x4_16b_bf16 v[188:191], v[222:223], v[102:103], v[44:47]\n"
    "v_cvt_pk_bf16_f32 v2, v180, v181\n"
    "v_cvt_pk_bf16_f32 v3, v182, v183\n"
    "v_mfma_f32_4x4x4_16b_bf16 v[176:179], v[216:217], v[104:105], v[48:51]\n"
    "v_cvt_pk_bf16_f32 v4, v184, v185\n"
    "v_cvt_pk_bf16_f32 v5, v186, v187\n"
    "v_mfma_f32_4x4x4_16b_bf16 v[180:183], v[218:219], v[106:107], v[52:55]\n"
    "v_cvt_pk_bf16_f32 v6, v188, v189\n"
    "v_mfma_f32_4x4x4_16b_bf16 v[224:227], v[228:229], v[210:211], v[224:227] cbsz:4 abid:1\n"
    "v_cvt_pk_bf16_f32 v7, v190, v191\n"
    "v_mfma_f32_4x4x4_16b_bf16 v[184:187], v[220:221], v[108:109], v[56:59]\n"
    "v_cvt_pk_bf16_f32 v8, v176, v177\n"
    "v_cvt_pk_bf16_f32 v9, v178, v179\n"
    "v_mfma_f32_4x4x4_16b_bf16 v[188:191], v[222:223], v[110:111], v[60:63]\n"
    "v_cvt_pk_bf16_f32 v10, v180, v181\n"
    "v_cvt_pk_bf16_f32 v11, v182, v183\n"
    "v_mfma_f32_4x4x4_16b_bf16 v[176:179], v[216:217], v[112:113], v[64:67]\n"
    "v_cvt_pk_bf16_f32 v12, v184, v185\n"
    "v_mfma_f32_4x4x4_16b_bf16 v[224:227], v[228:229], v[212:213], v[224:227] cbsz:4 abid:2\n"
    "v_cvt_pk_bf16_f32 v13, v186, v187\n"
    "s_setprio 1\n"
    "s_waitcnt lgkmcnt(0)\n"
    "v_mfma_f32_4x4x4_16b_bf16 v[180:183], v[218:219], v[114:115], v[68:71]\n"
    "v_cvt_pk_bf16_f32 v14, v188, v189\n"
    "v_cvt_pk_bf16_f32 v15, v190, v191\n"
    "v_mfma_f32_4x4x4_16b_bf16 v[184:187], v[220:221], v[116:117], v[72:75]\n"
    "v_cvt_pk_bf16_f32 v16, v176, v177\n"
    "v_cvt_pk_bf16_f32 v17, v178, v179\n"
    "v_mfma_f32_4x4x4_16b_bf16 v[188:191], v[222:223], v[118:119], v[76:79]\n"
    "v_cvt_pk_bf16_f32 v18, v180, v181\n"
    "v_mfma_f32_4x4x4_16b_bf16 v[224:227], v[228:229], v[214:215], v[224:227] cbsz:4 abid:3\n"
    "v_cvt_pk_bf16_f32 v19, v182, v183\n"
    "v_mfma_f32_4x4x4_16b_bf16 v[176:179], v[216:217], v[120:121], v[80:83]\n"
    "v_cvt_pk_bf16_f32 v20, v184, v185\n"
    "v_cvt_pk_bf16_f32 v21, v186, v187\n"
    "v_mfma_f32_4x4x4_16b_bf16 v[180:183], v[218:219], v[122:123], v[84:87]\n"
    "v_cvt_pk_bf16_f32 v22, v188, v189\n"
    "v_cvt_pk_bf16_f32 v23, v190, v191\n"
    "v_mfma_f32_4x4x4_16b_bf16 v[184:187], v[220:221], v[124:125], v[88:91]\n"
    "v_cvt_pk_bf16_f32 v24, v176, v177\n"
    "v_cvt_pk_bf16_f32 v25, v178, v179\n"
    "v_mfma_f32_4x4x4_16b_bf16 v[188:191], v[222:223], v[126:127], v[92:95]\n"
    "v_cvt_pk_bf16_f32 v26, v180, v181\n"
    "v_cvt_pk_bf16_f32 v27, v182, v183\n"
    "v_cvt_pk_bf16_f32 v28, v184, v185\n"
    "v_cvt_pk_bf16_f32 v29, v186, v187\n"
    "v_mfma_f32_16x16x32_bf16 v[176:179], v[0:3], v[192:195], v[144:147]\n"
    "s_add_u32 m0, s68, 2048\n"
    "v_cvt_pk_bf16_f32 v30, v188, v189\n"
    "v_cvt_pk_bf16_f32 v31, v190, v191\n"
    "v_exp_f32 v160, v160 clamp\n"
    "v_mfma_f32_16x16x32_bf16 v[176:179], v[4:7], v[196:199], v[176:179]\n"
    "s_add_u32 s66, s66, 16\n"
    "v_exp_f32 v161, v161 clamp\n"
    "v_exp_f32 v162, v162 clamp\n"
    "v_mfma_f32_16x16x32_bf16 v[180:183], v[8:11], v[192:195], v[148:151]\n"
    "s_add_u32 s76, s76, s73\n"
    "v_exp_f32 v163, v163 clamp\n"
    "v_exp_f32 v164, v164 clamp\n"
    "v_mfma_f32_16x16x32_bf16 v[180:183], v[12:15], v[196:199], v[180:183]\n"
    "s_lshl1_add_u32 s76, s72, s76\n"
    "v_exp_f32 v165, v165 clamp\n"
    "v_exp_f32 v166, v166 clamp\n"
    "v_mfma_f32_16x16x32_bf16 v[184:187], v[16:19], v[192:195], v[152:155]\n"
    "s_mov_b32 s80, s83\n"
    "v_exp_f32 v167, v167 clamp\n"
    "v_exp_f32 v168, v168 clamp\n"
    "v_mfma_f32_16x16x32_bf16 v[184:187], v[20:23], v[196:199], v[184:187]\n"
    "s_add_u32 s62, s62, 256\n"
    "v_exp_f32 v169, v169 clamp\n"
    "v_exp_f32 v170, v170 clamp\n"
    "v_mfma_f32_16x16x32_bf16 v[188:191], v[24:27], v[192:195], v[156:159]\n"
    "v_exp_f32 v171, v171 clamp\n"
    "v_exp_f32 v172, v172 clamp\n"
    "v_mfma_f32_16x16x32_bf16 v[188:191], v[28:31], v[196:199], v[188:191]\n"
    "v_exp_f32 v173, v173 clamp\n"
    "v_exp_f32 v174, v174 clamp\n"
    "v_exp_f32 v175, v175 clamp\n"
    "v_pk_fma_f32 v[160:161], v[160:161], s[70:71], v[128:129]\n"
    "v_pk_fma_f32 v[162:163], v[162:163], s[70:71], v[130:131]\n"
    "v_pk_add_f32 v[230:231], v[160:161], v[162:163]\n"
    "v_pk_mul_f32 v[232:233], v[160:161], v[162:163]\n"
    "v_pk_fma_f32 v[164:165], v[164:165], s[70:71], v[132:133]\n"
    "v_pk_fma_f32 v[230:231], v[230:231], v[164:165], v[232:233]\n"
    "v_pk_mul_f32 v[232:233], v[232:233], v[164:165]\n"
    "v_pk_fma_f32 v[166:167], v[166:167], s[70:71], v[134:135]\n"
    "v_pk_fma_f32 v[230:231], v[230:231], v[166:167], v[232:233]\n"
    "v_pk_mul_f32 v[232:233], v[232:233], v[166:167]\n"
    "v_pk_fma_f32 v[168:169], v[168:169], s[70:71], v[136:137]\n"
    "v_pk_fma_f32 v[230:231], v[230:231], v[168:169], v[232:233]\n"
    "v_pk_mul_f32 v[232:233], v[232:233], v[168:169]\n"
    "v_pk_fma_f32 v[170:171], v[170:171], s[70:71], v[138:139]\n"
    "v_pk_fma_f32 v[230:231], v[230:231], v[170:171], v[232:233]\n"
    "v_pk_mul_f32 v[232:233], v[232:233], v[170:171]\n"
    "v_pk_fma_f32 v[172:173], v[172:173], s[70:71], v[140:141]\n"
    "v_pk_fma_f32 v[230:231], v[230:231], v[172:173], v[232:233]\n"
    "v_pk_mul_f32 v[232:233], v[232:233], v[172:173]\n"
    "v_pk_fma_f32 v[174:175], v[174:175], s[70:71], v[142:143]\n"
    "v_pk_fma_f32 v[230:231], v[230:231], v[174:175], v[232:233]\n"
    "v_pk_mul_f32 v[232:233], v[232:233], v[174:175]\n"
    "v_rcp_f32 v232, v232\n"
    "v_rcp_f32 v233, v233\n"
    "v_fma_f32 v230, v230, v232, v247\n"
    "v_fmac_f32 v230, v231, v233\n"
    "v_mbcnt_lo_u32_b32 v232, -1, 0\n"
    "v_mbcnt_hi_u32_b32 v232, -1, v232\n"
    "v_bfe_u32 v233, v232, 3, 1\n"
    "v_and_b32 v232, 7, v232\n"
    "v_lshl_add_u32 v232, v232, 1, v233\n"
    "v_add_u32 v232, s81, v232\n"
    "ds_read_u8 v232, v232 offset:49664\n"
    "s_add_u32 s81, s81, 16\n"
    "s_waitcnt lgkmcnt(0)\n"
    "buffer_load_ushort v232, v232, s[44:47], s63 idxen\n"
    "s_waitcnt vmcnt(0)\n"
    "v_lshlrev_b32 v232, 16, v232\n"
    "v_mul_f32_e64 v230, v230, v232\n"
    "s_setprio 0\n"
    "ds_read_b64_tr_b16 v[208:209], v240 offset:2048\n"
    "ds_read_b64_tr_b16 v[210:211], v241 offset:2048\n"
    "ds_read_b64_tr_b16 v[212:213], v242 offset:2048\n"
    "ds_read_b64_tr_b16 v[214:215], v243 offset:2048\n"
    "ds_read_u8 v236, v251 offset:0\n"
    "ds_read_u8 v238, v251 offset:1\n"
    "ds_write_b16_d16_hi v245, v230\n"
    "ds_read_b64 v[228:229], v246\n"
    "ds_read_b64 v[216:217], v246 offset:128\n"
    "s_mov_b32 s81, s64\n"
    "s_lshr_b32 s63, s65, 6\n"
    "s_waitcnt vmcnt(2)\n"
    "ds_read_b128 v[192:195], v234 offset:4096\n"
    "ds_read_b128 v[196:199], v235 offset:4096\n"
    "s_waitcnt lgkmcnt(5)\n"
    "buffer_load_dwordx4 v[236:237], s[40:43], s67 idxen offen offset:0 nt lds\n"
    "buffer_load_dwordx4 v[238:239], s[40:43], s67 idxen offen offset:1024 nt lds\n"
    "s_waitcnt lgkmcnt(2)\n"
    "s_bitcmp1_b32 s60, 30\n"
    "s_cbranch_scc0 .Ldin_A1\n"
    ".Ldin_A1s:\n"
    "v_add_u32 v251, s66, v244\n"
    "v_readlane_b32 s60, v216, s59\n"
    "v_readlane_b32 s61, v217, s59\n"
    "v_mfma_f32_4x4x4_16b_bf16 v[224:227], v[228:229], v[200:201], v[224:227] cbsz:4 abid:0\n"
    "v_exp_f32 v176, v176 clamp\n"
    "v_exp_f32 v177, v177 clamp\n"
    "v_mfma_f32_4x4x4_16b_bf16 v[224:227], v[228:229], v[202:203], v[224:227] cbsz:4 abid:1\n"
    "v_exp_f32 v178, v178 clamp\n"
    "v_exp_f32 v179, v179 clamp\n"
    "v_mfma_f32_4x4x4_16b_bf16 v[224:227], v[228:229], v[204:205], v[224:227] cbsz:4 abid:2\n"
    "v_exp_f32 v180, v180 clamp\n"
    "v_exp_f32 v181, v181 clamp\n"
    "v_mfma_f32_4x4x4_16b_bf16 v[224:227], v[228:229], v[206:207], v[224:227] cbsz:4 abid:3\n"
    "s_waitcnt lgkmcnt(0)\n"
    "v_mfma_f32_16x16x32_bf16 v[160:163], v[0:3], v[192:195], v[144:147]\n"
    "s_setprio 1\n"
    "v_exp_f32 v182, v182 clamp\n"
    "v_mfma_f32_16x16x32_bf16 v[160:163], v[4:7], v[196:199], v[160:163]\n"
    "s_cmp_eq_u32 s60, -1\n"
    "v_exp_f32 v183, v183 clamp\n"
    "v_mfma_f32_16x16x32_bf16 v[164:167], v[8:11], v[192:195], v[148:151]\n"
    "s_cbranch_scc1 .Ldin_A1s_poll\n"
    ".Ldin_A1s_pr:\n"
    "v_exp_f32 v184, v184 clamp\n"
    "v_mfma_f32_16x16x32_bf16 v[164:167], v[12:15], v[196:199], v[164:167]\n"
    "s_lshr_b32 s94, s60, 12\n"
    "v_exp_f32 v185, v185 clamp\n"
    "v_mfma_f32_16x16x32_bf16 v[168:171], v[16:19], v[192:195], v[152:155]\n"
    "s_and_b32 s94, s94, 0x1ff0\n"
    "v_exp_f32 v186, v186 clamp\n"
    "v_mfma_f32_16x16x32_bf16 v[168:171], v[20:23], v[196:199], v[168:171]\n"
    "s_mov_b32 m0, s62\n"
    "v_exp_f32 v187, v187 clamp\n"
    "v_mfma_f32_16x16x32_bf16 v[172:175], v[24:27], v[192:195], v[156:159]\n"
    "s_add_u32 s66, s66, 16\n"
    "v_exp_f32 v188, v188 clamp\n"
    "v_mfma_f32_16x16x32_bf16 v[172:175], v[28:31], v[196:199], v[172:175]\n"
    "s_and_b32 s64, s60, 0xffff\n"
    "v_xor_b32 v252, s94, v250\n"
    "v_exp_f32 v189, v189 clamp\n"
    "v_exp_f32 v190, v190 clamp\n"
    "v_exp_f32 v191, v191 clamp\n"
    "v_pk_fma_f32 v[176:177], v[176:177], s[70:71], v[128:129]\n"
    "v_pk_fma_f32 v[178:179], v[178:179], s[70:71], v[130:131]\n"
    "v_pk_add_f32 v[230:231], v[176:177], v[178:179]\n"
    "v_pk_mul_f32 v[232:233], v[176:177], v[178:179]\n"
    "v_pk_add_f32 v[218:219], v[224:225], v[226:227]\n"
    "v_mov_b64 v[224:225], 0\n"
    "v_mov_b64 v[226:227], 0\n"
    "v_pk_fma_f32 v[180:181], v[180:181], s[70:71], v[132:133]\n"
    "v_pk_fma_f32 v[230:231], v[230:231], v[180:181], v[232:233]\n"
    "v_pk_mul_f32 v[232:233], v[232:233], v[180:181]\n"
    "v_pk_fma_f32 v[182:183], v[182:183], s[70:71], v[134:135]\n"
    "v_pk_fma_f32 v[230:231], v[230:231], v[182:183], v[232:233]\n"
    "v_pk_mul_f32 v[232:233], v[232:233], v[182:183]\n"
    "v_pk_fma_f32 v[184:185], v[184:185], s[70:71], v[136:137]\n"
    "v_pk_fma_f32 v[230:231], v[230:231], v[184:185], v[232:233]\n"
    "v_pk_mul_f32 v[232:233], v[232:233], v[184:185]\n"
    "v_pk_fma_f32 v[186:187], v[186:187], s[70:71], v[138:139]\n"
    "v_pk_fma_f32 v[230:231], v[230:231], v[186:187], v[232:233]\n"
    "v_pk_mul_f32 v[232:233], v[232:233], v[186:187]\n"
    "v_pk_fma_f32 v[188:189], v[188:189], s[70:71], v[140:141]\n"
    "v_pk_fma_f32 v[230:231], v[230:231], v[188:189], v[232:233]\n"
    "v_pk_mul_f32 v[232:233], v[232:233], v[188:189]\n"
    "v_pk_fma_f32 v[190:191], v[190:191], s[70:71], v[142:143]\n"
    "v_pk_fma_f32 v[230:231], v[230:231], v[190:191], v[232:233]\n"
    "v_pk_mul_f32 v[232:233], v[232:233], v[190:191]\n"
    "v_rcp_f32 v232, v232\n"
    "v_rcp_f32 v233, v233\n"
    "v_fma_f32 v230, v230, v232, v247\n"
    "v_fmac_f32 v230, v231, v233\n"
    "v_mbcnt_lo_u32_b32 v232, -1, 0\n"
    "v_mbcnt_hi_u32_b32 v232, -1, v232\n"
    "v_bfe_u32 v233, v232, 3, 1\n"
    "v_and_b32 v232, 7, v232\n"
    "v_lshl_add_u32 v232, v232, 1, v233\n"
    "v_add_u32 v232, s81, v232\n"
    "ds_read_u8 v232, v232 offset:49664\n"
    "s_add_u32 s81, s81, 16\n"
    "s_waitcnt lgkmcnt(0)\n"
    "buffer_load_ushort v232, v232, s[44:47], s63 idxen\n"
    "s_waitcnt vmcnt(0)\n"
    "v_lshlrev_b32 v232, 16, v232\n"
    "v_mul_f32 v230, v230, v232\n"
    "v_add_f32 v218, v218, v219\n"
    "s_setprio 0\n"
    "ds_read_b64_tr_b16 v[200:201], v240 offset:4096\n"
    "ds_read_b64_tr_b16 v[202:203], v241 offset:4096\n"
    "ds_read_b64_tr_b16 v[204:205], v242 offset:4096\n"
    "ds_read_b64_tr_b16 v[206:207], v243 offset:4096\n"
    "ds_read_u8 v236, v251 offset:0\n"
    "ds_read_u8 v238, v251 offset:1\n"
    "ds_write_b16_d16_hi v245, v230\n"
    "ds_read_b64 v[228:229], v246\n"
    "ds_write_addtid_b32 v218 offset:49152\n"
    "s_add_u32 m0, s68, 4096\n"
    "s_bfe_u32 s94, s60, 0x30013\n"
    "s_add_u32 s58, s58, s57\n"
    "s_mul_i32 s94, s94, s56\n"
    "s_add_u32 s82, s64, s9\n"
    "s_add_u32 s65, s58, s94\n"
    "s_waitcnt vmcnt(2)\n"
    "ds_read_b128 v[192:195], v234 offset:6144\n"
    "ds_read_b128 v[196:199], v235 offset:6144\n"
    "ds_read_b128 v[216:219], v252 offset:0\n"
    "ds_read_b128 v[220:223], v252 offset:64\n"
    "s_sub_u32 s95, s61, s76\n"
    "s_lshr_b32 s94, s61, 16\n"
    "s_sext_i32_i16 s95, s95\n"
    "s_cmp_eq_u32 s59, s10\n"
    "s_cbranch_scc1 .Ldin_A1s_skip\n"
    "s_waitcnt lgkmcnt(7)\n"
    "buffer_load_dwordx4 v[236:237], s[40:43], s67 idxen offen offset:0 nt lds\n"
    "buffer_load_dwordx4 v[238:239], s[40:43], s67 idxen offen offset:1024 nt lds\n"
    ".Ldin_A1s_skr:\n"
    "s_cmp_lt_i32 s95, -550\n"
    "s_cselect_b32 s94, 0, s94\n"
    "s_mul_i32 s95, s95, s94\n"
    "s_bfe_u32 s94, s60, 0x50019\n"
    "s_ashr_i32 s95, s95, 16\n"
    "s_lshl4_add_u32 s83, s94, s64\n"
    "s_max_i32 s95, s95, -14\n"
    "s_add_u32 s59, s59, 1\n"
    "s_min_i32 s95, s95, 10\n"
    "s_add_u32 s72, s77, s95\n"
    "s_cmp_lt_i32 s60, 0\n"
    "s_cselect_b32 s72, 0, s72\n"
    "s_cmp_eq_u32 s66, s80\n"
    "s_cselect_b64 s[66:67], s[64:65], s[66:67]\n"
    "s_waitcnt lgkmcnt(5)\n"
    "s_branch .Ldin_G2s\n"
    ".Ldin_A1s_poll:\n"
    "s_sleep 1\n"
    "ds_read_b64 v[216:217], v246 offset:128\n"
    "s_waitcnt lgkmcnt(0)\n"
    "v_readlane_b32 s60, v216, s59\n"
    "v_readlane_b32 s61, v217, s59\n"
    "s_cmp_eq_u32 s60, -1\n"
    "s_cbranch_scc1 .Ldin_A1s_poll\n"
    "s_branch .Ldin_A1s_pr\n"
    ".Ldin_A1s_skip:\n"
    "s_add_u32 s8, s8, 6144\n"
    "s_sub_u32 s68, s68, 2048\n"
    "s_mov_b32 s9, 48\n"
    "s_sub_u32 s82, s82, 16\n"
    "s_sub_u32 s66, s66, 16\n"
    "s_sub_u32 s76, s76, s72\n"
    "s_waitcnt lgkmcnt(0)\n"
    "s_branch .Ldin_A1s_skr\n"
    ".p2align 5\n"
    ".Ldin_L3s:\n"
    "s_cmp_lt_i32 s60, 0\n"
    "s_cbranch_scc1 .Ldin_E1s\n"
    "ds_read_b128 v[144:147], v252 offset:8192\n"
    "ds_read_b128 v[148:151], v252 offset:8256\n"
    "ds_read_b128 v[152:155], v252 offset:16384\n"
    "ds_read_b128 v[156:159], v252 offset:16448\n"
    "v_add_u32 v251, s66, v244\n"
    "v_and_b32 v216, v248, v216\n"
    "v_and_b32 v217, v249, v217\n"
    "v_and_b32 v218, v248, v218\n"
    "v_and_b32 v219, v249, v219\n"
    "v_mfma_f32_4x4x4_16b_bf16 v[160:163], v[216:217], v[96:97], v[32:35]\n"
    "v_and_b32 v220, v248, v220\n"
    "v_and_b32 v221, v249, v221\n"
    "v_mfma_f32_4x4x4_16b_bf16 v[164:167], v[218:219], v[98:99], v[36:39]\n"
    "v_and_b32 v222, v248, v222\n"
    "v_and_b32_e64 v223, v249, v223\n"
    "v_mfma_f32_4x4x4_16b_bf16 v[168:171], v[220:221], v[100:101], v[40:43]\n"
    "v_cvt_pk_bf16_f32 v0, v160, v161\n"
    "v_mfma_f32_4x4x4_16b_bf16 v[224:227], v[228:229], v[200:201], v[224:227] cbsz:4 abid:0\n"
    "v_cvt_pk_bf16_f32 v1, v162, v163\n"
    "v_mfma_f32_4x4x4_16b_bf16 v[172:175], v[222:223], v[102:103], v[44:47]\n"
    "v_cvt_pk_bf16_f32 v2, v164, v165\n"
    "v_cvt_pk_bf16_f32 v3, v166, v167\n"
    "v_mfma_f32_4x4x4_16b_bf16 v[160:163], v[216:217], v[104:105], v[48:51]\n"
    "v_cvt_pk_bf16_f32 v4, v168, v169\n"
    "v_cvt_pk_bf16_f32 v5, v170, v171\n"
    "v_mfma_f32_4x4x4_16b_bf16 v[164:167], v[218:219], v[106:107], v[52:55]\n"
    "v_cvt_pk_bf16_f32 v6, v172, v173\n"
    "v_mfma_f32_4x4x4_16b_bf16 v[224:227], v[228:229], v[202:203], v[224:227] cbsz:4 abid:1\n"
    "v_cvt_pk_bf16_f32 v7, v174, v175\n"
    "v_mfma_f32_4x4x4_16b_bf16 v[168:171], v[220:221], v[108:109], v[56:59]\n"
    "v_cvt_pk_bf16_f32 v8, v160, v161\n"
    "v_cvt_pk_bf16_f32 v9, v162, v163\n"
    "v_mfma_f32_4x4x4_16b_bf16 v[172:175], v[222:223], v[110:111], v[60:63]\n"
    "v_cvt_pk_bf16_f32 v10, v164, v165\n"
    "v_cvt_pk_bf16_f32 v11, v166, v167\n"
    "v_mfma_f32_4x4x4_16b_bf16 v[160:163], v[216:217], v[112:113], v[64:67]\n"
    "v_cvt_pk_bf16_f32 v12, v168, v169\n"
    "v_mfma_f32_4x4x4_16b_bf16 v[224:227], v[228:229], v[204:205], v[224:227] cbsz:4 abid:2\n"
    "v_cvt_pk_bf16_f32 v13, v170, v171\n"
    "s_setprio 1\n"
    "s_waitcnt lgkmcnt(0)\n"
    "v_mfma_f32_4x4x4_16b_bf16 v[164:167], v[218:219], v[114:115], v[68:71]\n"
    "v_cvt_pk_bf16_f32 v14, v172, v173\n"
    "v_cvt_pk_bf16_f32 v15, v174, v175\n"
    "v_mfma_f32_4x4x4_16b_bf16 v[168:171], v[220:221], v[116:117], v[72:75]\n"
    "v_cvt_pk_bf16_f32 v16, v160, v161\n"
    "v_cvt_pk_bf16_f32 v17, v162, v163\n"
    "v_mfma_f32_4x4x4_16b_bf16 v[172:175], v[222:223], v[118:119], v[76:79]\n"
    "v_cvt_pk_bf16_f32 v18, v164, v165\n"
    "v_mfma_f32_4x4x4_16b_bf16 v[224:227], v[228:229], v[206:207], v[224:227] cbsz:4 abid:3\n"
    "v_cvt_pk_bf16_f32 v19, v166, v167\n"
    "v_mfma_f32_4x4x4_16b_bf16 v[160:163], v[216:217], v[120:121], v[80:83]\n"
    "v_cvt_pk_bf16_f32 v20, v168, v169\n"
    "v_cvt_pk_bf16_f32 v21, v170, v171\n"
    "v_mfma_f32_4x4x4_16b_bf16 v[164:167], v[218:219], v[122:123], v[84:87]\n"
    "v_cvt_pk_bf16_f32 v22, v172, v173\n"
    "v_cvt_pk_bf16_f32 v23, v174, v175\n"
    "v_mfma_f32_4x4x4_16b_bf16 v[168:171], v[220:221], v[124:125], v[88:91]\n"
    "v_cvt_pk_bf16_f32 v24, v160, v161\n"
    "v_cvt_pk_bf16_f32 v25, v162, v163\n"
    "v_mfma_f32_4x4x4_16b_bf16 v[172:175], v[222:223], v[126:127], v[92:95]\n"
    "v_cvt_pk_bf16_f32 v26, v164, v165\n"
    "v_cvt_pk_bf16_f32 v27, v166, v167\n"
    "v_cvt_pk_bf16_f32 v28, v168, v169\n"
    "v_cvt_pk_bf16_f32 v29, v170, v171\n"
    "v_mfma_f32_16x16x32_bf16 v[160:163], v[0:3], v[192:195], v[144:147]\n"
    "s_add_u32 m0, s8, 0\n"
    "v_cvt_pk_bf16_f32 v30, v172, v173\n"
    "v_cvt_pk_bf16_f32 v31, v174, v175\n"
    "v_exp_f32 v176, v176 clamp\n"
    "v_mfma_f32_16x16x32_bf16 v[160:163], v[4:7], v[196:199], v[160:163]\n"
    "s_add_u32 s66, s66, 16\n"
    "v_exp_f32 v177, v177 clamp\n"
    "v_exp_f32 v178, v178 clamp\n"
    "v_mfma_f32_16x16x32_bf16 v[164:167], v[8:11], v[192:195], v[148:151]\n"
    "s_add_u32 s76, s76, s73\n"
    "v_exp_f32 v179, v179 clamp\n"
    "v_exp_f32 v180, v180 clamp\n"
    "v_mfma_f32_16x16x32_bf16 v[164:167], v[12:15], v[196:199], v[164:167]\n"
    "s_lshl1_add_u32 s76, s72, s76\n"
    "v_exp_f32 v181, v181 clamp\n"
    "v_exp_f32 v182, v182 clamp\n"
    "v_mfma_f32_16x16x32_bf16 v[168:171], v[16:19], v[192:195], v[152:155]\n"
    "s_mov_b32 s80, s83\n"
    "v_exp_f32 v183, v183 clamp\n"
    "v_exp_f32 v184, v184 clamp\n"
    "v_mfma_f32_16x16x32_bf16 v[168:171], v[20:23], v[196:199], v[168:171]\n"
    "s_add_u32 s62, s62, 256\n"
    "v_exp_f32 v185, v185 clamp\n"
    "v_exp_f32 v186, v186 clamp\n"
    "v_mfma_f32_16x16x32_bf16 v[172:175], v[24:27], v[192:195], v[156:159]\n"
    "v_exp_f32 v187, v187 clamp\n"
    "v_exp_f32 v188, v188 clamp\n"
    "v_mfma_f32_16x16x32_bf16 v[172:175], v[28:31], v[196:199], v[172:175]\n"
    "v_exp_f32 v189, v189 clamp\n"
    "v_exp_f32 v190, v190 clamp\n"
    "v_exp_f32 v191, v191 clamp\n"
    "s_nop 0\n"
    "v_pk_fma_f32 v[176:177], v[176:177], s[70:71], v[128:129]\n"
    "v_pk_fma_f32 v[178:179], v[178:179], s[70:71], v[130:131]\n"
    "v_pk_add_f32 v[230:231], v[176:177], v[178:179]\n"
    "v_pk_mul_f32 v[232:233], v[176:177], v[178:179]\n"
    "v_pk_fma_f32 v[180:181], v[180:181], s[70:71], v[132:133]\n"
    "v_pk_fma_f32 v[230:231], v[230:231], v[180:181], v[232:233]\n"
    "v_pk_mul_f32 v[232:233], v[232:233], v[180:181]\n"
    "v_pk_fma_f32 v[182:183], v[182:183], s[70:71], v[134:135]\n"
    "v_pk_fma_f32 v[230:231], v[230:231], v[182:183], v[232:233]\n"
    "v_pk_mul_f32 v[232:233], v[232:233], v[182:183]\n"
    "v_pk_fma_f32 v[184:185], v[184:185], s[70:71], v[136:137]\n"
    "v_pk_fma_f32 v[230:231], v[230:231], v[184:185], v[232:233]\n"
    "v_pk_mul_f32 v[232:233], v[232:233], v[184:185]\n"
    "v_pk_fma_f32 v[186:187], v[186:187], s[70:71], v[138:139]\n"
    "v_pk_fma_f32 v[230:231], v[230:231], v[186:187], v[232:233]\n"
    "v_pk_mul_f32 v[232:233], v[232:233], v[186:187]\n"
    "v_pk_fma_f32 v[188:189], v[188:189], s[70:71], v[140:141]\n"
    "v_pk_fma_f32 v[230:231], v[230:231], v[188:189], v[232:233]\n"
    "v_pk_mul_f32 v[232:233], v[232:233], v[188:189]\n"
    "v_pk_fma_f32 v[190:191], v[190:191], s[70:71], v[142:143]\n"
    "v_pk_fma_f32 v[230:231], v[230:231], v[190:191], v[232:233]\n"
    "v_pk_mul_f32 v[232:233], v[232:233], v[190:191]\n"
    "v_rcp_f32 v232, v232\n"
    "v_rcp_f32 v233, v233\n"
    "v_fma_f32 v230, v230, v232, v247\n"
    "v_fmac_f32 v230, v231, v233\n"
    "v_mbcnt_lo_u32_b32 v232, -1, 0\n"
    "v_mbcnt_hi_u32_b32 v232, -1, v232\n"
    "v_bfe_u32 v233, v232, 3, 1\n"
    "v_and_b32 v232, 7, v232\n"
    "v_lshl_add_u32 v232, v232, 1, v233\n"
    "v_add_u32 v232, s81, v232\n"
    "ds_read_u8 v232, v232 offset:49664\n"
    "s_add_u32 s81, s81, 16\n"
    "s_waitcnt lgkmcnt(0)\n"
    "buffer_load_ushort v232, v232, s[44:47], s63 idxen\n"
    "s_waitcnt vmcnt(0)\n"
    "v_lshlrev_b32 v232, 16, v232\n"
    "v_mul_f32_e64 v230, v230, v232\n"
    "s_setprio 0\n"
    "ds_read_b64_tr_b16 v[200:201], v240 offset:0\n"
    "ds_read_b64_tr_b16 v[202:203], v241 offset:0\n"
    "ds_read_b64_tr_b16 v[204:205], v242 offset:0\n"
    "ds_read_b64_tr_b16 v[206:207], v243 offset:0\n"
    "ds_read_u8 v236, v251 offset:0\n"
    "ds_read_u8 v238, v251 offset:1\n"
    "ds_write_b16_d16_hi v245, v230\n"
    "ds_read_b64 v[228:229], v246\n"
    "ds_read_b64 v[216:217], v246 offset:128\n"
    "s_mov_b32 s81, s64\n"
    "s_lshr_b32 s63, s65, 6\n"
    "s_waitcnt vmcnt(2)\n"
    "ds_read_b128 v[192:195], v234 offset:2048\n"
    "ds_read_b128 v[196:199], v235 offset:2048\n"
    "s_waitcnt lgkmcnt(5)\n"
    "buffer_load_dwordx4 v[236:237], s[40:43], s67 idxen offen offset:0 nt lds\n"
    "buffer_load_dwordx4 v[238:239], s[40:43], s67 idxen offen offset:1024 nt lds\n"
    "s_waitcnt lgkmcnt(2)\n"
    "s_bitcmp1_b32 s60, 30\n"
    "s_cbranch_scc0 .Ldin_A0\n"
    "s_branch .Ldin_A0s\n"
    ".Ldin_E0s:\n"
    "s_add_u32 m0, s62, 256\n"
    "v_exp_f32 v160, v160 clamp\n"
    "s_setprio 1\n"
    "v_exp_f32 v161, v161 clamp\n"
    "v_exp_f32 v162, v162 clamp\n"
    "v_exp_f32 v163, v163 clamp\n"
    "v_exp_f32 v164, v164 clamp\n"
    "v_exp_f32 v165, v165 clamp\n"
    "v_exp_f32 v166, v166 clamp\n"
    "v_exp_f32 v167, v167 clamp\n"
    "v_mfma_f32_4x4x4_16b_bf16 v[224:227], v[228:229], v[208:209], v[224:227] cbsz:4 abid:0\n"
    "v_exp_f32 v168, v168 clamp\n"
    "v_exp_f32 v169, v169 clamp\n"
    "v_mfma_f32_4x4x4_16b_bf16 v[224:227], v[228:229], v[210:211], v[224:227] cbsz:4 abid:1\n"
    "v_exp_f32 v170, v170 clamp\n"
    "v_exp_f32 v171, v171 clamp\n"
    "v_mfma_f32_4x4x4_16b_bf16 v[224:227], v[228:229], v[212:213], v[224:227] cbsz:4 abid:2\n"
    "v_exp_f32 v172, v172 clamp\n"
    "v_exp_f32 v173, v173 clamp\n"
    "v_mfma_f32_4x4x4_16b_bf16 v[224:227], v[228:229], v[214:215], v[224:227] cbsz:4 abid:3\n"
    "v_exp_f32 v174, v174 clamp\n"
    "v_exp_f32 v175, v175 clamp\n"
    "s_nop 0\n"
    "v_pk_fma_f32 v[160:161], v[160:161], s[70:71], v[128:129]\n"
    "v_pk_fma_f32 v[162:163], v[162:163], s[70:71], v[130:131]\n"
    "v_pk_add_f32 v[230:231], v[160:161], v[162:163]\n"
    "v_pk_mul_f32 v[232:233], v[160:161], v[162:163]\n"
    "v_pk_fma_f32 v[164:165], v[164:165], s[70:71], v[132:133]\n"
    "v_pk_fma_f32 v[230:231], v[230:231], v[164:165], v[232:233]\n"
    "v_pk_mul_f32 v[232:233], v[232:233], v[164:165]\n"
    "v_pk_fma_f32 v[166:167], v[166:167], s[70:71], v[134:135]\n"
    "v_pk_fma_f32 v[230:231], v[230:231], v[166:167], v[232:233]\n"
    "v_pk_mul_f32 v[232:233], v[232:233], v[166:167]\n"
    "v_pk_fma_f32 v[168:169], v[168:169], s[70:71], v[136:137]\n"
    "v_pk_fma_f32 v[230:231], v[230:231], v[168:169], v[232:233]\n"
    "v_pk_mul_f32 v[232:233], v[232:233], v[168:169]\n"
    "v_pk_fma_f32 v[170:171], v[170:171], s[70:71], v[138:139]\n"
    "v_pk_fma_f32 v[230:231], v[230:231], v[170:171], v[232:233]\n"
    "v_pk_mul_f32 v[232:233], v[232:233], v[170:171]\n"
    "v_pk_fma_f32 v[172:173], v[172:173], s[70:71], v[140:141]\n"
    "v_pk_fma_f32 v[230:231], v[230:231], v[172:173], v[232:233]\n"
    "v_pk_mul_f32 v[232:233], v[232:233], v[172:173]\n"
    "v_pk_fma_f32 v[174:175], v[174:175], s[70:71], v[142:143]\n"
    "v_pk_fma_f32 v[230:231], v[230:231], v[174:175], v[232:233]\n"
    "v_pk_mul_f32 v[232:233], v[232:233], v[174:175]\n"
    "v_rcp_f32 v232, v232\n"
    "v_rcp_f32 v233, v233\n"
    "v_fma_f32 v230, v230, v232, v247\n"
    "v_fmac_f32 v230, v231, v233\n"
    "v_mbcnt_lo_u32_b32 v232, -1, 0\n"
    "v_mbcnt_hi_u32_b32 v232, -1, v232\n"
    "v_bfe_u32 v233, v232, 3, 1\n"
    "v_and_b32 v232, 7, v232\n"
    "v_lshl_add_u32 v232, v232, 1, v233\n"
    "v_add_u32 v232, s81, v232\n"
    "ds_read_u8 v232, v232 offset:49664\n"
    "s_add_u32 s81, s81, 16\n"
    "s_waitcnt lgkmcnt(0)\n"
    "buffer_load_ushort v232, v232, s[44:47], s63 idxen\n"
    "s_waitcnt vmcnt(0)\n"
    "v_lshlrev_b32 v232, 16, v232\n"
    "v_mul_f32 v230, v230, v232\n"
    "ds_write_b16_d16_hi v245, v230\n"
    "ds_read_b64 v[228:229], v246\n"
    "s_branch .Ldin_drain0\n"
    ".Ldin_E1s:\n"
    "s_add_u32 m0, s62, 256\n"
    "v_exp_f32 v176, v176 clamp\n"
    "s_setprio 1\n"
    "v_exp_f32 v177, v177 clamp\n"
    "v_exp_f32 v178, v178 clamp\n"
    "v_exp_f32 v179, v179 clamp\n"
    "v_exp_f32 v180, v180 clamp\n"
    "v_exp_f32 v181, v181 clamp\n"
    "v_exp_f32 v182, v182 clamp\n"
    "v_exp_f32 v183, v183 clamp\n"
    "v_mfma_f32_4x4x4_16b_bf16 v[224:227], v[228:229], v[200:201], v[224:227] cbsz:4 abid:0\n"
    "v_exp_f32 v184, v184 clamp\n"
    "v_exp_f32 v185, v185 clamp\n"
    "v_mfma_f32_4x4x4_16b_bf16 v[224:227], v[228:229], v[202:203], v[224:227] cbsz:4 abid:1\n"
    "v_exp_f32 v186, v186 clamp\n"
    "v_exp_f32 v187, v187 clamp\n"
    "v_mfma_f32_4x4x4_16b_bf16 v[224:227], v[228:229], v[204:205], v[224:227] cbsz:4 abid:2\n"
    "v_exp_f32 v188, v188 clamp\n"
    "v_exp_f32 v189, v189 clamp\n"
    "v_mfma_f32_4x4x4_16b_bf16 v[224:227], v[228:229], v[206:207], v[224:227] cbsz:4 abid:3\n"
    "v_exp_f32 v190, v190 clamp\n"
    "v_exp_f32 v191, v191 clamp\n"
    "v_pk_fma_f32 v[176:177], v[176:177], s[70:71], v[128:129]\n"
    "v_pk_fma_f32 v[178:179], v[178:179], s[70:71], v[130:131]\n"
    "v_pk_add_f32 v[230:231], v[176:177], v[178:179]\n"
    "v_pk_mul_f32 v[232:233], v[176:177], v[178:179]\n"
    "v_pk_fma_f32 v[180:181], v[180:181], s[70:71], v[132:133]\n"
    "v_pk_fma_f32 v[230:231], v[230:231], v[180:181], v[232:233]\n"
    "v_pk_mul_f32 v[232:233], v[232:233], v[180:181]\n"
    "v_pk_fma_f32 v[182:183], v[182:183], s[70:71], v[134:135]\n"
    "v_pk_fma_f32 v[230:231], v[230:231], v[182:183], v[232:233]\n"
    "v_pk_mul_f32 v[232:233], v[232:233], v[182:183]\n"
    "v_pk_fma_f32 v[184:185], v[184:185], s[70:71], v[136:137]\n"
    "v_pk_fma_f32 v[230:231], v[230:231], v[184:185], v[232:233]\n"
    "v_pk_mul_f32 v[232:233], v[232:233], v[184:185]\n"
    "v_pk_fma_f32 v[186:187], v[186:187], s[70:71], v[138:139]\n"
    "v_pk_fma_f32 v[230:231], v[230:231], v[186:187], v[232:233]\n"
    "v_pk_mul_f32 v[232:233], v[232:233], v[186:187]\n"
    "v_pk_fma_f32 v[188:189], v[188:189], s[70:71], v[140:141]\n"
    "v_pk_fma_f32 v[230:231], v[230:231], v[188:189], v[232:233]\n"
    "v_pk_mul_f32 v[232:233], v[232:233], v[188:189]\n"
    "v_pk_fma_f32 v[190:191], v[190:191], s[70:71], v[142:143]\n"
    "v_pk_fma_f32 v[230:231], v[230:231], v[190:191], v[232:233]\n"
    "v_pk_mul_f32 v[232:233], v[232:233], v[190:191]\n"
    "v_rcp_f32 v232, v232\n"
    "v_rcp_f32 v233, v233\n"
    "v_fma_f32 v230, v230, v232, v247\n"
    "v_fmac_f32 v230, v231, v233\n"
    "v_mbcnt_lo_u32_b32 v232, -1, 0\n"
    "v_mbcnt_hi_u32_b32 v232, -1, v232\n"
    "v_bfe_u32 v233, v232, 3, 1\n"
    "v_and_b32 v232, 7, v232\n"
    "v_lshl_add_u32 v232, v232, 1, v233\n"
    "v_add_u32 v232, s81, v232\n"
    "ds_read_u8 v232, v232 offset:49664\n"
    "s_add_u32 s81, s81, 16\n"
    "s_waitcnt lgkmcnt(0)\n"
    "buffer_load_ushort v232, v232, s[44:47], s63 idxen\n"
    "s_waitcnt vmcnt(0)\n"
    "v_lshlrev_b32 v232, 16, v232\n"
    "v_mul_f32 v230, v230, v232\n"
    "ds_write_b16_d16_hi v245, v230\n"
    "ds_read_b64 v[228:229], v246\n"
    "s_branch .Ldin_drain1\n"
    ".Ldin_idle:\n"
    "s_waitcnt vmcnt(0)\n"
    ".Ldin_exit:\n"

      :
      : "{s[0:1]}"(__builtin_amdgcn_kernarg_segment_ptr()), "{s2}"(blockIdx.x), "{v0}"(threadIdx.x), "{s97}"((uint32_t)(uintptr_t)smem)
      : "memory", "vcc", "scc", "v1", "v2", "v3", "v4", "v5", "v6", "v7", "v8", "v9", "v10", "v11", "v12", "v13", "v14", "v15", "v16", "v17", "v18", "v19", "v20", "v21", "v22", "v23", "v24", "v25", "v26", "v27", "v28", "v29", "v30", "v31", "v32", "v33", "v34", "v35", "v36", "v37", "v38", "v39", "v40", "v41", "v42", "v43", "v44", "v45", "v46", "v47", "v48", "v49", "v50", "v51", "v52", "v53", "v54", "v55", "v56", "v57", "v58", "v59", "v60", "v61", "v62", "v63", "v64", "v65", "v66", "v67", "v68", "v69", "v70", "v71", "v72", "v73", "v74", "v75", "v76", "v77", "v78", "v79", "v80", "v81", "v82", "v83", "v84", "v85", "v86", "v87", "v88", "v89", "v90", "v91", "v92", "v93", "v94", "v95", "v96", "v97", "v98", "v99", "v100", "v101", "v102", "v103", "v104", "v105", "v106", "v107", "v108", "v109", "v110", "v111", "v112", "v113", "v114", "v115", "v116", "v117", "v118", "v119", "v120", "v121", "v122", "v123", "v124", "v125", "v126", "v127", "v128", "v129", "v130", "v131", "v132", "v133", "v134", "v135", "v136", "v137", "v138", "v139", "v140", "v141", "v142", "v143", "v144", "v145", "v146", "v147", "v148", "v149", "v150", "v151", "v152", "v153", "v154", "v155", "v156", "v157", "v158", "v159", "v160", "v161", "v162", "v163", "v164", "v165", "v166", "v167", "v168", "v169", "v170", "v171", "v172", "v173", "v174", "v175", "v176", "v177", "v178", "v179", "v180", "v181", "v182", "v183", "v184", "v185", "v186", "v187", "v188", "v189", "v190", "v191", "v192", "v193", "v194", "v195", "v196", "v197", "v198", "v199", "v200", "v201", "v202", "v203", "v204", "v205", "v206", "v207", "v208", "v209", "v210", "v211", "v212", "v213", "v214", "v215", "v216", "v217", "v218", "v219", "v220", "v221", "v222", "v223", "v224", "v225", "v226", "v227", "v228", "v229", "v230", "v231", "v232", "v233", "v234", "v235", "v236", "v237", "v238", "v239", "v240", "v241", "v242", "v243", "v244", "v245", "v246", "v247", "v248", "v249", "v250", "v251", "v252", "v253", "s3", "s4", "s5", "s6", "s7", "s8", "s9", "s10", "s11", "s12", "s13", "s14", "s15", "s16", "s17", "s18", "s19", "s20", "s21", "s22", "s23", "s24", "s25", "s26", "s27", "s28", "s29", "s30", "s31", "s33", "s34", "s35", "s36", "s37", "s38", "s39", "s40", "s41", "s42", "s43", "s44", "s45", "s46", "s47", "s48", "s49", "s50", "s51", "s52", "s53", "s54", "s55", "s56", "s57", "s58", "s59", "s60", "s61", "s62", "s63", "s64", "s65", "s66", "s67", "s68", "s69", "s70", "s71", "s72", "s73", "s74", "s75", "s76", "s77", "s78", "s79", "s80", "s81", "s82", "s83", "s84", "s85", "s86", "s87", "s88", "s89", "s90", "s91", "s92", "s93", "s94", "s95", "s96");
}

__global__ __launch_bounds__(64) void din_fallback_kernel(
    const unsigned short* __restrict__ target, const unsigned short* __restrict__ hist,
    const unsigned short* __restrict__ mask, const unsigned short* __restrict__ w1,
    const unsigned short* __restrict__ b1, const unsigned short* __restrict__ w2,
    const unsigned short* __restrict__ b2, unsigned short* __restrict__ out, int B, int L) {
  __shared__ float s_w[64 * 64];
  __shared__ float s_x[64];
  const int b = blockIdx.x, h = threadIdx.x;
  float bias = bf2f(b1[h]);
  for (int d = 0; d < 64; ++d) {
    const float t = bf2f(target[(size_t)b * 64 + d]);
    const float wc = bf2f(w1[(128 + d) * 64 + h]);
    bias += t * (bf2f(w1[(64 + d) * 64 + h]) - wc);
    s_w[d * 64 + h] = bf2f(w1[d * 64 + h]) + wc + t * bf2f(w1[(192 + d) * 64 + h]);
  }
  const float w2h = bf2f(w2[h]), b2v = bf2f(b2[0]);
  float acc = 0.f;
  for (int l = 0; l < L; ++l) {
    const float m = bf2f(mask[(size_t)b * L + l]);
    if (m == 0.f) continue;
    const float x = bf2f(hist[((size_t)b * L + l) * 64 + h]);
    __syncthreads();
    s_x[h] = x;
    __syncthreads();
    float z = bias;
    for (int d = 0; d < 64; ++d) z += s_x[d] * s_w[d * 64 + h];
    float s = w2h / (1.0f + __expf(-z));
    for (int o = 32; o > 0; o >>= 1) s += __shfl_xor(s, o);
    acc += (s + b2v) * m * x;
  }
  const uint32_t u = __float_as_uint(acc);
  out[(size_t)b * 64 + h] = (unsigned short)((u + 0x7fffu + ((u >> 16) & 1u)) >> 16);
}

extern "C" void kernel_launch(void* const* d_in, const int* in_sizes, int n_in, void* d_out,
                              int out_size, void* d_ws, size_t ws_size, hipStream_t stream) {
  const unsigned short* target = (const unsigned short*)d_in[0];
  const unsigned short* hist = (const unsigned short*)d_in[1];
  const unsigned short* mask = (const unsigned short*)d_in[2];
  const unsigned short* w1 = (const unsigned short*)d_in[3];
  const unsigned short* b1 = (const unsigned short*)d_in[4];
  const unsigned short* w2 = (const unsigned short*)d_in[5];
  const unsigned short* b2 = (const unsigned short*)d_in[6];
  int B = in_sizes[0] / 64;
  int L = in_sizes[2] / B;
  if ((L & 1) || L > 254 || B >= (1 << 24) - 16) {
    din_fallback_kernel<<<B, 64, 0, stream>>>(target, hist, mask, w1, b1, w2, b2, (unsigned short*)d_out, B, L);
    return;
  }
  const uint32_t G = (B + 63) / 64, L2 = 2u * L, lanes = (L2 + 15) / 16, F = 0x20000u;
  din_kernel<<<G, 512, 0, stream>>>(hist, (uint32_t)L, F, mask, (uint32_t)B * L2, F, w1, 32768u, F, (unsigned short*)d_out, (uint32_t)B * 128u, F,
                                    target, (uint32_t)B * 128u, F, b1, w2, b2, L2, G * 8u * L2, lanes >= 32 ? 0xffffffffu : (1u << lanes) - 1u,
                                    G * 8u * 128u, G >= 224 ? 0xffffffffu : 0u, L2 + 31u);
}
```

```cpp
#include <hip/hip_runtime.h>
#include <stdint.h>

__device__ __forceinline__ float bf2f(unsigned short u) { return __uint_as_float(((uint32_t)u) << 16); }

__global__ __launch_bounds__(512, 2) void din_kernel(
    const unsigned short* hist, uint32_t hn, uint32_t hf, const unsigned short* mask, uint32_t mn, uint32_t mf,
    const unsigned short* w1, uint32_t wn, uint32_t wf, unsigned short* out, uint32_t on, uint32_t of,
    const unsigned short* target, uint32_t tn, uint32_t tf, const unsigned short* b1, const unsigned short* w2, const unsigned short* b2,
    uint32_t row_bytes, uint32_t round_bytes, uint32_t row_lanes, uint32_t round_out, uint32_t paced, uint32_t first_row) {
  __shared__ __attribute__((aligned(1024))) char smem[154624];
  asm volatile(
    ".p2align 3\n"
    "s_load_dwordx16 s[40:55], s[0:1], 0x0\n"
    "s_load_dwordx16 s[4:19], s[0:1], 0x40\n"
    "s_memrealtime s[76:77]\n"
    "v_readfirstlane_b32 s3, v0\n"
    "v_and_b32 v1, 63, v0\n"
    "v_and_b32 v2, 48, v0\n"
    "v_lshlrev_b32 v251, 4, v1\n"
    "s_lshr_b32 s20, s3, 6\n"
    "s_lshl_b32 s78, s2, 3\n"
    "s_and_b32 s21, s20, 3\n"
    "s_lshr_b32 s75, s3, 8\n"
    "s_cselect_b64 vcc, -1, 0\n"
    "s_cbranch_scc0 .Ldin_p0\n"
    "s_setprio 1\n"
    ".Ldin_p0:\n"
    "s_cmp_lt_u32 s2, 32\n"
    "s_cbranch_scc0 .Ldin_ncp\n"
    "s_getpc_b64 s[34:35]\n"
    "v_lshlrev_b32 v5, 7, v1\n"
    "s_mul_i32 s23, s20, 3072\n"
    "s_add_u32 s34, s34, s23\n"
    "s_addc_u32 s35, s35, 0\n"
    "s_mov_b64 exec, 0xffffff\n"
    "global_load_dword v253, v5, s[34:35] offset:1024\n"
    "s_mov_b64 exec, -1\n"
    ".Ldin_ncp:\n"
    "v_lshlrev_b32 v6, 2, v0\n"
    "v_and_b32 v7, 32, v0\n"
    "v_and_b32 v6, 64, v6\n"
    "v_xor_b32 v5, v251, v2\n"
    "v_bitop3_b32 v6, v6, v7, v251 bitop3:0x96\n"
    "v_and_b32 v3, 0x1f0, v251\n"
    "v_xor_b32 v7, 32, v6\n"
    "v_mov_b64 v[40:41], 0\n"
    "v_mov_b64 v[42:43], 0\n"
    "v_mov_b64 v[44:45], 0\n"
    "v_mov_b64 v[46:47], 0\n"
    "v_bfe_u32 v50, v251, 3, 5\n"
    "s_lshl_b32 s23, s21, 5\n"
    "v_lshrrev_b32_e64 v51, 1, v2\n"
    "s_lshl_b32 s24, s20, 12\n"
    "v_or_b32 v51, s23, v51\n"
    "s_add_u32 m0, s24, 54144\n"
    "s_add_u32 s22, s78, s20\n"
    "s_lshl_b32 s25, s20, 10\n"
    "s_waitcnt lgkmcnt(0)\n"
    "s_add_u32 s22, s22, s19\n"
    "s_mul_i32 s67, s22, s14\n"
    "s_cbranch_vccz .Ldin_pm\n"
    "global_load_short_d16_hi v40, v50, s[10:11] offset:0\n"
    "global_load_short_d16_hi v41, v50, s[10:11] offset:32\n"
    "global_load_short_d16_hi v42, v50, s[10:11] offset:64\n"
    "global_load_short_d16_hi v43, v50, s[10:11] offset:96\n"
    "global_load_short_d16_hi v44, v45, s[12:13]\n"
    "global_load_dwordx2 v[46:47], v51, s[10:11]\n"
    "global_load_dwordx2 v[48:49], v51, s[8:9]\n"
    "s_branch .Ldin_p1\n"
    ".Ldin_pm:\n"
    "s_mov_b32 exec_lo, s16\n"
    "s_mov_b32 exec_hi, 0\n"
    "s_lshl2_add_u32 s28, s14, s67\n"
    "buffer_load_dwordx4 v[40:43], v3, s[44:47], s67 offen nt\n"
    "s_add_u32 s29, s67, s15\n"
    "buffer_load_dwordx4 v[44:47], v3, s[44:47], s28 offen nt\n"
    "s_mov_b32 exec_hi, s16\n"
    "s_mov_b32 exec_lo, 0\n"
    "s_add_u32 s30, s28, s15\n"
    "buffer_load_dwordx4 v[40:43], v3, s[44:47], s29 offen nt\n"
    "buffer_load_dwordx4 v[44:47], v3, s[44:47], s30 offen nt\n"
    "s_mov_b64 exec, -1\n"
    ".Ldin_p1:\n"
    "s_mul_i32 s23, s20, s17\n"
    "buffer_load_dwordx4 v6, s[48:51], s24 offen offset:0 lds\n"
    "buffer_load_dwordx4 v7, s[48:51], s24 offen offset:1024 lds\n"
    "buffer_load_dwordx4 v6, s[48:51], s24 offen offset:2048 lds\n"
    "buffer_load_dwordx4 v7, s[48:51], s24 offen offset:3072 lds\n"
    "s_add_u32 s78, s78, s19\n"
    "s_add_u32 m0, s25, 24576\n"
    "s_lshl_b32 s26, s78, 7\n"
    "s_add_u32 s23, s23, s26\n"
    "buffer_load_dwordx4 v5, s[4:7], s23 offen nt lds\n"
    ".Ldin_p2:\n"
    "s_lshl_b32 s67, s67, 6\n"
    "s_lshl_b32 s68, s20, 13\n"
    "s_lshl_b32 s23, s20, 8\n"
    "s_add_u32 s68, s68, 89088\n"
    "s_add_u32 s23, s23, 86912\n"
    "v_and_b32 v32, 0x70, v5\n"
    "v_lshrrev_b32 v4, 1, v3\n"
    "v_or_b32 v237, 0x400, v32\n"
    "v_xor_b32 v239, 64, v32\n"
    "v_and_b32 v32, 0x70, v4\n"
    "v_lshlrev_b32 v33, 6, v50\n"
    "v_bitop3_b32 v36, v32, v2, v33 bitop3:0x96\n"
    "v_and_b32 v32, 8, v0\n"
    "v_add_u32 v234, s68, v36\n"
    "v_mul_u32_u24 v33, 0x88, v32\n"
    "v_xor_b32 v235, 64, v234\n"
    "v_lshrrev_b32 v245, 1, v32\n"
    "v_lshlrev_b32 v32, 1, v2\n"
    "v_and_b32 v34, 0x18, v4\n"
    "v_bitop3_b32 v33, v33, v32, v34 bitop3:0x96\n"
    "v_or3_b32 v245, v245, v32, s23\n"
    "v_and_b32 v32, 0x40, v251\n"
    "v_lshl_or_b32 v246, v34, 2, s23\n"
    "v_lshl_or_b32 v33, v32, 1, v33\n"
    "v_and_b32 v32, 24, v50\n"
    "v_add_u32 v240, s68, v33\n"
    "v_or_b32 v246, v246, v32\n"
    "v_xor_b32 v241, 0x110, v240\n"
    "v_xor_b32 v242, 0x220, v240\n"
    "v_xor_b32 v243, 0x330, v240\n"
    "v_and_b32 v32, 12, v50\n"
    "v_and_b32 v33, 2, v50\n"
    "v_lshl_or_b32 v32, v32, 1, v33\n"
    "v_bfe_u32 v244, v0, 3, 3\n"
    "v_or_b32 v245, v245, v32\n"
    "s_mov_b32 s24, 49664\n"
    "v_lshl_add_u32 v244, v244, 1, s24\n"
    "v_or_b32 v250, 0x6000, v2\n"
    "s_mov_b64 s[26:27], 0xffff\n"
    "v_lshlrev_b64 v[248:249], v251, s[26:27]\n"
    "v_add_u32 v13, 1, v4\n"
    "v_mov_b64 v[14:15], -1\n"
    "v_lshl_add_u32 v8, v13, 16, v4\n"
    "v_add_u32 v9, 0x20002, v8\n"
    "v_add_u32 v10, 0x40004, v8\n"
    "v_add_u32 v11, 0x60006, v8\n"
    "v_mov_b32 v17, v237\n"
    "v_mov_b32 v19, v239\n"
    "v_mov_b32 v21, v237\n"
    "v_mov_b32 v23, v239\n"
    "v_mov_b32 v25, v237\n"
    "v_mov_b32 v27, v239\n"
    "v_mov_b32 v29, v237\n"
    "v_mov_b32 v31, v239\n"
    "v_mov_b32 v32, -1\n"
    "v_mov_b32 v33, 0\n"
    "s_mov_b64 exec, 0xff00\n"
    "v_mov_b32 v32, 0x8c000000\n"
    "s_mov_b64 exec, 0xfffc\n"
    "ds_write_b64 v246, v[32:33] offset:128\n"
    "s_mov_b64 exec, 0xfff\n"
    "ds_write_b64 v4, v[14:15] offset:49664\n"
    "ds_write_b32 v33, v33 offset:49416\n"
    "s_mov_b64 exec, -1\n"
    "s_mov_b32 s28, 0xc080c080\n"
    "s_mov_b32 s29, 0xffff0001\n"
    "s_mov_b32 s30, 0x01000100\n"
    "s_mov_b32 s31, 0x00010001\n"
    "s_add_u32 s40, s40, 0xfffffc00\n"
    "s_addc_u32 s41, s41, 0x7fffff\n"
    "s_lshl_b32 s56, s14, 6\n"
    "s_lshl_b32 s57, s15, 6\n"
    "s_ashr_i32 s79, s17, 7\n"
    "s_mul_i32 s58, s78, s56\n"
    "s_mov_b32 s59, 1\n"
    "s_mul_i32 s62, s20, 2304\n"
    "s_mov_b32 s70, 0x47000000\n"
    "s_mov_b32 s71, 0x47000000\n"
    "s_add_u32 s62, s62, 18016\n"
    "s_and_b32 s77, s18, 53\n"
    "s_and_b32 s73, s18, 8\n"
    "s_add_u32 s19, s14, 31\n"
    "s_lshr_b32 s74, s6, 7\n"
    "s_cbranch_vccnz .Ldin_py\n"
    "v_lshrrev_b32 v32, 5, v1\n"
    "s_mul_i32 s24, s20, 272\n"
    "v_mul_u32_u24 v12, 2176, v32\n"
    "v_and_b32 v60, 32, v0\n"
    "v_add3_u32 v12, v12, s24, 15\n"
    "v_add_u32 v60, s23, v60\n"
    "v_add_u32 v13, v13, v12\n"
    "v_mov_b32 v57, 0\n"
    "v_mov_b32_e64 v59, 0\n"
    "ds_write_b64 v13, v[14:15] offset:49760\n"
    "ds_write_b64 v13, v[14:15] offset:50848\n"
    "s_mul_i32 s23, s20, 0x80110\n"
    "s_lshr_b32 s24, s20, 1\n"
    "s_lshl_b32 s24, s24, 16\n"
    "s_add_u32 s23, s23, s24\n"
    "s_add_u32 s25, s22, s79\n"
    "s_add_u32 s26, s23, 0x70\n"
    "s_cmp_lt_u32 s22, s74\n"
    "s_cselect_b32 s26, s26, 0x80000000\n"
    "v_writelane_b32 v56, s26, 31\n"
    "s_add_u32 s26, s23, 0x4008f0\n"
    "s_cmp_lt_u32 s25, s74\n"
    "s_cselect_b32 s26, s26, 0x80000000\n"
    "v_writelane_b32 v56, s26, 63\n"
    "s_add_u32 s26, s23, 0x2204b0\n"
    "s_add_u32 s22, s22, 4\n"
    "s_cmp_lt_u32 s22, s74\n"
    "s_cselect_b32 s26, s26, 0x80000000\n"
    "v_writelane_b32 v58, s26, 31\n"
    "s_add_u32 s26, s23, 0x620d30\n"
    "s_add_u32 s25, s25, 4\n"
    "s_cmp_lt_u32 s25, s74\n"
    "s_cselect_b32 s26, s26, 0x80000000\n"
    "v_writelane_b32 v58, s26, 63\n"
    "s_cmp_lg_u32 s21, 0\n"
    "s_cbranch_scc1 .Ldin_tw0\n"
    "s_call_b64 s[34:35], .Ldin_stub_A0\n"
    "s_call_b64 s[34:35], .Ldin_stub_G1\n"
    "s_call_b64 s[34:35], .Ldin_stub_G1_slp\n"
    "s_branch .Ldin_twe\n"
    ".Ldin_tw0:\n"
    "s_cmp_lg_u32 s21, 1\n"
    "s_cbranch_scc1 .Ldin_tw1\n"
    "s_call_b64 s[34:35], .Ldin_stub_L2\n"
    "s_call_b64 s[34:35], .Ldin_stub_L1\n"
    "s_branch .Ldin_twe\n"
    ".Ldin_tw1:\n"
    "s_cmp_lg_u32 s21, 2\n"
    "s_cbranch_scc1 .Ldin_tw2\n"
    "s_call_b64 s[34:35], .Ldin_stub_L0\n"
    "s_call_b64 s[34:35], .Ldin_stub_L3\n"
    "s_call_b64 s[34:35], .Ldin_stub_ps\n"
    "s_branch .Ldin_twe\n"
    ".Ldin_tw2:\n"
    "s_call_b64 s[34:35], .Ldin_stub_E0\n"
    "s_call_b64 s[34:35], .Ldin_stub_E1\n"
    ".Ldin_twe:\n"
    "s_and_b32 s23, s14, 14\n"
    "s_waitcnt vmcnt(5)\n"
    "s_cbranch_scc1 .Ldin_trimo\n"
    ".Ldin_trimo_r:\n"
    "v_pk_min_u16 v48, v40, 1 op_sel_hi:[1,0]\n"
    "v_pk_min_u16 v49, v41, 1 op_sel_hi:[1,0]\n"
    "v_pk_min_u16 v50, v42, 1 op_sel_hi:[1,0]\n"
    "v_pk_min_u16 v51, v43, 1 op_sel_hi:[1,0]\n"
    "v_add3_u32 v64, v48, v49, v50\n"
    "v_add_u32_e64 v64, v64, v51\n"
    "v_dot2_u32_u16 v62, v64, s31, 0\n"
    "v_pk_min_u16 v52, v44, 1 op_sel_hi:[1,0]\n"
    "v_pk_min_u16 v53, v45, 1 op_sel_hi:[1,0]\n"
    "v_pk_min_u16 v54, v46, 1 op_sel_hi:[1,0]\n"
    "v_pk_min_u16 v55, v47, 1 op_sel_hi:[1,0]\n"
    "v_add3_u32 v64, v52, v53, v54\n"
    "v_add_u32_e64 v64, v64, v55\n"
    "v_dot2_u32_u16 v62, v64, s30, v62\n"
    "v_pk_mad_u16 v40, v48, s28, v40\n"
    "v_pk_mad_u16 v41, v49, s28, v41\n"
    "v_pk_mad_u16 v42, v50, s28, v42\n"
    "v_add_u32_dpp v63, v62, v62 row_shr:1 row_mask:0xf bank_mask:0xf bound_ctrl:1\n"
    "v_sub_u32 v64, 0x10101, v62\n"
    "v_pk_mad_u16 v43, v51, s28, v43\n"
    "v_add_u32_dpp v63, v63, v63 row_shr:2 row_mask:0xf bank_mask:0xf bound_ctrl:1\n"
    "v_or3_b32 v65, v40, v41, 0\n"
    "v_or3_b32 v65, v42, v43, v65\n"
    "v_add_u32_dpp v63, v63, v63 row_shr:4 row_mask:0xf bank_mask:0xf bound_ctrl:1\n"
    "v_pk_mad_u16 v44, v52, s28, v44\n"
    "v_pk_mad_u16 v45, v53, s28, v45\n"
    "v_add_u32_dpp v63, v63, v63 row_shr:8 row_mask:0xf bank_mask:0xf bound_ctrl:1\n"
    "v_pk_mad_u16 v46, v54, s28, v46\n"
    "v_pk_mad_u16 v47, v55, s28, v47\n"
    "v_add_u32_dpp v63, v63, v63 row_bcast:15 row_mask:0xa bank_mask:0xf\n"
    "v_or3_b32 v65, v44, v45, v65\n"
    "v_or3_b32 v65, v46, v47, v65\n"
    "v_add_u32_e64 v64, v64, v63\n"
    "s_mov_b32 s26, 0xc040c00\n"
    "v_perm_b32 v66, v63, v64, s26\n"
    "v_mad_u32_u16 v68, v48, v66, v12\n"
    "v_mad_u32_u16 v69, v48, v66, v12 op_sel:[1,1,0,0]\n"
    "v_pk_mad_u16 v66, v48, s29, v66\n"
    "ds_write_b8 v68, v8 offset:49760\n"
    "ds_write_b8_d16_hi v69, v8 offset:49760\n"
    "v_mad_u32_u16 v70, v49, v66, v12\n"
    "v_mad_u32_u16 v71, v49, v66, v12 op_sel:[1,1,0,0]\n"
    "v_pk_mad_u16 v66, v49, s29, v66\n"
    "ds_write_b8 v70, v9 offset:49760\n"
    "ds_write_b8_d16_hi v71, v9 offset:49760\n"
    "v_mad_u32_u16 v68, v50, v66, v12\n"
    "v_mad_u32_u16 v69, v50, v66, v12 op_sel:[1,1,0,0]\n"
    "v_pk_mad_u16 v66, v50, s29, v66\n"
    "ds_write_b8 v68, v10 offset:49760\n"
    "ds_write_b8_d16_hi v69, v10 offset:49760\n"
    "v_mad_u32_u16 v70, v51, v66, v12\n"
    "v_mad_u32_u16 v71, v51, v66, v12 op_sel:[1,1,0,0]\n"
    "ds_write_b8 v70, v11 offset:49760\n"
    "ds_write_b8_d16_hi v71, v11 offset:49760\n"
    "s_waitcnt vmcnt(0)\n"
    "s_barrier\n"
    "s_mul_i32 s23, s20, 272\n"
    "v_add_u32_e64 v66, s23, v244\n"
    "ds_read_u8 v16, v66 offset:112\n"
    "ds_read_u8 v18, v66 offset:113\n"
    "ds_read_u8 v20, v66 offset:128\n"
    "ds_read_u8 v22, v66 offset:129\n"
    "ds_read_u8 v24, v66 offset:144\n"
    "ds_read_u8 v26, v66 offset:145\n"
    "ds_read_u8 v28, v66 offset:160\n"
    "ds_read_u8 v30, v66 offset:161\n"
    "v_cmp_ne_u32 s[26:27], 0, v65\n"
    "s_mov_b32 s24, 0x0c010c00\n"
    "v_perm_b32 v62, v63, v63, s24\n"
    "s_cmp_lg_u64 s[26:27], 0\n"
    "v_pk_add_u16 v62, v62, 15 op_sel_hi:[1,0]\n"
    "s_cselect_b32 s24, 0x200020, 0\n"
    "v_pk_lshrrev_b16 v62, 4, v62 op_sel_hi:[0,1]\n"
    "v_pk_max_u16 v62, v62, 6 op_sel_hi:[1,0]\n"
    "v_or_b32 v62, s24, v62\n"
    "v_lshl_or_b32 v56, v62, 25, v56\n"
    "v_lshrrev_b32 v62, 16, v62\n"
    "s_add_u32 m0, s68, 0\n"
    "s_waitcnt lgkmcnt(6)\n"
    "buffer_load_dwordx4 v[16:17], s[40:43], s67 idxen offen offset:0 nt lds\n"
    "buffer_load_dwordx4 v[18:19], s[40:43], s67 idxen offen offset:1024 nt lds\n"
    "s_add_u32 m0, s68, 2048\n"
    "s_waitcnt lgkmcnt(4)\n"
    "buffer_load_dwordx4 v[20:21], s[40:43], s67 idxen offen offset:0 nt lds\n"
    "buffer_load_dwordx4 v[22:23], s[40:43], s67 idxen offen offset:1024 nt lds\n"
    "s_add_u32 m0, s68, 4096\n"
    "s_waitcnt lgkmcnt(2)\n"
    "buffer_load_dwordx4 v[24:25], s[40:43], s67 idxen offen offset:0 nt lds\n"
    "buffer_load_dwordx4 v[26:27], s[40:43], s67 idxen offen offset:1024 nt lds\n"
    "s_add_u32 m0, s68, 6144\n"
    "s_waitcnt lgkmcnt(0)\n"
    "buffer_load_dwordx4 v[28:29], s[40:43], s67 idxen offen offset:0 nt lds\n"
    "buffer_load_dwordx4 v[30:31], s[40:43], s67 idxen offen offset:1024 nt lds\n"
    "v_lshl_or_b32 v58, v62, 25, v58\n"
    "v_readlane_b32 s60, v56, 31\n"
    "s_mov_b32 s26, 0xc050c01\n"
    "v_perm_b32 v66, v63, v64, s26\n"
    "v_mad_u32_u16 v68, v52, v66, v12\n"
    "v_mad_u32_u16 v69, v52, v66, v12 op_sel:[1,1,0,0]\n"
    "v_pk_mad_u16 v66, v52, s29, v66\n"
    "ds_write_b8 v68, v8 offset:50848\n"
    "ds_write_b8_d16_hi v69, v8 offset:50848\n"
    "v_mad_u32_u16 v70, v53, v66, v12\n"
    "v_mad_u32_u16 v71, v53, v66, v12 op_sel:[1,1,0,0]\n"
    "v_pk_mad_u16 v66, v53, s29, v66\n"
    "ds_write_b8 v70, v9 offset:50848\n"
    "ds_write_b8_d16_hi v71, v9 offset:50848\n"
    "v_mad_u32_u16 v68, v54, v66, v12\n"
    "v_mad_u32_u16 v69, v54, v66, v12 op_sel:[1,1,0,0]\n"
    "v_pk_mad_u16 v66, v54, s29, v66\n"
    "ds_write_b8 v68, v10 offset:50848\n"
    "ds_write_b8_d16_hi v69, v10 offset:50848\n"
    "v_mad_u32_u16 v70, v55, v66, v12\n"
    "v_mad_u32_u16 v71, v55, v66, v12 op_sel:[1,1,0,0]\n"
    "ds_write_b8 v70, v11 offset:50848\n"
    "ds_write_b8_d16_hi v71, v11 offset:50848\n"
    "s_brev_b32 exec_lo, 1\n"
    "s_brev_b32 exec_hi, 1\n"
    "ds_write_b64 v60, v[56:57] offset:128\n"
    "ds_write_b64 v60, v[58:59] offset:1152\n"
    "s_mov_b64 exec, -1\n"
    "s_waitcnt lgkmcnt(0)\n"
    "s_barrier\n"
    ".Ldin_go:\n"
    "s_or_b32 s45, s45, 0x20000\n"
    "s_mov_b32 s46, s42\n"
    "s_cmp_lt_i32 s60, 0\n"
    "s_cbranch_scc1 .Ldin_idle\n"
    "s_lshr_b32 s94, s60, 12\n"
    "s_and_b32 s94, s94, 0x1ff0\n"
    "v_xor_b32 v252, s94, v250\n"
    "ds_read_b128 v[216:219], v252 offset:0\n"
    "ds_read_b128 v[220:223], v252 offset:64\n"
    "ds_read_b128 v[96:99], v251 offset:16384\n"
    "ds_read_b128 v[32:35], v251 offset:0\n"
    "ds_read_b128 v[36:39], v251 offset:1024\n"
    "ds_read_b128 v[100:103], v251 offset:17408\n"
    "ds_read_b128 v[40:43], v251 offset:2048\n"
    "ds_read_b128 v[44:47], v251 offset:3072\n"
    "ds_read_b128 v[104:107], v251 offset:18432\n"
    "ds_read_b128 v[48:51], v251 offset:4096\n"
    "ds_read_b128 v[52:55], v251 offset:5120\n"
    "ds_read_b128 v[108:111], v251 offset:19456\n"
    "ds_read_b128 v[56:59], v251 offset:6144\n"
    "ds_read_b128 v[60:63], v251 offset:7168\n"
    "s_and_b32 s66, s60, 0xffff\n"
    "s_bfe_u32 s95, s60, 0x50019\n"
    "s_mov_b64 s[14:15], 0\n"
    "s_cmp_eq_u32 s75, 0\n"
    "s_cselect_b32 s16, 18, 12\n"
    "s_cselect_b32 s17, 50, 0\n"
    "s_add_u32 s17, s17, 60\n"
    "s_mov_b32 s81, s66\n"
    "s_lshr_b32 s63, s67, 6\n"
    "s_lshl4_add_u32 s80, s95, s66\n"
    "v_add_u32 v230, s66, v244\n"
    "s_add_u32 s66, s66, 80\n"
    "s_mov_b32 s8, s68\n"
    "s_mov_b32 s9, 64\n"
    "s_lshl_b32 s94, s20, 2\n"
    "s_lshr_b32 s10, 0x34567654, s94\n"
    "s_and_b32 s10, s10, 15\n"
    "s_add_u32 m0, s8, 0\n"
    "s_waitcnt lgkmcnt(12)\n"
    "v_and_b32 v216, v248, v216\n"
    "v_and_b32 v217, v249, v217\n"
    "v_and_b32 v218, v248, v218\n"
    "v_and_b32 v219, v249, v219\n"
    "s_waitcnt lgkmcnt(9)\n"
    "ds_read_b128 v[112:115], v251 offset:20480\n"
    "ds_read_b128 v[64:67], v251 offset:8192\n"
    "ds_read_b128 v[68:71], v251 offset:9216\n"
    "v_mfma_f32_4x4x4_16b_bf16 v[176:179], v[216:217], v[96:97], v[32:35]\n"
    "v_and_b32 v220, v248, v220\n"
    "v_and_b32 v221, v249, v221\n"
    "v_mfma_f32_4x4x4_16b_bf16 v[180:183], v[218:219], v[98:99], v[36:39]\n"
    "v_and_b32 v222, v248, v222\n"
    "v_and_b32_e64 v223, v249, v223\n"
    "s_waitcnt lgkmcnt(9)\n"
    "ds_read_b128 v[116:119], v251 offset:21504\n"
    "ds_read_b128 v[72:75], v251 offset:10240\n"
    "ds_read_b128 v[76:79], v251 offset:11264\n"
    "v_mfma_f32_4x4x4_16b_bf16 v[184:187], v[220:221], v[100:101], v[40:43]\n"
    "v_cvt_pk_bf16_f32 v0, v176, v177\n"
    "v_cvt_pk_bf16_f32 v1, v178, v179\n"
    "v_mfma_f32_4x4x4_16b_bf16 v[188:191], v[222:223], v[102:103], v[44:47]\n"
    "v_cvt_pk_bf16_f32 v2, v180, v181\n"
    "v_cvt_pk_bf16_f32 v3, v182, v183\n"
    "s_waitcnt lgkmcnt(9)\n"
    "s_nop 0\n"
    "ds_read_b128 v[120:123], v251 offset:22528\n"
    "ds_read_b128 v[80:83], v251 offset:12288\n"
    "ds_read_b128 v[84:87], v251 offset:13312\n"
    "v_mfma_f32_4x4x4_16b_bf16 v[176:179], v[216:217], v[104:105], v[48:51]\n"
    "v_cvt_pk_bf16_f32 v4, v184, v185\n"
    "v_cvt_pk_bf16_f32 v5, v186, v187\n"
    "v_mfma_f32_4x4x4_16b_bf16 v[180:183], v[218:219], v[106:107], v[52:55]\n"
    "v_cvt_pk_bf16_f32 v6, v188, v189\n"
    "v_cvt_pk_bf16_f32 v7, v190, v191\n"
    "s_waitcnt lgkmcnt(9)\n"
    "s_nop 0\n"
    "ds_read_b128 v[124:127], v251 offset:23552\n"
    "ds_read_b128 v[88:91], v251 offset:14336\n"
    "ds_read_b128 v[92:95], v251 offset:15360\n"
    "v_mfma_f32_4x4x4_16b_bf16 v[184:187], v[220:221], v[108:109], v[56:59]\n"
    "v_cvt_pk_bf16_f32 v8, v176, v177\n"
    "v_cvt_pk_bf16_f32 v9, v178, v179\n"
    "v_mfma_f32_4x4x4_16b_bf16 v[188:191], v[222:223], v[110:111], v[60:63]\n"
    "v_cvt_pk_bf16_f32 v10, v180, v181\n"
    "v_cvt_pk_bf16_f32 v11, v182, v183\n"
    "s_waitcnt lgkmcnt(9)\n"
    "s_nop 0\n"
    "ds_read_b128 v[144:147], v252 offset:8192\n"
    "ds_read_b128 v[148:151], v252 offset:8256\n"
    "ds_read_b128 v[152:155], v252 offset:16384\n"
    "ds_read_b128 v[156:159], v252 offset:16448\n"
    "ds_read_u8 v236, v230 offset:64\n"
    "ds_read_u8 v238, v230 offset:65\n"
    "v_mfma_f32_4x4x4_16b_bf16 v[176:179], v[216:217], v[112:113], v[64:67]\n"
    "v_cvt_pk_bf16_f32 v12, v184, v185\n"
    "v_cvt_pk_bf16_f32 v13, v186, v187\n"
    "v_mfma_f32_4x4x4_16b_bf16 v[180:183], v[218:219], v[114:115], v[68:71]\n"
    "v_cvt_pk_bf16_f32 v14, v188, v189\n"
    "v_cvt_pk_bf16_f32 v15, v190, v191\n"
    "s_waitcnt lgkmcnt(12)\n"
    "s_waitcnt vmcnt(4)\n"
    "ds_read_b128 v[192:195], v234 offset:0\n"
    "ds_read_b128 v[196:199], v235 offset:0\n"
    "ds_read_b64_tr_b16 v[200:201], v240 offset:0\n"
    "ds_read_b64_tr_b16 v[202:203], v241 offset:0\n"
    "ds_read_b64_tr_b16 v[204:205], v242 offset:0\n"
    "ds_read_b64_tr_b16 v[206:207], v243 offset:0\n"
    "v_mfma_f32_4x4x4_16b_bf16 v[184:187], v[220:221], v[116:117], v[72:75]\n"
    "v_cvt_pk_bf16_f32 v16, v176, v177\n"
    "v_cvt_pk_bf16_f32 v17, v178, v179\n"
    "v_mfma_f32_4x4x4_16b_bf16 v[188:191], v[222:223], v[118:119], v[76:79]\n"
    "v_cvt_pk_bf16_f32 v18, v180, v181\n"
    "v_cvt_pk_bf16_f32 v19, v182, v183\n"
    "s_waitcnt lgkmcnt(15)\n"
    "s_nop 0\n"
    "ds_read_b128 v[128:131], v250 offset:24576\n"
    "ds_read_b128 v[132:135], v250 offset:24640\n"
    "ds_read_b128 v[136:139], v250 offset:24704\n"
    "ds_read_b128 v[140:143], v250 offset:24768\n"
    "ds_read_b64 v[232:233], v250 offset:24832\n"
    "v_mfma_f32_4x4x4_16b_bf16 v[176:179], v[216:217], v[120:121], v[80:83]\n"
    "v_cvt_pk_bf16_f32 v20, v184, v185\n"
    "v_cvt_pk_bf16_f32 v21, v186, v187\n"
    "v_mfma_f32_4x4x4_16b_bf16 v[180:183], v[218:219], v[122:123], v[84:87]\n"
    "v_cvt_pk_bf16_f32 v22, v188, v189\n"
    "v_cvt_pk_bf16_f32 v23, v190, v191\n"
    "s_waitcnt lgkmcnt(15)\n"
    "v_mfma_f32_4x4x4_16b_bf16 v[184:187], v[220:221], v[124:125], v[88:91]\n"
    "v_cvt_pk_bf16_f32 v24, v176, v177\n"
    "v_cvt_pk_bf16_f32 v25, v178, v179\n"
    "v_mfma_f32_4x4x4_16b_bf16 v[188:191], v[222:223], v[126:127], v[92:95]\n"
    "v_cvt_pk_bf16_f32 v26, v180, v181\n"
    "v_cvt_pk_bf16_f32 v27, v182, v183\n"
    "s_waitcnt lgkmcnt(5)\n"
    "buffer_load_dwordx4 v[236:237], s[40:43], s67 idxen offen offset:0 nt lds\n"
    "buffer_load_dwordx4 v[238:239], s[40:43], s67 idxen offen offset:1024 nt lds\n"
    "v_cvt_pk_bf16_f32 v28, v184, v185\n"
    "v_cvt_pk_bf16_f32 v29, v186, v187\n"
    "v_cvt_pk_bf16_f32 v30, v188, v189\n"
    "v_cvt_pk_bf16_f32 v31, v190, v191\n"
    "v_mfma_f32_16x16x32_bf16 v[160:163], v[0:3], v[192:195], v[144:147]\n"
    "v_mfma_f32_16x16x32_bf16 v[160:163], v[4:7], v[196:199], v[160:163]\n"
    "v_mfma_f32_16x16x32_bf16 v[164:167], v[8:11], v[192:195], v[148:151]\n"
    "v_mfma_f32_16x16x32_bf16 v[164:167], v[12:15], v[196:199], v[164:167]\n"
    "v_mfma_f32_16x16x32_bf16 v[168:171], v[16:19], v[192:195], v[152:155]\n"
    "v_mfma_f32_16x16x32_bf16 v[168:171], v[20:23], v[196:199], v[168:171]\n"
    "v_mfma_f32_16x16x32_bf16 v[172:175], v[24:27], v[192:195], v[156:159]\n"
    "v_mfma_f32_16x16x32_bf16 v[172:175], v[28:31], v[196:199], v[172:175]\n"
    "ds_read_b128 v[192:195], v234 offset:2048\n"
    "ds_read_b128 v[196:199], v235 offset:2048\n"
    "ds_read_b64 v[216:217], v246 offset:128\n"
    "s_memrealtime s[34:35]\n"
    "s_waitcnt lgkmcnt(0)\n"
    "s_mul_i32 s94, s75, 60\n"
    "s_add_u32 s94, s94, 0\n"
    "s_and_b32 s94, s18, s94\n"
    "s_add_u32 s76, s34, s94\n"
    "v_mov_b32 v247, v232\n"
    "v_readfirstlane_b32 s69, v233\n"
    "s_bitcmp1_b32 s60, 30\n"
    "s_nop 0\n"
    "s_cbranch_scc1 .Ldin_A0s\n"
    "s_branch .Ldin_A0\n"
    ".p2align 12\n"
    ".Ldin_stub_G1:\n"
    "s_setpc_b64 s[34:35]\n"
    ".p2align 5\n"
    ".Ldin_G1:\n"
    "v_add_u32 v251, s66, v244\n"
    "v_mfma_f32_4x4x4_16b_bf16 v[224:227], v[228:229], v[200:201], v[224:227] cbsz:4 abid:0\n"
    "v_exp_f32 v176, v176 clamp\n"
    "v_exp_f32 v177, v177 clamp\n"
    "v_mfma_f32_4x4x4_16b_bf16 v[224:227], v[228:229], v[202:203], v[224:227] cbsz:4 abid:1\n"
    "v_exp_f32 v178, v178 clamp\n"
    "v_exp_f32 v179, v179 clamp\n"
    "v_mfma_f32_4x4x4_16b_bf16 v[224:227], v[228:229], v[204:205], v[224:227] cbsz:4 abid:2\n"
    "v_exp_f32 v180, v180 clamp\n"
    "v_exp_f32 v181, v181 clamp\n"
    "v_mfma_f32_4x4x4_16b_bf16 v[224:227], v[228:229], v[206:207], v[224:227] cbsz:4 abid:3\n"
    "s_waitcnt lgkmcnt(0)\n"
    "v_mfma_f32_16x16x32_bf16 v[160:163], v[0:3], v[192:195], v[144:147]\n"
    "s_setprio 1\n"
    "v_exp_f32 v182, v182 clamp\n"
    "v_mfma_f32_16x16x32_bf16 v[160:163], v[4:7], v[196:199], v[160:163]\n"
    "s_add_u32 m0, s68, 4096\n"
    "v_exp_f32 v183, v183 clamp\n"
    "v_mfma_f32_16x16x32_bf16 v[164:167], v[8:11], v[192:195], v[148:151]\n"
    "s_add_u32 s66, s66, 16\n"
    "v_exp_f32 v184, v184 clamp\n"
    "v_mfma_f32_16x16x32_bf16 v[164:167], v[12:15], v[196:199], v[164:167]\n"
    "v_exp_f32 v185, v185 clamp\n"
    "v_mfma_f32_16x16x32_bf16 v[168:171], v[16:19], v[192:195], v[152:155]\n"
    "v_exp_f32 v186, v186 clamp\n"
    "v_mfma_f32_16x16x32_bf16 v[168:171], v[20:23], v[196:199], v[168:171]\n"
    "v_exp_f32 v187, v187 clamp\n"
    "v_mfma_f32_16x16x32_bf16 v[172:175], v[24:27], v[192:195], v[156:159]\n"
    "v_exp_f32 v188, v188 clamp\n"
    "v_mfma_f32_16x16x32_bf16 v[172:175], v[28:31], v[196:199], v[172:175]\n"
    "s_memrealtime s[34:35]\n"
    "v_exp_f32 v189, v189 clamp\n"
    "v_exp_f32 v190, v190 clamp\n"
    "v_exp_f32 v191, v191 clamp\n"
    "v_pk_fma_f32 v[176:177], v[176:177], s[70:71], v[128:129]\n"
    "v_pk_fma_f32 v[178:179], v[178:179], s[70:71], v[130:131]\n"
    "v_pk_add_f32 v[230:231], v[176:177], v[178:179]\n"
    "v_pk_mul_f32 v[232:233], v[176:177], v[178:179]\n"
    "v_pk_fma_f32 v[180:181], v[180:181], s[70:71], v[132:133]\n"
    "v_pk_fma_f32 v[230:231], v[230:231], v[180:181], v[232:233]\n"
    "v_pk_mul_f32 v[232:233], v[232:233], v[180:181]\n"
    "v_pk_fma_f32 v[182:183], v[182:183], s[70:71], v[134:135]\n"
    "v_pk_fma_f32 v[230:231], v[230:231], v[182:183], v[232:233]\n"
    "v_pk_mul_f32 v[232:233], v[232:233], v[182:183]\n"
    "v_pk_fma_f32 v[184:185], v[184:185], s[70:71], v[136:137]\n"
    "v_pk_fma_f32 v[230:231], v[230:231], v[184:185], v[232:233]\n"
    "v_pk_mul_f32 v[232:233], v[232:233], v[184:185]\n"
    "v_pk_fma_f32 v[186:187], v[186:187], s[70:71], v[138:139]\n"
    "v_pk_fma_f32 v[230:231], v[230:231], v[186:187], v[232:233]\n"
    "v_pk_mul_f32 v[232:233], v[232:233], v[186:187]\n"
    "v_pk_fma_f32 v[188:189], v[188:189], s[70:71], v[140:141]\n"
    "v_pk_fma_f32 v[230:231], v[230:231], v[188:189], v[232:233]\n"
    "v_pk_mul_f32 v[232:233], v[232:233], v[188:189]\n"
    "v_pk_fma_f32 v[190:191], v[190:191], s[70:71], v[142:143]\n"
    "v_pk_fma_f32 v[230:231], v[230:231], v[190:191], v[232:233]\n"
    "v_pk_mul_f32 v[232:233], v[232:233], v[190:191]\n"
    "v_rcp_f32 v232, v232\n"
    "v_rcp_f32 v233, v233\n"
    "v_fma_f32 v230, v230, v232, v247\n"
    "v_fmac_f32_e64 v230, v231, v233\n"
    "s_setprio 0\n"
    "s_waitcnt lgkmcnt(0)\n"
    "ds_read_b64_tr_b16 v[200:201], v240 offset:4096\n"
    "ds_read_b64_tr_b16 v[202:203], v241 offset:4096\n"
    "ds_read_b64_tr_b16 v[204:205], v242 offset:4096\n"
    "ds_read_b64_tr_b16 v[206:207], v243 offset:4096\n"
    "ds_read_u8 v236, v251 offset:0\n"
    "ds_read_u8 v238, v251 offset:1\n"
    "ds_write_b16_d16_hi v245, v230\n"
    "ds_read_b64 v[228:229], v246\n"
    "s_cmp_lt_u32 s34, s76\n"
    "s_cbranch_scc1 .Ldin_G1_wait\n"
    ".Ldin_G1_go:\n"
    "s_waitcnt vmcnt(2)\n"
    "ds_read_b128 v[192:195], v234 offset:6144\n"
    "ds_read_b128 v[196:199], v235 offset:6144\n"
    "s_waitcnt lgkmcnt(4)\n"
    "buffer_load_dwordx4 v[236:237], s[40:43], s67 idxen offen offset:0 nt lds\n"
    "buffer_load_dwordx4 v[238:239], s[40:43], s67 idxen offen offset:1024 nt lds\n"
    "s_add_u32 s76, s76, s72\n"
    "s_cmp_eq_u32 s66, s80\n"
    "s_cselect_b64 s[66:67], s[64:65], s[66:67]\n"
    "s_waitcnt lgkmcnt(2)\n"
    "s_cmp_eq_u32 s66, s82\n"
    "s_cbranch_scc1 .Ldin_L2\n"
    ".Ldin_G2:\n"
    "v_add_u32 v251, s66, v244\n"
    "v_mfma_f32_4x4x4_16b_bf16 v[224:227], v[228:229], v[208:209], v[224:227] cbsz:4 abid:0\n"
    "v_exp_f32 v160, v160 clamp\n"
    "v_exp_f32 v161, v161 clamp\n"
    "v_mfma_f32_4x4x4_16b_bf16 v[224:227], v[228:229], v[210:211], v[224:227] cbsz:4 abid:1\n"
    "v_exp_f32 v162, v162 clamp\n"
    "v_exp_f32 v163, v163 clamp\n"
    "v_mfma_f32_4x4x4_16b_bf16 v[224:227], v[228:229], v[212:213], v[224:227] cbsz:4 abid:2\n"
    "v_exp_f32 v164, v164 clamp\n"
    "v_exp_f32 v165, v165 clamp\n"
    "v_mfma_f32_4x4x4_16b_bf16 v[224:227], v[228:229], v[214:215], v[224:227] cbsz:4 abid:3\n"
    "s_waitcnt lgkmcnt(0)\n"
    "v_mfma_f32_16x16x32_bf16 v[176:179], v[0:3], v[192:195], v[144:147]\n"
    "s_setprio 1\n"
    "v_exp_f32 v166, v166 clamp\n"
    "v_mfma_f32_16x16x32_bf16 v[176:179], v[4:7], v[196:199], v[176:179]\n"
    "s_add_u32 m0, s68, 6144\n"
    "v_exp_f32 v167, v167 clamp\n"
    "v_mfma_f32_16x16x32_bf16 v[180:183], v[8:11], v[192:195], v[148:151]\n"
    "s_add_u32 s66, s66, 16\n"
    "v_exp_f32 v168, v168 clamp\n"
    "v_mfma_f32_16x16x32_bf16 v[180:183], v[12:15], v[196:199], v[180:183]\n"
    "v_exp_f32 v169, v169 clamp\n"
    "v_mfma_f32_16x16x32_bf16 v[184:187], v[16:19], v[192:195], v[152:155]\n"
    "v_exp_f32 v170, v170 clamp\n"
    "v_mfma_f32_16x16x32_bf16 v[184:187], v[20:23], v[196:199], v[184:187]\n"
    "v_exp_f32 v171, v171 clamp\n"
    "v_mfma_f32_16x16x32_bf16 v[188:191], v[24:27], v[192:195], v[156:159]\n"
    "v_exp_f32 v172, v172 clamp\n"
    "v_mfma_f32_16x16x32_bf16 v[188:191], v[28:31], v[196:199], v[188:191]\n"
    "s_memrealtime s[34:35]\n"
    "v_exp_f32 v173, v173 clamp\n"
    "v_exp_f32 v174, v174 clamp\n"
    "v_exp_f32 v175, v175 clamp\n"
    "v_pk_fma_f32 v[160:161], v[160:161], s[70:71], v[128:129]\n"
    "v_pk_fma_f32 v[162:163], v[162:163], s[70:71], v[130:131]\n"
    "v_pk_add_f32 v[230:231], v[160:161], v[162:163]\n"
    "v_pk_mul_f32 v[232:233], v[160:161], v[162:163]\n"
    "v_pk_fma_f32 v[164:165], v[164:165], s[70:71], v[132:133]\n"
    "v_pk_fma_f32 v[230:231], v[230:231], v[164:165], v[232:233]\n"
    "v_pk_mul_f32 v[232:233], v[232:233], v[164:165]\n"
    "v_pk_fma_f32 v[166:167], v[166:167], s[70:71], v[134:135]\n"
    "v_pk_fma_f32 v[230:231], v[230:231], v[166:167], v[232:233]\n"
    "v_pk_mul_f32 v[232:233], v[232:233], v[166:167]\n"
    "v_pk_fma_f32 v[168:169], v[168:169], s[70:71], v[136:137]\n"
    "v_pk_fma_f32 v[230:231], v[230:231], v[168:169], v[232:233]\n"
    "v_pk_mul_f32 v[232:233], v[232:233], v[168:169]\n"
    "v_pk_fma_f32 v[170:171], v[170:171], s[70:71], v[138:139]\n"
    "v_pk_fma_f32 v[230:231], v[230:231], v[170:171], v[232:233]\n"
    "v_pk_mul_f32 v[232:233], v[232:233], v[170:171]\n"
    "v_pk_fma_f32 v[172:173], v[172:173], s[70:71], v[140:141]\n"
    "v_pk_fma_f32 v[230:231], v[230:231], v[172:173], v[232:233]\n"
    "v_pk_mul_f32 v[232:233], v[232:233], v[172:173]\n"
    "v_pk_fma_f32 v[174:175], v[174:175], s[70:71], v[142:143]\n"
    "v_pk_fma_f32 v[230:231], v[230:231], v[174:175], v[232:233]\n"
    "v_pk_mul_f32 v[232:233], v[232:233], v[174:175]\n"
    "v_rcp_f32 v232, v232\n"
    "v_rcp_f32 v233, v233\n"
    "v_fma_f32 v230, v230, v232, v247\n"
    "v_fmac_f32_e64 v230, v231, v233\n"
    "s_setprio 0\n"
    "s_waitcnt lgkmcnt(0)\n"
    "ds_read_b64_tr_b16 v[208:209], v240 offset:6144\n"
    "ds_read_b64_tr_b16 v[210:211], v241 offset:6144\n"
    "ds_read_b64_tr_b16 v[212:213], v242 offset:6144\n"
    "ds_read_b64_tr_b16 v[214:215], v243 offset:6144\n"
    "ds_read_u8 v236, v251 offset:0\n"
    "ds_read_u8 v238, v251 offset:1\n"
    "ds_write_b16_d16_hi v245, v230\n"
    "ds_read_b64 v[228:229], v246\n"
    "s_cmp_lt_u32 s34, s76\n"
    "s_cbranch_scc1 .Ldin_G2_wait\n"
    ".Ldin_G2_go:\n"
    "s_waitcnt vmcnt(2)\n"
    "ds_read_b128 v[192:195], v234 offset:0\n"
    "ds_read_b128 v[196:199], v235 offset:0\n"
    "s_waitcnt lgkmcnt(4)\n"
    "buffer_load_dwordx4 v[236:237], s[40:43], s67 idxen offen offset:0 nt lds\n"
    "buffer_load_dwordx4 v[238:239], s[40:43], s67 idxen offen offset:1024 nt lds\n"
    "s_sub_u32 s94, s34, 30\n"
    "s_max_u32 s76, s76, s94\n"
    "s_add_u32 s76, s76, s72\n"
    "s_cmp_eq_u32 s66, s80\n"
    "s_cselect_b64 s[66:67], s[64:65], s[66:67]\n"
    "s_waitcnt lgkmcnt(2)\n"
    "s_cmp_eq_u32 s66, s82\n"
    "s_cbranch_scc1 .Ldin_L3\n"
    ".Ldin_G3:\n"
    "v_add_u32_e64 v251, s66, v244\n"
    "v_mfma_f32_4x4x4_16b_bf16 v[224:227], v[228:229], v[200:201], v[224:227] cbsz:4 abid:0\n"
    "v_exp_f32 v176, v176 clamp\n"
    "v_exp_f32 v177, v177 clamp\n"
    "v_mfma_f32_4x4x4_16b_bf16 v[224:227], v[228:229], v[202:203], v[224:227] cbsz:4 abid:1\n"
    "v_exp_f32 v178, v178 clamp\n"
    "v_exp_f32 v179, v179 clamp\n"
    "v_mfma_f32_4x4x4_16b_bf16 v[224:227], v[228:229], v[204:205], v[224:227] cbsz:4 abid:2\n"
    "v_exp_f32 v180, v180 clamp\n"
    "v_exp_f32 v181, v181 clamp\n"
    "v_mfma_f32_4x4x4_16b_bf16 v[224:227], v[228:229], v[206:207], v[224:227] cbsz:4 abid:3\n"
    "s_waitcnt lgkmcnt(0)\n"
    "v_mfma_f32_16x16x32_bf16 v[160:163], v[0:3], v[192:195], v[144:147]\n"
    "s_setprio 1\n"
    "v_exp_f32 v182, v182 clamp\n"
    "v_mfma_f32_16x16x32_bf16 v[160:163], v[4:7], v[196:199], v[160:163]\n"
    "s_add_u32 m0, s8, 0\n"
    "v_exp_f32 v183, v183 clamp\n"
    "v_mfma_f32_16x16x32_bf16 v[164:167], v[8:11], v[192:195], v[148:151]\n"
    "s_add_u32 s66, s66, 16\n"
    "v_exp_f32 v184, v184 clamp\n"
    "v_mfma_f32_16x16x32_bf16 v[164:167], v[12:15], v[196:199], v[164:167]\n"
    "v_exp_f32 v185, v185 clamp\n"
    "v_mfma_f32_16x16x32_bf16 v[168:171], v[16:19], v[192:195], v[152:155]\n"
    "v_exp_f32 v186, v186 clamp\n"
    "v_mfma_f32_16x16x32_bf16 v[168:171], v[20:23], v[196:199], v[168:171]\n"
    "v_exp_f32 v187, v187 clamp\n"
    "v_mfma_f32_16x16x32_bf16 v[172:175], v[24:27], v[192:195], v[156:159]\n"
    "v_exp_f32 v188, v188 clamp\n"
    "v_mfma_f32_16x16x32_bf16 v[172:175], v[28:31], v[196:199], v[172:175]\n"
    "s_memrealtime s[34:35]\n"
    "v_exp_f32 v189, v189 clamp\n"
    "v_exp_f32 v190, v190 clamp\n"
    "v_exp_f32 v191, v191 clamp\n"
    "v_pk_fma_f32 v[176:177], v[176:177], s[70:71], v[128:129]\n"
    "v_pk_fma_f32 v[178:179], v[178:179], s[70:71], v[130:131]\n"
    "v_pk_add_f32 v[230:231], v[176:177], v[178:179]\n"
    "v_pk_mul_f32 v[232:233], v[176:177], v[178:179]\n"
    "v_pk_fma_f32 v[180:181], v[180:181], s[70:71], v[132:133]\n"
    "v_pk_fma_f32 v[230:231], v[230:231], v[180:181], v[232:233]\n"
    "v_pk_mul_f32 v[232:233], v[232:233], v[180:181]\n"
    "v_pk_fma_f32 v[182:183], v[182:183], s[70:71], v[134:135]\n"
    "v_pk_fma_f32 v[230:231], v[230:231], v[182:183], v[232:233]\n"
    "v_pk_mul_f32 v[232:233], v[232:233], v[182:183]\n"
    "v_pk_fma_f32 v[184:185], v[184:185], s[70:71], v[136:137]\n"
    "v_pk_fma_f32 v[230:231], v[230:231], v[184:185], v[232:233]\n"
    "v_pk_mul_f32 v[232:233], v[232:233], v[184:185]\n"
    "v_pk_fma_f32 v[186:187], v[186:187], s[70:71], v[138:139]\n"
    "v_pk_fma_f32 v[230:231], v[230:231], v[186:187], v[232:233]\n"
    "v_pk_mul_f32 v[232:233], v[232:233], v[186:187]\n"
    "v_pk_fma_f32 v[188:189], v[188:189], s[70:71], v[140:141]\n"
    "v_pk_fma_f32 v[230:231], v[230:231], v[188:189], v[232:233]\n"
    "v_pk_mul_f32 v[232:233], v[232:233], v[188:189]\n"
    "v_pk_fma_f32 v[190:191], v[190:191], s[70:71], v[142:143]\n"
    "v_pk_fma_f32 v[230:231], v[230:231], v[190:191], v[232:233]\n"
    "v_pk_mul_f32 v[232:233], v[232:233], v[190:191]\n"
    "v_rcp_f32 v232, v232\n"
    "v_rcp_f32 v233, v233\n"
    "v_fma_f32 v230, v230, v232, v247\n"
    "v_fmac_f32_e64 v230, v231, v233\n"
    "s_setprio 0\n"
    "s_waitcnt lgkmcnt(0)\n"
    "ds_read_b64_tr_b16 v[200:201], v240 offset:0\n"
    "ds_read_b64_tr_b16 v[202:203], v241 offset:0\n"
    "ds_read_b64_tr_b16 v[204:205], v242 offset:0\n"
    "ds_read_b64_tr_b16 v[206:207], v243 offset:0\n"
    "ds_read_u8 v236, v251 offset:0\n"
    "ds_read_u8 v238, v251 offset:1\n"
    "ds_write_b16_d16_hi v245, v230\n"
    "ds_read_b64 v[228:229], v246\n"
    "s_cmp_lt_u32 s34, s76\n"
    "s_cbranch_scc1 .Ldin_G3_wait\n"
    ".Ldin_G3_go:\n"
    "s_waitcnt vmcnt(2)\n"
    "ds_read_b128 v[192:195], v234 offset:2048\n"
    "ds_read_b128 v[196:199], v235 offset:2048\n"
    "s_waitcnt lgkmcnt(4)\n"
    "buffer_load_dwordx4 v[236:237], s[40:43], s67 idxen offen offset:0 nt lds\n"
    "buffer_load_dwordx4 v[238:239], s[40:43], s67 idxen offen offset:1024 nt lds\n"
    "s_add_u32 s76, s76, s72\n"
    "s_cmp_eq_u32 s66, s80\n"
    "s_cselect_b64 s[66:67], s[64:65], s[66:67]\n"
    "s_waitcnt lgkmcnt(2)\n"
    "s_cmp_eq_u32 s66, s82\n"
    "s_cbranch_scc1 .Ldin_L0\n"
    ".Ldin_G0:\n"
    "v_add_u32 v251, s66, v244\n"
    "v_mfma_f32_4x4x4_16b_bf16 v[224:227], v[228:229], v[208:209], v[224:227] cbsz:4 abid:0\n"
    "v_exp_f32 v160, v160 clamp\n"
    "v_exp_f32 v161, v161 clamp\n"
    "v_mfma_f32_4x4x4_16b_bf16 v[224:227], v[228:229], v[210:211], v[224:227] cbsz:4 abid:1\n"
    "v_exp_f32 v162, v162 clamp\n"
    "v_exp_f32 v163, v163 clamp\n"
    "v_mfma_f32_4x4x4_16b_bf16 v[224:227], v[228:229], v[212:213], v[224:227] cbsz:4 abid:2\n"
    "v_exp_f32 v164, v164 clamp\n"
    "v_exp_f32 v165, v165 clamp\n"
    "v_mfma_f32_4x4x4_16b_bf16 v[224:227], v[228:229], v[214:215], v[224:227] cbsz:4 abid:3\n"
    "s_waitcnt lgkmcnt(0)\n"
    "v_mfma_f32_16x16x32_bf16 v[176:179], v[0:3], v[192:195], v[144:147]\n"
    "s_setprio 1\n"
    "v_exp_f32 v166, v166 clamp\n"
    "v_mfma_f32_16x16x32_bf16 v[176:179], v[4:7], v[196:199], v[176:179]\n"
    "s_add_u32 m0, s68, 2048\n"
    "v_exp_f32 v167, v167 clamp\n"
    "v_mfma_f32_16x16x32_bf16 v[180:183], v[8:11], v[192:195], v[148:151]\n"
    "s_add_u32 s66, s66, 16\n"
    "v_exp_f32 v168, v168 clamp\n"
    "v_mfma_f32_16x16x32_bf16 v[180:183], v[12:15], v[196:199], v[180:183]\n"
    "v_exp_f32 v169, v169 clamp\n"
    "v_mfma_f32_16x16x32_bf16 v[184:187], v[16:19], v[192:195], v[152:155]\n"
    "v_exp_f32 v170, v170 clamp\n"
    "v_mfma_f32_16x16x32_bf16 v[184:187], v[20:23], v[196:199], v[184:187]\n"
    "v_exp_f32 v171, v171 clamp\n"
    "v_mfma_f32_16x16x32_bf16 v[188:191], v[24:27], v[192:195], v[156:159]\n"
    "v_exp_f32 v172, v172 clamp\n"
    "v_mfma_f32_16x16x32_bf16 v[188:191], v[28:31], v[196:199], v[188:191]\n"
    "s_memrealtime s[34:35]\n"
    "v_exp_f32 v173, v173 clamp\n"
    "v_exp_f32 v174, v174 clamp\n"
    "v_exp_f32 v175, v175 clamp\n"
    "v_pk_fma_f32 v[160:161], v[160:161], s[70:71], v[128:129]\n"
    "v_pk_fma_f32 v[162:163], v[162:163], s[70:71], v[130:131]\n"
    "v_pk_add_f32 v[230:231], v[160:161], v[162:163]\n"
    "v_pk_mul_f32 v[232:233], v[160:161], v[162:163]\n"
    "v_pk_fma_f32 v[164:165], v[164:165], s[70:71], v[132:133]\n"
    "v_pk_fma_f32 v[230:231], v[230:231], v[164:165], v[232:233]\n"
    "v_pk_mul_f32 v[232:233], v[232:233], v[164:165]\n"
    "v_pk_fma_f32 v[166:167], v[166:167], s[70:71], v[134:135]\n"
    "v_pk_fma_f32 v[230:231], v[230:231], v[166:167], v[232:233]\n"
    "v_pk_mul_f32 v[232:233], v[232:233], v[166:167]\n"
    "v_pk_fma_f32 v[168:169], v[168:169], s[70:71], v[136:137]\n"
    "v_pk_fma_f32 v[230:231], v[230:231], v[168:169], v[232:233]\n"
    "v_pk_mul_f32 v[232:233], v[232:233], v[168:169]\n"
    "v_pk_fma_f32 v[170:171], v[170:171], s[70:71], v[138:139]\n"
    "v_pk_fma_f32 v[230:231], v[230:231], v[170:171], v[232:233]\n"
    "v_pk_mul_f32 v[232:233], v[232:233], v[170:171]\n"
    "v_pk_fma_f32 v[172:173], v[172:173], s[70:71], v[140:141]\n"
    "v_pk_fma_f32 v[230:231], v[230:231], v[172:173], v[232:233]\n"
    "v_pk_mul_f32 v[232:233], v[232:233], v[172:173]\n"
    "v_pk_fma_f32 v[174:175], v[174:175], s[70:71], v[142:143]\n"
    "v_pk_fma_f32 v[230:231], v[230:231], v[174:175], v[232:233]\n"
    "v_pk_mul_f32 v[232:233], v[232:233], v[174:175]\n"
    "v_rcp_f32 v232, v232\n"
    "v_rcp_f32 v233, v233\n"
    "v_fma_f32 v230, v230, v232, v247\n"
    "v_fmac_f32_e64 v230, v231, v233\n"
    "s_setprio 0\n"
    "s_waitcnt lgkmcnt(0)\n"
    "ds_read_b64_tr_b16 v[208:209], v240 offset:2048\n"
    "ds_read_b64_tr_b16 v[210:211], v241 offset:2048\n"
    "ds_read_b64_tr_b16 v[212:213], v242 offset:2048\n"
    "ds_read_b64_tr_b16 v[214:215], v243 offset:2048\n"
    "ds_read_u8 v236, v251 offset:0\n"
    "ds_read_u8 v238, v251 offset:1\n"
    "ds_write_b16_d16_hi v245, v230\n"
    "ds_read_b64 v[228:229], v246\n"
    "s_cmp_lt_u32 s34, s76\n"
    "s_cbranch_scc1 .Ldin_G0_wait\n"
    ".Ldin_G0_go:\n"
    "s_waitcnt vmcnt(2)\n"
    "ds_read_b128 v[192:195], v234 offset:4096\n"
    "ds_read_b128 v[196:199], v235 offset:4096\n"
    "s_waitcnt lgkmcnt(4)\n"
    "buffer_load_dwordx4 v[236:237], s[40:43], s67 idxen offen offset:0 nt lds\n"
    "buffer_load_dwordx4 v[238:239], s[40:43], s67 idxen offen offset:1024 nt lds\n"
    "s_sub_u32 s94, s34, 30\n"
    "s_max_u32 s76, s76, s94\n"
    "s_add_u32 s76, s76, s72\n"
    "s_cmp_eq_u32 s66, s80\n"
    "s_cselect_b64 s[66:67], s[64:65], s[66:67]\n"
    "s_waitcnt lgkmcnt(2)\n"
    "s_cmp_eq_u32 s66, s82\n"
    "s_cbranch_scc1 .Ldin_L1\n"
    "s_branch .Ldin_G1\n"
    ".Ldin_stub_G1_slp:\n"
    "s_setpc_b64 s[34:35]\n"
    ".Ldin_G1_slp:\n"
    "s_sleep 1\n"
    ".Ldin_G1_wait:\n"
    "s_memrealtime s[34:35]\n"
    "s_waitcnt lgkmcnt(0)\n"
    "s_sub_u32 s94, s34, s76\n"
    "s_cmp_lt_i32 s94, 0\n"
    "s_cbranch_scc1 .Ldin_G1_slp\n"
    "s_getreg_b32 s94, hwreg(HW_REG_IB_STS, 0, 4)\n"
    "s_cmp_gt_u32 s94, 2\n"
    "s_addc_u32 s15, s15, 0\n"
    "s_branch .Ldin_G1_go\n"
    ".Ldin_G2_slp:\n"
    "s_sleep 1\n"
    ".Ldin_G2_wait:\n"
    "s_memrealtime s[34:35]\n"
    "s_waitcnt lgkmcnt(0)\n"
    "s_sub_u32 s94, s34, s76\n"
    "s_cmp_lt_i32 s94, 0\n"
    "s_cbranch_scc1 .Ldin_G2_slp\n"
    "s_getreg_b32 s94, hwreg(HW_REG_IB_STS, 0, 4)\n"
    "s_cmp_gt_u32 s94, 2\n"
    "s_addc_u32 s15, s15, 0\n"
    "s_branch .Ldin_G2_go\n"
    ".Ldin_G3_slp:\n"
    "s_sleep 1\n"
    ".Ldin_G3_wait:\n"
    "s_memrealtime s[34:35]\n"
    "s_waitcnt lgkmcnt(0)\n"
    "s_sub_u32 s94, s34, s76\n"
    "s_cmp_lt_i32 s94, 0\n"
    "s_cbranch_scc1 .Ldin_G3_slp\n"
    "s_getreg_b32 s94, hwreg(HW_REG_IB_STS, 0, 4)\n"
    "s_cmp_gt_u32 s94, 2\n"
    "s_addc_u32 s15, s15, 0\n"
    "s_branch .Ldin_G3_go\n"
    ".Ldin_G0_slp:\n"
    "s_sleep 1\n"
    ".Ldin_G0_wait:\n"
    "s_memrealtime s[34:35]\n"
    "s_waitcnt lgkmcnt(0)\n"
    "s_sub_u32 s94, s34, s76\n"
    "s_cmp_lt_i32 s94, 0\n"
    "s_cbranch_scc1 .Ldin_G0_slp\n"
    "s_getreg_b32 s94, hwreg(HW_REG_IB_STS, 0, 4)\n"
    "s_cmp_gt_u32 s94, 2\n"
    "s_addc_u32 s15, s15, 0\n"
    "s_branch .Ldin_G0_go\n"
    ".Ldin_stub_E0:\n"
    "s_setpc_b64 s[34:35]\n"
    ".p2align 5\n"
    ".Ldin_E0:\n"
    "s_add_u32 m0, s62, 256\n"
    "v_exp_f32 v160, v160 clamp\n"
    "s_setprio 1\n"
    "v_exp_f32 v161, v161 clamp\n"
    "v_exp_f32 v162, v162 clamp\n"
    "v_exp_f32 v163, v163 clamp\n"
    "v_exp_f32 v164, v164 clamp\n"
    "v_exp_f32 v165, v165 clamp\n"
    "v_exp_f32 v166, v166 clamp\n"
    "v_exp_f32 v167, v167 clamp\n"
    "v_mfma_f32_4x4x4_16b_bf16 v[224:227], v[228:229], v[208:209], v[224:227] cbsz:4 abid:0\n"
    "v_exp_f32 v168, v168 clamp\n"
    "v_exp_f32 v169, v169 clamp\n"
    "v_mfma_f32_4x4x4_16b_bf16 v[224:227], v[228:229], v[210:211], v[224:227] cbsz:4 abid:1\n"
    "v_exp_f32 v170, v170 clamp\n"
    "v_exp_f32 v171, v171 clamp\n"
    "v_mfma_f32_4x4x4_16b_bf16 v[224:227], v[228:229], v[212:213], v[224:227] cbsz:4 abid:2\n"
    "v_exp_f32 v172, v172 clamp\n"
    "v_exp_f32 v173, v173 clamp\n"
    "v_mfma_f32_4x4x4_16b_bf16 v[224:227], v[228:229], v[214:215], v[224:227] cbsz:4 abid:3\n"
    "v_exp_f32 v174, v174 clamp\n"
    "v_exp_f32 v175, v175 clamp\n"
    "s_nop 0\n"
    "v_pk_fma_f32 v[160:161], v[160:161], s[70:71], v[128:129]\n"
    "v_pk_fma_f32 v[162:163], v[162:163], s[70:71], v[130:131]\n"
    "v_pk_add_f32 v[230:231], v[160:161], v[162:163]\n"
    "v_pk_mul_f32 v[232:233], v[160:161], v[162:163]\n"
    "v_pk_fma_f32 v[164:165], v[164:165], s[70:71], v[132:133]\n"
    "v_pk_fma_f32 v[230:231], v[230:231], v[164:165], v[232:233]\n"
    "v_pk_mul_f32 v[232:233], v[232:233], v[164:165]\n"
    "v_pk_fma_f32 v[166:167], v[166:167], s[70:71], v[134:135]\n"
    "v_pk_fma_f32 v[230:231], v[230:231], v[166:167], v[232:233]\n"
    "v_pk_mul_f32 v[232:233], v[232:233], v[166:167]\n"
    "v_pk_fma_f32 v[168:169], v[168:169], s[70:71], v[136:137]\n"
    "v_pk_fma_f32 v[230:231], v[230:231], v[168:169], v[232:233]\n"
    "v_pk_mul_f32 v[232:233], v[232:233], v[168:169]\n"
    "v_pk_fma_f32 v[170:171], v[170:171], s[70:71], v[138:139]\n"
    "v_pk_fma_f32 v[230:231], v[230:231], v[170:171], v[232:233]\n"
    "v_pk_mul_f32 v[232:233], v[232:233], v[170:171]\n"
    "v_pk_fma_f32 v[172:173], v[172:173], s[70:71], v[140:141]\n"
    "v_pk_fma_f32 v[230:231], v[230:231], v[172:173], v[232:233]\n"
    "v_pk_mul_f32 v[232:233], v[232:233], v[172:173]\n"
    "v_pk_fma_f32 v[174:175], v[174:175], s[70:71], v[142:143]\n"
    "v_pk_fma_f32 v[230:231], v[230:231], v[174:175], v[232:233]\n"
    "v_pk_mul_f32 v[232:233], v[232:233], v[174:175]\n"
    "v_rcp_f32 v232, v232\n"
    "v_rcp_f32 v233, v233\n"
    "v_fma_f32 v230, v230, v232, v247\n"
    "v_fmac_f32 v230, v231, v233\n"
    "ds_write_b16_d16_hi v245, v230\n"
    "ds_read_b64 v[228:229], v246\n"
    ".Ldin_drain0:\n"
    "v_mbcnt_lo_u32_b32 v0, -1, 0\n"
    "v_mbcnt_hi_u32_b32 v0, -1, v0\n"
    "v_lshrrev_b32 v1, 3, v0\n"
    "v_lshlrev_b32 v2, 2, v1\n"
    "ds_read_b32 v3, v246 offset:128\n"
    "s_lshl_b32 s94, s59, 8\n"
    "s_sub_u32 s94, s62, s94\n"
    "s_waitcnt lgkmcnt(0)\n"
    "ds_bpermute_b32 v3, v2, v3\n"
    "v_mfma_f32_4x4x4_16b_bf16 v[224:227], v[228:229], v[200:201], v[224:227] cbsz:4 abid:0\n"
    "v_lshlrev_b32 v2, 5, v0\n"
    "v_mul_lo_u32 v1, v1, s79\n"
    "v_mfma_f32_4x4x4_16b_bf16 v[224:227], v[228:229], v[202:203], v[224:227] cbsz:4 abid:1\n"
    "v_add_u32 v2, s94, v2\n"
    "v_and_b32 v0, 7, v0\n"
    "v_mfma_f32_4x4x4_16b_bf16 v[224:227], v[228:229], v[204:205], v[224:227] cbsz:4 abid:2\n"
    "v_add_u32 v1, s78, v1\n"
    "v_lshlrev_b32 v0, 4, v0\n"
    "v_mfma_f32_4x4x4_16b_bf16 v[224:227], v[228:229], v[206:207], v[224:227] cbsz:4 abid:3\n"
    "s_waitcnt lgkmcnt(0)\n"
    "v_bfe_u32 v4, v3, 19, 3\n"
    "v_cmp_le_i32 vcc, 0, v3\n"
    "v_add_u32 v1, v1, v4\n"
    "s_mov_b32 s70, s69\n"
    "s_mov_b32 s71, s69\n"
    "v_pk_add_f32 v[232:233], v[224:225], v[226:227]\n"
    "v_add_f32 v232, v232, v233\n"
    "ds_write_addtid_b32 v232 offset:49152\n"
    "ds_read_b128 v[8:11], v2 offset:49920\n"
    "ds_read_b128 v[12:15], v2 offset:49936\n"
    "v_lshl_add_u32 v1, v1, 7, v0\n"
    "v_bfrev_b32 v4, 1\n"
    "v_cndmask_b32 v1, v4, v1, vcc\n"
    "s_waitcnt vmcnt(0) lgkmcnt(0)\n"
    "v_pk_mul_f32 v[8:9], v[8:9], s[70:71]\n"
    "v_pk_mul_f32 v[10:11], v[10:11], s[70:71]\n"
    "v_pk_mul_f32 v[12:13], v[12:13], s[70:71]\n"
    "v_pk_mul_f32 v[14:15], v[14:15], s[70:71]\n"
    "v_cvt_pk_bf16_f32 v16, v8, v9\n"
    "v_cvt_pk_bf16_f32 v17, v10, v11\n"
    "v_cvt_pk_bf16_f32 v18, v12, v13\n"
    "v_cvt_pk_bf16_f32 v19, v14, v15\n"
    "buffer_store_dwordx4 v[16:19], v1, s[52:55], 0 offen sc1\n"
    "s_endpgm\n"
    ".Ldin_stub_E1:\n"
    "s_setpc_b64 s[34:35]\n"
    ".p2align 5\n"
    ".Ldin_E1:\n"
    "s_add_u32 m0, s62, 256\n"
    "v_exp_f32 v176, v176 clamp\n"
    "s_setprio 1\n"
    "v_exp_f32 v177, v177 clamp\n"
    "v_exp_f32 v178, v178 clamp\n"
    "v_exp_f32 v179, v179 clamp\n"
    "v_exp_f32 v180, v180 clamp\n"
    "v_exp_f32 v181, v181 clamp\n"
    "v_exp_f32 v182, v182 clamp\n"
    "v_exp_f32 v183, v183 clamp\n"
    "v_mfma_f32_4x4x4_16b_bf16 v[224:227], v[228:229], v[200:201], v[224:227] cbsz:4 abid:0\n"
    "v_exp_f32 v184, v184 clamp\n"
    "v_exp_f32 v185, v185 clamp\n"
    "v_mfma_f32_4x4x4_16b_bf16 v[224:227], v[228:229], v[202:203], v[224:227] cbsz:4 abid:1\n"
    "v_exp_f32 v186, v186 clamp\n"
    "v_exp_f32 v187, v187 clamp\n"
    "v_mfma_f32_4x4x4_16b_bf16 v[224:227], v[228:229], v[204:205], v[224:227] cbsz:4 abid:2\n"
    "v_exp_f32 v188, v188 clamp\n"
    "v_exp_f32 v189, v189 clamp\n"
    "v_mfma_f32_4x4x4_16b_bf16 v[224:227], v[228:229], v[206:207], v[224:227] cbsz:4 abid:3\n"
    "v_exp_f32 v190, v190 clamp\n"
    "v_exp_f32 v191, v191 clamp\n"
    "s_nop 0\n"
    "v_pk_fma_f32 v[176:177], v[176:177], s[70:71], v[128:129]\n"
    "v_pk_fma_f32 v[178:179], v[178:179], s[70:71], v[130:131]\n"
    "v_pk_add_f32 v[230:231], v[176:177], v[178:179]\n"
    "v_pk_mul_f32 v[232:233], v[176:177], v[178:179]\n"
    "v_pk_fma_f32 v[180:181], v[180:181], s[70:71], v[132:133]\n"
    "v_pk_fma_f32 v[230:231], v[230:231], v[180:181], v[232:233]\n"
    "v_pk_mul_f32 v[232:233], v[232:233], v[180:181]\n"
    "v_pk_fma_f32 v[182:183], v[182:183], s[70:71], v[134:135]\n"
    "v_pk_fma_f32 v[230:231], v[230:231], v[182:183], v[232:233]\n"
    "v_pk_mul_f32 v[232:233], v[232:233], v[182:183]\n"
    "v_pk_fma_f32 v[184:185], v[184:185], s[70:71], v[136:137]\n"
    "v_pk_fma_f32 v[230:231], v[230:231], v[184:185], v[232:233]\n"
    "v_pk_mul_f32 v[232:233], v[232:233], v[184:185]\n"
    "v_pk_fma_f32 v[186:187], v[186:187], s[70:71], v[138:139]\n"
    "v_pk_fma_f32 v[230:231], v[230:231], v[186:187], v[232:233]\n"
    "v_pk_mul_f32 v[232:233], v[232:233], v[186:187]\n"
    "v_pk_fma_f32 v[188:189], v[188:189], s[70:71], v[140:141]\n"
    "v_pk_fma_f32 v[230:231], v[230:231], v[188:189], v[232:233]\n"
    "v_pk_mul_f32 v[232:233], v[232:233], v[188:189]\n"
    "v_pk_fma_f32 v[190:191], v[190:191], s[70:71], v[142:143]\n"
    "v_pk_fma_f32 v[230:231], v[230:231], v[190:191], v[232:233]\n"
    "v_pk_mul_f32 v[232:233], v[232:233], v[190:191]\n"
    "v_rcp_f32 v232, v232\n"
    "v_rcp_f32 v233, v233\n"
    "v_fma_f32 v230, v230, v232, v247\n"
    "v_fmac_f32 v230, v231, v233\n"
    "ds_write_b16_d16_hi v245, v230\n"
    "ds_read_b64 v[228:229], v246\n"
    ".Ldin_drain1:\n"
    "v_mbcnt_lo_u32_b32 v0, -1, 0\n"
    "v_mbcnt_hi_u32_b32 v0, -1, v0\n"
    "v_lshrrev_b32 v1, 3, v0\n"
    "v_lshlrev_b32 v2, 2, v1\n"
    "ds_read_b32 v3, v246 offset:128\n"
    "s_lshl_b32 s94, s59, 8\n"
    "s_sub_u32 s94, s62, s94\n"
    "s_waitcnt lgkmcnt(0)\n"
    "ds_bpermute_b32 v3, v2, v3\n"
    "v_mfma_f32_4x4x4_16b_bf16 v[224:227], v[228:229], v[208:209], v[224:227] cbsz:4 abid:0\n"
    "v_lshlrev_b32 v2, 5, v0\n"
    "v_mul_lo_u32 v1, v1, s79\n"
    "v_mfma_f32_4x4x4_16b_bf16 v[224:227], v[228:229], v[210:211], v[224:227] cbsz:4 abid:1\n"
    "v_add_u32 v2, s94, v2\n"
    "v_and_b32 v0, 7, v0\n"
    "v_mfma_f32_4x4x4_16b_bf16 v[224:227], v[228:229], v[212:213], v[224:227] cbsz:4 abid:2\n"
    "v_add_u32 v1, s78, v1\n"
    "v_lshlrev_b32 v0, 4, v0\n"
    "v_mfma_f32_4x4x4_16b_bf16 v[224:227], v[228:229], v[214:215], v[224:227] cbsz:4 abid:3\n"
    "s_waitcnt lgkmcnt(0)\n"
    "v_bfe_u32 v4, v3, 19, 3\n"
    "v_cmp_le_i32 vcc, 0, v3\n"
    "v_add_u32 v1, v1, v4\n"
    "s_mov_b32 s70, s69\n"
    "s_mov_b32 s71, s69\n"
    "v_pk_add_f32 v[232:233], v[224:225], v[226:227]\n"
    "v_add_f32 v232, v232, v233\n"
    "ds_write_addtid_b32 v232 offset:49152\n"
    "ds_read_b128 v[8:11], v2 offset:49920\n"
    "ds_read_b128 v[12:15], v2 offset:49936\n"
    "v_lshl_add_u32 v1, v1, 7, v0\n"
    "v_bfrev_b32 v4, 1\n"
    "v_cndmask_b32 v1, v4, v1, vcc\n"
    "s_waitcnt vmcnt(0) lgkmcnt(0)\n"
    "v_pk_mul_f32 v[8:9], v[8:9], s[70:71]\n"
    "v_pk_mul_f32 v[10:11], v[10:11], s[70:71]\n"
    "v_pk_mul_f32 v[12:13], v[12:13], s[70:71]\n"
    "v_pk_mul_f32 v[14:15], v[14:15], s[70:71]\n"
    "v_cvt_pk_bf16_f32 v16, v8, v9\n"
    "v_cvt_pk_bf16_f32 v17, v10, v11\n"
    "v_cvt_pk_bf16_f32 v18, v12, v13\n"
    "v_cvt_pk_bf16_f32 v19, v14, v15\n"
    "buffer_store_dwordx4 v[16:19], v1, s[52:55], 0 offen sc1\n"
    "s_endpgm\n"
    ".p2align 12\n"
    ".Ldin_stub_L2:\n"
    "s_setpc_b64 s[34:35]\n"
    ".p2align 5\n"
    ".Ldin_L2:\n"
    "s_cmp_lt_i32 s60, 0\n"
    "s_cbranch_scc1 .Ldin_E0\n"
    "ds_read_b128 v[144:147], v252 offset:8192\n"
    "ds_read_b128 v[148:151], v252 offset:8256\n"
    "ds_read_b128 v[152:155], v252 offset:16384\n"
    "ds_read_b128 v[156:159], v252 offset:16448\n"
    "v_add_u32 v251, s66, v244\n"
    "v_and_b32 v216, v248, v216\n"
    "v_and_b32 v217, v249, v217\n"
    "v_and_b32 v218, v248, v218\n"
    "v_and_b32 v219, v249, v219\n"
    "v_mfma_f32_4x4x4_16b_bf16 v[176:179], v[216:217], v[96:97], v[32:35]\n"
    "v_and_b32 v220, v248, v220\n"
    "v_and_b32 v221, v249, v221\n"
    "v_mfma_f32_4x4x4_16b_bf16 v[180:183], v[218:219], v[98:99], v[36:39]\n"
    "v_and_b32 v222, v248, v222\n"
    "v_and_b32_e64 v223, v249, v223\n"
    "v_mfma_f32_4x4x4_16b_bf16 v[184:187], v[220:221], v[100:101], v[40:43]\n"
    "v_cvt_pk_bf16_f32 v0, v176, v177\n"
    "v_mfma_f32_4x4x4_16b_bf16 v[224:227], v[228:229], v[208:209], v[224:227] cbsz:4 abid:0\n"
    "v_cvt_pk_bf16_f32 v1, v178, v179\n"
    "v_mfma_f32_4x4x4_16b_bf16 v[188:191], v[222:223], v[102:103], v[44:47]\n"
    "v_cvt_pk_bf16_f32 v2, v180, v181\n"
    "v_cvt_pk_bf16_f32 v3, v182, v183\n"
    "v_mfma_f32_4x4x4_16b_bf16 v[176:179], v[216:217], v[104:105], v[48:51]\n"
    "v_cvt_pk_bf16_f32 v4, v184, v185\n"
    "v_cvt_pk_bf16_f32 v5, v186, v187\n"
    "v_mfma_f32_4x4x4_16b_bf16 v[180:183], v[218:219], v[106:107], v[52:55]\n"
    "v_cvt_pk_bf16_f32 v6, v188, v189\n"
    "v_mfma_f32_4x4x4_16b_bf16 v[224:227], v[228:229], v[210:211], v[224:227] cbsz:4 abid:1\n"
    "v_cvt_pk_bf16_f32 v7, v190, v191\n"
    "v_mfma_f32_4x4x4_16b_bf16 v[184:187], v[220:221], v[108:109], v[56:59]\n"
    "v_cvt_pk_bf16_f32 v8, v176, v177\n"
    "v_cvt_pk_bf16_f32 v9, v178, v179\n"
    "v_mfma_f32_4x4x4_16b_bf16 v[188:191], v[222:223], v[110:111], v[60:63]\n"
    "v_cvt_pk_bf16_f32 v10, v180, v181\n"
    "v_cvt_pk_bf16_f32 v11, v182, v183\n"
    "v_mfma_f32_4x4x4_16b_bf16 v[176:179], v[216:217], v[112:113], v[64:67]\n"
    "v_cvt_pk_bf16_f32 v12, v184, v185\n"
    "v_mfma_f32_4x4x4_16b_bf16 v[224:227], v[228:229], v[212:213], v[224:227] cbsz:4 abid:2\n"
    "v_cvt_pk_bf16_f32 v13, v186, v187\n"
    "s_setprio 1\n"
    "s_waitcnt lgkmcnt(0)\n"
    "v_mfma_f32_4x4x4_16b_bf16 v[180:183], v[218:219], v[114:115], v[68:71]\n"
    "v_cvt_pk_bf16_f32 v14, v188, v189\n"
    "v_cvt_pk_bf16_f32 v15, v190, v191\n"
    "v_mfma_f32_4x4x4_16b_bf16 v[184:187], v[220:221], v[116:117], v[72:75]\n"
    "v_cvt_pk_bf16_f32 v16, v176, v177\n"
    "v_cvt_pk_bf16_f32 v17, v178, v179\n"
    "v_mfma_f32_4x4x4_16b_bf16 v[188:191], v[222:223], v[118:119], v[76:79]\n"
    "v_cvt_pk_bf16_f32 v18, v180, v181\n"
    "v_mfma_f32_4x4x4_16b_bf16 v[224:227], v[228:229], v[214:215], v[224:227] cbsz:4 abid:3\n"
    "v_cvt_pk_bf16_f32 v19, v182, v183\n"
    "v_mfma_f32_4x4x4_16b_bf16 v[176:179], v[216:217], v[120:121], v[80:83]\n"
    "v_cvt_pk_bf16_f32 v20, v184, v185\n"
    "v_cvt_pk_bf16_f32 v21, v186, v187\n"
    "v_mfma_f32_4x4x4_16b_bf16 v[180:183], v[218:219], v[122:123], v[84:87]\n"
    "v_cvt_pk_bf16_f32 v22, v188, v189\n"
    "v_cvt_pk_bf16_f32 v23, v190, v191\n"
    "v_mfma_f32_4x4x4_16b_bf16 v[184:187], v[220:221], v[124:125], v[88:91]\n"
    "v_cvt_pk_bf16_f32 v24, v176, v177\n"
    "v_cvt_pk_bf16_f32 v25, v178, v179\n"
    "v_mfma_f32_4x4x4_16b_bf16 v[188:191], v[222:223], v[126:127], v[92:95]\n"
    "v_cvt_pk_bf16_f32 v26, v180, v181\n"
    "v_cvt_pk_bf16_f32 v27, v182, v183\n"
    "v_cvt_pk_bf16_f32 v28, v184, v185\n"
    "v_cvt_pk_bf16_f32 v29, v186, v187\n"
    "v_mfma_f32_16x16x32_bf16 v[176:179], v[0:3], v[192:195], v[144:147]\n"
    "s_add_u32 m0, s68, 6144\n"
    "v_cvt_pk_bf16_f32 v30, v188, v189\n"
    "v_cvt_pk_bf16_f32 v31, v190, v191\n"
    "v_exp_f32 v160, v160 clamp\n"
    "v_mfma_f32_16x16x32_bf16 v[176:179], v[4:7], v[196:199], v[176:179]\n"
    "s_add_u32 s66, s66, 16\n"
    "v_exp_f32 v161, v161 clamp\n"
    "v_exp_f32 v162, v162 clamp\n"
    "v_mfma_f32_16x16x32_bf16 v[180:183], v[8:11], v[192:195], v[148:151]\n"
    "s_add_u32 s76, s76, s73\n"
    "v_exp_f32 v163, v163 clamp\n"
    "v_exp_f32 v164, v164 clamp\n"
    "v_mfma_f32_16x16x32_bf16 v[180:183], v[12:15], v[196:199], v[180:183]\n"
    "s_lshl1_add_u32 s76, s72, s76\n"
    "v_exp_f32 v165, v165 clamp\n"
    "v_exp_f32 v166, v166 clamp\n"
    "v_mfma_f32_16x16x32_bf16 v[184:187], v[16:19], v[192:195], v[152:155]\n"
    "s_mov_b32 s80, s83\n"
    "v_exp_f32 v167, v167 clamp\n"
    "v_exp_f32 v168, v168 clamp\n"
    "v_mfma_f32_16x16x32_bf16 v[184:187], v[20:23], v[196:199], v[184:187]\n"
    "s_add_u32 s62, s62, 256\n"
    "v_exp_f32 v169, v169 clamp\n"
    "v_exp_f32 v170, v170 clamp\n"
    "v_mfma_f32_16x16x32_bf16 v[188:191], v[24:27], v[192:195], v[156:159]\n"
    "s_mov_b32 s81, s64\n"
    "v_exp_f32 v171, v171 clamp\n"
    "v_exp_f32 v172, v172 clamp\n"
    "v_mfma_f32_16x16x32_bf16 v[188:191], v[28:31], v[196:199], v[188:191]\n"
    "s_lshr_b32 s63, s65, 6\n"
    "v_exp_f32 v173, v173 clamp\n"
    "v_exp_f32 v174, v174 clamp\n"
    "v_exp_f32 v175, v175 clamp\n"
    "v_pk_fma_f32 v[160:161], v[160:161], s[70:71], v[128:129]\n"
    "v_pk_fma_f32 v[162:163], v[162:163], s[70:71], v[130:131]\n"
    "v_pk_add_f32 v[230:231], v[160:161], v[162:163]\n"
    "v_pk_mul_f32 v[232:233], v[160:161], v[162:163]\n"
    "v_pk_fma_f32 v[164:165], v[164:165], s[70:71], v[132:133]\n"
    "v_pk_fma_f32 v[230:231], v[230:231], v[164:165], v[232:233]\n"
    "v_pk_mul_f32 v[232:233], v[232:233], v[164:165]\n"
    "v_pk_fma_f32 v[166:167], v[166:167], s[70:71], v[134:135]\n"
    "v_pk_fma_f32 v[230:231], v[230:231], v[166:167], v[232:233]\n"
    "v_pk_mul_f32 v[232:233], v[232:233], v[166:167]\n"
    "v_pk_fma_f32 v[168:169], v[168:169], s[70:71], v[136:137]\n"
    "v_pk_fma_f32 v[230:231], v[230:231], v[168:169], v[232:233]\n"
    "v_pk_mul_f32 v[232:233], v[232:233], v[168:169]\n"
    "v_pk_fma_f32 v[170:171], v[170:171], s[70:71], v[138:139]\n"
    "v_pk_fma_f32 v[230:231], v[230:231], v[170:171], v[232:233]\n"
    "v_pk_mul_f32 v[232:233], v[232:233], v[170:171]\n"
    "v_pk_fma_f32 v[172:173], v[172:173], s[70:71], v[140:141]\n"
    "v_pk_fma_f32 v[230:231], v[230:231], v[172:173], v[232:233]\n"
    "v_pk_mul_f32 v[232:233], v[232:233], v[172:173]\n"
    "v_pk_fma_f32 v[174:175], v[174:175], s[70:71], v[142:143]\n"
    "v_pk_fma_f32 v[230:231], v[230:231], v[174:175], v[232:233]\n"
    "v_pk_mul_f32 v[232:233], v[232:233], v[174:175]\n"
    "v_rcp_f32 v232, v232\n"
    "v_rcp_f32 v233, v233\n"
    "v_fma_f32 v230, v230, v232, v247\n"
    "v_fmac_f32 v230, v231, v233\n"
    "s_setprio 0\n"
    "ds_read_b64_tr_b16 v[208:209], v240 offset:6144\n"
    "ds_read_b64_tr_b16 v[210:211], v241 offset:6144\n"
    "ds_read_b64_tr_b16 v[212:213], v242 offset:6144\n"
    "ds_read_b64_tr_b16 v[214:215], v243 offset:6144\n"
    "ds_read_u8 v236, v251 offset:0\n"
    "ds_read_u8 v238, v251 offset:1\n"
    "ds_write_b16_d16_hi v245, v230\n"
    "ds_read_b64 v[228:229], v246\n"
    "ds_read_b64 v[216:217], v246 offset:128\n"
    "s_waitcnt vmcnt(2)\n"
    "ds_read_b128 v[192:195], v234 offset:0\n"
    "ds_read_b128 v[196:199], v235 offset:0\n"
    "s_waitcnt lgkmcnt(5)\n"
    "buffer_load_dwordx4 v[236:237], s[40:43], s67 idxen offen offset:0 nt lds\n"
    "buffer_load_dwordx4 v[238:239], s[40:43], s67 idxen offen offset:1024 nt lds\n"
    "s_waitcnt lgkmcnt(2)\n"
    "s_bitcmp1_b32 s60, 30\n"
    "s_cbranch_scc1 .Ldin_A3s\n"
    ".Ldin_A3:\n"
    "v_add_u32 v251, s66, v244\n"
    "v_readlane_b32 s60, v216, s59\n"
    "v_readlane_b32 s61, v217, s59\n"
    "v_mfma_f32_4x4x4_16b_bf16 v[224:227], v[228:229], v[200:201], v[224:227] cbsz:4 abid:0\n"
    "v_exp_f32 v176, v176 clamp\n"
    "v_exp_f32 v177, v177 clamp\n"
    "v_mfma_f32_4x4x4_16b_bf16 v[224:227], v[228:229], v[202:203], v[224:227] cbsz:4 abid:1\n"
    "v_exp_f32 v178, v178 clamp\n"
    "v_exp_f32 v179, v179 clamp\n"
    "v_mfma_f32_4x4x4_16b_bf16 v[224:227], v[228:229], v[204:205], v[224:227] cbsz:4 abid:2\n"
    "v_exp_f32 v180, v180 clamp\n"
    "v_exp_f32 v181, v181 clamp\n"
    "v_mfma_f32_4x4x4_16b_bf16 v[224:227], v[228:229], v[206:207], v[224:227] cbsz:4 abid:3\n"
    "s_waitcnt lgkmcnt(0)\n"
    "v_mfma_f32_16x16x32_bf16 v[160:163], v[0:3], v[192:195], v[144:147]\n"
    "s_setprio 1\n"
    "v_exp_f32 v182, v182 clamp\n"
    "v_mfma_f32_16x16x32_bf16 v[160:163], v[4:7], v[196:199], v[160:163]\n"
    "s_cmp_eq_u32 s60, -1\n"
    "v_exp_f32 v183, v183 clamp\n"
    "v_mfma_f32_16x16x32_bf16 v[164:167], v[8:11], v[192:195], v[148:151]\n"
    "s_cbranch_scc1 .Ldin_A3_poll\n"
    ".Ldin_A3_pr:\n"
    "v_exp_f32 v184, v184 clamp\n"
    "v_mfma_f32_16x16x32_bf16 v[164:167], v[12:15], v[196:199], v[164:167]\n"
    "s_lshr_b32 s94, s60, 12\n"
    "v_exp_f32 v185, v185 clamp\n"
    "v_mfma_f32_16x16x32_bf16 v[168:171], v[16:19], v[192:195], v[152:155]\n"
    "s_and_b32 s94, s94, 0x1ff0\n"
    "v_exp_f32 v186, v186 clamp\n"
    "v_mfma_f32_16x16x32_bf16 v[168:171], v[20:23], v[196:199], v[168:171]\n"
    "s_mov_b32 m0, s62\n"
    "v_exp_f32 v187, v187 clamp\n"
    "v_mfma_f32_16x16x32_bf16 v[172:175], v[24:27], v[192:195], v[156:159]\n"
    "s_add_u32 s66, s66, 16\n"
    "v_exp_f32 v188, v188 clamp\n"
    "v_mfma_f32_16x16x32_bf16 v[172:175], v[28:31], v[196:199], v[172:175]\n"
    "s_and_b32 s64, s60, 0xffff\n"
    "v_xor_b32 v252, s94, v250\n"
    "v_exp_f32 v189, v189 clamp\n"
    "v_exp_f32 v190, v190 clamp\n"
    "v_exp_f32 v191, v191 clamp\n"
    "v_pk_fma_f32 v[176:177], v[176:177], s[70:71], v[128:129]\n"
    "v_pk_fma_f32 v[178:179], v[178:179], s[70:71], v[130:131]\n"
    "v_pk_add_f32 v[230:231], v[176:177], v[178:179]\n"
    "v_pk_mul_f32 v[232:233], v[176:177], v[178:179]\n"
    "v_pk_add_f32 v[218:219], v[224:225], v[226:227]\n"
    "v_mov_b64 v[224:225], 0\n"
    "v_mov_b64 v[226:227], 0\n"
    "v_pk_fma_f32 v[180:181], v[180:181], s[70:71], v[132:133]\n"
    "v_pk_fma_f32 v[230:231], v[230:231], v[180:181], v[232:233]\n"
    "v_pk_mul_f32 v[232:233], v[232:233], v[180:181]\n"
    "v_pk_fma_f32 v[182:183], v[182:183], s[70:71], v[134:135]\n"
    "v_pk_fma_f32 v[230:231], v[230:231], v[182:183], v[232:233]\n"
    "v_pk_mul_f32 v[232:233], v[232:233], v[182:183]\n"
    "v_pk_fma_f32 v[184:185], v[184:185], s[70:71], v[136:137]\n"
    "v_pk_fma_f32 v[230:231], v[230:231], v[184:185], v[232:233]\n"
    "v_pk_mul_f32 v[232:233], v[232:233], v[184:185]\n"
    "v_pk_fma_f32 v[186:187], v[186:187], s[70:71], v[138:139]\n"
    "v_pk_fma_f32 v[230:231], v[230:231], v[186:187], v[232:233]\n"
    "v_pk_mul_f32 v[232:233], v[232:233], v[186:187]\n"
    "v_pk_fma_f32 v[188:189], v[188:189], s[70:71], v[140:141]\n"
    "v_pk_fma_f32 v[230:231], v[230:231], v[188:189], v[232:233]\n"
    "v_pk_mul_f32 v[232:233], v[232:233], v[188:189]\n"
    "v_pk_fma_f32 v[190:191], v[190:191], s[70:71], v[142:143]\n"
    "v_pk_fma_f32 v[230:231], v[230:231], v[190:191], v[232:233]\n"
    "v_pk_mul_f32 v[232:233], v[232:233], v[190:191]\n"
    "v_rcp_f32 v232, v232\n"
    "v_rcp_f32 v233, v233\n"
    "v_fma_f32 v230, v230, v232, v247\n"
    "v_fmac_f32 v230, v231, v233\n"
    "v_add_f32_e64 v218, v218, v219\n"
    "s_setprio 0\n"
    "ds_read_b64_tr_b16 v[200:201], v240 offset:0\n"
    "ds_read_b64_tr_b16 v[202:203], v241 offset:0\n"
    "ds_read_b64_tr_b16 v[204:205], v242 offset:0\n"
    "ds_read_b64_tr_b16 v[206:207], v243 offset:0\n"
    "ds_read_u8 v236, v251 offset:0\n"
    "ds_read_u8 v238, v251 offset:1\n"
    "ds_write_b16_d16_hi v245, v230\n"
    "ds_read_b64 v[228:229], v246\n"
    "ds_write_addtid_b32 v218 offset:49152\n"
    "s_add_u32 m0, s8, 0\n"
    "s_bfe_u32 s94, s60, 0x30013\n"
    "s_add_u32 s58, s58, s57\n"
    "s_mul_i32 s94, s94, s56\n"
    "s_add_u32 s82, s64, s9\n"
    "s_add_u32 s65, s58, s94\n"
    "s_waitcnt vmcnt(2)\n"
    "ds_read_b128 v[192:195], v234 offset:2048\n"
    "ds_read_b128 v[196:199], v235 offset:2048\n"
    "ds_read_b128 v[216:219], v252 offset:0\n"
    "ds_read_b128 v[220:223], v252 offset:64\n"
    "s_mul_i32 s15, s15, s16\n"
    "s_cmp_lt_u32 s72, 49\n"
    "s_cselect_b32 s15, 0, s15\n"
    "s_add_u32 s14, s14, s15\n"
    "s_mov_b32 s15, 0\n"
    "s_min_u32 s14, s14, s17\n"
    "s_add_u32 s95, s61, s14\n"
    "s_sub_u32 s95, s95, s76\n"
    "s_lshr_b32 s94, s61, 16\n"
    "s_sext_i32_i16 s95, s95\n"
    "s_cmp_eq_u32 s59, s10\n"
    "s_cbranch_scc1 .Ldin_A3_skip\n"
    "s_waitcnt lgkmcnt(7)\n"
    "buffer_load_dwordx4 v[236:237], s[40:43], s67 idxen offen offset:0 nt lds\n"
    "buffer_load_dwordx4 v[238:239], s[40:43], s67 idxen offen offset:1024 nt lds\n"
    ".Ldin_A3_skr:\n"
    "s_mul_i32 s95, s95, s94\n"
    "s_bfe_u32 s94, s60, 0x50019\n"
    "s_ashr_i32 s95, s95, 16\n"
    "s_lshl4_add_u32 s83, s94, s64\n"
    "s_max_i32 s95, s95, -14\n"
    "s_add_u32 s59, s59, 1\n"
    "s_min_i32 s95, s95, 14\n"
    "s_add_u32 s72, s77, s95\n"
    "s_cmp_lt_i32 s60, 0\n"
    "s_cselect_b32 s72, 0, s72\n"
    "s_cmp_eq_u32 s66, s80\n"
    "s_cselect_b64 s[66:67], s[64:65], s[66:67]\n"
    "s_waitcnt lgkmcnt(5)\n"
    "s_branch .Ldin_G0\n"
    ".Ldin_A3_poll:\n"
    "s_sleep 1\n"
    "ds_read_b64 v[216:217], v246 offset:128\n"
    "s_waitcnt lgkmcnt(0)\n"
    "v_readlane_b32 s60, v216, s59\n"
    "v_readlane_b32 s61, v217, s59\n"
    "s_cmp_eq_u32 s60, -1\n"
    "s_cbranch_scc1 .Ldin_A3_poll\n"
    "s_branch .Ldin_A3_pr\n"
    ".Ldin_A3_skip:\n"
    "s_add_u32 s8, s8, 6144\n"
    "s_sub_u32 s68, s68, 2048\n"
    "s_mov_b32 s9, 48\n"
    "s_sub_u32 s82, s82, 16\n"
    "s_sub_u32 s66, s66, 16\n"
    "s_sub_u32 s76, s76, s72\n"
    "s_waitcnt lgkmcnt(0)\n"
    "s_branch .Ldin_A3_skr\n"
    ".Ldin_stub_L1:\n"
    "s_setpc_b64 s[34:35]\n"
    ".p2align 5\n"
    ".Ldin_L1:\n"
    "s_cmp_lt_i32 s60, 0\n"
    "s_cbranch_scc1 .Ldin_E1\n"
    "ds_read_b128 v[144:147], v252 offset:8192\n"
    "ds_read_b128 v[148:151], v252 offset:8256\n"
    "ds_read_b128 v[152:155], v252 offset:16384\n"
    "ds_read_b128 v[156:159], v252 offset:16448\n"
    "v_add_u32 v251, s66, v244\n"
    "v_and_b32 v216, v248, v216\n"
    "v_and_b32 v217, v249, v217\n"
    "v_and_b32 v218, v248, v218\n"
    "v_and_b32 v219, v249, v219\n"
    "v_mfma_f32_4x4x4_16b_bf16 v[160:163], v[216:217], v[96:97], v[32:35]\n"
    "v_and_b32 v220, v248, v220\n"
    "v_and_b32 v221, v249, v221\n"
    "v_mfma_f32_4x4x4_16b_bf16 v[164:167], v[218:219], v[98:99], v[36:39]\n"
    "v_and_b32 v222, v248, v222\n"
    "v_and_b32_e64 v223, v249, v223\n"
    "v_mfma_f32_4x4x4_16b_bf16 v[168:171], v[220:221], v[100:101], v[40:43]\n"
    "v_cvt_pk_bf16_f32 v0, v160, v161\n"
    "v_mfma_f32_4x4x4_16b_bf16 v[224:227], v[228:229], v[200:201], v[224:227] cbsz:4 abid:0\n"
    "v_cvt_pk_bf16_f32 v1, v162, v163\n"
    "v_mfma_f32_4x4x4_16b_bf16 v[172:175], v[222:223], v[102:103], v[44:47]\n"
    "v_cvt_pk_bf16_f32 v2, v164, v165\n"
    "v_cvt_pk_bf16_f32 v3, v166, v167\n"
    "v_mfma_f32_4x4x4_16b_bf16 v[160:163], v[216:217], v[104:105], v[48:51]\n"
    "v_cvt_pk_bf16_f32 v4, v168, v169\n"
    "v_cvt_pk_bf16_f32 v5, v170, v171\n"
    "v_mfma_f32_4x4x4_16b_bf16 v[164:167], v[218:219], v[106:107], v[52:55]\n"
    "v_cvt_pk_bf16_f32 v6, v172, v173\n"
    "v_mfma_f32_4x4x4_16b_bf16 v[224:227], v[228:229], v[202:203], v[224:227] cbsz:4 abid:1\n"
    "v_cvt_pk_bf16_f32 v7, v174, v175\n"
    "v_mfma_f32_4x4x4_16b_bf16 v[168:171], v[220:221], v[108:109], v[56:59]\n"
    "v_cvt_pk_bf16_f32 v8, v160, v161\n"
    "v_cvt_pk_bf16_f32 v9, v162, v163\n"
    "v_mfma_f32_4x4x4_16b_bf16 v[172:175], v[222:223], v[110:111], v[60:63]\n"
    "v_cvt_pk_bf16_f32 v10, v164, v165\n"
    "v_cvt_pk_bf16_f32 v11, v166, v167\n"
    "v_mfma_f32_4x4x4_16b_bf16 v[160:163], v[216:217], v[112:113], v[64:67]\n"
    "v_cvt_pk_bf16_f32 v12, v168, v169\n"
    "v_mfma_f32_4x4x4_16b_bf16 v[224:227], v[228:229], v[204:205], v[224:227] cbsz:4 abid:2\n"
    "v_cvt_pk_bf16_f32 v13, v170, v171\n"
    "s_setprio 1\n"
    "s_waitcnt lgkmcnt(0)\n"
    "v_mfma_f32_4x4x4_16b_bf16 v[164:167], v[218:219], v[114:115], v[68:71]\n"
    "v_cvt_pk_bf16_f32 v14, v172, v173\n"
    "v_cvt_pk_bf16_f32 v15, v174, v175\n"
    "v_mfma_f32_4x4x4_16b_bf16 v[168:171], v[220:221], v[116:117], v[72:75]\n"
    "v_cvt_pk_bf16_f32 v16, v160, v161\n"
    "v_cvt_pk_bf16_f32 v17, v162, v163\n"
    "v_mfma_f32_4x4x4_16b_bf16 v[172:175], v[222:223], v[118:119], v[76:79]\n"
    "v_cvt_pk_bf16_f32 v18, v164, v165\n"
    "v_mfma_f32_4x4x4_16b_bf16 v[224:227], v[228:229], v[206:207], v[224:227] cbsz:4 abid:3\n"
    "v_cvt_pk_bf16_f32 v19, v166, v167\n"
    "v_mfma_f32_4x4x4_16b_bf16 v[160:163], v[216:217], v[120:121], v[80:83]\n"
    "v_cvt_pk_bf16_f32 v20, v168, v169\n"
    "v_cvt_pk_bf16_f32 v21, v170, v171\n"
    "v_mfma_f32_4x4x4_16b_bf16 v[164:167], v[218:219], v[122:123], v[84:87]\n"
    "v_cvt_pk_bf16_f32 v22, v172, v173\n"
    "v_cvt_pk_bf16_f32 v23, v174, v175\n"
    "v_mfma_f32_4x4x4_16b_bf16 v[168:171], v[220:221], v[124:125], v[88:91]\n"
    "v_cvt_pk_bf16_f32 v24, v160, v161\n"
    "v_cvt_pk_bf16_f32 v25, v162, v163\n"
    "v_mfma_f32_4x4x4_16b_bf16 v[172:175], v[222:223], v[126:127], v[92:95]\n"
    "v_cvt_pk_bf16_f32 v26, v164, v165\n"
    "v_cvt_pk_bf16_f32 v27, v166, v167\n"
    "v_cvt_pk_bf16_f32 v28, v168, v169\n"
    "v_cvt_pk_bf16_f32 v29, v170, v171\n"
    "v_mfma_f32_16x16x32_bf16 v[160:163], v[0:3], v[192:195], v[144:147]\n"
    "s_add_u32 m0, s68, 4096\n"
    "v_cvt_pk_bf16_f32 v30, v172, v173\n"
    "v_cvt_pk_bf16_f32 v31, v174, v175\n"
    "v_exp_f32 v176, v176 clamp\n"
    "v_mfma_f32_16x16x32_bf16 v[160:163], v[4:7], v[196:199], v[160:163]\n"
    "s_add_u32 s66, s66, 16\n"
    "v_exp_f32 v177, v177 clamp\n"
    "v_exp_f32 v178, v178 clamp\n"
    "v_mfma_f32_16x16x32_bf16 v[164:167], v[8:11], v[192:195], v[148:151]\n"
    "s_add_u32 s76, s76, s73\n"
    "v_exp_f32 v179, v179 clamp\n"
    "v_exp_f32 v180, v180 clamp\n"
    "v_mfma_f32_16x16x32_bf16 v[164:167], v[12:15], v[196:199], v[164:167]\n"
    "s_lshl1_add_u32 s76, s72, s76\n"
    "v_exp_f32 v181, v181 clamp\n"
    "v_exp_f32 v182, v182 clamp\n"
    "v_mfma_f32_16x16x32_bf16 v[168:171], v[16:19], v[192:195], v[152:155]\n"
    "s_mov_b32 s80, s83\n"
    "v_exp_f32 v183, v183 clamp\n"
    "v_exp_f32 v184, v184 clamp\n"
    "v_mfma_f32_16x16x32_bf16 v[168:171], v[20:23], v[196:199], v[168:171]\n"
    "s_add_u32 s62, s62, 256\n"
    "v_exp_f32 v185, v185 clamp\n"
    "v_exp_f32 v186, v186 clamp\n"
    "v_mfma_f32_16x16x32_bf16 v[172:175], v[24:27], v[192:195], v[156:159]\n"
    "s_mov_b32 s81, s64\n"
    "v_exp_f32 v187, v187 clamp\n"
    "v_exp_f32 v188, v188 clamp\n"
    "v_mfma_f32_16x16x32_bf16 v[172:175], v[28:31], v[196:199], v[172:175]\n"
    "s_lshr_b32 s63, s65, 6\n"
    "v_exp_f32 v189, v189 clamp\n"
    "v_exp_f32 v190, v190 clamp\n"
    "v_exp_f32 v191, v191 clamp\n"
    "v_pk_fma_f32 v[176:177], v[176:177], s[70:71], v[128:129]\n"
    "v_pk_fma_f32 v[178:179], v[178:179], s[70:71], v[130:131]\n"
    "v_pk_add_f32 v[230:231], v[176:177], v[178:179]\n"
    "v_pk_mul_f32 v[232:233], v[176:177], v[178:179]\n"
    "v_pk_fma_f32 v[180:181], v[180:181], s[70:71], v[132:133]\n"
    "v_pk_fma_f32 v[230:231], v[230:231], v[180:181], v[232:233]\n"
    "v_pk_mul_f32 v[232:233], v[232:233], v[180:181]\n"
    "v_pk_fma_f32 v[182:183], v[182:183], s[70:71], v[134:135]\n"
    "v_pk_fma_f32 v[230:231], v[230:231], v[182:183], v[232:233]\n"
    "v_pk_mul_f32 v[232:233], v[232:233], v[182:183]\n"
    "v_pk_fma_f32 v[184:185], v[184:185], s[70:71], v[136:137]\n"
    "v_pk_fma_f32 v[230:231], v[230:231], v[184:185], v[232:233]\n"
    "v_pk_mul_f32 v[232:233], v[232:233], v[184:185]\n"
    "v_pk_fma_f32 v[186:187], v[186:187], s[70:71], v[138:139]\n"
    "v_pk_fma_f32 v[230:231], v[230:231], v[186:187], v[232:233]\n"
    "v_pk_mul_f32 v[232:233], v[232:233], v[186:187]\n"
    "v_pk_fma_f32 v[188:189], v[188:189], s[70:71], v[140:141]\n"
    "v_pk_fma_f32 v[230:231], v[230:231], v[188:189], v[232:233]\n"
    "v_pk_mul_f32 v[232:233], v[232:233], v[188:189]\n"
    "v_pk_fma_f32 v[190:191], v[190:191], s[70:71], v[142:143]\n"
    "v_pk_fma_f32 v[230:231], v[230:231], v[190:191], v[232:233]\n"
    "v_pk_mul_f32 v[232:233], v[232:233], v[190:191]\n"
    "v_rcp_f32 v232, v232\n"
    "v_rcp_f32 v233, v233\n"
    "v_fma_f32 v230, v230, v232, v247\n"
    "v_fmac_f32 v230, v231, v233\n"
    "s_setprio 0\n"
    "ds_read_b64_tr_b16 v[200:201], v240 offset:4096\n"
    "ds_read_b64_tr_b16 v[202:203], v241 offset:4096\n"
    "ds_read_b64_tr_b16 v[204:205], v242 offset:4096\n"
    "ds_read_b64_tr_b16 v[206:207], v243 offset:4096\n"
    "ds_read_u8 v236, v251 offset:0\n"
    "ds_read_u8 v238, v251 offset:1\n"
    "ds_write_b16_d16_hi v245, v230\n"
    "ds_read_b64 v[228:229], v246\n"
    "ds_read_b64 v[216:217], v246 offset:128\n"
    "s_waitcnt vmcnt(2)\n"
    "ds_read_b128 v[192:195], v234 offset:6144\n"
    "ds_read_b128 v[196:199], v235 offset:6144\n"
    "s_waitcnt lgkmcnt(5)\n"
    "buffer_load_dwordx4 v[236:237], s[40:43], s67 idxen offen offset:0 nt lds\n"
    "buffer_load_dwordx4 v[238:239], s[40:43], s67 idxen offen offset:1024 nt lds\n"
    "s_waitcnt lgkmcnt(2)\n"
    "s_bitcmp1_b32 s60, 30\n"
    "s_cbranch_scc1 .Ldin_A2s\n"
    ".Ldin_A2:\n"
    "v_add_u32 v251, s66, v244\n"
    "v_readlane_b32 s60, v216, s59\n"
    "v_readlane_b32 s61, v217, s59\n"
    "v_mfma_f32_4x4x4_16b_bf16 v[224:227], v[228:229], v[208:209], v[224:227] cbsz:4 abid:0\n"
    "v_exp_f32 v160, v160 clamp\n"
    "v_exp_f32 v161, v161 clamp\n"
    "v_mfma_f32_4x4x4_16b_bf16 v[224:227], v[228:229], v[210:211], v[224:227] cbsz:4 abid:1\n"
    "v_exp_f32 v162, v162 clamp\n"
    "v_exp_f32 v163, v163 clamp\n"
    "v_mfma_f32_4x4x4_16b_bf16 v[224:227], v[228:229], v[212:213], v[224:227] cbsz:4 abid:2\n"
    "v_exp_f32 v164, v164 clamp\n"
    "v_exp_f32 v165, v165 clamp\n"
    "v_mfma_f32_4x4x4_16b_bf16 v[224:227], v[228:229], v[214:215], v[224:227] cbsz:4 abid:3\n"
    "s_waitcnt lgkmcnt(0)\n"
    "v_mfma_f32_16x16x32_bf16 v[176:179], v[0:3], v[192:195], v[144:147]\n"
    "s_setprio 1\n"
    "v_exp_f32 v166, v166 clamp\n"
    "v_mfma_f32_16x16x32_bf16 v[176:179], v[4:7], v[196:199], v[176:179]\n"
    "s_cmp_eq_u32 s60, -1\n"
    "v_exp_f32 v167, v167 clamp\n"
    "v_mfma_f32_16x16x32_bf16 v[180:183], v[8:11], v[192:195], v[148:151]\n"
    "s_cbranch_scc1 .Ldin_A2_poll\n"
    ".Ldin_A2_pr:\n"
    "v_exp_f32 v168, v168 clamp\n"
    "v_mfma_f32_16x16x32_bf16 v[180:183], v[12:15], v[196:199], v[180:183]\n"
    "s_lshr_b32 s94, s60, 12\n"
    "v_exp_f32 v169, v169 clamp\n"
    "v_mfma_f32_16x16x32_bf16 v[184:187], v[16:19], v[192:195], v[152:155]\n"
    "s_and_b32 s94, s94, 0x1ff0\n"
    "v_exp_f32 v170, v170 clamp\n"
    "v_mfma_f32_16x16x32_bf16 v[184:187], v[20:23], v[196:199], v[184:187]\n"
    "s_mov_b32 m0, s62\n"
    "v_exp_f32 v171, v171 clamp\n"
    "v_mfma_f32_16x16x32_bf16 v[188:191], v[24:27], v[192:195], v[156:159]\n"
    "s_add_u32 s66, s66, 16\n"
    "v_exp_f32 v172, v172 clamp\n"
    "v_mfma_f32_16x16x32_bf16 v[188:191], v[28:31], v[196:199], v[188:191]\n"
    "s_and_b32 s64, s60, 0xffff\n"
    "v_xor_b32 v252, s94, v250\n"
    "v_exp_f32 v173, v173 clamp\n"
    "v_exp_f32 v174, v174 clamp\n"
    "v_exp_f32 v175, v175 clamp\n"
    "v_pk_fma_f32 v[160:161], v[160:161], s[70:71], v[128:129]\n"
    "v_pk_fma_f32 v[162:163], v[162:163], s[70:71], v[130:131]\n"
    "v_pk_add_f32 v[230:231], v[160:161], v[162:163]\n"
    "v_pk_mul_f32 v[232:233], v[160:161], v[162:163]\n"
    "v_pk_add_f32 v[218:219], v[224:225], v[226:227]\n"
    "v_mov_b64 v[224:225], 0\n"
    "v_mov_b64 v[226:227], 0\n"
    "v_pk_fma_f32 v[164:165], v[164:165], s[70:71], v[132:133]\n"
    "v_pk_fma_f32 v[230:231], v[230:231], v[164:165], v[232:233]\n"
    "v_pk_mul_f32 v[232:233], v[232:233], v[164:165]\n"
    "v_pk_fma_f32 v[166:167], v[166:167], s[70:71], v[134:135]\n"
    "v_pk_fma_f32 v[230:231], v[230:231], v[166:167], v[232:233]\n"
    "v_pk_mul_f32 v[232:233], v[232:233], v[166:167]\n"
    "v_pk_fma_f32 v[168:169], v[168:169], s[70:71], v[136:137]\n"
    "v_pk_fma_f32 v[230:231], v[230:231], v[168:169], v[232:233]\n"
    "v_pk_mul_f32 v[232:233], v[232:233], v[168:169]\n"
    "v_pk_fma_f32 v[170:171], v[170:171], s[70:71], v[138:139]\n"
    "v_pk_fma_f32 v[230:231], v[230:231], v[170:171], v[232:233]\n"
    "v_pk_mul_f32 v[232:233], v[232:233], v[170:171]\n"
    "v_pk_fma_f32 v[172:173], v[172:173], s[70:71], v[140:141]\n"
    "v_pk_fma_f32 v[230:231], v[230:231], v[172:173], v[232:233]\n"
    "v_pk_mul_f32 v[232:233], v[232:233], v[172:173]\n"
    "v_pk_fma_f32 v[174:175], v[174:175], s[70:71], v[142:143]\n"
    "v_pk_fma_f32 v[230:231], v[230:231], v[174:175], v[232:233]\n"
    "v_pk_mul_f32 v[232:233], v[232:233], v[174:175]\n"
    "v_rcp_f32 v232, v232\n"
    "v_rcp_f32 v233, v233\n"
    "v_fma_f32 v230, v230, v232, v247\n"
    "v_fmac_f32 v230, v231, v233\n"
    "v_add_f32_e64 v218, v218, v219\n"
    "s_setprio 0\n"
    "ds_read_b64_tr_b16 v[208:209], v240 offset:6144\n"
    "ds_read_b64_tr_b16 v[210:211], v241 offset:6144\n"
    "ds_read_b64_tr_b16 v[212:213], v242 offset:6144\n"
    "ds_read_b64_tr_b16 v[214:215], v243 offset:6144\n"
    "ds_read_u8 v236, v251 offset:0\n"
    "ds_read_u8 v238, v251 offset:1\n"
    "ds_write_b16_d16_hi v245, v230\n"
    "ds_read_b64 v[228:229], v246\n"
    "ds_write_addtid_b32 v218 offset:49152\n"
    "s_add_u32 m0, s68, 6144\n"
    "s_bfe_u32 s94, s60, 0x30013\n"
    "s_add_u32 s58, s58, s57\n"
    "s_mul_i32 s94, s94, s56\n"
    "s_add_u32 s82, s64, s9\n"
    "s_add_u32 s65, s58, s94\n"
    "s_waitcnt vmcnt(2)\n"
    "ds_read_b128 v[192:195], v234 offset:0\n"
    "ds_read_b128 v[196:199], v235 offset:0\n"
    "ds_read_b128 v[216:219], v252 offset:0\n"
    "ds_read_b128 v[220:223], v252 offset:64\n"
    "s_mul_i32 s15, s15, s16\n"
    "s_cmp_lt_u32 s72, 49\n"
    "s_cselect_b32 s15, 0, s15\n"
    "s_add_u32 s14, s14, s15\n"
    "s_mov_b32 s15, 0\n"
    "s_min_u32 s14, s14, s17\n"
    "s_add_u32 s95, s61, s14\n"
    "s_sub_u32 s95, s95, s76\n"
    "s_lshr_b32 s94, s61, 16\n"
    "s_sext_i32_i16 s95, s95\n"
    "s_cmp_eq_u32 s59, s10\n"
    "s_cbranch_scc1 .Ldin_A2_skip\n"
    "s_waitcnt lgkmcnt(7)\n"
    "buffer_load_dwordx4 v[236:237], s[40:43], s67 idxen offen offset:0 nt lds\n"
    "buffer_load_dwordx4 v[238:239], s[40:43], s67 idxen offen offset:1024 nt lds\n"
    ".Ldin_A2_skr:\n"
    "s_mul_i32 s95, s95, s94\n"
    "s_bfe_u32 s94, s60, 0x50019\n"
    "s_ashr_i32 s95, s95, 16\n"
    "s_lshl4_add_u32 s83, s94, s64\n"
    "s_max_i32 s95, s95, -14\n"
    "s_add_u32 s59, s59, 1\n"
    "s_min_i32 s95, s95, 14\n"
    "s_add_u32 s72, s77, s95\n"
    "s_cmp_lt_i32 s60, 0\n"
    "s_cselect_b32 s72, 0, s72\n"
    "s_cmp_eq_u32 s66, s80\n"
    "s_cselect_b64 s[66:67], s[64:65], s[66:67]\n"
    "s_waitcnt lgkmcnt(5)\n"
    "s_branch .Ldin_G3\n"
    ".Ldin_A2_poll:\n"
    "s_sleep 1\n"
    "ds_read_b64 v[216:217], v246 offset:128\n"
    "s_waitcnt lgkmcnt(0)\n"
    "v_readlane_b32 s60, v216, s59\n"
    "v_readlane_b32 s61, v217, s59\n"
    "s_cmp_eq_u32 s60, -1\n"
    "s_cbranch_scc1 .Ldin_A2_poll\n"
    "s_branch .Ldin_A2_pr\n"
    ".Ldin_A2_skip:\n"
    "s_add_u32 s8, s8, 6144\n"
    "s_sub_u32 s68, s68, 2048\n"
    "s_mov_b32 s9, 48\n"
    "s_sub_u32 s82, s82, 16\n"
    "s_sub_u32 s66, s66, 16\n"
    "s_sub_u32 s76, s76, s72\n"
    "s_waitcnt lgkmcnt(0)\n"
    "s_branch .Ldin_A2_skr\n"
    ".p2align 12\n"
    ".Ldin_stub_L0:\n"
    "s_setpc_b64 s[34:35]\n"
    ".p2align 5\n"
    ".Ldin_L0:\n"
    "s_cmp_lt_i32 s60, 0\n"
    "s_cbranch_scc1 .Ldin_E0\n"
    "ds_read_b128 v[144:147], v252 offset:8192\n"
    "ds_read_b128 v[148:151], v252 offset:8256\n"
    "ds_read_b128 v[152:155], v252 offset:16384\n"
    "ds_read_b128 v[156:159], v252 offset:16448\n"
    "v_add_u32 v251, s66, v244\n"
    "v_and_b32 v216, v248, v216\n"
    "v_and_b32 v217, v249, v217\n"
    "v_and_b32 v218, v248, v218\n"
    "v_and_b32 v219, v249, v219\n"
    "v_mfma_f32_4x4x4_16b_bf16 v[176:179], v[216:217], v[96:97], v[32:35]\n"
    "v_and_b32 v220, v248, v220\n"
    "v_and_b32 v221, v249, v221\n"
    "v_mfma_f32_4x4x4_16b_bf16 v[180:183], v[218:219], v[98:99], v[36:39]\n"
    "v_and_b32 v222, v248, v222\n"
    "v_and_b32_e64 v223, v249, v223\n"
    "v_mfma_f32_4x4x4_16b_bf16 v[184:187], v[220:221], v[100:101], v[40:43]\n"
    "v_cvt_pk_bf16_f32 v0, v176, v177\n"
    "v_mfma_f32_4x4x4_16b_bf16 v[224:227], v[228:229], v[208:209], v[224:227] cbsz:4 abid:0\n"
    "v_cvt_pk_bf16_f32 v1, v178, v179\n"
    "v_mfma_f32_4x4x4_16b_bf16 v[188:191], v[222:223], v[102:103], v[44:47]\n"
    "v_cvt_pk_bf16_f32 v2, v180, v181\n"
    "v_cvt_pk_bf16_f32 v3, v182, v183\n"
    "v_mfma_f32_4x4x4_16b_bf16 v[176:179], v[216:217], v[104:105], v[48:51]\n"
    "v_cvt_pk_bf16_f32 v4, v184, v185\n"
    "v_cvt_pk_bf16_f32 v5, v186, v187\n"
    "v_mfma_f32_4x4x4_16b_bf16 v[180:183], v[218:219], v[106:107], v[52:55]\n"
    "v_cvt_pk_bf16_f32 v6, v188, v189\n"
    "v_mfma_f32_4x4x4_16b_bf16 v[224:227], v[228:229], v[210:211], v[224:227] cbsz:4 abid:1\n"
    "v_cvt_pk_bf16_f32 v7, v190, v191\n"
    "v_mfma_f32_4x4x4_16b_bf16 v[184:187], v[220:221], v[108:109], v[56:59]\n"
    "v_cvt_pk_bf16_f32 v8, v176, v177\n"
    "v_cvt_pk_bf16_f32 v9, v178, v179\n"
    "v_mfma_f32_4x4x4_16b_bf16 v[188:191], v[222:223], v[110:111], v[60:63]\n"
    "v_cvt_pk_bf16_f32 v10, v180, v181\n"
    "v_cvt_pk_bf16_f32 v11, v182, v183\n"
    "v_mfma_f32_4x4x4_16b_bf16 v[176:179], v[216:217], v[112:113], v[64:67]\n"
    "v_cvt_pk_bf16_f32 v12, v184, v185\n"
    "v_mfma_f32_4x4x4_16b_bf16 v[224:227], v[228:229], v[212:213], v[224:227] cbsz:4 abid:2\n"
    "v_cvt_pk_bf16_f32 v13, v186, v187\n"
    "s_setprio 1\n"
    "s_waitcnt lgkmcnt(0)\n"
    "v_mfma_f32_4x4x4_16b_bf16 v[180:183], v[218:219], v[114:115], v[68:71]\n"
    "v_cvt_pk_bf16_f32 v14, v188, v189\n"
    "v_cvt_pk_bf16_f32 v15, v190, v191\n"
    "v_mfma_f32_4x4x4_16b_bf16 v[184:187], v[220:221], v[116:117], v[72:75]\n"
    "v_cvt_pk_bf16_f32 v16, v176, v177\n"
    "v_cvt_pk_bf16_f32 v17, v178, v179\n"
    "v_mfma_f32_4x4x4_16b_bf16 v[188:191], v[222:223], v[118:119], v[76:79]\n"
    "v_cvt_pk_bf16_f32 v18, v180, v181\n"
    "v_mfma_f32_4x4x4_16b_bf16 v[224:227], v[228:229], v[214:215], v[224:227] cbsz:4 abid:3\n"
    "v_cvt_pk_bf16_f32 v19, v182, v183\n"
    "v_mfma_f32_4x4x4_16b_bf16 v[176:179], v[216:217], v[120:121], v[80:83]\n"
    "v_cvt_pk_bf16_f32 v20, v184, v185\n"
    "v_cvt_pk_bf16_f32 v21, v186, v187\n"
    "v_mfma_f32_4x4x4_16b_bf16 v[180:183], v[218:219], v[122:123], v[84:87]\n"
    "v_cvt_pk_bf16_f32 v22, v188, v189\n"
    "v_cvt_pk_bf16_f32 v23, v190, v191\n"
    "v_mfma_f32_4x4x4_16b_bf16 v[184:187], v[220:221], v[124:125], v[88:91]\n"
    "v_cvt_pk_bf16_f32 v24, v176, v177\n"
    "v_cvt_pk_bf16_f32 v25, v178, v179\n"
    "v_mfma_f32_4x4x4_16b_bf16 v[188:191], v[222:223], v[126:127], v[92:95]\n"
    "v_cvt_pk_bf16_f32 v26, v180, v181\n"
    "v_cvt_pk_bf16_f32 v27, v182, v183\n"
    "v_cvt_pk_bf16_f32 v28, v184, v185\n"
    "v_cvt_pk_bf16_f32 v29, v186, v187\n"
    "v_mfma_f32_16x16x32_bf16 v[176:179], v[0:3], v[192:195], v[144:147]\n"
    "s_add_u32 m0, s68, 2048\n"
    "v_cvt_pk_bf16_f32 v30, v188, v189\n"
    "v_cvt_pk_bf16_f32 v31, v190, v191\n"
    "v_exp_f32 v160, v160 clamp\n"
    "v_mfma_f32_16x16x32_bf16 v[176:179], v[4:7], v[196:199], v[176:179]\n"
    "s_add_u32 s66, s66, 16\n"
    "v_exp_f32 v161, v161 clamp\n"
    "v_exp_f32 v162, v162 clamp\n"
    "v_mfma_f32_16x16x32_bf16 v[180:183], v[8:11], v[192:195], v[148:151]\n"
    "s_add_u32 s76, s76, s73\n"
    "v_exp_f32 v163, v163 clamp\n"
    "v_exp_f32 v164, v164 clamp\n"
    "v_mfma_f32_16x16x32_bf16 v[180:183], v[12:15], v[196:199], v[180:183]\n"
    "s_lshl1_add_u32 s76, s72, s76\n"
    "v_exp_f32 v165, v165 clamp\n"
    "v_exp_f32 v166, v166 clamp\n"
    "v_mfma_f32_16x16x32_bf16 v[184:187], v[16:19], v[192:195], v[152:155]\n"
    "s_mov_b32 s80, s83\n"
    "v_exp_f32 v167, v167 clamp\n"
    "v_exp_f32 v168, v168 clamp\n"
    "v_mfma_f32_16x16x32_bf16 v[184:187], v[20:23], v[196:199], v[184:187]\n"
    "s_add_u32 s62, s62, 256\n"
    "v_exp_f32 v169, v169 clamp\n"
    "v_exp_f32 v170, v170 clamp\n"
    "v_mfma_f32_16x16x32_bf16 v[188:191], v[24:27], v[192:195], v[156:159]\n"
    "s_mov_b32 s81, s64\n"
    "v_exp_f32 v171, v171 clamp\n"
    "v_exp_f32 v172, v172 clamp\n"
    "v_mfma_f32_16x16x32_bf16 v[188:191], v[28:31], v[196:199], v[188:191]\n"
    "s_lshr_b32 s63, s65, 6\n"
    "v_exp_f32 v173, v173 clamp\n"
    "v_exp_f32 v174, v174 clamp\n"
    "v_exp_f32 v175, v175 clamp\n"
    "v_pk_fma_f32 v[160:161], v[160:161], s[70:71], v[128:129]\n"
    "v_pk_fma_f32 v[162:163], v[162:163], s[70:71], v[130:131]\n"
    "v_pk_add_f32 v[230:231], v[160:161], v[162:163]\n"
    "v_pk_mul_f32 v[232:233], v[160:161], v[162:163]\n"
    "v_pk_fma_f32 v[164:165], v[164:165], s[70:71], v[132:133]\n"
    "v_pk_fma_f32 v[230:231], v[230:231], v[164:165], v[232:233]\n"
    "v_pk_mul_f32 v[232:233], v[232:233], v[164:165]\n"
    "v_pk_fma_f32 v[166:167], v[166:167], s[70:71], v[134:135]\n"
    "v_pk_fma_f32 v[230:231], v[230:231], v[166:167], v[232:233]\n"
    "v_pk_mul_f32 v[232:233], v[232:233], v[166:167]\n"
    "v_pk_fma_f32 v[168:169], v[168:169], s[70:71], v[136:137]\n"
    "v_pk_fma_f32 v[230:231], v[230:231], v[168:169], v[232:233]\n"
    "v_pk_mul_f32 v[232:233], v[232:233], v[168:169]\n"
    "v_pk_fma_f32 v[170:171], v[170:171], s[70:71], v[138:139]\n"
    "v_pk_fma_f32 v[230:231], v[230:231], v[170:171], v[232:233]\n"
    "v_pk_mul_f32 v[232:233], v[232:233], v[170:171]\n"
    "v_pk_fma_f32 v[172:173], v[172:173], s[70:71], v[140:141]\n"
    "v_pk_fma_f32 v[230:231], v[230:231], v[172:173], v[232:233]\n"
    "v_pk_mul_f32 v[232:233], v[232:233], v[172:173]\n"
    "v_pk_fma_f32 v[174:175], v[174:175], s[70:71], v[142:143]\n"
    "v_pk_fma_f32 v[230:231], v[230:231], v[174:175], v[232:233]\n"
    "v_pk_mul_f32 v[232:233], v[232:233], v[174:175]\n"
    "v_rcp_f32 v232, v232\n"
    "v_rcp_f32 v233, v233\n"
    "v_fma_f32 v230, v230, v232, v247\n"
    "v_fmac_f32 v230, v231, v233\n"
    "s_setprio 0\n"
    "ds_read_b64_tr_b16 v[208:209], v240 offset:2048\n"
    "ds_read_b64_tr_b16 v[210:211], v241 offset:2048\n"
    "ds_read_b64_tr_b16 v[212:213], v242 offset:2048\n"
    "ds_read_b64_tr_b16 v[214:215], v243 offset:2048\n"
    "ds_read_u8 v236, v251 offset:0\n"
    "ds_read_u8 v238, v251 offset:1\n"
    "ds_write_b16_d16_hi v245, v230\n"
    "ds_read_b64 v[228:229], v246\n"
    "ds_read_b64 v[216:217], v246 offset:128\n"
    "s_waitcnt vmcnt(2)\n"
    "ds_read_b128 v[192:195], v234 offset:4096\n"
    "ds_read_b128 v[196:199], v235 offset:4096\n"
    "s_waitcnt lgkmcnt(5)\n"
    "buffer_load_dwordx4 v[236:237], s[40:43], s67 idxen offen offset:0 nt lds\n"
    "buffer_load_dwordx4 v[238:239], s[40:43], s67 idxen offen offset:1024 nt lds\n"
    "s_waitcnt lgkmcnt(2)\n"
    "s_bitcmp1_b32 s60, 30\n"
    "s_cbranch_scc1 .Ldin_A1s\n"
    ".Ldin_A1:\n"
    "v_add_u32 v251, s66, v244\n"
    "v_readlane_b32 s60, v216, s59\n"
    "v_readlane_b32 s61, v217, s59\n"
    "v_mfma_f32_4x4x4_16b_bf16 v[224:227], v[228:229], v[200:201], v[224:227] cbsz:4 abid:0\n"
    "v_exp_f32 v176, v176 clamp\n"
    "v_exp_f32 v177, v177 clamp\n"
    "v_mfma_f32_4x4x4_16b_bf16 v[224:227], v[228:229], v[202:203], v[224:227] cbsz:4 abid:1\n"
    "v_exp_f32 v178, v178 clamp\n"
    "v_exp_f32 v179, v179 clamp\n"
    "v_mfma_f32_4x4x4_16b_bf16 v[224:227], v[228:229], v[204:205], v[224:227] cbsz:4 abid:2\n"
    "v_exp_f32 v180, v180 clamp\n"
    "v_exp_f32 v181, v181 clamp\n"
    "v_mfma_f32_4x4x4_16b_bf16 v[224:227], v[228:229], v[206:207], v[224:227] cbsz:4 abid:3\n"
    "s_waitcnt lgkmcnt(0)\n"
    "v_mfma_f32_16x16x32_bf16 v[160:163], v[0:3], v[192:195], v[144:147]\n"
    "s_setprio 1\n"
    "v_exp_f32 v182, v182 clamp\n"
    "v_mfma_f32_16x16x32_bf16 v[160:163], v[4:7], v[196:199], v[160:163]\n"
    "s_cmp_eq_u32 s60, -1\n"
    "v_exp_f32 v183, v183 clamp\n"
    "v_mfma_f32_16x16x32_bf16 v[164:167], v[8:11], v[192:195], v[148:151]\n"
    "s_cbranch_scc1 .Ldin_A1_poll\n"
    ".Ldin_A1_pr:\n"
    "v_exp_f32 v184, v184 clamp\n"
    "v_mfma_f32_16x16x32_bf16 v[164:167], v[12:15], v[196:199], v[164:167]\n"
    "s_lshr_b32 s94, s60, 12\n"
    "v_exp_f32 v185, v185 clamp\n"
    "v_mfma_f32_16x16x32_bf16 v[168:171], v[16:19], v[192:195], v[152:155]\n"
    "s_and_b32 s94, s94, 0x1ff0\n"
    "v_exp_f32 v186, v186 clamp\n"
    "v_mfma_f32_16x16x32_bf16 v[168:171], v[20:23], v[196:199], v[168:171]\n"
    "s_mov_b32 m0, s62\n"
    "v_exp_f32 v187, v187 clamp\n"
    "v_mfma_f32_16x16x32_bf16 v[172:175], v[24:27], v[192:195], v[156:159]\n"
    "s_add_u32 s66, s66, 16\n"
    "v_exp_f32 v188, v188 clamp\n"
    "v_mfma_f32_16x16x32_bf16 v[172:175], v[28:31], v[196:199], v[172:175]\n"
    "s_and_b32 s64, s60, 0xffff\n"
    "v_xor_b32 v252, s94, v250\n"
    "v_exp_f32 v189, v189 clamp\n"
    "v_exp_f32 v190, v190 clamp\n"
    "v_exp_f32 v191, v191 clamp\n"
    "v_pk_fma_f32 v[176:177], v[176:177], s[70:71], v[128:129]\n"
    "v_pk_fma_f32 v[178:179], v[178:179], s[70:71], v[130:131]\n"
    "v_pk_add_f32 v[230:231], v[176:177], v[178:179]\n"
    "v_pk_mul_f32 v[232:233], v[176:177], v[178:179]\n"
    "v_pk_add_f32 v[218:219], v[224:225], v[226:227]\n"
    "v_mov_b64 v[224:225], 0\n"
    "v_mov_b64 v[226:227], 0\n"
    "v_pk_fma_f32 v[180:181], v[180:181], s[70:71], v[132:133]\n"
    "v_pk_fma_f32 v[230:231], v[230:231], v[180:181], v[232:233]\n"
    "v_pk_mul_f32 v[232:233], v[232:233], v[180:181]\n"
    "v_pk_fma_f32 v[182:183], v[182:183], s[70:71], v[134:135]\n"
    "v_pk_fma_f32 v[230:231], v[230:231], v[182:183], v[232:233]\n"
    "v_pk_mul_f32 v[232:233], v[232:233], v[182:183]\n"
    "v_pk_fma_f32 v[184:185], v[184:185], s[70:71], v[136:137]\n"
    "v_pk_fma_f32 v[230:231], v[230:231], v[184:185], v[232:233]\n"
    "v_pk_mul_f32 v[232:233], v[232:233], v[184:185]\n"
    "v_pk_fma_f32 v[186:187], v[186:187], s[70:71], v[138:139]\n"
    "v_pk_fma_f32 v[230:231], v[230:231], v[186:187], v[232:233]\n"
    "v_pk_mul_f32 v[232:233], v[232:233], v[186:187]\n"
    "v_pk_fma_f32 v[188:189], v[188:189], s[70:71], v[140:141]\n"
    "v_pk_fma_f32 v[230:231], v[230:231], v[188:189], v[232:233]\n"
    "v_pk_mul_f32 v[232:233], v[232:233], v[188:189]\n"
    "v_pk_fma_f32 v[190:191], v[190:191], s[70:71], v[142:143]\n"
    "v_pk_fma_f32 v[230:231], v[230:231], v[190:191], v[232:233]\n"
    "v_pk_mul_f32 v[232:233], v[232:233], v[190:191]\n"
    "v_rcp_f32 v232, v232\n"
    "v_rcp_f32 v233, v233\n"
    "v_fma_f32 v230, v230, v232, v247\n"
    "v_fmac_f32 v230, v231, v233\n"
    "v_add_f32_e64 v218, v218, v219\n"
    "s_setprio 0\n"
    "ds_read_b64_tr_b16 v[200:201], v240 offset:4096\n"
    "ds_read_b64_tr_b16 v[202:203], v241 offset:4096\n"
    "ds_read_b64_tr_b16 v[204:205], v242 offset:4096\n"
    "ds_read_b64_tr_b16 v[206:207], v243 offset:4096\n"
    "ds_read_u8 v236, v251 offset:0\n"
    "ds_read_u8 v238, v251 offset:1\n"
    "ds_write_b16_d16_hi v245, v230\n"
    "ds_read_b64 v[228:229], v246\n"
    "ds_write_addtid_b32 v218 offset:49152\n"
    "s_add_u32 m0, s68, 4096\n"
    "s_bfe_u32 s94, s60, 0x30013\n"
    "s_add_u32 s58, s58, s57\n"
    "s_mul_i32 s94, s94, s56\n"
    "s_add_u32 s82, s64, s9\n"
    "s_add_u32 s65, s58, s94\n"
    "s_waitcnt vmcnt(2)\n"
    "ds_read_b128 v[192:195], v234 offset:6144\n"
    "ds_read_b128 v[196:199], v235 offset:6144\n"
    "ds_read_b128 v[216:219], v252 offset:0\n"
    "ds_read_b128 v[220:223], v252 offset:64\n"
    "s_mul_i32 s15, s15, s16\n"
    "s_cmp_lt_u32 s72, 49\n"
    "s_cselect_b32 s15, 0, s15\n"
    "s_add_u32 s14, s14, s15\n"
    "s_mov_b32 s15, 0\n"
    "s_min_u32 s14, s14, s17\n"
    "s_add_u32 s95, s61, s14\n"
    "s_sub_u32 s95, s95, s76\n"
    "s_lshr_b32 s94, s61, 16\n"
    "s_sext_i32_i16 s95, s95\n"
    "s_cmp_eq_u32 s59, s10\n"
    "s_cbranch_scc1 .Ldin_A1_skip\n"
    "s_waitcnt lgkmcnt(7)\n"
    "buffer_load_dwordx4 v[236:237], s[40:43], s67 idxen offen offset:0 nt lds\n"
    "buffer_load_dwordx4 v[238:239], s[40:43], s67 idxen offen offset:1024 nt lds\n"
    ".Ldin_A1_skr:\n"
    "s_mul_i32 s95, s95, s94\n"
    "s_bfe_u32 s94, s60, 0x50019\n"
    "s_ashr_i32 s95, s95, 16\n"
    "s_lshl4_add_u32 s83, s94, s64\n"
    "s_max_i32 s95, s95, -14\n"
    "s_add_u32 s59, s59, 1\n"
    "s_min_i32 s95, s95, 14\n"
    "s_add_u32 s72, s77, s95\n"
    "s_cmp_lt_i32 s60, 0\n"
    "s_cselect_b32 s72, 0, s72\n"
    "s_cmp_eq_u32 s66, s80\n"
    "s_cselect_b64 s[66:67], s[64:65], s[66:67]\n"
    "s_waitcnt lgkmcnt(5)\n"
    "s_branch .Ldin_G2\n"
    ".Ldin_A1_poll:\n"
    "s_sleep 1\n"
    "ds_read_b64 v[216:217], v246 offset:128\n"
    "s_waitcnt lgkmcnt(0)\n"
    "v_readlane_b32 s60, v216, s59\n"
    "v_readlane_b32 s61, v217, s59\n"
    "s_cmp_eq_u32 s60, -1\n"
    "s_cbranch_scc1 .Ldin_A1_poll\n"
    "s_branch .Ldin_A1_pr\n"
    ".Ldin_A1_skip:\n"
    "s_add_u32 s8, s8, 6144\n"
    "s_sub_u32 s68, s68, 2048\n"
    "s_mov_b32 s9, 48\n"
    "s_sub_u32 s82, s82, 16\n"
    "s_sub_u32 s66, s66, 16\n"
    "s_sub_u32 s76, s76, s72\n"
    "s_waitcnt lgkmcnt(0)\n"
    "s_branch .Ldin_A1_skr\n"
    ".Ldin_stub_L3:\n"
    "s_setpc_b64 s[34:35]\n"
    ".p2align 5\n"
    ".Ldin_L3:\n"
    "s_cmp_lt_i32 s60, 0\n"
    "s_cbranch_scc1 .Ldin_E1\n"
    "ds_read_b128 v[144:147], v252 offset:8192\n"
    "ds_read_b128 v[148:151], v252 offset:8256\n"
    "ds_read_b128 v[152:155], v252 offset:16384\n"
    "ds_read_b128 v[156:159], v252 offset:16448\n"
    "v_add_u32 v251, s66, v244\n"
    "v_and_b32 v216, v248, v216\n"
    "v_and_b32 v217, v249, v217\n"
    "v_and_b32 v218, v248, v218\n"
    "v_and_b32 v219, v249, v219\n"
    "v_mfma_f32_4x4x4_16b_bf16 v[160:163], v[216:217], v[96:97], v[32:35]\n"
    "v_and_b32 v220, v248, v220\n"
    "v_and_b32 v221, v249, v221\n"
    "v_mfma_f32_4x4x4_16b_bf16 v[164:167], v[218:219], v[98:99], v[36:39]\n"
    "v_and_b32 v222, v248, v222\n"
    "v_and_b32_e64 v223, v249, v223\n"
    "v_mfma_f32_4x4x4_16b_bf16 v[168:171], v[220:221], v[100:101], v[40:43]\n"
    "v_cvt_pk_bf16_f32 v0, v160, v161\n"
    "v_mfma_f32_4x4x4_16b_bf16 v[224:227], v[228:229], v[200:201], v[224:227] cbsz:4 abid:0\n"
    "v_cvt_pk_bf16_f32 v1, v162, v163\n"
    "v_mfma_f32_4x4x4_16b_bf16 v[172:175], v[222:223], v[102:103], v[44:47]\n"
    "v_cvt_pk_bf16_f32 v2, v164, v165\n"
    "v_cvt_pk_bf16_f32 v3, v166, v167\n"
    "v_mfma_f32_4x4x4_16b_bf16 v[160:163], v[216:217], v[104:105], v[48:51]\n"
    "v_cvt_pk_bf16_f32 v4, v168, v169\n"
    "v_cvt_pk_bf16_f32 v5, v170, v171\n"
    "v_mfma_f32_4x4x4_16b_bf16 v[164:167], v[218:219], v[106:107], v[52:55]\n"
    "v_cvt_pk_bf16_f32 v6, v172, v173\n"
    "v_mfma_f32_4x4x4_16b_bf16 v[224:227], v[228:229], v[202:203], v[224:227] cbsz:4 abid:1\n"
    "v_cvt_pk_bf16_f32 v7, v174, v175\n"
    "v_mfma_f32_4x4x4_16b_bf16 v[168:171], v[220:221], v[108:109], v[56:59]\n"
    "v_cvt_pk_bf16_f32 v8, v160, v161\n"
    "v_cvt_pk_bf16_f32 v9, v162, v163\n"
    "v_mfma_f32_4x4x4_16b_bf16 v[172:175], v[222:223], v[110:111], v[60:63]\n"
    "v_cvt_pk_bf16_f32 v10, v164, v165\n"
    "v_cvt_pk_bf16_f32 v11, v166, v167\n"
    "v_mfma_f32_4x4x4_16b_bf16 v[160:163], v[216:217], v[112:113], v[64:67]\n"
    "v_cvt_pk_bf16_f32 v12, v168, v169\n"
    "v_mfma_f32_4x4x4_16b_bf16 v[224:227], v[228:229], v[204:205], v[224:227] cbsz:4 abid:2\n"
    "v_cvt_pk_bf16_f32 v13, v170, v171\n"
    "s_setprio 1\n"
    "s_waitcnt lgkmcnt(0)\n"
    "v_mfma_f32_4x4x4_16b_bf16 v[164:167], v[218:219], v[114:115], v[68:71]\n"
    "v_cvt_pk_bf16_f32 v14, v172, v173\n"
    "v_cvt_pk_bf16_f32 v15, v174, v175\n"
    "v_mfma_f32_4x4x4_16b_bf16 v[168:171], v[220:221], v[116:117], v[72:75]\n"
    "v_cvt_pk_bf16_f32 v16, v160, v161\n"
    "v_cvt_pk_bf16_f32 v17, v162, v163\n"
    "v_mfma_f32_4x4x4_16b_bf16 v[172:175], v[222:223], v[118:119], v[76:79]\n"
    "v_cvt_pk_bf16_f32 v18, v164, v165\n"
    "v_mfma_f32_4x4x4_16b_bf16 v[224:227], v[228:229], v[206:207], v[224:227] cbsz:4 abid:3\n"
    "v_cvt_pk_bf16_f32 v19, v166, v167\n"
    "v_mfma_f32_4x4x4_16b_bf16 v[160:163], v[216:217], v[120:121], v[80:83]\n"
    "v_cvt_pk_bf16_f32 v20, v168, v169\n"
    "v_cvt_pk_bf16_f32 v21, v170, v171\n"
    "v_mfma_f32_4x4x4_16b_bf16 v[164:167], v[218:219], v[122:123], v[84:87]\n"
    "v_cvt_pk_bf16_f32 v22, v172, v173\n"
    "v_cvt_pk_bf16_f32 v23, v174, v175\n"
    "v_mfma_f32_4x4x4_16b_bf16 v[168:171], v[220:221], v[124:125], v[88:91]\n"
    "v_cvt_pk_bf16_f32 v24, v160, v161\n"
    "v_cvt_pk_bf16_f32 v25, v162, v163\n"
    "v_mfma_f32_4x4x4_16b_bf16 v[172:175], v[222:223], v[126:127], v[92:95]\n"
    "v_cvt_pk_bf16_f32 v26, v164, v165\n"
    "v_cvt_pk_bf16_f32 v27, v166, v167\n"
    "v_cvt_pk_bf16_f32 v28, v168, v169\n"
    "v_cvt_pk_bf16_f32 v29, v170, v171\n"
    "v_mfma_f32_16x16x32_bf16 v[160:163], v[0:3], v[192:195], v[144:147]\n"
    "s_add_u32 m0, s8, 0\n"
    "v_cvt_pk_bf16_f32 v30, v172, v173\n"
    "v_cvt_pk_bf16_f32 v31, v174, v175\n"
    "v_exp_f32 v176, v176 clamp\n"
    "v_mfma_f32_16x16x32_bf16 v[160:163], v[4:7], v[196:199], v[160:163]\n"
    "s_add_u32 s66, s66, 16\n"
    "v_exp_f32 v177, v177 clamp\n"
    "v_exp_f32 v178, v178 clamp\n"
    "v_mfma_f32_16x16x32_bf16 v[164:167], v[8:11], v[192:195], v[148:151]\n"
    "s_add_u32 s76, s76, s73\n"
    "v_exp_f32 v179, v179 clamp\n"
    "v_exp_f32 v180, v180 clamp\n"
    "v_mfma_f32_16x16x32_bf16 v[164:167], v[12:15], v[196:199], v[164:167]\n"
    "s_lshl1_add_u32 s76, s72, s76\n"
    "v_exp_f32 v181, v181 clamp\n"
    "v_exp_f32 v182, v182 clamp\n"
    "v_mfma_f32_16x16x32_bf16 v[168:171], v[16:19], v[192:195], v[152:155]\n"
    "s_mov_b32 s80, s83\n"
    "v_exp_f32 v183, v183 clamp\n"
    "v_exp_f32 v184, v184 clamp\n"
    "v_mfma_f32_16x16x32_bf16 v[168:171], v[20:23], v[196:199], v[168:171]\n"
    "s_add_u32 s62, s62, 256\n"
    "v_exp_f32 v185, v185 clamp\n"
    "v_exp_f32 v186, v186 clamp\n"
    "v_mfma_f32_16x16x32_bf16 v[172:175], v[24:27], v[192:195], v[156:159]\n"
    "s_mov_b32 s81, s64\n"
    "v_exp_f32 v187, v187 clamp\n"
    "v_exp_f32 v188, v188 clamp\n"
    "v_mfma_f32_16x16x32_bf16 v[172:175], v[28:31], v[196:199], v[172:175]\n"
    "s_lshr_b32 s63, s65, 6\n"
    "v_exp_f32 v189, v189 clamp\n"
    "v_exp_f32 v190, v190 clamp\n"
    "v_exp_f32 v191, v191 clamp\n"
    "s_nop 0\n"
    "v_pk_fma_f32 v[176:177], v[176:177], s[70:71], v[128:129]\n"
    "v_pk_fma_f32 v[178:179], v[178:179], s[70:71], v[130:131]\n"
    "v_pk_add_f32 v[230:231], v[176:177], v[178:179]\n"
    "v_pk_mul_f32 v[232:233], v[176:177], v[178:179]\n"
    "v_pk_fma_f32 v[180:181], v[180:181], s[70:71], v[132:133]\n"
    "v_pk_fma_f32 v[230:231], v[230:231], v[180:181], v[232:233]\n"
    "v_pk_mul_f32 v[232:233], v[232:233], v[180:181]\n"
    "v_pk_fma_f32 v[182:183], v[182:183], s[70:71], v[134:135]\n"
    "v_pk_fma_f32 v[230:231], v[230:231], v[182:183], v[232:233]\n"
    "v_pk_mul_f32 v[232:233], v[232:233], v[182:183]\n"
    "v_pk_fma_f32 v[184:185], v[184:185], s[70:71], v[136:137]\n"
    "v_pk_fma_f32 v[230:231], v[230:231], v[184:185], v[232:233]\n"
    "v_pk_mul_f32 v[232:233], v[232:233], v[184:185]\n"
    "v_pk_fma_f32 v[186:187], v[186:187], s[70:71], v[138:139]\n"
    "v_pk_fma_f32 v[230:231], v[230:231], v[186:187], v[232:233]\n"
    "v_pk_mul_f32 v[232:233], v[232:233], v[186:187]\n"
    "v_pk_fma_f32 v[188:189], v[188:189], s[70:71], v[140:141]\n"
    "v_pk_fma_f32 v[230:231], v[230:231], v[188:189], v[232:233]\n"
    "v_pk_mul_f32 v[232:233], v[232:233], v[188:189]\n"
    "v_pk_fma_f32 v[190:191], v[190:191], s[70:71], v[142:143]\n"
    "v_pk_fma_f32 v[230:231], v[230:231], v[190:191], v[232:233]\n"
    "v_pk_mul_f32 v[232:233], v[232:233], v[190:191]\n"
    "v_rcp_f32 v232, v232\n"
    "v_rcp_f32 v233, v233\n"
    "v_fma_f32 v230, v230, v232, v247\n"
    "v_fmac_f32 v230, v231, v233\n"
    "s_setprio 0\n"
    "ds_read_b64_tr_b16 v[200:201], v240 offset:0\n"
    "ds_read_b64_tr_b16 v[202:203], v241 offset:0\n"
    "ds_read_b64_tr_b16 v[204:205], v242 offset:0\n"
    "ds_read_b64_tr_b16 v[206:207], v243 offset:0\n"
    "ds_read_u8 v236, v251 offset:0\n"
    "ds_read_u8 v238, v251 offset:1\n"
    "ds_write_b16_d16_hi v245, v230\n"
    "ds_read_b64 v[228:229], v246\n"
    "ds_read_b64 v[216:217], v246 offset:128\n"
    "s_waitcnt vmcnt(2)\n"
    "ds_read_b128 v[192:195], v234 offset:2048\n"
    "ds_read_b128 v[196:199], v235 offset:2048\n"
    "s_waitcnt lgkmcnt(5)\n"
    "buffer_load_dwordx4 v[236:237], s[40:43], s67 idxen offen offset:0 nt lds\n"
    "buffer_load_dwordx4 v[238:239], s[40:43], s67 idxen offen offset:1024 nt lds\n"
    "s_waitcnt lgkmcnt(2)\n"
    "s_bitcmp1_b32 s60, 30\n"
    "s_cbranch_scc1 .Ldin_A0s\n"
    "s_branch .Ldin_A0\n"
    ".Ldin_stub_A0:\n"
    "s_setpc_b64 s[34:35]\n"
    ".Ldin_A0:\n"
    "v_add_u32 v251, s66, v244\n"
    "v_readlane_b32 s60, v216, s59\n"
    "v_readlane_b32 s61, v217, s59\n"
    "v_mfma_f32_4x4x4_16b_bf16 v[224:227], v[228:229], v[208:209], v[224:227] cbsz:4 abid:0\n"
    "v_exp_f32 v160, v160 clamp\n"
    "v_exp_f32 v161, v161 clamp\n"
    "v_mfma_f32_4x4x4_16b_bf16 v[224:227], v[228:229], v[210:211], v[224:227] cbsz:4 abid:1\n"
    "v_exp_f32 v162, v162 clamp\n"
    "v_exp_f32 v163, v163 clamp\n"
    "v_mfma_f32_4x4x4_16b_bf16 v[224:227], v[228:229], v[212:213], v[224:227] cbsz:4 abid:2\n"
    "v_exp_f32 v164, v164 clamp\n"
    "v_exp_f32 v165, v165 clamp\n"
    "v_mfma_f32_4x4x4_16b_bf16 v[224:227], v[228:229], v[214:215], v[224:227] cbsz:4 abid:3\n"
    "s_waitcnt lgkmcnt(0)\n"
    "v_mfma_f32_16x16x32_bf16 v[176:179], v[0:3], v[192:195], v[144:147]\n"
    "s_setprio 1\n"
    "v_exp_f32 v166, v166 clamp\n"
    "v_mfma_f32_16x16x32_bf16 v[176:179], v[4:7], v[196:199], v[176:179]\n"
    "s_cmp_eq_u32 s60, -1\n"
    "v_exp_f32 v167, v167 clamp\n"
    "v_mfma_f32_16x16x32_bf16 v[180:183], v[8:11], v[192:195], v[148:151]\n"
    "s_cbranch_scc1 .Ldin_A0_poll\n"
    ".Ldin_A0_pr:\n"
    "v_exp_f32 v168, v168 clamp\n"
    "v_mfma_f32_16x16x32_bf16 v[180:183], v[12:15], v[196:199], v[180:183]\n"
    "s_lshr_b32 s94, s60, 12\n"
    "v_exp_f32 v169, v169 clamp\n"
    "v_mfma_f32_16x16x32_bf16 v[184:187], v[16:19], v[192:195], v[152:155]\n"
    "s_and_b32 s94, s94, 0x1ff0\n"
    "v_exp_f32 v170, v170 clamp\n"
    "v_mfma_f32_16x16x32_bf16 v[184:187], v[20:23], v[196:199], v[184:187]\n"
    "s_mov_b32 m0, s62\n"
    "v_exp_f32 v171, v171 clamp\n"
    "v_mfma_f32_16x16x32_bf16 v[188:191], v[24:27], v[192:195], v[156:159]\n"
    "s_add_u32 s66, s66, 16\n"
    "v_exp_f32 v172, v172 clamp\n"
    "v_mfma_f32_16x16x32_bf16 v[188:191], v[28:31], v[196:199], v[188:191]\n"
    "s_and_b32 s64, s60, 0xffff\n"
    "v_xor_b32 v252, s94, v250\n"
    "v_exp_f32 v173, v173 clamp\n"
    "v_exp_f32 v174, v174 clamp\n"
    "v_exp_f32 v175, v175 clamp\n"
    "v_pk_fma_f32 v[160:161], v[160:161], s[70:71], v[128:129]\n"
    "v_pk_fma_f32 v[162:163], v[162:163], s[70:71], v[130:131]\n"
    "v_pk_add_f32 v[230:231], v[160:161], v[162:163]\n"
    "v_pk_mul_f32 v[232:233], v[160:161], v[162:163]\n"
    "v_pk_add_f32 v[218:219], v[224:225], v[226:227]\n"
    "v_mov_b64 v[224:225], 0\n"
    "v_mov_b64 v[226:227], 0\n"
    "v_pk_fma_f32 v[164:165], v[164:165], s[70:71], v[132:133]\n"
    "v_pk_fma_f32 v[230:231], v[230:231], v[164:165], v[232:233]\n"
    "v_pk_mul_f32 v[232:233], v[232:233], v[164:165]\n"
    "v_pk_fma_f32 v[166:167], v[166:167], s[70:71], v[134:135]\n"
    "v_pk_fma_f32 v[230:231], v[230:231], v[166:167], v[232:233]\n"
    "v_pk_mul_f32 v[232:233], v[232:233], v[166:167]\n"
    "v_pk_fma_f32 v[168:169], v[168:169], s[70:71], v[136:137]\n"
    "v_pk_fma_f32 v[230:231], v[230:231], v[168:169], v[232:233]\n"
    "v_pk_mul_f32 v[232:233], v[232:233], v[168:169]\n"
    "v_pk_fma_f32 v[170:171], v[170:171], s[70:71], v[138:139]\n"
    "v_pk_fma_f32 v[230:231], v[230:231], v[170:171], v[232:233]\n"
    "v_pk_mul_f32 v[232:233], v[232:233], v[170:171]\n"
    "v_pk_fma_f32 v[172:173], v[172:173], s[70:71], v[140:141]\n"
    "v_pk_fma_f32 v[230:231], v[230:231], v[172:173], v[232:233]\n"
    "v_pk_mul_f32 v[232:233], v[232:233], v[172:173]\n"
    "v_pk_fma_f32 v[174:175], v[174:175], s[70:71], v[142:143]\n"
    "v_pk_fma_f32 v[230:231], v[230:231], v[174:175], v[232:233]\n"
    "v_pk_mul_f32 v[232:233], v[232:233], v[174:175]\n"
    "v_rcp_f32 v232, v232\n"
    "v_rcp_f32 v233, v233\n"
    "v_fma_f32 v230, v230, v232, v247\n"
    "v_fmac_f32 v230, v231, v233\n"
    "v_add_f32_e64 v218, v218, v219\n"
    "s_setprio 0\n"
    "ds_read_b64_tr_b16 v[208:209], v240 offset:2048\n"
    "ds_read_b64_tr_b16 v[210:211], v241 offset:2048\n"
    "ds_read_b64_tr_b16 v[212:213], v242 offset:2048\n"
    "ds_read_b64_tr_b16 v[214:215], v243 offset:2048\n"
    "ds_read_u8 v236, v251 offset:0\n"
    "ds_read_u8 v238, v251 offset:1\n"
    "ds_write_b16_d16_hi v245, v230\n"
    "ds_read_b64 v[228:229], v246\n"
    "ds_write_addtid_b32 v218 offset:49152\n"
    "s_add_u32 m0, s68, 2048\n"
    "s_bfe_u32 s94, s60, 0x30013\n"
    "s_add_u32 s58, s58, s57\n"
    "s_mul_i32 s94, s94, s56\n"
    "s_add_u32 s82, s64, s9\n"
    "s_add_u32 s65, s58, s94\n"
    "s_waitcnt vmcnt(2)\n"
    "ds_read_b128 v[192:195], v234 offset:4096\n"
    "ds_read_b128 v[196:199], v235 offset:4096\n"
    "ds_read_b128 v[216:219], v252 offset:0\n"
    "ds_read_b128 v[220:223], v252 offset:64\n"
    "s_mul_i32 s15, s15, s16\n"
    "s_cmp_lt_u32 s72, 49\n"
    "s_cselect_b32 s15, 0, s15\n"
    "s_add_u32 s14, s14, s15\n"
    "s_mov_b32 s15, 0\n"
    "s_min_u32 s14, s14, s17\n"
    "s_add_u32 s95, s61, s14\n"
    "s_sub_u32 s95, s95, s76\n"
    "s_lshr_b32 s94, s61, 16\n"
    "s_sext_i32_i16 s95, s95\n"
    "s_cmp_eq_u32 s59, s10\n"
    "s_cbranch_scc1 .Ldin_A0_skip\n"
    "s_waitcnt lgkmcnt(7)\n"
    "buffer_load_dwordx4 v[236:237], s[40:43], s67 idxen offen offset:0 nt lds\n"
    "buffer_load_dwordx4 v[238:239], s[40:43], s67 idxen offen offset:1024 nt lds\n"
    ".Ldin_A0_skr:\n"
    "s_mul_i32 s95, s95, s94\n"
    "s_bfe_u32 s94, s60, 0x50019\n"
    "s_ashr_i32 s95, s95, 16\n"
    "s_lshl4_add_u32 s83, s94, s64\n"
    "s_max_i32 s95, s95, -14\n"
    "s_add_u32 s59, s59, 1\n"
    "s_min_i32 s95, s95, 14\n"
    "s_add_u32 s72, s77, s95\n"
    "s_cmp_lt_i32 s60, 0\n"
    "s_cselect_b32 s72, 0, s72\n"
    "s_cmp_eq_u32 s66, s80\n"
    "s_cselect_b64 s[66:67], s[64:65], s[66:67]\n"
    "s_waitcnt lgkmcnt(5)\n"
    "s_branch .Ldin_G1\n"
    ".Ldin_A0_poll:\n"
    "s_sleep 1\n"
    "ds_read_b64 v[216:217], v246 offset:128\n"
    "s_waitcnt lgkmcnt(0)\n"
    "v_readlane_b32 s60, v216, s59\n"
    "v_readlane_b32 s61, v217, s59\n"
    "s_cmp_eq_u32 s60, -1\n"
    "s_cbranch_scc1 .Ldin_A0_poll\n"
    "s_branch .Ldin_A0_pr\n"
    ".Ldin_A0_skip:\n"
    "s_add_u32 s8, s8, 6144\n"
    "s_sub_u32 s68, s68, 2048\n"
    "s_mov_b32 s9, 48\n"
    "s_sub_u32 s82, s82, 16\n"
    "s_sub_u32 s66, s66, 16\n"
    "s_sub_u32 s76, s76, s72\n"
    "s_waitcnt lgkmcnt(0)\n"
    "s_branch .Ldin_A0_skr\n"
    ".p2align 12\n"
    ".Ldin_py:\n"
    "v_and_b32 v32, 0x58, v4\n"
    "v_lshlrev_b32 v33, 1, v2\n"
    "v_and_b32 v34, 0x3c0, v251\n"
    "v_bitop3_b32 v35, v33, v0, 32 bitop3:0x28\n"
    "v_lshl_or_b32 v34, v34, 1, v32\n"
    "s_lshl_b32 s23, s21, 11\n"
    "s_add_u32 s23, s23, 54144\n"
    "v_add3_u32 v52, v34, v35, s23\n"
    "v_and_or_b32 v32, v33, 32, v32\n"
    "v_xor_b32 v53, 32, v52\n"
    "v_xor_b32 v54, 64, v52\n"
    "v_xor_b32 v55, 96, v52\n"
    "v_and_b32 v34, 0xc0, v251\n"
    "v_lshl_or_b32 v32, v2, 6, v32\n"
    "s_lshl_b32 s23, s21, 5\n"
    "v_lshl_or_b32 v32, v34, 1, v32\n"
    "v_and_b32 v58, 0x7bf, v36\n"
    "v_xor_b32 v32, s23, v32\n"
    "s_mul_i32 s23, s21, 0x1040\n"
    "v_add_u32 v56, 54144, v32\n"
    "s_and_b32 s23, s23, 0x2040\n"
    "v_xor_b32 v57, 32, v56\n"
    "v_add_u32 v59, s23, v58\n"
    "v_and_b32 v32, 0xf0, v251\n"
    "v_and_b32 v33, 32, v0\n"
    "v_and_b32 v34, 16, v0\n"
    "v_lshl_or_b32 v32, v33, 3, v32\n"
    "s_lshl_b32 s24, s21, 9\n"
    "s_and_b32 s23, s24, 0x400\n"
    "v_lshl_or_b32 v60, v34, 6, v32\n"
    "s_add_u32 s23, s23, s24\n"
    "v_lshrrev_b32 v34, 1, v34\n"
    "v_or_b32 v60, s23, v60\n"
    "v_or3_b32 v61, v32, v34, s24\n"
    "v_and_b32 v62, 0x3f803f80, v248\n"
    "v_and_b32 v63, 0x3f803f80, v249\n"
    "v_lshrrev_b32 v32, 5, v1\n"
    "s_mul_i32 s23, s21, 544\n"
    "v_mul_u32_u24 v12, 272, v32\n"
    "v_mad_u32_u24 v64, v32, s14, v3\n"
    "v_add3_u32 v12, v12, s23, 15\n"
    "s_not_b32 exec_lo, s16\n"
    "s_not_b32 exec_hi, s16\n"
    "v_bfrev_b32 v64, 1\n"
    "s_mov_b64 exec, -1\n"
    "v_add_u32 v13, v13, v12\n"
    "s_lshl1_add_u32 s23, s21, s78\n"
    "s_mul_i32 s23, s23, s14\n"
    "s_lshl1_add_u32 s22, s15, s23\n"
    "s_mov_b32 s26, 0xbfb8aa3b\n"
    "v_bfrev_b32 v35, 1\n"
    "s_waitcnt vmcnt(5)\n"
    "v_mul_f32 v32, 0x35800000, v44\n"
    "v_max3_f32 v33, |v40|, |v41|, |v42|\n"
    "v_max3_f32 v33, v33, |v43|, |v32|\n"
    "v_lshlrev_b32 v76, 16, v46\n"
    "v_and_b32 v77, 0xffff0000, v46\n"
    "v_max_f32_dpp v33, v33, v33 quad_perm:[1,0,3,2] row_mask:0xf bank_mask:0xf bound_ctrl:1\n"
    "v_lshlrev_b32_e64 v78, 16, v47\n"
    "v_and_b32 v79, 0xffff0000, v47\n"
    "v_max_f32_dpp v33, v33, v33 quad_perm:[2,3,0,1] row_mask:0xf bank_mask:0xf bound_ctrl:1\n"
    "v_bitop3_b32 v68, v76, s26, v35 bitop3:0x6c\n"
    "v_bitop3_b32 v69, v77, s26, v35 bitop3:0x6c\n"
    "v_max_f32_dpp v33, v33, v33 row_half_mirror row_mask:0xf bank_mask:0xf bound_ctrl:1\n"
    "v_bitop3_b32 v70, v78, s26, v35 bitop3:0x6c\n"
    "v_bitop3_b32 v71, v79, s26, v35 bitop3:0x6c\n"
    "v_max_f32_dpp v33, v33, v33 row_mirror row_mask:0xf bank_mask:0xf bound_ctrl:1\n"
    "v_rcp_f32_e64 v76, |v76|\n"
    "v_rcp_f32_e64 v77, |v77|\n"
    "v_rcp_f32_e64 v78, |v78|\n"
    "v_rcp_f32_e64 v79, |v79|\n"
    "v_lshrrev_b32 v33, 23, v33\n"
    "s_movk_i32 s23, 253\n"
    "v_med3_u32 v33, v33, 15, s23\n"
    "v_add_lshl_u32 v33, v33, -14, 23\n"
    "s_lshl_b32 s23, s21, 6\n"
    "v_pk_mul_f32 v[76:77], v[76:77], v[32:33] op_sel:[0,1] op_sel_hi:[1,1]\n"
    "v_pk_mul_f32 v[78:79], v[78:79], v[32:33] op_sel:[0,1] op_sel_hi:[1,1]\n"
    "v_or_b32 v34, s23, v2\n"
    "v_min_f32 v76, 0x46800000, v76\n"
    "v_min_f32 v77, 0x46800000, v77\n"
    "v_min_f32 v78, 0x46800000, v78\n"
    "v_min_f32 v79, 0x46800000, v79\n"
    "ds_write_b128 v34, v[76:79] offset:49152\n"
    "v_log_f32 v72, v76\n"
    "v_log_f32 v73, v77\n"
    "v_log_f32 v74, v78\n"
    "v_log_f32 v75, v79\n"
    "v_bitop3_b32 v80, v40, s26, v35 bitop3:0x6c\n"
    "v_bitop3_b32 v81, v41, s26, v35 bitop3:0x6c\n"
    "v_bitop3_b32 v82, v42, s26, v35 bitop3:0x6c\n"
    "v_bitop3_b32 v83, v43, s26, v35 bitop3:0x6c\n"
    "s_cmp_eq_u32 s21, 0\n"
    "v_add_f32 v72, 0xc1700000, v72\n"
    "v_add_f32 v73, 0xc1700000, v73\n"
    "v_add_f32 v74, 0xc1700000, v74\n"
    "v_add_f32 v75, 0xc1700000, v75\n"
    "v_lshlrev_b32 v84, 16, v48\n"
    "v_and_b32 v85, 0xffff0000, v48\n"
    "v_lshlrev_b32 v86, 16, v49\n"
    "v_and_b32 v87, 0xffff0000, v49\n"
    "s_cbranch_scc0 .Ldin_cg_r\n"
    "v_min_f32 v40, 0, v40\n"
    "v_min_f32 v41, 0, v41\n"
    "v_min_f32 v42, 0, v42\n"
    "v_min_f32 v43, 0, v43\n"
    "v_pk_add_f32 v[40:41], v[40:41], v[42:43]\n"
    "v_sub_u32 v34, 0x7f000000, v33\n"
    "v_add_f32 v32, v40, v41\n"
    "v_and_b32 v42, 12, v0\n"
    "v_lshlrev_b32 v42, 2, v42\n"
    "v_add_f32_dpp v32, v32, v32 quad_perm:[1,0,3,2] row_mask:0xf bank_mask:0xf bound_ctrl:1\n"
    "v_or_b32 v33, 23600, v33\n"
    "s_nop 0\n"
    "v_add_f32_dpp v32, v32, v32 quad_perm:[2,3,0,1] row_mask:0xf bank_mask:0xf bound_ctrl:1\n"
    "s_mov_b64 exec, 15\n"
    "v_add_f32 v32, v32, v44\n"
    "s_mov_b64 exec, 0xffff\n"
    "v_mul_f32_e64 v32, v32, v34\n"
    "ds_write_b64 v42, v[32:33] offset:49408\n"
    "s_mov_b64 exec, -1\n"
    ".Ldin_cg_r:\n"
    "s_waitcnt vmcnt(0) lgkmcnt(0)\n"
    "buffer_load_dwordx4 v[96:99], v64, s[44:47], s22 offen nt\n"
    "s_add_u32 s22, s22, s15\n"
    "buffer_load_dwordx4 v[100:103], v64, s[44:47], s22 offen nt\n"
    "s_add_u32 s22, s22, s15\n"
    "buffer_load_dwordx4 v[104:107], v64, s[44:47], s22 offen nt\n"
    "s_add_u32 s22, s22, s15\n"
    "buffer_load_dwordx4 v[108:111], v64, s[44:47], s22 offen nt\n"
    "s_add_u32 s22, s22, s15\n"
    "buffer_load_dwordx4 v[112:115], v64, s[44:47], s22 offen nt\n"
    "s_add_u32 s22, s22, s15\n"
    "buffer_load_dwordx4 v[116:119], v64, s[44:47], s22 offen nt\n"
    "s_barrier\n"
    "ds_read_b64_tr_b16 v[120:121], v52 offset:0\n"
    "ds_read_b64_tr_b16 v[122:123], v52 offset:16384\n"
    "ds_read_b64_tr_b16 v[124:125], v52 offset:24576\n"
    "ds_read_b64_tr_b16 v[126:127], v53 offset:0\n"
    "ds_read_b64_tr_b16 v[128:129], v53 offset:16384\n"
    "ds_read_b64_tr_b16 v[130:131], v53 offset:24576\n"
    "ds_read_b64_tr_b16 v[132:133], v54 offset:0\n"
    "ds_read_b64_tr_b16 v[134:135], v54 offset:16384\n"
    "ds_read_b64_tr_b16 v[136:137], v54 offset:24576\n"
    "ds_read_b64_tr_b16 v[138:139], v55 offset:0\n"
    "ds_read_b64_tr_b16 v[140:141], v55 offset:16384\n"
    "ds_read_b64_tr_b16 v[142:143], v55 offset:24576\n"
    "s_waitcnt lgkmcnt(9)\n"
    "v_mfma_f32_4x4x4_16b_bf16 v[192:195], v[62:63], v[120:121], 0\n"
    "v_mfma_f32_4x4x4_16b_bf16 v[196:199], v[62:63], v[124:125], 0\n"
    "s_nop 0\n"
    "v_mfma_f32_4x4x4_16b_bf16 v[192:195], v[62:63], v[122:123], v[192:195]\n"
    "ds_read_b64_tr_b16 v[144:145], v56 offset:8192\n"
    "ds_read_b64_tr_b16 v[146:147], v57 offset:8704\n"
    "ds_read_b64_tr_b16 v[148:149], v56 offset:12288\n"
    "ds_read_b64_tr_b16 v[150:151], v57 offset:12800\n"
    "s_waitcnt lgkmcnt(10)\n"
    "v_mfma_f32_4x4x4_16b_bf16 v[200:203], v[62:63], v[126:127], 0\n"
    "v_mfma_f32_4x4x4_16b_bf16 v[204:207], v[62:63], v[130:131], 0\n"
    "s_nop 0\n"
    "v_mfma_f32_4x4x4_16b_bf16 v[200:203], v[62:63], v[128:129], v[200:203]\n"
    "ds_read_b128 v[160:163], v58 offset:24576\n"
    "ds_read_b128 v[164:167], v58 offset:24640\n"
    "s_waitcnt lgkmcnt(9)\n"
    "v_mfma_f32_4x4x4_16b_bf16 v[208:211], v[62:63], v[132:133], 0\n"
    "v_mfma_f32_4x4x4_16b_bf16 v[212:215], v[62:63], v[136:137], 0\n"
    "s_nop 0\n"
    "v_mfma_f32_4x4x4_16b_bf16 v[208:211], v[62:63], v[134:135], v[208:211]\n"
    "ds_read_b64_tr_b16 v[152:153], v56 offset:16384\n"
    "ds_read_b64_tr_b16 v[154:155], v57 offset:16896\n"
    "ds_read_b64_tr_b16 v[156:157], v56 offset:20480\n"
    "ds_read_b64_tr_b16 v[158:159], v57 offset:20992\n"
    "s_waitcnt lgkmcnt(10)\n"
    "v_mfma_f32_4x4x4_16b_bf16 v[216:219], v[62:63], v[138:139], 0\n"
    "v_mfma_f32_4x4x4_16b_bf16 v[220:223], v[62:63], v[142:143], 0\n"
    "s_nop 0\n"
    "v_mfma_f32_4x4x4_16b_bf16 v[216:219], v[62:63], v[140:141], v[216:219]\n"
    "s_waitcnt lgkmcnt(4)\n"
    "v_mfma_f32_16x16x32_bf16 v[224:227], v[144:147], v[160:163], v[84:87]\n"
    "v_mfma_f32_16x16x32_bf16 v[224:227], v[148:151], v[164:167], v[224:227]\n"
    "s_waitcnt lgkmcnt(0)\n"
    "v_mfma_f32_16x16x32_bf16 v[228:231], v[152:155], v[160:163], 0\n"
    "v_mfma_f32_16x16x32_bf16 v[228:231], v[156:159], v[164:167], v[228:231]\n"
    "v_pk_mul_f32 v[192:193], v[192:193], v[80:81] op_sel_hi:[1,0]\n"
    "v_pk_mul_f32 v[194:195], v[194:195], v[80:81] op_sel_hi:[1,0]\n"
    "ds_write_b128 v60, v[192:195] offset:0\n"
    "v_pk_mul_f32 v[196:197], v[196:197], v[80:81] op_sel_hi:[1,0]\n"
    "v_pk_mul_f32 v[198:199], v[198:199], v[80:81] op_sel_hi:[1,0]\n"
    "v_cvt_pk_bf16_f32 v196, v196, v197\n"
    "v_cvt_pk_bf16_f32 v197, v198, v199\n"
    "ds_write_b64 v61, v[196:197] offset:16384\n"
    "v_pk_mul_f32 v[200:201], v[200:201], v[80:81] op_sel:[0,1] op_sel_hi:[1,1]\n"
    "v_pk_mul_f32 v[202:203], v[202:203], v[80:81] op_sel:[0,1] op_sel_hi:[1,1]\n"
    "ds_write_b128 v60, v[200:203] offset:4096\n"
    "v_pk_mul_f32 v[204:205], v[204:205], v[80:81] op_sel:[0,1] op_sel_hi:[1,1]\n"
    "v_pk_mul_f32 v[206:207], v[206:207], v[80:81] op_sel:[0,1] op_sel_hi:[1,1]\n"
    "v_cvt_pk_bf16_f32 v204, v204, v205\n"
    "v_cvt_pk_bf16_f32 v205, v206, v207\n"
    "ds_write_b64 v61, v[204:205] offset:18432\n"
    "v_pk_mul_f32 v[208:209], v[208:209], v[82:83] op_sel_hi:[1,0]\n"
    "v_pk_mul_f32 v[210:211], v[210:211], v[82:83] op_sel_hi:[1,0]\n"
    "ds_write_b128 v60, v[208:211] offset:8192\n"
    "v_pk_mul_f32 v[212:213], v[212:213], v[82:83] op_sel_hi:[1,0]\n"
    "v_pk_mul_f32 v[214:215], v[214:215], v[82:83] op_sel_hi:[1,0]\n"
    "v_cvt_pk_bf16_f32 v212, v212, v213\n"
    "v_cvt_pk_bf16_f32 v213, v214, v215\n"
    "ds_write_b64 v61, v[212:213] offset:20480\n"
    "v_pk_mul_f32 v[216:217], v[216:217], v[82:83] op_sel:[0,1] op_sel_hi:[1,1]\n"
    "v_pk_mul_f32 v[218:219], v[218:219], v[82:83] op_sel:[0,1] op_sel_hi:[1,1]\n"
    "ds_write_b128 v60, v[216:219] offset:12288\n"
    "v_pk_mul_f32 v[220:221], v[220:221], v[82:83] op_sel:[0,1] op_sel_hi:[1,1]\n"
    "v_pk_mul_f32 v[222:223], v[222:223], v[82:83] op_sel:[0,1] op_sel_hi:[1,1]\n"
    "v_cvt_pk_bf16_f32 v220, v220, v221\n"
    "v_cvt_pk_bf16_f32 v221, v222, v223\n"
    "ds_write_b64 v61, v[220:221] offset:22528\n"
    "v_pk_add_f32 v[224:225], v[224:225], v[228:229] neg_lo:[0,1] neg_hi:[0,1]\n"
    "v_pk_add_f32 v[226:227], v[226:227], v[230:231] neg_lo:[0,1] neg_hi:[0,1]\n"
    "v_pk_fma_f32 v[224:225], v[224:225], v[68:69], v[72:73]\n"
    "v_pk_fma_f32 v[226:227], v[226:227], v[70:71], v[74:75]\n"
    "ds_write_b128 v59, v[224:227] offset:32768\n"
    "s_waitcnt lgkmcnt(0)\n"
    "s_barrier\n"
    "s_setprio 0\n"
    "s_mul_i32 s23, s20, 272\n"
    "v_add_u32 v32, s23, v244\n"
    "ds_read_u8 v16, v32 offset:112\n"
    "ds_read_u8 v18, v32 offset:113\n"
    "ds_read_u8 v20, v32 offset:128\n"
    "ds_read_u8 v22, v32 offset:129\n"
    "ds_read_u8 v24, v32 offset:144\n"
    "ds_read_u8 v26, v32 offset:145\n"
    "ds_read_u8 v28, v32 offset:160\n"
    "ds_read_u8 v30, v32 offset:161\n"
    "ds_read_b32 v33, v246 offset:128\n"
    "ds_read_b128 v[168:171], v58 offset:26624\n"
    "ds_read_b128 v[172:175], v58 offset:26688\n"
    "ds_read_b128 v[176:179], v58 offset:28672\n"
    "ds_read_b128 v[180:183], v58 offset:28736\n"
    "ds_read_b128 v[184:187], v58 offset:30720\n"
    "ds_read_b128 v[188:191], v58 offset:30784\n"
    "s_add_u32 m0, s68, 0\n"
    "s_waitcnt lgkmcnt(13)\n"
    "buffer_load_dwordx4 v[16:17], s[40:43], s67 idxen offen offset:0 nt lds\n"
    "buffer_load_dwordx4 v[18:19], s[40:43], s67 idxen offen offset:1024 nt lds\n"
    "s_add_u32 m0, s68, 2048\n"
    "s_waitcnt lgkmcnt(11)\n"
    "s_nop 0\n"
    "buffer_load_dwordx4 v[20:21], s[40:43], s67 idxen offen offset:0 nt lds\n"
    "buffer_load_dwordx4 v[22:23], s[40:43], s67 idxen offen offset:1024 nt lds\n"
    "ds_write_b64 v13, v[14:15] offset:54112\n"
    "ds_write_b64 v13, v[14:15] offset:56288\n"
    "ds_write_b64 v13, v[14:15] offset:58464\n"
    "ds_write_b64 v13, v[14:15] offset:60640\n"
    "ds_write_b64 v13, v[14:15] offset:62816\n"
    "ds_write_b64 v13, v[14:15] offset:64992\n"
    "s_waitcnt lgkmcnt(6)\n"
    "v_readfirstlane_b32 s60, v33\n"
    "v_mfma_f32_16x16x32_bf16 v[120:123], v[144:147], v[168:171], v[84:87]\n"
    "v_mfma_f32_16x16x32_bf16 v[120:123], v[148:151], v[172:175], v[120:123]\n"
    "v_mfma_f32_16x16x32_bf16 v[124:127], v[152:155], v[168:171], 0\n"
    "v_mfma_f32_16x16x32_bf16 v[124:127], v[156:159], v[172:175], v[124:127]\n"
    "v_mfma_f32_16x16x32_bf16 v[128:131], v[144:147], v[176:179], v[84:87]\n"
    "v_mfma_f32_16x16x32_bf16 v[128:131], v[148:151], v[180:183], v[128:131]\n"
    "v_mfma_f32_16x16x32_bf16 v[132:135], v[152:155], v[176:179], 0\n"
    "v_mfma_f32_16x16x32_bf16 v[132:135], v[156:159], v[180:183], v[132:135]\n"
    "v_mfma_f32_16x16x32_bf16 v[136:139], v[144:147], v[184:187], v[84:87]\n"
    "v_mfma_f32_16x16x32_bf16 v[136:139], v[148:151], v[188:191], v[136:139]\n"
    "v_mfma_f32_16x16x32_bf16 v[140:143], v[152:155], v[184:187], 0\n"
    "v_mfma_f32_16x16x32_bf16 v[140:143], v[156:159], v[188:191], v[140:143]\n"
    "s_and_b32 s23, s14, 14\n"
    "s_waitcnt vmcnt(4)\n"
    "s_cbranch_scc1 .Ldin_trimy\n"
    ".Ldin_trimy_r:\n"
    "v_pk_min_u16 v192, v96, 1 op_sel_hi:[1,0]\n"
    "v_pk_min_u16 v193, v97, 1 op_sel_hi:[1,0]\n"
    "v_pk_min_u16 v194, v98, 1 op_sel_hi:[1,0]\n"
    "v_pk_min_u16 v195, v99, 1 op_sel_hi:[1,0]\n"
    "v_add3_u32 v44, v192, v193, v194\n"
    "v_add_u32 v44, v44, v195\n"
    "v_dot2_u32_u16 v40, v44, s31, 0\n"
    "v_pk_min_u16 v196, v100, 1 op_sel_hi:[1,0]\n"
    "v_pk_min_u16 v197, v101, 1 op_sel_hi:[1,0]\n"
    "v_pk_min_u16 v198, v102, 1 op_sel_hi:[1,0]\n"
    "v_pk_min_u16 v199, v103, 1 op_sel_hi:[1,0]\n"
    "v_add3_u32 v44, v196, v197, v198\n"
    "v_add_u32_e64 v44, v44, v199\n"
    "v_dot2_u32_u16 v40, v44, s30, v40\n"
    "v_pk_min_u16 v200, v104, 1 op_sel_hi:[1,0]\n"
    "v_pk_min_u16 v201, v105, 1 op_sel_hi:[1,0]\n"
    "v_pk_min_u16 v202, v106, 1 op_sel_hi:[1,0]\n"
    "v_pk_min_u16 v203, v107, 1 op_sel_hi:[1,0]\n"
    "v_add3_u32 v42, v200, v201, v202\n"
    "v_add_u32 v42, v42, v203\n"
    "v_lshl_add_u32 v42, v42, 16, v42\n"
    "v_and_b32 v42, 0xffff0000, v42\n"
    "v_or_b32 v40, v40, v42\n"
    "v_pk_min_u16 v204, v108, 1 op_sel_hi:[1,0]\n"
    "v_pk_min_u16 v205, v109, 1 op_sel_hi:[1,0]\n"
    "v_pk_min_u16 v206, v110, 1 op_sel_hi:[1,0]\n"
    "v_pk_min_u16 v207, v111, 1 op_sel_hi:[1,0]\n"
    "v_add3_u32 v45, v204, v205, v206\n"
    "v_add_u32_e64 v45, v45, v207\n"
    "v_dot2_u32_u16 v41, v45, s31, 0\n"
    "v_pk_min_u16 v208, v112, 1 op_sel_hi:[1,0]\n"
    "v_pk_min_u16 v209, v113, 1 op_sel_hi:[1,0]\n"
    "v_pk_min_u16 v210, v114, 1 op_sel_hi:[1,0]\n"
    "v_pk_min_u16 v211, v115, 1 op_sel_hi:[1,0]\n"
    "v_add3_u32 v45, v208, v209, v210\n"
    "v_add_u32_e64 v45, v45, v211\n"
    "v_dot2_u32_u16 v41, v45, s30, v41\n"
    "v_pk_min_u16 v212, v116, 1 op_sel_hi:[1,0]\n"
    "v_pk_min_u16 v213, v117, 1 op_sel_hi:[1,0]\n"
    "v_pk_min_u16 v214, v118, 1 op_sel_hi:[1,0]\n"
    "v_pk_min_u16 v215, v119, 1 op_sel_hi:[1,0]\n"
    "v_add3_u32 v43, v212, v213, v214\n"
    "v_add_u32 v43, v43, v215\n"
    "v_lshl_add_u32 v43, v43, 16, v43\n"
    "v_and_b32 v43, 0xffff0000, v43\n"
    "v_or_b32 v41, v41, v43\n"
    "v_pk_add_f32 v[120:121], v[120:121], v[124:125] neg_lo:[0,1] neg_hi:[0,1]\n"
    "v_pk_add_f32 v[122:123], v[122:123], v[126:127] neg_lo:[0,1] neg_hi:[0,1]\n"
    "v_pk_fma_f32 v[120:121], v[120:121], v[68:69], v[72:73]\n"
    "v_pk_fma_f32 v[122:123], v[122:123], v[70:71], v[74:75]\n"
    "ds_write_b128 v59, v[120:123] offset:34816\n"
    "v_pk_add_f32 v[128:129], v[128:129], v[132:133] neg_lo:[0,1] neg_hi:[0,1]\n"
    "v_pk_add_f32 v[130:131], v[130:131], v[134:135] neg_lo:[0,1] neg_hi:[0,1]\n"
    "v_pk_fma_f32 v[128:129], v[128:129], v[68:69], v[72:73]\n"
    "v_pk_fma_f32 v[130:131], v[130:131], v[70:71], v[74:75]\n"
    "ds_write_b128 v59, v[128:131] offset:36864\n"
    "v_pk_add_f32 v[136:137], v[136:137], v[140:141] neg_lo:[0,1] neg_hi:[0,1]\n"
    "v_pk_add_f32 v[138:139], v[138:139], v[142:143] neg_lo:[0,1] neg_hi:[0,1]\n"
    "v_pk_fma_f32 v[136:137], v[136:137], v[68:69], v[72:73]\n"
    "v_pk_fma_f32 v[138:139], v[138:139], v[70:71], v[74:75]\n"
    "ds_write_b128 v59, v[136:139] offset:38912\n"
    "v_sub_u32 v44, 0x10101, v40\n"
    "v_sub_u32 v45, 0x10101, v41\n"
    "v_add_u32_dpp v42, v40, v40 row_shr:1 row_mask:0xf bank_mask:0xf bound_ctrl:1\n"
    "v_add_u32_dpp v43, v41, v41 row_shr:1 row_mask:0xf bank_mask:0xf bound_ctrl:1\n"
    "v_pk_mad_u16 v96, v192, s28, v96\n"
    "v_add_u32_dpp v42, v42, v42 row_shr:2 row_mask:0xf bank_mask:0xf bound_ctrl:1\n"
    "v_add_u32_dpp v43, v43, v43 row_shr:2 row_mask:0xf bank_mask:0xf bound_ctrl:1\n"
    "v_pk_mad_u16 v97, v193, s28, v97\n"
    "v_add_u32_dpp v42, v42, v42 row_shr:4 row_mask:0xf bank_mask:0xf bound_ctrl:1\n"
    "v_add_u32_dpp v43, v43, v43 row_shr:4 row_mask:0xf bank_mask:0xf bound_ctrl:1\n"
    "v_pk_mad_u16 v98, v194, s28, v98\n"
    "v_add_u32_dpp v42, v42, v42 row_shr:8 row_mask:0xf bank_mask:0xf bound_ctrl:1\n"
    "v_add_u32_dpp v43, v43, v43 row_shr:8 row_mask:0xf bank_mask:0xf bound_ctrl:1\n"
    "v_pk_mad_u16 v99, v195, s28, v99\n"
    "v_add_u32_dpp v42, v42, v42 row_bcast:15 row_mask:0xa bank_mask:0xf\n"
    "v_add_u32_dpp v43, v43, v43 row_bcast:15 row_mask:0xa bank_mask:0xf\n"
    "v_or3_b32 v46, v96, v97, 0\n"
    "v_add_u32 v44, v44, v42\n"
    "v_add_u32 v45, v45, v43\n"
    "v_lshrrev_b32 v32, 3, v1\n"
    "v_and_b32_e64 v32, 4, v32\n"
    "v_lshl_add_u32 v32, s21, 3, v32\n"
    "s_brev_b32 exec_lo, 1\n"
    "s_brev_b32 exec_hi, 1\n"
    "ds_write_b32 v32, v42 offset:49472\n"
    "ds_write_b32 v32, v43 offset:49504\n"
    "s_mov_b64 exec, -1\n"
    "s_add_u32 m0, s68, 4096\n"
    "s_nop 0\n"
    "buffer_load_dwordx4 v[24:25], s[40:43], s67 idxen offen offset:0 nt lds\n"
    "buffer_load_dwordx4 v[26:27], s[40:43], s67 idxen offen offset:1024 nt lds\n"
    "s_add_u32 m0, s68, 6144\n"
    "s_nop 0\n"
    "buffer_load_dwordx4 v[28:29], s[40:43], s67 idxen offen offset:0 nt lds\n"
    "buffer_load_dwordx4 v[30:31], s[40:43], s67 idxen offen offset:1024 nt lds\n"
    "s_mov_b32 s26, 0xc040c00\n"
    "v_perm_b32 v47, v42, v44, s26\n"
    "v_mad_u32_u16 v48, v192, v47, v12\n"
    "v_mad_u32_u16 v49, v192, v47, v12 op_sel:[1,1,0,0]\n"
    "v_pk_mad_u16 v47, v192, s29, v47\n"
    "ds_write_b8 v48, v8 offset:54112\n"
    "ds_write_b8_d16_hi v49, v8 offset:54112\n"
    "v_mad_u32_u16 v50, v193, v47, v12\n"
    "v_mad_u32_u16 v51, v193, v47, v12 op_sel:[1,1,0,0]\n"
    "v_pk_mad_u16 v47, v193, s29, v47\n"
    "ds_write_b8 v50, v9 offset:54112\n"
    "ds_write_b8_d16_hi v51, v9 offset:54112\n"
    "v_mad_u32_u16 v48, v194, v47, v12\n"
    "v_mad_u32_u16 v49, v194, v47, v12 op_sel:[1,1,0,0]\n"
    "v_pk_mad_u16 v47, v194, s29, v47\n"
    "ds_write_b8 v48, v10 offset:54112\n"
    "ds_write_b8_d16_hi v49, v10 offset:54112\n"
    "v_mad_u32_u16 v50, v195, v47, v12\n"
    "v_mad_u32_u16 v51, v195, v47, v12 op_sel:[1,1,0,0]\n"
    "ds_write_b8 v50, v11 offset:54112\n"
    "ds_write_b8_d16_hi v51, v11 offset:54112\n"
    "v_or3_b32 v46, v98, v99, v46\n"
    "v_pk_mad_u16 v100, v196, s28, v100\n"
    "v_pk_mad_u16 v101, v197, s28, v101\n"
    "v_pk_mad_u16 v102, v198, s28, v102\n"
    "v_pk_mad_u16 v103, v199, s28, v103\n"
    "v_or3_b32 v46, v100, v101, v46\n"
    "s_setprio 2\n"
    "s_nop 0\n"
    "s_mov_b32 s26, 0xc050c01\n"
    "v_perm_b32 v47, v42, v44, s26\n"
    "v_mad_u32_u16 v48, v196, v47, v12\n"
    "v_mad_u32_u16 v49, v196, v47, v12 op_sel:[1,1,0,0]\n"
    "v_pk_mad_u16 v47, v196, s29, v47\n"
    "ds_write_b8 v48, v8 offset:56288\n"
    "ds_write_b8_d16_hi v49, v8 offset:56288\n"
    "v_mad_u32_u16 v50, v197, v47, v12\n"
    "v_mad_u32_u16 v51, v197, v47, v12 op_sel:[1,1,0,0]\n"
    "v_pk_mad_u16 v47, v197, s29, v47\n"
    "ds_write_b8 v50, v9 offset:56288\n"
    "ds_write_b8_d16_hi v51, v9 offset:56288\n"
    "v_mad_u32_u16 v48, v198, v47, v12\n"
    "v_mad_u32_u16 v49, v198, v47, v12 op_sel:[1,1,0,0]\n"
    "v_pk_mad_u16 v47, v198, s29, v47\n"
    "ds_write_b8 v48, v10 offset:56288\n"
    "ds_write_b8_d16_hi v49, v10 offset:56288\n"
    "v_mad_u32_u16 v50, v199, v47, v12\n"
    "v_mad_u32_u16 v51, v199, v47, v12 op_sel:[1,1,0,0]\n"
    "ds_write_b8 v50, v11 offset:56288\n"
    "ds_write_b8_d16_hi v51, v11 offset:56288\n"
    "v_or3_b32 v46, v102, v103, v46\n"
    "v_pk_mad_u16 v104, v200, s28, v104\n"
    "v_pk_mad_u16 v105, v201, s28, v105\n"
    "v_pk_mad_u16 v106, v202, s28, v106\n"
    "v_pk_mad_u16 v107, v203, s28, v107\n"
    "v_or3_b32 v46, v104, v105, v46\n"
    "s_mov_b32 s26, 0xc060c02\n"
    "v_perm_b32 v47, v42, v44, s26\n"
    "v_mad_u32_u16 v48, v200, v47, v12\n"
    "v_mad_u32_u16 v49, v200, v47, v12 op_sel:[1,1,0,0]\n"
    "v_pk_mad_u16 v47, v200, s29, v47\n"
    "ds_write_b8 v48, v8 offset:58464\n"
    "ds_write_b8_d16_hi v49, v8 offset:58464\n"
    "v_mad_u32_u16 v50, v201, v47, v12\n"
    "v_mad_u32_u16 v51, v201, v47, v12 op_sel:[1,1,0,0]\n"
    "v_pk_mad_u16 v47, v201, s29, v47\n"
    "ds_write_b8 v50, v9 offset:58464\n"
    "ds_write_b8_d16_hi v51, v9 offset:58464\n"
    "v_mad_u32_u16 v48, v202, v47, v12\n"
    "v_mad_u32_u16 v49, v202, v47, v12 op_sel:[1,1,0,0]\n"
    "v_pk_mad_u16 v47, v202, s29, v47\n"
    "ds_write_b8 v48, v10 offset:58464\n"
    "ds_write_b8_d16_hi v49, v10 offset:58464\n"
    "v_mad_u32_u16 v50, v203, v47, v12\n"
    "v_mad_u32_u16 v51, v203, v47, v12 op_sel:[1,1,0,0]\n"
    "ds_write_b8 v50, v11 offset:58464\n"
    "ds_write_b8_d16_hi v51, v11 offset:58464\n"
    "v_or3_b32 v46, v106, v107, v46\n"
    "v_pk_mad_u16 v108, v204, s28, v108\n"
    "v_pk_mad_u16 v109, v205, s28, v109\n"
    "v_pk_mad_u16 v110, v206, s28, v110\n"
    "v_pk_mad_u16 v111, v207, s28, v111\n"
    "v_or3_b32 v46, v108, v109, v46\n"
    "s_mov_b32 s26, 0xc040c00\n"
    "v_perm_b32 v47, v43, v45, s26\n"
    "v_mad_u32_u16 v48, v204, v47, v12\n"
    "v_mad_u32_u16 v49, v204, v47, v12 op_sel:[1,1,0,0]\n"
    "v_pk_mad_u16 v47, v204, s29, v47\n"
    "ds_write_b8 v48, v8 offset:60640\n"
    "ds_write_b8_d16_hi v49, v8 offset:60640\n"
    "v_mad_u32_u16 v50, v205, v47, v12\n"
    "v_mad_u32_u16 v51, v205, v47, v12 op_sel:[1,1,0,0]\n"
    "v_pk_mad_u16 v47, v205, s29, v47\n"
    "ds_write_b8 v50, v9 offset:60640\n"
    "ds_write_b8_d16_hi v51, v9 offset:60640\n"
    "v_mad_u32_u16 v48, v206, v47, v12\n"
    "v_mad_u32_u16 v49, v206, v47, v12 op_sel:[1,1,0,0]\n"
    "v_pk_mad_u16 v47, v206, s29, v47\n"
    "ds_write_b8 v48, v10 offset:60640\n"
    "ds_write_b8_d16_hi v49, v10 offset:60640\n"
    "v_mad_u32_u16 v50, v207, v47, v12\n"
    "v_mad_u32_u16 v51, v207, v47, v12 op_sel:[1,1,0,0]\n"
    "ds_write_b8 v50, v11 offset:60640\n"
    "ds_write_b8_d16_hi v51, v11 offset:60640\n"
    "v_or3_b32 v46, v110, v111, v46\n"
    "v_pk_mad_u16 v112, v208, s28, v112\n"
    "v_pk_mad_u16 v113, v209, s28, v113\n"
    "v_pk_mad_u16 v114, v210, s28, v114\n"
    "v_pk_mad_u16 v115, v211, s28, v115\n"
    "v_or3_b32 v46, v112, v113, v46\n"
    "s_mov_b32 s26, 0xc050c01\n"
    "v_perm_b32 v47, v43, v45, s26\n"
    "v_mad_u32_u16 v48, v208, v47, v12\n"
    "v_mad_u32_u16 v49, v208, v47, v12 op_sel:[1,1,0,0]\n"
    "v_pk_mad_u16 v47, v208, s29, v47\n"
    "ds_write_b8 v48, v8 offset:62816\n"
    "ds_write_b8_d16_hi v49, v8 offset:62816\n"
    "v_mad_u32_u16 v50, v209, v47, v12\n"
    "v_mad_u32_u16 v51, v209, v47, v12 op_sel:[1,1,0,0]\n"
    "v_pk_mad_u16 v47, v209, s29, v47\n"
    "ds_write_b8 v50, v9 offset:62816\n"
    "ds_write_b8_d16_hi v51, v9 offset:62816\n"
    "v_mad_u32_u16 v48, v210, v47, v12\n"
    "v_mad_u32_u16 v49, v210, v47, v12 op_sel:[1,1,0,0]\n"
    "v_pk_mad_u16 v47, v210, s29, v47\n"
    "ds_write_b8 v48, v10 offset:62816\n"
    "ds_write_b8_d16_hi v49, v10 offset:62816\n"
    "v_mad_u32_u16 v50, v211, v47, v12\n"
    "v_mad_u32_u16 v51, v211, v47, v12 op_sel:[1,1,0,0]\n"
    "ds_write_b8 v50, v11 offset:62816\n"
    "ds_write_b8_d16_hi v51, v11 offset:62816\n"
    "v_or3_b32 v46, v114, v115, v46\n"
    "v_pk_mad_u16 v116, v212, s28, v116\n"
    "v_pk_mad_u16 v117, v213, s28, v117\n"
    "v_pk_mad_u16 v118, v214, s28, v118\n"
    "v_pk_mad_u16 v119, v215, s28, v119\n"
    "v_or3_b32 v46, v116, v117, v46\n"
    "s_mov_b32 s26, 0xc060c02\n"
    "v_perm_b32 v47, v43, v45, s26\n"
    "v_mad_u32_u16 v48, v212, v47, v12\n"
    "v_mad_u32_u16 v49, v212, v47, v12 op_sel:[1,1,0,0]\n"
    "v_pk_mad_u16 v47, v212, s29, v47\n"
    "ds_write_b8 v48, v8 offset:64992\n"
    "ds_write_b8_d16_hi v49, v8 offset:64992\n"
    "v_mad_u32_u16 v50, v213, v47, v12\n"
    "v_mad_u32_u16 v51, v213, v47, v12 op_sel:[1,1,0,0]\n"
    "v_pk_mad_u16 v47, v213, s29, v47\n"
    "ds_write_b8 v50, v9 offset:64992\n"
    "ds_write_b8_d16_hi v51, v9 offset:64992\n"
    "v_mad_u32_u16 v48, v214, v47, v12\n"
    "v_mad_u32_u16 v49, v214, v47, v12 op_sel:[1,1,0,0]\n"
    "v_pk_mad_u16 v47, v214, s29, v47\n"
    "ds_write_b8 v48, v10 offset:64992\n"
    "ds_write_b8_d16_hi v49, v10 offset:64992\n"
    "v_mad_u32_u16 v50, v215, v47, v12\n"
    "v_mad_u32_u16 v51, v215, v47, v12 op_sel:[1,1,0,0]\n"
    "ds_write_b8 v50, v11 offset:64992\n"
    "ds_write_b8_d16_hi v51, v11 offset:64992\n"
    "v_or3_b32 v46, v118, v119, v46\n"
    "v_cmp_ne_u32 vcc, 0, v46\n"
    "v_mov_b32 v32, 0\n"
    "s_cmp_lg_u64 vcc, 0\n"
    "s_cselect_b32 s23, 0x101, 1\n"
    "s_mov_b64 exec, 1\n"
    "v_mov_b32 v33, s23\n"
    "ds_add_u32 v32, v33 offset:49416\n"
    "s_mov_b64 exec, -1\n"
    "s_cmp_lg_u32 s21, 3\n"
    "s_cbranch_scc1 .Ldin_gate\n"
    "s_branch .Ldin_ps\n"
    ".p2align 12\n"
    ".Ldin_stub_ps:\n"
    "s_setpc_b64 s[34:35]\n"
    ".Ldin_ps:\n"
    "s_setprio 2\n"
    "s_branch .Ldin_psr\n"
    ".Ldin_psw:\n"
    "s_sleep 1\n"
    ".Ldin_psr:\n"
    "ds_read_b32 v33, v32 offset:49416\n"
    "s_waitcnt lgkmcnt(0)\n"
    "v_readfirstlane_b32 s24, v33\n"
    "s_bitcmp1_b32 s24, 2\n"
    "s_cbranch_scc0 .Ldin_psw\n"
    "v_lshrrev_b32 v40, 3, v1\n"
    "v_and_b32 v41, 7, v0\n"
    "v_lshl_add_u32 v60, v41, 2, v40\n"
    "s_mov_b32 s23, 86912\n"
    "s_mov_b32 exec_lo, 0xff000000\n"
    "v_add_u32 v60, 29, v60\n"
    "s_mov_b32 exec_lo, -1\n"
    "v_lshl_add_u32 v62, v40, 8, s23\n"
    "v_lshl_add_u32 v55, v41, 8, s23\n"
    "ds_read_u8 v44, v60 offset:49472\n"
    "ds_read2_b32 v[46:47], v62 offset0:32 offset1:40\n"
    "ds_read2_b32 v[48:49], v55 offset0:32 offset1:40\n"
    "s_cmp_gt_u32 s24, 255\n"
    "s_cselect_b32 s24, 2.0, 0\n"
    "s_add_u32 s24, s24, 0x801170\n"
    "v_mul_u32_u24 v52, 0x80110, v1\n"
    "v_bfe_u32 v60, v0, 1, 2\n"
    "v_mul_lo_u32 v62, v40, s79\n"
    "v_lshl_add_u32 v52, v60, 16, v52\n"
    "s_lshl1_add_u32 s23, s79, s78\n"
    "v_add_u32 v52, s24, v52\n"
    "v_add3_u32 v62, v62, v41, s23\n"
    "v_and_b32 v42, 56, v0\n"
    "v_cmp_gt_u32 vcc, s74, v62\n"
    "v_lshlrev_b32 v43, 3, v41\n"
    "s_waitcnt lgkmcnt(2)\n"
    "v_add_u32 v44, 15, v44\n"
    "v_bfrev_b32 v60, 1\n"
    "v_lshrrev_b32 v44, 4, v44\n"
    "v_cndmask_b32 v52, v60, v52, vcc\n"
    "v_max_u32 v44, 6, v44\n"
    "v_lshl_or_b32 v52, v44, 25, v52\n"
    "v_add_u32_e64 v44, -5, v44\n"
    "v_cndmask_b32 v44, 0, v44, vcc\n"
    "v_lshl_or_b32 v53, v44, 3, v41\n"
    "s_waitcnt lgkmcnt(0)\n"
    "s_mov_b32 s25, 0x64320000\n"
    "v_bfe_u32 v46, v46, 25, 5\n"
    "v_bfe_u32 v47, v47, 25, 5\n"
    "v_mov_b32_dpp v54, v53 row_half_mirror row_mask:0xf bank_mask:0xf bound_ctrl:1\n"
    "v_sub_u32_dpp v64, v53, v53 quad_perm:[1,0,3,2] row_mask:0xf bank_mask:0xf bound_ctrl:1\n"
    "v_sub_u32_dpp v65, v53, v53 quad_perm:[2,3,0,1] row_mask:0xf bank_mask:0xf bound_ctrl:1\n"
    "v_sub_u32_dpp v66, v53, v53 quad_perm:[3,2,1,0] row_mask:0xf bank_mask:0xf bound_ctrl:1\n"
    "v_sub_u32 v67, v54, v53\n"
    "v_sub_u32_dpp v68, v54, v53 quad_perm:[1,0,3,2] row_mask:0xf bank_mask:0xf bound_ctrl:1\n"
    "v_sub_u32_dpp v69, v54, v53 quad_perm:[2,3,0,1] row_mask:0xf bank_mask:0xf bound_ctrl:1\n"
    "v_sub_u32_dpp v70, v54, v53 quad_perm:[3,2,1,0] row_mask:0xf bank_mask:0xf bound_ctrl:1\n"
    "v_lshrrev_b32 v56, 31, v64\n"
    "v_alignbit_b32 v56, v56, v65, 31\n"
    "v_alignbit_b32 v56, v56, v66, 31\n"
    "v_alignbit_b32 v56, v56, v67, 31\n"
    "v_alignbit_b32 v56, v56, v68, 31\n"
    "v_alignbit_b32 v56, v56, v69, 31\n"
    "v_alignbit_b32 v56, v56, v70, 31\n"
    "v_bcnt_u32_b32 v56, v56, -7\n"
    "v_mul_i32_i24 v56, -4, v56\n"
    "v_lshlrev_b32 v44, v56, v44\n"
    "v_lshl_add_u32 v56, v42, 2, v56\n"
    "ds_permute_b32 v52, v56, v52\n"
    "v_or_b32_dpp v44, v44, v44 quad_perm:[1,0,3,2] row_mask:0xf bank_mask:0xf bound_ctrl:1\n"
    "v_lshlrev_b32 v50, 2, v40\n"
    "v_bfe_u32 v50, s25, v50, 4\n"
    "v_or_b32_dpp v44, v44, v44 quad_perm:[2,3,0,1] row_mask:0xf bank_mask:0xf bound_ctrl:1\n"
    "v_add_u32 v50, v50, v46\n"
    "v_lshl_add_u32 v50, v50, 3, v40\n"
    "v_or_b32_dpp v44, v44, v44 row_half_mirror row_mask:0xf bank_mask:0xf bound_ctrl:1\n"
    "v_bfe_u32 v48, v48, 25, 5\n"
    "v_bfe_u32 v49, v49, 25, 5\n"
    "v_lshlrev_b32 v51, 2, v41\n"
    "v_bfe_u32 v51, s25, v51, 4\n"
    "v_add_u32 v51, v51, v48\n"
    "v_lshl_add_u32 v51, v51, 3, v41\n"
    "v_readlane_b32 s4, v44, 0\n"
    "v_readlane_b32 s5, v44, 8\n"
    "v_readlane_b32 s6, v44, 16\n"
    "v_readlane_b32 s7, v44, 24\n"
    "v_readlane_b32 s8, v44, 32\n"
    "v_readlane_b32 s9, v44, 40\n"
    "v_add_u32 v57, 40, v51\n"
    "v_lshl_add_u32 v50, v47, 3, v50\n"
    "v_lshl_add_u32 v51, v49, 3, v51\n"
    "v_cmp_lt_u32 vcc, v51, v50\n"
    "s_lshl_b64 exec, 0xff, 8\n"
    "v_mov_b32 v57, v51\n"
    "s_mov_b64 exec, -1\n"
    "v_lshrrev_b64 v[62:63], v43, vcc\n"
    "v_lshrrev_b64 v[60:61], v42, vcc\n"
    "v_and_b32 v62, 0xff, v62\n"
    "v_and_b32 v60, 0xff, v60\n"
    "v_bcnt_u32_b32 v62, v62, 0\n"
    "v_bcnt_u32_b32 v60, v60, 0\n"
    "v_lshlrev_b32 v62, 2, v62\n"
    "v_lshlrev_b32 v60, 2, v60\n"
    "v_bfe_u32 v63, s4, v62, 4\n"
    "v_bfe_u32 v61, s4, v60, 4\n"
    "s_lshl_b64 exec, 0xff, 0\n"
    "v_lshl_add_u32 v58, v42, 2, v62\n"
    "s_mov_b64 exec, -1\n"
    "v_lshl_add_u32 v51, v63, 3, v51\n"
    "v_lshl_add_u32 v50, v61, 3, v50\n"
    "v_cmp_lt_u32 vcc, v51, v50\n"
    "s_lshl_b64 exec, 0xff, 16\n"
    "v_mov_b32 v57, v51\n"
    "s_mov_b64 exec, -1\n"
    "v_lshrrev_b64 v[62:63], v43, vcc\n"
    "v_lshrrev_b64 v[60:61], v42, vcc\n"
    "v_and_b32 v62, 0xff, v62\n"
    "v_and_b32 v60, 0xff, v60\n"
    "v_bcnt_u32_b32 v62, v62, 0\n"
    "v_bcnt_u32_b32 v60, v60, 0\n"
    "v_lshlrev_b32 v62, 2, v62\n"
    "v_lshlrev_b32 v60, 2, v60\n"
    "v_bfe_u32 v63, s5, v62, 4\n"
    "v_bfe_u32 v61, s5, v60, 4\n"
    "s_lshl_b64 exec, 0xff, 8\n"
    "v_lshl_add_u32 v58, v42, 2, v62\n"
    "s_mov_b64 exec, -1\n"
    "v_lshl_add_u32 v51, v63, 3, v51\n"
    "v_lshl_add_u32 v50, v61, 3, v50\n"
    "v_cmp_lt_u32 vcc, v51, v50\n"
    "s_lshl_b64 exec, 0xff, 24\n"
    "v_mov_b32 v57, v51\n"
    "s_mov_b64 exec, -1\n"
    "v_lshrrev_b64 v[62:63], v43, vcc\n"
    "v_lshrrev_b64 v[60:61], v42, vcc\n"
    "v_and_b32 v62, 0xff, v62\n"
    "v_and_b32 v60, 0xff, v60\n"
    "v_bcnt_u32_b32 v62, v62, 0\n"
    "v_bcnt_u32_b32 v60, v60, 0\n"
    "v_lshlrev_b32 v62, 2, v62\n"
    "v_lshlrev_b32 v60, 2, v60\n"
    "v_bfe_u32 v63, s6, v62, 4\n"
    "v_bfe_u32 v61, s6, v60, 4\n"
    "s_lshl_b64 exec, 0xff, 16\n"
    "v_lshl_add_u32 v58, v42, 2, v62\n"
    "s_mov_b64 exec, -1\n"
    "v_lshl_add_u32 v51, v63, 3, v51\n"
    "v_lshl_add_u32 v50, v61, 3, v50\n"
    "v_cmp_lt_u32 vcc, v51, v50\n"
    "s_lshl_b64 exec, 0xff, 32\n"
    "v_mov_b32 v57, v51\n"
    "s_mov_b64 exec, -1\n"
    "v_lshrrev_b64 v[62:63], v43, vcc\n"
    "v_lshrrev_b64 v[60:61], v42, vcc\n"
    "v_and_b32 v62, 0xff, v62\n"
    "v_and_b32 v60, 0xff, v60\n"
    "v_bcnt_u32_b32 v62, v62, 0\n"
    "v_bcnt_u32_b32 v60, v60, 0\n"
    "v_lshlrev_b32 v62, 2, v62\n"
    "v_lshlrev_b32 v60, 2, v60\n"
    "v_bfe_u32 v63, s7, v62, 4\n"
    "v_bfe_u32 v61, s7, v60, 4\n"
    "s_lshl_b64 exec, 0xff, 24\n"
    "v_lshl_add_u32 v58, v42, 2, v62\n"
    "s_mov_b64 exec, -1\n"
    "v_lshl_add_u32 v51, v63, 3, v51\n"
    "v_lshl_add_u32 v50, v61, 3, v50\n"
    "v_cmp_lt_u32 vcc, v51, v50\n"
    "s_lshl_b64 exec, 0xff, 40\n"
    "v_mov_b32 v57, v51\n"
    "s_mov_b64 exec, -1\n"
    "v_lshrrev_b64 v[62:63], v43, vcc\n"
    "v_lshrrev_b64 v[60:61], v42, vcc\n"
    "v_and_b32 v62, 0xff, v62\n"
    "v_and_b32 v60, 0xff, v60\n"
    "v_bcnt_u32_b32 v62, v62, 0\n"
    "v_bcnt_u32_b32 v60, v60, 0\n"
    "v_lshlrev_b32 v62, 2, v62\n"
    "v_lshlrev_b32 v60, 2, v60\n"
    "v_bfe_u32 v63, s8, v62, 4\n"
    "v_bfe_u32 v61, s8, v60, 4\n"
    "s_lshl_b64 exec, 0xff, 32\n"
    "v_lshl_add_u32 v58, v42, 2, v62\n"
    "s_mov_b64 exec, -1\n"
    "v_lshl_add_u32 v51, v63, 3, v51\n"
    "v_lshl_add_u32 v50, v61, 3, v50\n"
    "v_cmp_lt_u32 vcc, v51, v50\n"
    "s_nop 1\n"
    "v_lshrrev_b64 v[62:63], v43, vcc\n"
    "v_and_b32 v62, 0xff, v62\n"
    "v_bcnt_u32_b32 v62, v62, 0\n"
    "v_lshlrev_b32 v62, 2, v62\n"
    "v_bfe_u32 v63, s9, v62, 4\n"
    "s_lshl_b64 exec, 0xff, 40\n"
    "v_lshl_add_u32 v58, v42, 2, v62\n"
    "s_mov_b64 exec, -1\n"
    "s_waitcnt lgkmcnt(0)\n"
    "ds_bpermute_b32 v52, v58, v52\n"
    "v_lshl_add_u32 v60, v63, 3, v51\n"
    "v_sub_u32 v57, v51, v57\n"
    "v_sub_u32 v50, 6, v40\n"
    "v_add_u32_dpp v60, v60, v60 quad_perm:[1,0,3,2] row_mask:0xf bank_mask:0xf bound_ctrl:1\n"
    "v_mad_u32_u24 v57, v50, 40, v57\n"
    "s_mov_b32 exec_lo, 0x3e7fffff\n"
    "s_mov_b32 exec_hi, 0x81c\n"
    "v_add_u32 v57, -8, v57\n"
    "s_mov_b64 exec, -1\n"
    "v_mul_i32_i24 v50, 8, v50\n"
    "v_add_u32_dpp v60, v60, v60 quad_perm:[2,3,0,1] row_mask:0xf bank_mask:0xf bound_ctrl:1\n"
    "v_cvt_f32_u32 v54, v57\n"
    "v_mul_u32_u24 v57, 105, v57\n"
    "v_add_u32_dpp v60, v60, v60 row_half_mirror row_mask:0xf bank_mask:0xf bound_ctrl:1\n"
    "v_rcp_f32 v54, v54\n"
    "v_mad_u32_u24 v50, v63, 30, v50\n"
    "v_lshrrev_b32 v60, 3, v60\n"
    "v_lshrrev_b32 v57, 4, v57\n"
    "v_add_u32 v60, 222, v60\n"
    "v_mul_f32 v54, 0x49000000, v54\n"
    "v_lshrrev_b32 v56, 5, v60\n"
    "v_cmp_lt_u32 vcc, 0, v63\n"
    "v_cvt_u32_f32 v54, v54\n"
    "v_sub_u32 v44, v60, v56\n"
    "v_add_u32 v60, v60, v56\n"
    "v_cndmask_b32 v54, 0, v54, vcc\n"
    "v_med3_u32 v60, s19, v44, v60\n"
    "v_and_b32 v54, s18, v54\n"
    "v_mul_u32_u24 v60, 401, v60\n"
    "s_add_u32 s23, s76, 350\n"
    "v_add_u32 v50, v50, v57\n"
    "s_mov_b32 s4, 0x4b4b4b00\n"
    "s_mov_b32 s5, 0x284b\n"
    "v_lshrrev_b64 v[62:63], v42, s[4:5]\n"
    "s_mov_b32 s6, 0xffff1111\n"
    "v_lshlrev_b32 v57, 2, v41\n"
    "v_and_b32 v62, 0xff, v62\n"
    "v_bfe_i32 v57, s6, v57, 4\n"
    "v_mad_i32_i24 v50, v62, v57, v50\n"
    "v_lshrrev_b32 v60, 6, v60\n"
    "v_add_u32 v56, 16, v42\n"
    "v_sub_u32 v60, v60, v50\n"
    "v_and_b32 v44, 24, v56\n"
    "v_add_u32 v60, s23, v60\n"
    "v_lshl_add_u32 v55, v44, 2, v55\n"
    "v_and_b32 v60, 0xffff, v60\n"
    "v_lshrrev_b32 v56, 5, v56\n"
    "v_lshl_or_b32 v53, v54, 16, v60\n"
    "v_lshl_add_u32 v55, v56, 3, v55\n"
    "s_mov_b32 exec_hi, 0xffff\n"
    "s_waitcnt lgkmcnt(0)\n"
    "ds_write_b64 v55, v[52:53] offset:128\n"
    "s_mov_b32 exec_hi, -1\n"
    ".Ldin_gate:\n"
    "s_bfe_u32 s23, s2, 0x10003\n"
    "s_lshl1_add_u32 s23, s21, s23\n"
    "s_mul_i32 s23, s23, 50\n"
    "s_add_u32 s23, s23, 380\n"
    "s_and_b32 s23, s23, s18\n"
    "s_add_u32 s23, s23, s76\n"
    "s_setprio 2\n"
    ".Ldin_gw:\n"
    "s_memrealtime s[34:35]\n"
    "s_waitcnt lgkmcnt(0)\n"
    "s_sub_u32 s24, s34, s23\n"
    "s_cmp_ge_i32 s24, 0\n"
    "s_cbranch_scc1 .Ldin_go\n"
    "s_sleep 2\n"
    "s_branch .Ldin_gw\n"
    ".Ldin_trimo:\n"
    "v_add_u32 v66, 4, v3\n"
    "v_cmp_gt_u32 vcc, s14, v66\n"
    "s_nop 1\n"
    "v_cndmask_b32 v41, 0, v41, vcc\n"
    "v_cndmask_b32 v45, 0, v45, vcc\n"
    "v_add_u32 v66, 8, v3\n"
    "v_cmp_gt_u32 vcc, s14, v66\n"
    "s_nop 1\n"
    "v_cndmask_b32 v42, 0, v42, vcc\n"
    "v_cndmask_b32 v46, 0, v46, vcc\n"
    "v_add_u32 v66, 12, v3\n"
    "v_cmp_gt_u32 vcc, s14, v66\n"
    "s_nop 1\n"
    "v_cndmask_b32 v43, 0, v43, vcc\n"
    "v_cndmask_b32 v47, 0, v47, vcc\n"
    "s_branch .Ldin_trimo_r\n"
    ".Ldin_trimy:\n"
    "v_add_u32 v32, 4, v3\n"
    "v_cmp_gt_u32 vcc, s14, v32\n"
    "s_nop 1\n"
    "v_cndmask_b32 v97, 0, v97, vcc\n"
    "v_cndmask_b32 v101, 0, v101, vcc\n"
    "v_cndmask_b32 v105, 0, v105, vcc\n"
    "v_cndmask_b32 v109, 0, v109, vcc\n"
    "v_cndmask_b32 v113, 0, v113, vcc\n"
    "v_cndmask_b32 v117, 0, v117, vcc\n"
    "v_add_u32 v32, 8, v3\n"
    "v_cmp_gt_u32 vcc, s14, v32\n"
    "s_nop 1\n"
    "v_cndmask_b32 v98, 0, v98, vcc\n"
    "v_cndmask_b32 v102, 0, v102, vcc\n"
    "v_cndmask_b32 v106, 0, v106, vcc\n"
    "v_cndmask_b32 v110, 0, v110, vcc\n"
    "v_cndmask_b32 v114, 0, v114, vcc\n"
    "v_cndmask_b32 v118, 0, v118, vcc\n"
    "v_add_u32 v32, 12, v3\n"
    "v_cmp_gt_u32 vcc, s14, v32\n"
    "s_nop 1\n"
    "v_cndmask_b32 v99, 0, v99, vcc\n"
    "v_cndmask_b32 v103, 0, v103, vcc\n"
    "v_cndmask_b32 v107, 0, v107, vcc\n"
    "v_cndmask_b32 v111, 0, v111, vcc\n"
    "v_cndmask_b32 v115, 0, v115, vcc\n"
    "v_cndmask_b32 v119, 0, v119, vcc\n"
    "s_branch .Ldin_trimy_r\n"
    ".Ldin_A0s:\n"
    "v_add_u32 v251, s66, v244\n"
    "v_readlane_b32 s60, v216, s59\n"
    "v_readlane_b32 s61, v217, s59\n"
    "v_mfma_f32_4x4x4_16b_bf16 v[224:227], v[228:229], v[208:209], v[224:227] cbsz:4 abid:0\n"
    "v_exp_f32 v160, v160 clamp\n"
    "v_exp_f32 v161, v161 clamp\n"
    "v_mfma_f32_4x4x4_16b_bf16 v[224:227], v[228:229], v[210:211], v[224:227] cbsz:4 abid:1\n"
    "v_exp_f32 v162, v162 clamp\n"
    "v_exp_f32 v163, v163 clamp\n"
    "v_mfma_f32_4x4x4_16b_bf16 v[224:227], v[228:229], v[212:213], v[224:227] cbsz:4 abid:2\n"
    "v_exp_f32 v164, v164 clamp\n"
    "v_exp_f32 v165, v165 clamp\n"
    "v_mfma_f32_4x4x4_16b_bf16 v[224:227], v[228:229], v[214:215], v[224:227] cbsz:4 abid:3\n"
    "s_waitcnt lgkmcnt(0)\n"
    "v_mfma_f32_16x16x32_bf16 v[176:179], v[0:3], v[192:195], v[144:147]\n"
    "s_setprio 1\n"
    "v_exp_f32 v166, v166 clamp\n"
    "v_mfma_f32_16x16x32_bf16 v[176:179], v[4:7], v[196:199], v[176:179]\n"
    "s_cmp_eq_u32 s60, -1\n"
    "v_exp_f32 v167, v167 clamp\n"
    "v_mfma_f32_16x16x32_bf16 v[180:183], v[8:11], v[192:195], v[148:151]\n"
    "s_cbranch_scc1 .Ldin_A0s_poll\n"
    ".Ldin_A0s_pr:\n"
    "v_exp_f32 v168, v168 clamp\n"
    "v_mfma_f32_16x16x32_bf16 v[180:183], v[12:15], v[196:199], v[180:183]\n"
    "s_lshr_b32 s94, s60, 12\n"
    "v_exp_f32 v169, v169 clamp\n"
    "v_mfma_f32_16x16x32_bf16 v[184:187], v[16:19], v[192:195], v[152:155]\n"
    "s_and_b32 s94, s94, 0x1ff0\n"
    "v_exp_f32 v170, v170 clamp\n"
    "v_mfma_f32_16x16x32_bf16 v[184:187], v[20:23], v[196:199], v[184:187]\n"
    "s_mov_b32 m0, s62\n"
    "v_exp_f32 v171, v171 clamp\n"
    "v_mfma_f32_16x16x32_bf16 v[188:191], v[24:27], v[192:195], v[156:159]\n"
    "s_add_u32 s66, s66, 16\n"
    "v_exp_f32 v172, v172 clamp\n"
    "v_mfma_f32_16x16x32_bf16 v[188:191], v[28:31], v[196:199], v[188:191]\n"
    "s_and_b32 s64, s60, 0xffff\n"
    "v_xor_b32 v252, s94, v250\n"
    "v_exp_f32 v173, v173 clamp\n"
    "v_exp_f32 v174, v174 clamp\n"
    "v_exp_f32 v175, v175 clamp\n"
    "v_pk_fma_f32 v[160:161], v[160:161], s[70:71], v[128:129]\n"
    "v_pk_fma_f32 v[162:163], v[162:163], s[70:71], v[130:131]\n"
    "v_pk_add_f32 v[230:231], v[160:161], v[162:163]\n"
    "v_pk_mul_f32 v[232:233], v[160:161], v[162:163]\n"
    "v_pk_add_f32 v[218:219], v[224:225], v[226:227]\n"
    "v_mov_b64 v[224:225], 0\n"
    "v_mov_b64 v[226:227], 0\n"
    "v_pk_fma_f32 v[164:165], v[164:165], s[70:71], v[132:133]\n"
    "v_pk_fma_f32 v[230:231], v[230:231], v[164:165], v[232:233]\n"
    "v_pk_mul_f32 v[232:233], v[232:233], v[164:165]\n"
    "v_pk_fma_f32 v[166:167], v[166:167], s[70:71], v[134:135]\n"
    "v_pk_fma_f32 v[230:231], v[230:231], v[166:167], v[232:233]\n"
    "v_pk_mul_f32 v[232:233], v[232:233], v[166:167]\n"
    "v_pk_fma_f32 v[168:169], v[168:169], s[70:71], v[136:137]\n"
    "v_pk_fma_f32 v[230:231], v[230:231], v[168:169], v[232:233]\n"
    "v_pk_mul_f32 v[232:233], v[232:233], v[168:169]\n"
    "v_pk_fma_f32 v[170:171], v[170:171], s[70:71], v[138:139]\n"
    "v_pk_fma_f32 v[230:231], v[230:231], v[170:171], v[232:233]\n"
    "v_pk_mul_f32 v[232:233], v[232:233], v[170:171]\n"
    "v_pk_fma_f32 v[172:173], v[172:173], s[70:71], v[140:141]\n"
    "v_pk_fma_f32 v[230:231], v[230:231], v[172:173], v[232:233]\n"
    "v_pk_mul_f32 v[232:233], v[232:233], v[172:173]\n"
    "v_pk_fma_f32 v[174:175], v[174:175], s[70:71], v[142:143]\n"
    "v_pk_fma_f32 v[230:231], v[230:231], v[174:175], v[232:233]\n"
    "v_pk_mul_f32 v[232:233], v[232:233], v[174:175]\n"
    "v_rcp_f32 v232, v232\n"
    "v_rcp_f32 v233, v233\n"
    "v_fma_f32 v230, v230, v232, v247\n"
    "v_fmac_f32 v230, v231, v233\n"
    "v_mbcnt_lo_u32_b32 v232, -1, 0\n"
    "v_mbcnt_hi_u32_b32 v232, -1, v232\n"
    "v_bfe_u32 v233, v232, 3, 1\n"
    "v_and_b32 v232, 7, v232\n"
    "v_lshl_add_u32 v232, v232, 1, v233\n"
    "v_add_u32 v232, s81, v232\n"
    "ds_read_u8 v232, v232 offset:49664\n"
    "s_add_u32 s81, s81, 16\n"
    "s_waitcnt lgkmcnt(0)\n"
    "buffer_load_ushort v232, v232, s[44:47], s63 idxen\n"
    "s_waitcnt vmcnt(0)\n"
    "v_lshlrev_b32 v232, 16, v232\n"
    "v_mul_f32 v230, v230, v232\n"
    "v_add_f32 v218, v218, v219\n"
    "s_setprio 0\n"
    "ds_read_b64_tr_b16 v[208:209], v240 offset:2048\n"
    "ds_read_b64_tr_b16 v[210:211], v241 offset:2048\n"
    "ds_read_b64_tr_b16 v[212:213], v242 offset:2048\n"
    "ds_read_b64_tr_b16 v[214:215], v243 offset:2048\n"
    "ds_read_u8 v236, v251 offset:0\n"
    "ds_read_u8 v238, v251 offset:1\n"
    "ds_write_b16_d16_hi v245, v230\n"
    "ds_read_b64 v[228:229], v246\n"
    "ds_write_addtid_b32 v218 offset:49152\n"
    "s_add_u32 m0, s68, 2048\n"
    "s_bfe_u32 s94, s60, 0x30013\n"
    "s_add_u32 s58, s58, s57\n"
    "s_mul_i32 s94, s94, s56\n"
    "s_add_u32 s82, s64, s9\n"
    "s_add_u32 s65, s58, s94\n"
    "s_waitcnt vmcnt(2)\n"
    "ds_read_b128 v[192:195], v234 offset:4096\n"
    "ds_read_b128 v[196:199], v235 offset:4096\n"
    "ds_read_b128 v[216:219], v252 offset:0\n"
    "ds_read_b128 v[220:223], v252 offset:64\n"
    "s_mul_i32 s15, s15, s16\n"
    "s_cmp_lt_u32 s72, 49\n"
    "s_cselect_b32 s15, 0, s15\n"
    "s_add_u32 s14, s14, s15\n"
    "s_mov_b32 s15, 0\n"
    "s_min_u32 s14, s14, s17\n"
    "s_add_u32 s95, s61, s14\n"
    "s_sub_u32 s95, s95, s76\n"
    "s_lshr_b32 s94, s61, 16\n"
    "s_sext_i32_i16 s95, s95\n"
    "s_cmp_eq_u32 s59, s10\n"
    "s_cbranch_scc1 .Ldin_A0s_skip\n"
    "s_waitcnt lgkmcnt(7)\n"
    "buffer_load_dwordx4 v[236:237], s[40:43], s67 idxen offen offset:0 nt lds\n"
    "buffer_load_dwordx4 v[238:239], s[40:43], s67 idxen offen offset:1024 nt lds\n"
    ".Ldin_A0s_skr:\n"
    "s_mul_i32 s95, s95, s94\n"
    "s_bfe_u32 s94, s60, 0x50019\n"
    "s_ashr_i32 s95, s95, 16\n"
    "s_lshl4_add_u32 s83, s94, s64\n"
    "s_max_i32 s95, s95, -14\n"
    "s_add_u32 s59, s59, 1\n"
    "s_min_i32 s95, s95, 14\n"
    "s_add_u32 s72, s77, s95\n"
    "s_cmp_lt_i32 s60, 0\n"
    "s_cselect_b32 s72, 0, s72\n"
    "s_cmp_eq_u32 s66, s80\n"
    "s_cselect_b64 s[66:67], s[64:65], s[66:67]\n"
    "s_waitcnt lgkmcnt(5)\n"
    ".Ldin_G1s:\n"
    "v_add_u32 v251, s66, v244\n"
    "v_mfma_f32_4x4x4_16b_bf16 v[224:227], v[228:229], v[200:201], v[224:227] cbsz:4 abid:0\n"
    "v_exp_f32 v176, v176 clamp\n"
    "v_exp_f32 v177, v177 clamp\n"
    "v_mfma_f32_4x4x4_16b_bf16 v[224:227], v[228:229], v[202:203], v[224:227] cbsz:4 abid:1\n"
    "v_exp_f32 v178, v178 clamp\n"
    "v_exp_f32 v179, v179 clamp\n"
    "v_mfma_f32_4x4x4_16b_bf16 v[224:227], v[228:229], v[204:205], v[224:227] cbsz:4 abid:2\n"
    "v_exp_f32 v180, v180 clamp\n"
    "v_exp_f32 v181, v181 clamp\n"
    "v_mfma_f32_4x4x4_16b_bf16 v[224:227], v[228:229], v[206:207], v[224:227] cbsz:4 abid:3\n"
    "s_waitcnt lgkmcnt(0)\n"
    "v_mfma_f32_16x16x32_bf16 v[160:163], v[0:3], v[192:195], v[144:147]\n"
    "s_setprio 1\n"
    "v_exp_f32 v182, v182 clamp\n"
    "v_mfma_f32_16x16x32_bf16 v[160:163], v[4:7], v[196:199], v[160:163]\n"
    "s_add_u32 m0, s68, 4096\n"
    "v_exp_f32 v183, v183 clamp\n"
    "v_mfma_f32_16x16x32_bf16 v[164:167], v[8:11], v[192:195], v[148:151]\n"
    "s_add_u32 s66, s66, 16\n"
    "v_exp_f32 v184, v184 clamp\n"
    "v_mfma_f32_16x16x32_bf16 v[164:167], v[12:15], v[196:199], v[164:167]\n"
    "v_exp_f32 v185, v185 clamp\n"
    "v_mfma_f32_16x16x32_bf16 v[168:171], v[16:19], v[192:195], v[152:155]\n"
    "v_exp_f32 v186, v186 clamp\n"
    "v_mfma_f32_16x16x32_bf16 v[168:171], v[20:23], v[196:199], v[168:171]\n"
    "v_exp_f32 v187, v187 clamp\n"
    "v_mfma_f32_16x16x32_bf16 v[172:175], v[24:27], v[192:195], v[156:159]\n"
    "v_exp_f32 v188, v188 clamp\n"
    "v_mfma_f32_16x16x32_bf16 v[172:175], v[28:31], v[196:199], v[172:175]\n"
    "s_memrealtime s[34:35]\n"
    "v_exp_f32 v189, v189 clamp\n"
    "v_exp_f32 v190, v190 clamp\n"
    "v_exp_f32 v191, v191 clamp\n"
    "v_pk_fma_f32 v[176:177], v[176:177], s[70:71], v[128:129]\n"
    "v_pk_fma_f32 v[178:179], v[178:179], s[70:71], v[130:131]\n"
    "v_pk_add_f32 v[230:231], v[176:177], v[178:179]\n"
    "v_pk_mul_f32 v[232:233], v[176:177], v[178:179]\n"
    "v_pk_fma_f32 v[180:181], v[180:181], s[70:71], v[132:133]\n"
    "v_pk_fma_f32 v[230:231], v[230:231], v[180:181], v[232:233]\n"
    "v_pk_mul_f32 v[232:233], v[232:233], v[180:181]\n"
    "v_pk_fma_f32 v[182:183], v[182:183], s[70:71], v[134:135]\n"
    "v_pk_fma_f32 v[230:231], v[230:231], v[182:183], v[232:233]\n"
    "v_pk_mul_f32 v[232:233], v[232:233], v[182:183]\n"
    "v_pk_fma_f32 v[184:185], v[184:185], s[70:71], v[136:137]\n"
    "v_pk_fma_f32 v[230:231], v[230:231], v[184:185], v[232:233]\n"
    "v_pk_mul_f32 v[232:233], v[232:233], v[184:185]\n"
    "v_pk_fma_f32 v[186:187], v[186:187], s[70:71], v[138:139]\n"
    "v_pk_fma_f32 v[230:231], v[230:231], v[186:187], v[232:233]\n"
    "v_pk_mul_f32 v[232:233], v[232:233], v[186:187]\n"
    "v_pk_fma_f32 v[188:189], v[188:189], s[70:71], v[140:141]\n"
    "v_pk_fma_f32 v[230:231], v[230:231], v[188:189], v[232:233]\n"
    "v_pk_mul_f32 v[232:233], v[232:233], v[188:189]\n"
    "v_pk_fma_f32 v[190:191], v[190:191], s[70:71], v[142:143]\n"
    "v_pk_fma_f32 v[230:231], v[230:231], v[190:191], v[232:233]\n"
    "v_pk_mul_f32 v[232:233], v[232:233], v[190:191]\n"
    "v_rcp_f32 v232, v232\n"
    "v_rcp_f32 v233, v233\n"
    "v_fma_f32 v230, v230, v232, v247\n"
    "v_fmac_f32 v230, v231, v233\n"
    "v_mbcnt_lo_u32_b32 v232, -1, 0\n"
    "v_mbcnt_hi_u32_b32 v232, -1, v232\n"
    "v_bfe_u32 v233, v232, 3, 1\n"
    "v_and_b32 v232, 7, v232\n"
    "v_lshl_add_u32 v232, v232, 1, v233\n"
    "v_add_u32 v232, s81, v232\n"
    "ds_read_u8 v232, v232 offset:49664\n"
    "s_add_u32 s81, s81, 16\n"
    "s_waitcnt lgkmcnt(0)\n"
    "buffer_load_ushort v232, v232, s[44:47], s63 idxen\n"
    "s_waitcnt vmcnt(0)\n"
    "v_lshlrev_b32 v232, 16, v232\n"
    "v_mul_f32 v230, v230, v232\n"
    "s_setprio 0\n"
    "s_waitcnt lgkmcnt(0)\n"
    "ds_read_b64_tr_b16 v[200:201], v240 offset:4096\n"
    "ds_read_b64_tr_b16 v[202:203], v241 offset:4096\n"
    "ds_read_b64_tr_b16 v[204:205], v242 offset:4096\n"
    "ds_read_b64_tr_b16 v[206:207], v243 offset:4096\n"
    "ds_read_u8 v236, v251 offset:0\n"
    "ds_read_u8 v238, v251 offset:1\n"
    "ds_write_b16_d16_hi v245, v230\n"
    "ds_read_b64 v[228:229], v246\n"
    "s_cmp_lt_u32 s34, s76\n"
    "s_cbranch_scc1 .Ldin_G1s_wait\n"
    ".Ldin_G1s_go:\n"
    "s_waitcnt vmcnt(2)\n"
    "ds_read_b128 v[192:195], v234 offset:6144\n"
    "ds_read_b128 v[196:199], v235 offset:6144\n"
    "s_waitcnt lgkmcnt(4)\n"
    "buffer_load_dwordx4 v[236:237], s[40:43], s67 idxen offen offset:0 nt lds\n"
    "buffer_load_dwordx4 v[238:239], s[40:43], s67 idxen offen offset:1024 nt lds\n"
    "s_add_u32 s76, s76, s72\n"
    "s_cmp_eq_u32 s66, s80\n"
    "s_cselect_b64 s[66:67], s[64:65], s[66:67]\n"
    "s_waitcnt lgkmcnt(2)\n"
    "s_cmp_eq_u32 s66, s82\n"
    "s_cbranch_scc1 .Ldin_L2s\n"
    ".Ldin_G2s:\n"
    "v_add_u32 v251, s66, v244\n"
    "v_mfma_f32_4x4x4_16b_bf16 v[224:227], v[228:229], v[208:209], v[224:227] cbsz:4 abid:0\n"
    "v_exp_f32 v160, v160 clamp\n"
    "v_exp_f32 v161, v161 clamp\n"
    "v_mfma_f32_4x4x4_16b_bf16 v[224:227], v[228:229], v[210:211], v[224:227] cbsz:4 abid:1\n"
    "v_exp_f32 v162, v162 clamp\n"
    "v_exp_f32 v163, v163 clamp\n"
    "v_mfma_f32_4x4x4_16b_bf16 v[224:227], v[228:229], v[212:213], v[224:227] cbsz:4 abid:2\n"
    "v_exp_f32 v164, v164 clamp\n"
    "v_exp_f32 v165, v165 clamp\n"
    "v_mfma_f32_4x4x4_16b_bf16 v[224:227], v[228:229], v[214:215], v[224:227] cbsz:4 abid:3\n"
    "s_waitcnt lgkmcnt(0)\n"
    "v_mfma_f32_16x16x32_bf16 v[176:179], v[0:3], v[192:195], v[144:147]\n"
    "s_setprio 1\n"
    "v_exp_f32 v166, v166 clamp\n"
    "v_mfma_f32_16x16x32_bf16 v[176:179], v[4:7], v[196:199], v[176:179]\n"
    "s_add_u32 m0, s68, 6144\n"
    "v_exp_f32 v167, v167 clamp\n"
    "v_mfma_f32_16x16x32_bf16 v[180:183], v[8:11], v[192:195], v[148:151]\n"
    "s_add_u32 s66, s66, 16\n"
    "v_exp_f32 v168, v168 clamp\n"
    "v_mfma_f32_16x16x32_bf16 v[180:183], v[12:15], v[196:199], v[180:183]\n"
    "v_exp_f32 v169, v169 clamp\n"
    "v_mfma_f32_16x16x32_bf16 v[184:187], v[16:19], v[192:195], v[152:155]\n"
    "v_exp_f32 v170, v170 clamp\n"
    "v_mfma_f32_16x16x32_bf16 v[184:187], v[20:23], v[196:199], v[184:187]\n"
    "v_exp_f32 v171, v171 clamp\n"
    "v_mfma_f32_16x16x32_bf16 v[188:191], v[24:27], v[192:195], v[156:159]\n"
    "v_exp_f32 v172, v172 clamp\n"
    "v_mfma_f32_16x16x32_bf16 v[188:191], v[28:31], v[196:199], v[188:191]\n"
    "s_memrealtime s[34:35]\n"
    "v_exp_f32 v173, v173 clamp\n"
    "v_exp_f32 v174, v174 clamp\n"
    "v_exp_f32 v175, v175 clamp\n"
    "v_pk_fma_f32 v[160:161], v[160:161], s[70:71], v[128:129]\n"
    "v_pk_fma_f32 v[162:163], v[162:163], s[70:71], v[130:131]\n"
    "v_pk_add_f32 v[230:231], v[160:161], v[162:163]\n"
    "v_pk_mul_f32 v[232:233], v[160:161], v[162:163]\n"
    "v_pk_fma_f32 v[164:165], v[164:165], s[70:71], v[132:133]\n"
    "v_pk_fma_f32 v[230:231], v[230:231], v[164:165], v[232:233]\n"
    "v_pk_mul_f32 v[232:233], v[232:233], v[164:165]\n"
    "v_pk_fma_f32 v[166:167], v[166:167], s[70:71], v[134:135]\n"
    "v_pk_fma_f32 v[230:231], v[230:231], v[166:167], v[232:233]\n"
    "v_pk_mul_f32 v[232:233], v[232:233], v[166:167]\n"
    "v_pk_fma_f32 v[168:169], v[168:169], s[70:71], v[136:137]\n"
    "v_pk_fma_f32 v[230:231], v[230:231], v[168:169], v[232:233]\n"
    "v_pk_mul_f32 v[232:233], v[232:233], v[168:169]\n"
    "v_pk_fma_f32 v[170:171], v[170:171], s[70:71], v[138:139]\n"
    "v_pk_fma_f32 v[230:231], v[230:231], v[170:171], v[232:233]\n"
    "v_pk_mul_f32 v[232:233], v[232:233], v[170:171]\n"
    "v_pk_fma_f32 v[172:173], v[172:173], s[70:71], v[140:141]\n"
    "v_pk_fma_f32 v[230:231], v[230:231], v[172:173], v[232:233]\n"
    "v_pk_mul_f32 v[232:233], v[232:233], v[172:173]\n"
    "v_pk_fma_f32 v[174:175], v[174:175], s[70:71], v[142:143]\n"
    "v_pk_fma_f32 v[230:231], v[230:231], v[174:175], v[232:233]\n"
    "v_pk_mul_f32 v[232:233], v[232:233], v[174:175]\n"
    "v_rcp_f32 v232, v232\n"
    "v_rcp_f32 v233, v233\n"
    "v_fma_f32 v230, v230, v232, v247\n"
    "v_fmac_f32 v230, v231, v233\n"
    "v_mbcnt_lo_u32_b32 v232, -1, 0\n"
    "v_mbcnt_hi_u32_b32 v232, -1, v232\n"
    "v_bfe_u32 v233, v232, 3, 1\n"
    "v_and_b32 v232, 7, v232\n"
    "v_lshl_add_u32 v232, v232, 1, v233\n"
    "v_add_u32 v232, s81, v232\n"
    "ds_read_u8 v232, v232 offset:49664\n"
    "s_add_u32 s81, s81, 16\n"
    "s_waitcnt lgkmcnt(0)\n"
    "buffer_load_ushort v232, v232, s[44:47], s63 idxen\n"
    "s_waitcnt vmcnt(0)\n"
    "v_lshlrev_b32 v232, 16, v232\n"
    "v_mul_f32 v230, v230, v232\n"
    "s_setprio 0\n"
    "s_waitcnt lgkmcnt(0)\n"
    "ds_read_b64_tr_b16 v[208:209], v240 offset:6144\n"
    "ds_read_b64_tr_b16 v[210:211], v241 offset:6144\n"
    "ds_read_b64_tr_b16 v[212:213], v242 offset:6144\n"
    "ds_read_b64_tr_b16 v[214:215], v243 offset:6144\n"
    "ds_read_u8 v236, v251 offset:0\n"
    "ds_read_u8 v238, v251 offset:1\n"
    "ds_write_b16_d16_hi v245, v230\n"
    "ds_read_b64 v[228:229], v246\n"
    "s_cmp_lt_u32 s34, s76\n"
    "s_cbranch_scc1 .Ldin_G2s_wait\n"
    ".Ldin_G2s_go:\n"
    "s_waitcnt vmcnt(2)\n"
    "ds_read_b128 v[192:195], v234 offset:0\n"
    "ds_read_b128 v[196:199], v235 offset:0\n"
    "s_waitcnt lgkmcnt(4)\n"
    "buffer_load_dwordx4 v[236:237], s[40:43], s67 idxen offen offset:0 nt lds\n"
    "buffer_load_dwordx4 v[238:239], s[40:43], s67 idxen offen offset:1024 nt lds\n"
    "s_sub_u32 s94, s34, 30\n"
    "s_max_u32 s76, s76, s94\n"
    "s_add_u32 s76, s76, s72\n"
    "s_cmp_eq_u32 s66, s80\n"
    "s_cselect_b64 s[66:67], s[64:65], s[66:67]\n"
    "s_waitcnt lgkmcnt(2)\n"
    "s_cmp_eq_u32 s66, s82\n"
    "s_cbranch_scc1 .Ldin_L3s\n"
    ".Ldin_G3s:\n"
    "v_add_u32_e64 v251, s66, v244\n"
    "v_mfma_f32_4x4x4_16b_bf16 v[224:227], v[228:229], v[200:201], v[224:227] cbsz:4 abid:0\n"
    "v_exp_f32 v176, v176 clamp\n"
    "v_exp_f32 v177, v177 clamp\n"
    "v_mfma_f32_4x4x4_16b_bf16 v[224:227], v[228:229], v[202:203], v[224:227] cbsz:4 abid:1\n"
    "v_exp_f32 v178, v178 clamp\n"
    "v_exp_f32 v179, v179 clamp\n"
    "v_mfma_f32_4x4x4_16b_bf16 v[224:227], v[228:229], v[204:205], v[224:227] cbsz:4 abid:2\n"
    "v_exp_f32 v180, v180 clamp\n"
    "v_exp_f32 v181, v181 clamp\n"
    "v_mfma_f32_4x4x4_16b_bf16 v[224:227], v[228:229], v[206:207], v[224:227] cbsz:4 abid:3\n"
    "s_waitcnt lgkmcnt(0)\n"
    "v_mfma_f32_16x16x32_bf16 v[160:163], v[0:3], v[192:195], v[144:147]\n"
    "s_setprio 1\n"
    "v_exp_f32 v182, v182 clamp\n"
    "v_mfma_f32_16x16x32_bf16 v[160:163], v[4:7], v[196:199], v[160:163]\n"
    "s_add_u32 m0, s8, 0\n"
    "v_exp_f32 v183, v183 clamp\n"
    "v_mfma_f32_16x16x32_bf16 v[164:167], v[8:11], v[192:195], v[148:151]\n"
    "s_add_u32 s66, s66, 16\n"
    "v_exp_f32 v184, v184 clamp\n"
    "v_mfma_f32_16x16x32_bf16 v[164:167], v[12:15], v[196:199], v[164:167]\n"
    "v_exp_f32 v185, v185 clamp\n"
    "v_mfma_f32_16x16x32_bf16 v[168:171], v[16:19], v[192:195], v[152:155]\n"
    "v_exp_f32 v186, v186 clamp\n"
    "v_mfma_f32_16x16x32_bf16 v[168:171], v[20:23], v[196:199], v[168:171]\n"
    "v_exp_f32 v187, v187 clamp\n"
    "v_mfma_f32_16x16x32_bf16 v[172:175], v[24:27], v[192:195], v[156:159]\n"
    "v_exp_f32 v188, v188 clamp\n"
    "v_mfma_f32_16x16x32_bf16 v[172:175], v[28:31], v[196:199], v[172:175]\n"
    "s_memrealtime s[34:35]\n"
    "v_exp_f32 v189, v189 clamp\n"
    "v_exp_f32 v190, v190 clamp\n"
    "v_exp_f32 v191, v191 clamp\n"
    "v_pk_fma_f32 v[176:177], v[176:177], s[70:71], v[128:129]\n"
    "v_pk_fma_f32 v[178:179], v[178:179], s[70:71], v[130:131]\n"
    "v_pk_add_f32 v[230:231], v[176:177], v[178:179]\n"
    "v_pk_mul_f32 v[232:233], v[176:177], v[178:179]\n"
    "v_pk_fma_f32 v[180:181], v[180:181], s[70:71], v[132:133]\n"
    "v_pk_fma_f32 v[230:231], v[230:231], v[180:181], v[232:233]\n"
    "v_pk_mul_f32 v[232:233], v[232:233], v[180:181]\n"
    "v_pk_fma_f32 v[182:183], v[182:183], s[70:71], v[134:135]\n"
    "v_pk_fma_f32 v[230:231], v[230:231], v[182:183], v[232:233]\n"
    "v_pk_mul_f32 v[232:233], v[232:233], v[182:183]\n"
    "v_pk_fma_f32 v[184:185], v[184:185], s[70:71], v[136:137]\n"
    "v_pk_fma_f32 v[230:231], v[230:231], v[184:185], v[232:233]\n"
    "v_pk_mul_f32 v[232:233], v[232:233], v[184:185]\n"
    "v_pk_fma_f32 v[186:187], v[186:187], s[70:71], v[138:139]\n"
    "v_pk_fma_f32 v[230:231], v[230:231], v[186:187], v[232:233]\n"
    "v_pk_mul_f32 v[232:233], v[232:233], v[186:187]\n"
    "v_pk_fma_f32 v[188:189], v[188:189], s[70:71], v[140:141]\n"
    "v_pk_fma_f32 v[230:231], v[230:231], v[188:189], v[232:233]\n"
    "v_pk_mul_f32 v[232:233], v[232:233], v[188:189]\n"
    "v_pk_fma_f32 v[190:191], v[190:191], s[70:71], v[142:143]\n"
    "v_pk_fma_f32 v[230:231], v[230:231], v[190:191], v[232:233]\n"
    "v_pk_mul_f32 v[232:233], v[232:233], v[190:191]\n"
    "v_rcp_f32 v232, v232\n"
    "v_rcp_f32 v233, v233\n"
    "v_fma_f32 v230, v230, v232, v247\n"
    "v_fmac_f32 v230, v231, v233\n"
    "v_mbcnt_lo_u32_b32 v232, -1, 0\n"
    "v_mbcnt_hi_u32_b32 v232, -1, v232\n"
    "v_bfe_u32 v233, v232, 3, 1\n"
    "v_and_b32 v232, 7, v232\n"
    "v_lshl_add_u32 v232, v232, 1, v233\n"
    "v_add_u32 v232, s81, v232\n"
    "ds_read_u8 v232, v232 offset:49664\n"
    "s_add_u32 s81, s81, 16\n"
    "s_waitcnt lgkmcnt(0)\n"
    "buffer_load_ushort v232, v232, s[44:47], s63 idxen\n"
    "s_waitcnt vmcnt(0)\n"
    "v_lshlrev_b32 v232, 16, v232\n"
    "v_mul_f32 v230, v230, v232\n"
    "s_setprio 0\n"
    "s_waitcnt lgkmcnt(0)\n"
    "ds_read_b64_tr_b16 v[200:201], v240 offset:0\n"
    "ds_read_b64_tr_b16 v[202:203], v241 offset:0\n"
    "ds_read_b64_tr_b16 v[204:205], v242 offset:0\n"
    "ds_read_b64_tr_b16 v[206:207], v243 offset:0\n"
    "ds_read_u8 v236, v251 offset:0\n"
    "ds_read_u8 v238, v251 offset:1\n"
    "ds_write_b16_d16_hi v245, v230\n"
    "ds_read_b64 v[228:229], v246\n"
    "s_cmp_lt_u32 s34, s76\n"
    "s_cbranch_scc1 .Ldin_G3s_wait\n"
    ".Ldin_G3s_go:\n"
    "s_waitcnt vmcnt(2)\n"
    "ds_read_b128 v[192:195], v234 offset:2048\n"
    "ds_read_b128 v[196:199], v235 offset:2048\n"
    "s_waitcnt lgkmcnt(4)\n"
    "buffer_load_dwordx4 v[236:237], s[40:43], s67 idxen offen offset:0 nt lds\n"
    "buffer_load_dwordx4 v[238:239], s[40:43], s67 idxen offen offset:1024 nt lds\n"
    "s_add_u32 s76, s76, s72\n"
    "s_cmp_eq_u32 s66, s80\n"
    "s_cselect_b64 s[66:67], s[64:65], s[66:67]\n"
    "s_waitcnt lgkmcnt(2)\n"
    "s_cmp_eq_u32 s66, s82\n"
    "s_cbranch_scc1 .Ldin_L0s\n"
    ".Ldin_G0s:\n"
    "v_add_u32 v251, s66, v244\n"
    "v_mfma_f32_4x4x4_16b_bf16 v[224:227], v[228:229], v[208:209], v[224:227] cbsz:4 abid:0\n"
    "v_exp_f32 v160, v160 clamp\n"
    "v_exp_f32 v161, v161 clamp\n"
    "v_mfma_f32_4x4x4_16b_bf16 v[224:227], v[228:229], v[210:211], v[224:227] cbsz:4 abid:1\n"
    "v_exp_f32 v162, v162 clamp\n"
    "v_exp_f32 v163, v163 clamp\n"
    "v_mfma_f32_4x4x4_16b_bf16 v[224:227], v[228:229], v[212:213], v[224:227] cbsz:4 abid:2\n"
    "v_exp_f32 v164, v164 clamp\n"
    "v_exp_f32 v165, v165 clamp\n"
    "v_mfma_f32_4x4x4_16b_bf16 v[224:227], v[228:229], v[214:215], v[224:227] cbsz:4 abid:3\n"
    "s_waitcnt lgkmcnt(0)\n"
    "v_mfma_f32_16x16x32_bf16 v[176:179], v[0:3], v[192:195], v[144:147]\n"
    "s_setprio 1\n"
    "v_exp_f32 v166, v166 clamp\n"
    "v_mfma_f32_16x16x32_bf16 v[176:179], v[4:7], v[196:199], v[176:179]\n"
    "s_add_u32 m0, s68, 2048\n"
    "v_exp_f32 v167, v167 clamp\n"
    "v_mfma_f32_16x16x32_bf16 v[180:183], v[8:11], v[192:195], v[148:151]\n"
    "s_add_u32 s66, s66, 16\n"
    "v_exp_f32 v168, v168 clamp\n"
    "v_mfma_f32_16x16x32_bf16 v[180:183], v[12:15], v[196:199], v[180:183]\n"
    "v_exp_f32 v169, v169 clamp\n"
    "v_mfma_f32_16x16x32_bf16 v[184:187], v[16:19], v[192:195], v[152:155]\n"
    "v_exp_f32 v170, v170 clamp\n"
    "v_mfma_f32_16x16x32_bf16 v[184:187], v[20:23], v[196:199], v[184:187]\n"
    "v_exp_f32 v171, v171 clamp\n"
    "v_mfma_f32_16x16x32_bf16 v[188:191], v[24:27], v[192:195], v[156:159]\n"
    "v_exp_f32 v172, v172 clamp\n"
    "v_mfma_f32_16x16x32_bf16 v[188:191], v[28:31], v[196:199], v[188:191]\n"
    "s_memrealtime s[34:35]\n"
    "v_exp_f32 v173, v173 clamp\n"
    "v_exp_f32 v174, v174 clamp\n"
    "v_exp_f32 v175, v175 clamp\n"
    "v_pk_fma_f32 v[160:161], v[160:161], s[70:71], v[128:129]\n"
    "v_pk_fma_f32 v[162:163], v[162:163], s[70:71], v[130:131]\n"
    "v_pk_add_f32 v[230:231], v[160:161], v[162:163]\n"
    "v_pk_mul_f32 v[232:233], v[160:161], v[162:163]\n"
    "v_pk_fma_f32 v[164:165], v[164:165], s[70:71], v[132:133]\n"
    "v_pk_fma_f32 v[230:231], v[230:231], v[164:165], v[232:233]\n"
    "v_pk_mul_f32 v[232:233], v[232:233], v[164:165]\n"
    "v_pk_fma_f32 v[166:167], v[166:167], s[70:71], v[134:135]\n"
    "v_pk_fma_f32 v[230:231], v[230:231], v[166:167], v[232:233]\n"
    "v_pk_mul_f32 v[232:233], v[232:233], v[166:167]\n"
    "v_pk_fma_f32 v[168:169], v[168:169], s[70:71], v[136:137]\n"
    "v_pk_fma_f32 v[230:231], v[230:231], v[168:169], v[232:233]\n"
    "v_pk_mul_f32 v[232:233], v[232:233], v[168:169]\n"
    "v_pk_fma_f32 v[170:171], v[170:171], s[70:71], v[138:139]\n"
    "v_pk_fma_f32 v[230:231], v[230:231], v[170:171], v[232:233]\n"
    "v_pk_mul_f32 v[232:233], v[232:233], v[170:171]\n"
    "v_pk_fma_f32 v[172:173], v[172:173], s[70:71], v[140:141]\n"
    "v_pk_fma_f32 v[230:231], v[230:231], v[172:173], v[232:233]\n"
    "v_pk_mul_f32 v[232:233], v[232:233], v[172:173]\n"
    "v_pk_fma_f32 v[174:175], v[174:175], s[70:71], v[142:143]\n"
    "v_pk_fma_f32 v[230:231], v[230:231], v[174:175], v[232:233]\n"
    "v_pk_mul_f32 v[232:233], v[232:233], v[174:175]\n"
    "v_rcp_f32 v232, v232\n"
    "v_rcp_f32 v233, v233\n"
    "v_fma_f32 v230, v230, v232, v247\n"
    "v_fmac_f32 v230, v231, v233\n"
    "v_mbcnt_lo_u32_b32 v232, -1, 0\n"
    "v_mbcnt_hi_u32_b32 v232, -1, v232\n"
    "v_bfe_u32 v233, v232, 3, 1\n"
    "v_and_b32 v232, 7, v232\n"
    "v_lshl_add_u32 v232, v232, 1, v233\n"
    "v_add_u32 v232, s81, v232\n"
    "ds_read_u8 v232, v232 offset:49664\n"
    "s_add_u32 s81, s81, 16\n"
    "s_waitcnt lgkmcnt(0)\n"
    "buffer_load_ushort v232, v232, s[44:47], s63 idxen\n"
    "s_waitcnt vmcnt(0)\n"
    "v_lshlrev_b32 v232, 16, v232\n"
    "v_mul_f32 v230, v230, v232\n"
    "s_setprio 0\n"
    "s_waitcnt lgkmcnt(0)\n"
    "ds_read_b64_tr_b16 v[208:209], v240 offset:2048\n"
    "ds_read_b64_tr_b16 v[210:211], v241 offset:2048\n"
    "ds_read_b64_tr_b16 v[212:213], v242 offset:2048\n"
    "ds_read_b64_tr_b16 v[214:215], v243 offset:2048\n"
    "ds_read_u8 v236, v251 offset:0\n"
    "ds_read_u8 v238, v251 offset:1\n"
    "ds_write_b16_d16_hi v245, v230\n"
    "ds_read_b64 v[228:229], v246\n"
    "s_cmp_lt_u32 s34, s76\n"
    "s_cbranch_scc1 .Ldin_G0s_wait\n"
    ".Ldin_G0s_go:\n"
    "s_waitcnt vmcnt(2)\n"
    "ds_read_b128 v[192:195], v234 offset:4096\n"
    "ds_read_b128 v[196:199], v235 offset:4096\n"
    "s_waitcnt lgkmcnt(4)\n"
    "buffer_load_dwordx4 v[236:237], s[40:43], s67 idxen offen offset:0 nt lds\n"
    "buffer_load_dwordx4 v[238:239], s[40:43], s67 idxen offen offset:1024 nt lds\n"
    "s_sub_u32 s94, s34, 30\n"
    "s_max_u32 s76, s76, s94\n"
    "s_add_u32 s76, s76, s72\n"
    "s_cmp_eq_u32 s66, s80\n"
    "s_cselect_b64 s[66:67], s[64:65], s[66:67]\n"
    "s_waitcnt lgkmcnt(2)\n"
    "s_cmp_eq_u32 s66, s82\n"
    "s_cbranch_scc1 .Ldin_L1s\n"
    "s_branch .Ldin_G1s\n"
    ".Ldin_A0s_poll:\n"
    "s_sleep 1\n"
    "ds_read_b64 v[216:217], v246 offset:128\n"
    "s_waitcnt lgkmcnt(0)\n"
    "v_readlane_b32 s60, v216, s59\n"
    "v_readlane_b32 s61, v217, s59\n"
    "s_cmp_eq_u32 s60, -1\n"
    "s_cbranch_scc1 .Ldin_A0s_poll\n"
    "s_branch .Ldin_A0s_pr\n"
    ".Ldin_A0s_skip:\n"
    "s_add_u32 s8, s8, 6144\n"
    "s_sub_u32 s68, s68, 2048\n"
    "s_mov_b32 s9, 48\n"
    "s_sub_u32 s82, s82, 16\n"
    "s_sub_u32 s66, s66, 16\n"
    "s_sub_u32 s76, s76, s72\n"
    "s_waitcnt lgkmcnt(0)\n"
    "s_branch .Ldin_A0s_skr\n"
    ".Ldin_G1s_slp:\n"
    "s_sleep 1\n"
    ".Ldin_G1s_wait:\n"
    "s_memrealtime s[34:35]\n"
    "s_waitcnt lgkmcnt(0)\n"
    "s_sub_u32 s94, s34, s76\n"
    "s_cmp_lt_i32 s94, 0\n"
    "s_cbranch_scc1 .Ldin_G1s_slp\n"
    "s_getreg_b32 s94, hwreg(HW_REG_IB_STS, 0, 4)\n"
    "s_cmp_gt_u32 s94, 2\n"
    "s_addc_u32 s15, s15, 0\n"
    "s_branch .Ldin_G1s_go\n"
    ".Ldin_G2s_slp:\n"
    "s_sleep 1\n"
    ".Ldin_G2s_wait:\n"
    "s_memrealtime s[34:35]\n"
    "s_waitcnt lgkmcnt(0)\n"
    "s_sub_u32 s94, s34, s76\n"
    "s_cmp_lt_i32 s94, 0\n"
    "s_cbranch_scc1 .Ldin_G2s_slp\n"
    "s_getreg_b32 s94, hwreg(HW_REG_IB_STS, 0, 4)\n"
    "s_cmp_gt_u32 s94, 2\n"
    "s_addc_u32 s15, s15, 0\n"
    "s_branch .Ldin_G2s_go\n"
    ".Ldin_G3s_slp:\n"
    "s_sleep 1\n"
    ".Ldin_G3s_wait:\n"
    "s_memrealtime s[34:35]\n"
    "s_waitcnt lgkmcnt(0)\n"
    "s_sub_u32 s94, s34, s76\n"
    "s_cmp_lt_i32 s94, 0\n"
    "s_cbranch_scc1 .Ldin_G3s_slp\n"
    "s_getreg_b32 s94, hwreg(HW_REG_IB_STS, 0, 4)\n"
    "s_cmp_gt_u32 s94, 2\n"
    "s_addc_u32 s15, s15, 0\n"
    "s_branch .Ldin_G3s_go\n"
    ".Ldin_G0s_slp:\n"
    "s_sleep 1\n"
    ".Ldin_G0s_wait:\n"
    "s_memrealtime s[34:35]\n"
    "s_waitcnt lgkmcnt(0)\n"
    "s_sub_u32 s94, s34, s76\n"
    "s_cmp_lt_i32 s94, 0\n"
    "s_cbranch_scc1 .Ldin_G0s_slp\n"
    "s_getreg_b32 s94, hwreg(HW_REG_IB_STS, 0, 4)\n"
    "s_cmp_gt_u32 s94, 2\n"
    "s_addc_u32 s15, s15, 0\n"
    "s_branch .Ldin_G0s_go\n"
    ".p2align 5\n"
    ".Ldin_L2s:\n"
    "s_cmp_lt_i32 s60, 0\n"
    "s_cbranch_scc1 .Ldin_E0s\n"
    "ds_read_b128 v[144:147], v252 offset:8192\n"
    "ds_read_b128 v[148:151], v252 offset:8256\n"
    "ds_read_b128 v[152:155], v252 offset:16384\n"
    "ds_read_b128 v[156:159], v252 offset:16448\n"
    "v_add_u32 v251, s66, v244\n"
    "v_and_b32 v216, v248, v216\n"
    "v_and_b32 v217, v249, v217\n"
    "v_and_b32 v218, v248, v218\n"
    "v_and_b32 v219, v249, v219\n"
    "v_mfma_f32_4x4x4_16b_bf16 v[176:179], v[216:217], v[96:97], v[32:35]\n"
    "v_and_b32 v220, v248, v220\n"
    "v_and_b32 v221, v249, v221\n"
    "v_mfma_f32_4x4x4_16b_bf16 v[180:183], v[218:219], v[98:99], v[36:39]\n"
    "v_and_b32 v222, v248, v222\n"
    "v_and_b32_e64 v223, v249, v223\n"
    "v_mfma_f32_4x4x4_16b_bf16 v[184:187], v[220:221], v[100:101], v[40:43]\n"
    "v_cvt_pk_bf16_f32 v0, v176, v177\n"
    "v_mfma_f32_4x4x4_16b_bf16 v[224:227], v[228:229], v[208:209], v[224:227] cbsz:4 abid:0\n"
    "v_cvt_pk_bf16_f32 v1, v178, v179\n"
    "v_mfma_f32_4x4x4_16b_bf16 v[188:191], v[222:223], v[102:103], v[44:47]\n"
    "v_cvt_pk_bf16_f32 v2, v180, v181\n"
    "v_cvt_pk_bf16_f32 v3, v182, v183\n"
    "v_mfma_f32_4x4x4_16b_bf16 v[176:179], v[216:217], v[104:105], v[48:51]\n"
    "v_cvt_pk_bf16_f32 v4, v184, v185\n"
    "v_cvt_pk_bf16_f32 v5, v186, v187\n"
    "v_mfma_f32_4x4x4_16b_bf16 v[180:183], v[218:219], v[106:107], v[52:55]\n"
    "v_cvt_pk_bf16_f32 v6, v188, v189\n"
    "v_mfma_f32_4x4x4_16b_bf16 v[224:227], v[228:229], v[210:211], v[224:227] cbsz:4 abid:1\n"
    "v_cvt_pk_bf16_f32 v7, v190, v191\n"
    "v_mfma_f32_4x4x4_16b_bf16 v[184:187], v[220:221], v[108:109], v[56:59]\n"
    "v_cvt_pk_bf16_f32 v8, v176, v177\n"
    "v_cvt_pk_bf16_f32 v9, v178, v179\n"
    "v_mfma_f32_4x4x4_16b_bf16 v[188:191], v[222:223], v[110:111], v[60:63]\n"
    "v_cvt_pk_bf16_f32 v10, v180, v181\n"
    "v_cvt_pk_bf16_f32 v11, v182, v183\n"
    "v_mfma_f32_4x4x4_16b_bf16 v[176:179], v[216:217], v[112:113], v[64:67]\n"
    "v_cvt_pk_bf16_f32 v12, v184, v185\n"
    "v_mfma_f32_4x4x4_16b_bf16 v[224:227], v[228:229], v[212:213], v[224:227] cbsz:4 abid:2\n"
    "v_cvt_pk_bf16_f32 v13, v186, v187\n"
    "s_setprio 1\n"
    "s_waitcnt lgkmcnt(0)\n"
    "v_mfma_f32_4x4x4_16b_bf16 v[180:183], v[218:219], v[114:115], v[68:71]\n"
    "v_cvt_pk_bf16_f32 v14, v188, v189\n"
    "v_cvt_pk_bf16_f32 v15, v190, v191\n"
    "v_mfma_f32_4x4x4_16b_bf16 v[184:187], v[220:221], v[116:117], v[72:75]\n"
    "v_cvt_pk_bf16_f32 v16, v176, v177\n"
    "v_cvt_pk_bf16_f32 v17, v178, v179\n"
    "v_mfma_f32_4x4x4_16b_bf16 v[188:191], v[222:223], v[118:119], v[76:79]\n"
    "v_cvt_pk_bf16_f32 v18, v180, v181\n"
    "v_mfma_f32_4x4x4_16b_bf16 v[224:227], v[228:229], v[214:215], v[224:227] cbsz:4 abid:3\n"
    "v_cvt_pk_bf16_f32 v19, v182, v183\n"
    "v_mfma_f32_4x4x4_16b_bf16 v[176:179], v[216:217], v[120:121], v[80:83]\n"
    "v_cvt_pk_bf16_f32 v20, v184, v185\n"
    "v_cvt_pk_bf16_f32 v21, v186, v187\n"
    "v_mfma_f32_4x4x4_16b_bf16 v[180:183], v[218:219], v[122:123], v[84:87]\n"
    "v_cvt_pk_bf16_f32 v22, v188, v189\n"
    "v_cvt_pk_bf16_f32 v23, v190, v191\n"
    "v_mfma_f32_4x4x4_16b_bf16 v[184:187], v[220:221], v[124:125], v[88:91]\n"
    "v_cvt_pk_bf16_f32 v24, v176, v177\n"
    "v_cvt_pk_bf16_f32 v25, v178, v179\n"
    "v_mfma_f32_4x4x4_16b_bf16 v[188:191], v[222:223], v[126:127], v[92:95]\n"
    "v_cvt_pk_bf16_f32 v26, v180, v181\n"
    "v_cvt_pk_bf16_f32 v27, v182, v183\n"
    "v_cvt_pk_bf16_f32 v28, v184, v185\n"
    "v_cvt_pk_bf16_f32 v29, v186, v187\n"
    "v_mfma_f32_16x16x32_bf16 v[176:179], v[0:3], v[192:195], v[144:147]\n"
    "s_add_u32 m0, s68, 6144\n"
    "v_cvt_pk_bf16_f32 v30, v188, v189\n"
    "v_cvt_pk_bf16_f32 v31, v190, v191\n"
    "v_exp_f32 v160, v160 clamp\n"
    "v_mfma_f32_16x16x32_bf16 v[176:179], v[4:7], v[196:199], v[176:179]\n"
    "s_add_u32 s66, s66, 16\n"
    "v_exp_f32 v161, v161 clamp\n"
    "v_exp_f32 v162, v162 clamp\n"
    "v_mfma_f32_16x16x32_bf16 v[180:183], v[8:11], v[192:195], v[148:151]\n"
    "s_add_u32 s76, s76, s73\n"
    "v_exp_f32 v163, v163 clamp\n"
    "v_exp_f32 v164, v164 clamp\n"
    "v_mfma_f32_16x16x32_bf16 v[180:183], v[12:15], v[196:199], v[180:183]\n"
    "s_lshl1_add_u32 s76, s72, s76\n"
    "v_exp_f32 v165, v165 clamp\n"
    "v_exp_f32 v166, v166 clamp\n"
    "v_mfma_f32_16x16x32_bf16 v[184:187], v[16:19], v[192:195], v[152:155]\n"
    "s_mov_b32 s80, s83\n"
    "v_exp_f32 v167, v167 clamp\n"
    "v_exp_f32 v168, v168 clamp\n"
    "v_mfma_f32_16x16x32_bf16 v[184:187], v[20:23], v[196:199], v[184:187]\n"
    "s_add_u32 s62, s62, 256\n"
    "v_exp_f32 v169, v169 clamp\n"
    "v_exp_f32 v170, v170 clamp\n"
    "v_mfma_f32_16x16x32_bf16 v[188:191], v[24:27], v[192:195], v[156:159]\n"
    "v_exp_f32 v171, v171 clamp\n"
    "v_exp_f32 v172, v172 clamp\n"
    "v_mfma_f32_16x16x32_bf16 v[188:191], v[28:31], v[196:199], v[188:191]\n"
    "v_exp_f32 v173, v173 clamp\n"
    "v_exp_f32 v174, v174 clamp\n"
    "v_exp_f32 v175, v175 clamp\n"
    "v_pk_fma_f32 v[160:161], v[160:161], s[70:71], v[128:129]\n"
    "v_pk_fma_f32 v[162:163], v[162:163], s[70:71], v[130:131]\n"
    "v_pk_add_f32 v[230:231], v[160:161], v[162:163]\n"
    "v_pk_mul_f32 v[232:233], v[160:161], v[162:163]\n"
    "v_pk_fma_f32 v[164:165], v[164:165], s[70:71], v[132:133]\n"
    "v_pk_fma_f32 v[230:231], v[230:231], v[164:165], v[232:233]\n"
    "v_pk_mul_f32 v[232:233], v[232:233], v[164:165]\n"
    "v_pk_fma_f32 v[166:167], v[166:167], s[70:71], v[134:135]\n"
    "v_pk_fma_f32 v[230:231], v[230:231], v[166:167], v[232:233]\n"
    "v_pk_mul_f32 v[232:233], v[232:233], v[166:167]\n"
    "v_pk_fma_f32 v[168:169], v[168:169], s[70:71], v[136:137]\n"
    "v_pk_fma_f32 v[230:231], v[230:231], v[168:169], v[232:233]\n"
    "v_pk_mul_f32 v[232:233], v[232:233], v[168:169]\n"
    "v_pk_fma_f32 v[170:171], v[170:171], s[70:71], v[138:139]\n"
    "v_pk_fma_f32 v[230:231], v[230:231], v[170:171], v[232:233]\n"
    "v_pk_mul_f32 v[232:233], v[232:233], v[170:171]\n"
    "v_pk_fma_f32 v[172:173], v[172:173], s[70:71], v[140:141]\n"
    "v_pk_fma_f32 v[230:231], v[230:231], v[172:173], v[232:233]\n"
    "v_pk_mul_f32 v[232:233], v[232:233], v[172:173]\n"
    "v_pk_fma_f32 v[174:175], v[174:175], s[70:71], v[142:143]\n"
    "v_pk_fma_f32 v[230:231], v[230:231], v[174:175], v[232:233]\n"
    "v_pk_mul_f32 v[232:233], v[232:233], v[174:175]\n"
    "v_rcp_f32 v232, v232\n"
    "v_rcp_f32 v233, v233\n"
    "v_fma_f32 v230, v230, v232, v247\n"
    "v_fmac_f32 v230, v231, v233\n"
    "v_mbcnt_lo_u32_b32 v232, -1, 0\n"
    "v_mbcnt_hi_u32_b32 v232, -1, v232\n"
    "v_bfe_u32 v233, v232, 3, 1\n"
    "v_and_b32 v232, 7, v232\n"
    "v_lshl_add_u32 v232, v232, 1, v233\n"
    "v_add_u32 v232, s81, v232\n"
    "ds_read_u8 v232, v232 offset:49664\n"
    "s_add_u32 s81, s81, 16\n"
    "s_waitcnt lgkmcnt(0)\n"
    "buffer_load_ushort v232, v232, s[44:47], s63 idxen\n"
    "s_waitcnt vmcnt(0)\n"
    "v_lshlrev_b32 v232, 16, v232\n"
    "v_mul_f32_e64 v230, v230, v232\n"
    "s_setprio 0\n"
    "ds_read_b64_tr_b16 v[208:209], v240 offset:6144\n"
    "ds_read_b64_tr_b16 v[210:211], v241 offset:6144\n"
    "ds_read_b64_tr_b16 v[212:213], v242 offset:6144\n"
    "ds_read_b64_tr_b16 v[214:215], v243 offset:6144\n"
    "ds_read_u8 v236, v251 offset:0\n"
    "ds_read_u8 v238, v251 offset:1\n"
    "ds_write_b16_d16_hi v245, v230\n"
    "ds_read_b64 v[228:229], v246\n"
    "ds_read_b64 v[216:217], v246 offset:128\n"
    "s_mov_b32 s81, s64\n"
    "s_lshr_b32 s63, s65, 6\n"
    "s_waitcnt vmcnt(2)\n"
    "ds_read_b128 v[192:195], v234 offset:0\n"
    "ds_read_b128 v[196:199], v235 offset:0\n"
    "s_waitcnt lgkmcnt(5)\n"
    "buffer_load_dwordx4 v[236:237], s[40:43], s67 idxen offen offset:0 nt lds\n"
    "buffer_load_dwordx4 v[238:239], s[40:43], s67 idxen offen offset:1024 nt lds\n"
    "s_waitcnt lgkmcnt(2)\n"
    "s_bitcmp1_b32 s60, 30\n"
    "s_cbranch_scc0 .Ldin_A3\n"
    ".Ldin_A3s:\n"
    "v_add_u32 v251, s66, v244\n"
    "v_readlane_b32 s60, v216, s59\n"
    "v_readlane_b32 s61, v217, s59\n"
    "v_mfma_f32_4x4x4_16b_bf16 v[224:227], v[228:229], v[200:201], v[224:227] cbsz:4 abid:0\n"
    "v_exp_f32 v176, v176 clamp\n"
    "v_exp_f32 v177, v177 clamp\n"
    "v_mfma_f32_4x4x4_16b_bf16 v[224:227], v[228:229], v[202:203], v[224:227] cbsz:4 abid:1\n"
    "v_exp_f32 v178, v178 clamp\n"
    "v_exp_f32 v179, v179 clamp\n"
    "v_mfma_f32_4x4x4_16b_bf16 v[224:227], v[228:229], v[204:205], v[224:227] cbsz:4 abid:2\n"
    "v_exp_f32 v180, v180 clamp\n"
    "v_exp_f32 v181, v181 clamp\n"
    "v_mfma_f32_4x4x4_16b_bf16 v[224:227], v[228:229], v[206:207], v[224:227] cbsz:4 abid:3\n"
    "s_waitcnt lgkmcnt(0)\n"
    "v_mfma_f32_16x16x32_bf16 v[160:163], v[0:3], v[192:195], v[144:147]\n"
    "s_setprio 1\n"
    "v_exp_f32 v182, v182 clamp\n"
    "v_mfma_f32_16x16x32_bf16 v[160:163], v[4:7], v[196:199], v[160:163]\n"
    "s_cmp_eq_u32 s60, -1\n"
    "v_exp_f32 v183, v183 clamp\n"
    "v_mfma_f32_16x16x32_bf16 v[164:167], v[8:11], v[192:195], v[148:151]\n"
    "s_cbranch_scc1 .Ldin_A3s_poll\n"
    ".Ldin_A3s_pr:\n"
    "v_exp_f32 v184, v184 clamp\n"
    "v_mfma_f32_16x16x32_bf16 v[164:167], v[12:15], v[196:199], v[164:167]\n"
    "s_lshr_b32 s94, s60, 12\n"
    "v_exp_f32 v185, v185 clamp\n"
    "v_mfma_f32_16x16x32_bf16 v[168:171], v[16:19], v[192:195], v[152:155]\n"
    "s_and_b32 s94, s94, 0x1ff0\n"
    "v_exp_f32 v186, v186 clamp\n"
    "v_mfma_f32_16x16x32_bf16 v[168:171], v[20:23], v[196:199], v[168:171]\n"
    "s_mov_b32 m0, s62\n"
    "v_exp_f32 v187, v187 clamp\n"
    "v_mfma_f32_16x16x32_bf16 v[172:175], v[24:27], v[192:195], v[156:159]\n"
    "s_add_u32 s66, s66, 16\n"
    "v_exp_f32 v188, v188 clamp\n"
    "v_mfma_f32_16x16x32_bf16 v[172:175], v[28:31], v[196:199], v[172:175]\n"
    "s_and_b32 s64, s60, 0xffff\n"
    "v_xor_b32 v252, s94, v250\n"
    "v_exp_f32 v189, v189 clamp\n"
    "v_exp_f32 v190, v190 clamp\n"
    "v_exp_f32 v191, v191 clamp\n"
    "v_pk_fma_f32 v[176:177], v[176:177], s[70:71], v[128:129]\n"
    "v_pk_fma_f32 v[178:179], v[178:179], s[70:71], v[130:131]\n"
    "v_pk_add_f32 v[230:231], v[176:177], v[178:179]\n"
    "v_pk_mul_f32 v[232:233], v[176:177], v[178:179]\n"
    "v_pk_add_f32 v[218:219], v[224:225], v[226:227]\n"
    "v_mov_b64 v[224:225], 0\n"
    "v_mov_b64 v[226:227], 0\n"
    "v_pk_fma_f32 v[180:181], v[180:181], s[70:71], v[132:133]\n"
    "v_pk_fma_f32 v[230:231], v[230:231], v[180:181], v[232:233]\n"
    "v_pk_mul_f32 v[232:233], v[232:233], v[180:181]\n"
    "v_pk_fma_f32 v[182:183], v[182:183], s[70:71], v[134:135]\n"
    "v_pk_fma_f32 v[230:231], v[230:231], v[182:183], v[232:233]\n"
    "v_pk_mul_f32 v[232:233], v[232:233], v[182:183]\n"
    "v_pk_fma_f32 v[184:185], v[184:185], s[70:71], v[136:137]\n"
    "v_pk_fma_f32 v[230:231], v[230:231], v[184:185], v[232:233]\n"
    "v_pk_mul_f32 v[232:233], v[232:233], v[184:185]\n"
    "v_pk_fma_f32 v[186:187], v[186:187], s[70:71], v[138:139]\n"
    "v_pk_fma_f32 v[230:231], v[230:231], v[186:187], v[232:233]\n"
    "v_pk_mul_f32 v[232:233], v[232:233], v[186:187]\n"
    "v_pk_fma_f32 v[188:189], v[188:189], s[70:71], v[140:141]\n"
    "v_pk_fma_f32 v[230:231], v[230:231], v[188:189], v[232:233]\n"
    "v_pk_mul_f32 v[232:233], v[232:233], v[188:189]\n"
    "v_pk_fma_f32 v[190:191], v[190:191], s[70:71], v[142:143]\n"
    "v_pk_fma_f32 v[230:231], v[230:231], v[190:191], v[232:233]\n"
    "v_pk_mul_f32 v[232:233], v[232:233], v[190:191]\n"
    "v_rcp_f32 v232, v232\n"
    "v_rcp_f32 v233, v233\n"
    "v_fma_f32 v230, v230, v232, v247\n"
    "v_fmac_f32 v230, v231, v233\n"
    "v_mbcnt_lo_u32_b32 v232, -1, 0\n"
    "v_mbcnt_hi_u32_b32 v232, -1, v232\n"
    "v_bfe_u32 v233, v232, 3, 1\n"
    "v_and_b32 v232, 7, v232\n"
    "v_lshl_add_u32 v232, v232, 1, v233\n"
    "v_add_u32 v232, s81, v232\n"
    "ds_read_u8 v232, v232 offset:49664\n"
    "s_add_u32 s81, s81, 16\n"
    "s_waitcnt lgkmcnt(0)\n"
    "buffer_load_ushort v232, v232, s[44:47], s63 idxen\n"
    "s_waitcnt vmcnt(0)\n"
    "v_lshlrev_b32 v232, 16, v232\n"
    "v_mul_f32 v230, v230, v232\n"
    "v_add_f32 v218, v218, v219\n"
    "s_setprio 0\n"
    "ds_read_b64_tr_b16 v[200:201], v240 offset:0\n"
    "ds_read_b64_tr_b16 v[202:203], v241 offset:0\n"
    "ds_read_b64_tr_b16 v[204:205], v242 offset:0\n"
    "ds_read_b64_tr_b16 v[206:207], v243 offset:0\n"
    "ds_read_u8 v236, v251 offset:0\n"
    "ds_read_u8 v238, v251 offset:1\n"
    "ds_write_b16_d16_hi v245, v230\n"
    "ds_read_b64 v[228:229], v246\n"
    "ds_write_addtid_b32 v218 offset:49152\n"
    "s_add_u32 m0, s8, 0\n"
    "s_bfe_u32 s94, s60, 0x30013\n"
    "s_add_u32 s58, s58, s57\n"
    "s_mul_i32 s94, s94, s56\n"
    "s_add_u32 s82, s64, s9\n"
    "s_add_u32 s65, s58, s94\n"
    "s_waitcnt vmcnt(2)\n"
    "ds_read_b128 v[192:195], v234 offset:2048\n"
    "ds_read_b128 v[196:199], v235 offset:2048\n"
    "ds_read_b128 v[216:219], v252 offset:0\n"
    "ds_read_b128 v[220:223], v252 offset:64\n"
    "s_mul_i32 s15, s15, s16\n"
    "s_cmp_lt_u32 s72, 49\n"
    "s_cselect_b32 s15, 0, s15\n"
    "s_add_u32 s14, s14, s15\n"
    "s_mov_b32 s15, 0\n"
    "s_min_u32 s14, s14, s17\n"
    "s_add_u32 s95, s61, s14\n"
    "s_sub_u32 s95, s95, s76\n"
    "s_lshr_b32 s94, s61, 16\n"
    "s_sext_i32_i16 s95, s95\n"
    "s_cmp_eq_u32 s59, s10\n"
    "s_cbranch_scc1 .Ldin_A3s_skip\n"
    "s_waitcnt lgkmcnt(7)\n"
    "buffer_load_dwordx4 v[236:237], s[40:43], s67 idxen offen offset:0 nt lds\n"
    "buffer_load_dwordx4 v[238:239], s[40:43], s67 idxen offen offset:1024 nt lds\n"
    ".Ldin_A3s_skr:\n"
    "s_mul_i32 s95, s95, s94\n"
    "s_bfe_u32 s94, s60, 0x50019\n"
    "s_ashr_i32 s95, s95, 16\n"
    "s_lshl4_add_u32 s83, s94, s64\n"
    "s_max_i32 s95, s95, -14\n"
    "s_add_u32 s59, s59, 1\n"
    "s_min_i32 s95, s95, 14\n"
    "s_add_u32 s72, s77, s95\n"
    "s_cmp_lt_i32 s60, 0\n"
    "s_cselect_b32 s72, 0, s72\n"
    "s_cmp_eq_u32 s66, s80\n"
    "s_cselect_b64 s[66:67], s[64:65], s[66:67]\n"
    "s_waitcnt lgkmcnt(5)\n"
    "s_branch .Ldin_G0s\n"
    ".Ldin_A3s_poll:\n"
    "s_sleep 1\n"
    "ds_read_b64 v[216:217], v246 offset:128\n"
    "s_waitcnt lgkmcnt(0)\n"
    "v_readlane_b32 s60, v216, s59\n"
    "v_readlane_b32 s61, v217, s59\n"
    "s_cmp_eq_u32 s60, -1\n"
    "s_cbranch_scc1 .Ldin_A3s_poll\n"
    "s_branch .Ldin_A3s_pr\n"
    ".Ldin_A3s_skip:\n"
    "s_add_u32 s8, s8, 6144\n"
    "s_sub_u32 s68, s68, 2048\n"
    "s_mov_b32 s9, 48\n"
    "s_sub_u32 s82, s82, 16\n"
    "s_sub_u32 s66, s66, 16\n"
    "s_sub_u32 s76, s76, s72\n"
    "s_waitcnt lgkmcnt(0)\n"
    "s_branch .Ldin_A3s_skr\n"
    ".p2align 5\n"
    ".Ldin_L1s:\n"
    "s_cmp_lt_i32 s60, 0\n"
    "s_cbranch_scc1 .Ldin_E1s\n"
    "ds_read_b128 v[144:147], v252 offset:8192\n"
    "ds_read_b128 v[148:151], v252 offset:8256\n"
    "ds_read_b128 v[152:155], v252 offset:16384\n"
    "ds_read_b128 v[156:159], v252 offset:16448\n"
    "v_add_u32 v251, s66, v244\n"
    "v_and_b32 v216, v248, v216\n"
    "v_and_b32 v217, v249, v217\n"
    "v_and_b32 v218, v248, v218\n"
    "v_and_b32 v219, v249, v219\n"
    "v_mfma_f32_4x4x4_16b_bf16 v[160:163], v[216:217], v[96:97], v[32:35]\n"
    "v_and_b32 v220, v248, v220\n"
    "v_and_b32 v221, v249, v221\n"
    "v_mfma_f32_4x4x4_16b_bf16 v[164:167], v[218:219], v[98:99], v[36:39]\n"
    "v_and_b32 v222, v248, v222\n"
    "v_and_b32_e64 v223, v249, v223\n"
    "v_mfma_f32_4x4x4_16b_bf16 v[168:171], v[220:221], v[100:101], v[40:43]\n"
    "v_cvt_pk_bf16_f32 v0, v160, v161\n"
    "v_mfma_f32_4x4x4_16b_bf16 v[224:227], v[228:229], v[200:201], v[224:227] cbsz:4 abid:0\n"
    "v_cvt_pk_bf16_f32 v1, v162, v163\n"
    "v_mfma_f32_4x4x4_16b_bf16 v[172:175], v[222:223], v[102:103], v[44:47]\n"
    "v_cvt_pk_bf16_f32 v2, v164, v165\n"
    "v_cvt_pk_bf16_f32 v3, v166, v167\n"
    "v_mfma_f32_4x4x4_16b_bf16 v[160:163], v[216:217], v[104:105], v[48:51]\n"
    "v_cvt_pk_bf16_f32 v4, v168, v169\n"
    "v_cvt_pk_bf16_f32 v5, v170, v171\n"
    "v_mfma_f32_4x4x4_16b_bf16 v[164:167], v[218:219], v[106:107], v[52:55]\n"
    "v_cvt_pk_bf16_f32 v6, v172, v173\n"
    "v_mfma_f32_4x4x4_16b_bf16 v[224:227], v[228:229], v[202:203], v[224:227] cbsz:4 abid:1\n"
    "v_cvt_pk_bf16_f32 v7, v174, v175\n"
    "v_mfma_f32_4x4x4_16b_bf16 v[168:171], v[220:221], v[108:109], v[56:59]\n"
    "v_cvt_pk_bf16_f32 v8, v160, v161\n"
    "v_cvt_pk_bf16_f32 v9, v162, v163\n"
    "v_mfma_f32_4x4x4_16b_bf16 v[172:175], v[222:223], v[110:111], v[60:63]\n"
    "v_cvt_pk_bf16_f32 v10, v164, v165\n"
    "v_cvt_pk_bf16_f32 v11, v166, v167\n"
    "v_mfma_f32_4x4x4_16b_bf16 v[160:163], v[216:217], v[112:113], v[64:67]\n"
    "v_cvt_pk_bf16_f32 v12, v168, v169\n"
    "v_mfma_f32_4x4x4_16b_bf16 v[224:227], v[228:229], v[204:205], v[224:227] cbsz:4 abid:2\n"
    "v_cvt_pk_bf16_f32 v13, v170, v171\n"
    "s_setprio 1\n"
    "s_waitcnt lgkmcnt(0)\n"
    "v_mfma_f32_4x4x4_16b_bf16 v[164:167], v[218:219], v[114:115], v[68:71]\n"
    "v_cvt_pk_bf16_f32 v14, v172, v173\n"
    "v_cvt_pk_bf16_f32 v15, v174, v175\n"
    "v_mfma_f32_4x4x4_16b_bf16 v[168:171], v[220:221], v[116:117], v[72:75]\n"
    "v_cvt_pk_bf16_f32 v16, v160, v161\n"
    "v_cvt_pk_bf16_f32 v17, v162, v163\n"
    "v_mfma_f32_4x4x4_16b_bf16 v[172:175], v[222:223], v[118:119], v[76:79]\n"
    "v_cvt_pk_bf16_f32 v18, v164, v165\n"
    "v_mfma_f32_4x4x4_16b_bf16 v[224:227], v[228:229], v[206:207], v[224:227] cbsz:4 abid:3\n"
    "v_cvt_pk_bf16_f32 v19, v166, v167\n"
    "v_mfma_f32_4x4x4_16b_bf16 v[160:163], v[216:217], v[120:121], v[80:83]\n"
    "v_cvt_pk_bf16_f32 v20, v168, v169\n"
    "v_cvt_pk_bf16_f32 v21, v170, v171\n"
    "v_mfma_f32_4x4x4_16b_bf16 v[164:167], v[218:219], v[122:123], v[84:87]\n"
    "v_cvt_pk_bf16_f32 v22, v172, v173\n"
    "v_cvt_pk_bf16_f32 v23, v174, v175\n"
    "v_mfma_f32_4x4x4_16b_bf16 v[168:171], v[220:221], v[124:125], v[88:91]\n"
    "v_cvt_pk_bf16_f32 v24, v160, v161\n"
    "v_cvt_pk_bf16_f32 v25, v162, v163\n"
    "v_mfma_f32_4x4x4_16b_bf16 v[172:175], v[222:223], v[126:127], v[92:95]\n"
    "v_cvt_pk_bf16_f32 v26, v164, v165\n"
    "v_cvt_pk_bf16_f32 v27, v166, v167\n"
    "v_cvt_pk_bf16_f32 v28, v168, v169\n"
    "v_cvt_pk_bf16_f32 v29, v170, v171\n"
    "v_mfma_f32_16x16x32_bf16 v[160:163], v[0:3], v[192:195], v[144:147]\n"
    "s_add_u32 m0, s68, 4096\n"
    "v_cvt_pk_bf16_f32 v30, v172, v173\n"
    "v_cvt_pk_bf16_f32 v31, v174, v175\n"
    "v_exp_f32 v176, v176 clamp\n"
    "v_mfma_f32_16x16x32_bf16 v[160:163], v[4:7], v[196:199], v[160:163]\n"
    "s_add_u32 s66, s66, 16\n"
    "v_exp_f32 v177, v177 clamp\n"
    "v_exp_f32 v178, v178 clamp\n"
    "v_mfma_f32_16x16x32_bf16 v[164:167], v[8:11], v[192:195], v[148:151]\n"
    "s_add_u32 s76, s76, s73\n"
    "v_exp_f32 v179, v179 clamp\n"
    "v_exp_f32 v180, v180 clamp\n"
    "v_mfma_f32_16x16x32_bf16 v[164:167], v[12:15], v[196:199], v[164:167]\n"
    "s_lshl1_add_u32 s76, s72, s76\n"
    "v_exp_f32 v181, v181 clamp\n"
    "v_exp_f32 v182, v182 clamp\n"
    "v_mfma_f32_16x16x32_bf16 v[168:171], v[16:19], v[192:195], v[152:155]\n"
    "s_mov_b32 s80, s83\n"
    "v_exp_f32 v183, v183 clamp\n"
    "v_exp_f32 v184, v184 clamp\n"
    "v_mfma_f32_16x16x32_bf16 v[168:171], v[20:23], v[196:199], v[168:171]\n"
    "s_add_u32 s62, s62, 256\n"
    "v_exp_f32 v185, v185 clamp\n"
    "v_exp_f32 v186, v186 clamp\n"
    "v_mfma_f32_16x16x32_bf16 v[172:175], v[24:27], v[192:195], v[156:159]\n"
    "v_exp_f32 v187, v187 clamp\n"
    "v_exp_f32 v188, v188 clamp\n"
    "v_mfma_f32_16x16x32_bf16 v[172:175], v[28:31], v[196:199], v[172:175]\n"
    "v_exp_f32 v189, v189 clamp\n"
    "v_exp_f32 v190, v190 clamp\n"
    "v_exp_f32 v191, v191 clamp\n"
    "v_pk_fma_f32 v[176:177], v[176:177], s[70:71], v[128:129]\n"
    "v_pk_fma_f32 v[178:179], v[178:179], s[70:71], v[130:131]\n"
    "v_pk_add_f32 v[230:231], v[176:177], v[178:179]\n"
    "v_pk_mul_f32 v[232:233], v[176:177], v[178:179]\n"
    "v_pk_fma_f32 v[180:181], v[180:181], s[70:71], v[132:133]\n"
    "v_pk_fma_f32 v[230:231], v[230:231], v[180:181], v[232:233]\n"
    "v_pk_mul_f32 v[232:233], v[232:233], v[180:181]\n"
    "v_pk_fma_f32 v[182:183], v[182:183], s[70:71], v[134:135]\n"
    "v_pk_fma_f32 v[230:231], v[230:231], v[182:183], v[232:233]\n"
    "v_pk_mul_f32 v[232:233], v[232:233], v[182:183]\n"
    "v_pk_fma_f32 v[184:185], v[184:185], s[70:71], v[136:137]\n"
    "v_pk_fma_f32 v[230:231], v[230:231], v[184:185], v[232:233]\n"
    "v_pk_mul_f32 v[232:233], v[232:233], v[184:185]\n"
    "v_pk_fma_f32 v[186:187], v[186:187], s[70:71], v[138:139]\n"
    "v_pk_fma_f32 v[230:231], v[230:231], v[186:187], v[232:233]\n"
    "v_pk_mul_f32 v[232:233], v[232:233], v[186:187]\n"
    "v_pk_fma_f32 v[188:189], v[188:189], s[70:71], v[140:141]\n"
    "v_pk_fma_f32 v[230:231], v[230:231], v[188:189], v[232:233]\n"
    "v_pk_mul_f32 v[232:233], v[232:233], v[188:189]\n"
    "v_pk_fma_f32 v[190:191], v[190:191], s[70:71], v[142:143]\n"
    "v_pk_fma_f32 v[230:231], v[230:231], v[190:191], v[232:233]\n"
    "v_pk_mul_f32 v[232:233], v[232:233], v[190:191]\n"
    "v_rcp_f32 v232, v232\n"
    "v_rcp_f32 v233, v233\n"
    "v_fma_f32 v230, v230, v232, v247\n"
    "v_fmac_f32 v230, v231, v233\n"
    "v_mbcnt_lo_u32_b32 v232, -1, 0\n"
    "v_mbcnt_hi_u32_b32 v232, -1, v232\n"
    "v_bfe_u32 v233, v232, 3, 1\n"
    "v_and_b32 v232, 7, v232\n"
    "v_lshl_add_u32 v232, v232, 1, v233\n"
    "v_add_u32 v232, s81, v232\n"
    "ds_read_u8 v232, v232 offset:49664\n"
    "s_add_u32 s81, s81, 16\n"
    "s_waitcnt lgkmcnt(0)\n"
    "buffer_load_ushort v232, v232, s[44:47], s63 idxen\n"
    "s_waitcnt vmcnt(0)\n"
    "v_lshlrev_b32 v232, 16, v232\n"
    "v_mul_f32_e64 v230, v230, v232\n"
    "s_setprio 0\n"
    "ds_read_b64_tr_b16 v[200:201], v240 offset:4096\n"
    "ds_read_b64_tr_b16 v[202:203], v241 offset:4096\n"
    "ds_read_b64_tr_b16 v[204:205], v242 offset:4096\n"
    "ds_read_b64_tr_b16 v[206:207], v243 offset:4096\n"
    "ds_read_u8 v236, v251 offset:0\n"
    "ds_read_u8 v238, v251 offset:1\n"
    "ds_write_b16_d16_hi v245, v230\n"
    "ds_read_b64 v[228:229], v246\n"
    "ds_read_b64 v[216:217], v246 offset:128\n"
    "s_mov_b32 s81, s64\n"
    "s_lshr_b32 s63, s65, 6\n"
    "s_waitcnt vmcnt(2)\n"
    "ds_read_b128 v[192:195], v234 offset:6144\n"
    "ds_read_b128 v[196:199], v235 offset:6144\n"
    "s_waitcnt lgkmcnt(5)\n"
    "buffer_load_dwordx4 v[236:237], s[40:43], s67 idxen offen offset:0 nt lds\n"
    "buffer_load_dwordx4 v[238:239], s[40:43], s67 idxen offen offset:1024 nt lds\n"
    "s_waitcnt lgkmcnt(2)\n"
    "s_bitcmp1_b32 s60, 30\n"
    "s_cbranch_scc0 .Ldin_A2\n"
    ".Ldin_A2s:\n"
    "v_add_u32 v251, s66, v244\n"
    "v_readlane_b32 s60, v216, s59\n"
    "v_readlane_b32 s61, v217, s59\n"
    "v_mfma_f32_4x4x4_16b_bf16 v[224:227], v[228:229], v[208:209], v[224:227] cbsz:4 abid:0\n"
    "v_exp_f32 v160, v160 clamp\n"
    "v_exp_f32 v161, v161 clamp\n"
    "v_mfma_f32_4x4x4_16b_bf16 v[224:227], v[228:229], v[210:211], v[224:227] cbsz:4 abid:1\n"
    "v_exp_f32 v162, v162 clamp\n"
    "v_exp_f32 v163, v163 clamp\n"
    "v_mfma_f32_4x4x4_16b_bf16 v[224:227], v[228:229], v[212:213], v[224:227] cbsz:4 abid:2\n"
    "v_exp_f32 v164, v164 clamp\n"
    "v_exp_f32 v165, v165 clamp\n"
    "v_mfma_f32_4x4x4_16b_bf16 v[224:227], v[228:229], v[214:215], v[224:227] cbsz:4 abid:3\n"
    "s_waitcnt lgkmcnt(0)\n"
    "v_mfma_f32_16x16x32_bf16 v[176:179], v[0:3], v[192:195], v[144:147]\n"
    "s_setprio 1\n"
    "v_exp_f32 v166, v166 clamp\n"
    "v_mfma_f32_16x16x32_bf16 v[176:179], v[4:7], v[196:199], v[176:179]\n"
    "s_cmp_eq_u32 s60, -1\n"
    "v_exp_f32 v167, v167 clamp\n"
    "v_mfma_f32_16x16x32_bf16 v[180:183], v[8:11], v[192:195], v[148:151]\n"
    "s_cbranch_scc1 .Ldin_A2s_poll\n"
    ".Ldin_A2s_pr:\n"
    "v_exp_f32 v168, v168 clamp\n"
    "v_mfma_f32_16x16x32_bf16 v[180:183], v[12:15], v[196:199], v[180:183]\n"
    "s_lshr_b32 s94, s60, 12\n"
    "v_exp_f32 v169, v169 clamp\n"
    "v_mfma_f32_16x16x32_bf16 v[184:187], v[16:19], v[192:195], v[152:155]\n"
    "s_and_b32 s94, s94, 0x1ff0\n"
    "v_exp_f32 v170, v170 clamp\n"
    "v_mfma_f32_16x16x32_bf16 v[184:187], v[20:23], v[196:199], v[184:187]\n"
    "s_mov_b32 m0, s62\n"
    "v_exp_f32 v171, v171 clamp\n"
    "v_mfma_f32_16x16x32_bf16 v[188:191], v[24:27], v[192:195], v[156:159]\n"
    "s_add_u32 s66, s66, 16\n"
    "v_exp_f32 v172, v172 clamp\n"
    "v_mfma_f32_16x16x32_bf16 v[188:191], v[28:31], v[196:199], v[188:191]\n"
    "s_and_b32 s64, s60, 0xffff\n"
    "v_xor_b32 v252, s94, v250\n"
    "v_exp_f32 v173, v173 clamp\n"
    "v_exp_f32 v174, v174 clamp\n"
    "v_exp_f32 v175, v175 clamp\n"
    "v_pk_fma_f32 v[160:161], v[160:161], s[70:71], v[128:129]\n"
    "v_pk_fma_f32 v[162:163], v[162:163], s[70:71], v[130:131]\n"
    "v_pk_add_f32 v[230:231], v[160:161], v[162:163]\n"
    "v_pk_mul_f32 v[232:233], v[160:161], v[162:163]\n"
    "v_pk_add_f32 v[218:219], v[224:225], v[226:227]\n"
    "v_mov_b64 v[224:225], 0\n"
    "v_mov_b64 v[226:227], 0\n"
    "v_pk_fma_f32 v[164:165], v[164:165], s[70:71], v[132:133]\n"
    "v_pk_fma_f32 v[230:231], v[230:231], v[164:165], v[232:233]\n"
    "v_pk_mul_f32 v[232:233], v[232:233], v[164:165]\n"
    "v_pk_fma_f32 v[166:167], v[166:167], s[70:71], v[134:135]\n"
    "v_pk_fma_f32 v[230:231], v[230:231], v[166:167], v[232:233]\n"
    "v_pk_mul_f32 v[232:233], v[232:233], v[166:167]\n"
    "v_pk_fma_f32 v[168:169], v[168:169], s[70:71], v[136:137]\n"
    "v_pk_fma_f32 v[230:231], v[230:231], v[168:169], v[232:233]\n"
    "v_pk_mul_f32 v[232:233], v[232:233], v[168:169]\n"
    "v_pk_fma_f32 v[170:171], v[170:171], s[70:71], v[138:139]\n"
    "v_pk_fma_f32 v[230:231], v[230:231], v[170:171], v[232:233]\n"
    "v_pk_mul_f32 v[232:233], v[232:233], v[170:171]\n"
    "v_pk_fma_f32 v[172:173], v[172:173], s[70:71], v[140:141]\n"
    "v_pk_fma_f32 v[230:231], v[230:231], v[172:173], v[232:233]\n"
    "v_pk_mul_f32 v[232:233], v[232:233], v[172:173]\n"
    "v_pk_fma_f32 v[174:175], v[174:175], s[70:71], v[142:143]\n"
    "v_pk_fma_f32 v[230:231], v[230:231], v[174:175], v[232:233]\n"
    "v_pk_mul_f32 v[232:233], v[232:233], v[174:175]\n"
    "v_rcp_f32 v232, v232\n"
    "v_rcp_f32 v233, v233\n"
    "v_fma_f32 v230, v230, v232, v247\n"
    "v_fmac_f32 v230, v231, v233\n"
    "v_mbcnt_lo_u32_b32 v232, -1, 0\n"
    "v_mbcnt_hi_u32_b32 v232, -1, v232\n"
    "v_bfe_u32 v233, v232, 3, 1\n"
    "v_and_b32 v232, 7, v232\n"
    "v_lshl_add_u32 v232, v232, 1, v233\n"
    "v_add_u32 v232, s81, v232\n"
    "ds_read_u8 v232, v232 offset:49664\n"
    "s_add_u32 s81, s81, 16\n"
    "s_waitcnt lgkmcnt(0)\n"
    "buffer_load_ushort v232, v232, s[44:47], s63 idxen\n"
    "s_waitcnt vmcnt(0)\n"
    "v_lshlrev_b32 v232, 16, v232\n"
    "v_mul_f32 v230, v230, v232\n"
    "v_add_f32 v218, v218, v219\n"
    "s_setprio 0\n"
    "ds_read_b64_tr_b16 v[208:209], v240 offset:6144\n"
    "ds_read_b64_tr_b16 v[210:211], v241 offset:6144\n"
    "ds_read_b64_tr_b16 v[212:213], v242 offset:6144\n"
    "ds_read_b64_tr_b16 v[214:215], v243 offset:6144\n"
    "ds_read_u8 v236, v251 offset:0\n"
    "ds_read_u8 v238, v251 offset:1\n"
    "ds_write_b16_d16_hi v245, v230\n"
    "ds_read_b64 v[228:229], v246\n"
    "ds_write_addtid_b32 v218 offset:49152\n"
    "s_add_u32 m0, s68, 6144\n"
    "s_bfe_u32 s94, s60, 0x30013\n"
    "s_add_u32 s58, s58, s57\n"
    "s_mul_i32 s94, s94, s56\n"
    "s_add_u32 s82, s64, s9\n"
    "s_add_u32 s65, s58, s94\n"
    "s_waitcnt vmcnt(2)\n"
    "ds_read_b128 v[192:195], v234 offset:0\n"
    "ds_read_b128 v[196:199], v235 offset:0\n"
    "ds_read_b128 v[216:219], v252 offset:0\n"
    "ds_read_b128 v[220:223], v252 offset:64\n"
    "s_mul_i32 s15, s15, s16\n"
    "s_cmp_lt_u32 s72, 49\n"
    "s_cselect_b32 s15, 0, s15\n"
    "s_add_u32 s14, s14, s15\n"
    "s_mov_b32 s15, 0\n"
    "s_min_u32 s14, s14, s17\n"
    "s_add_u32 s95, s61, s14\n"
    "s_sub_u32 s95, s95, s76\n"
    "s_lshr_b32 s94, s61, 16\n"
    "s_sext_i32_i16 s95, s95\n"
    "s_cmp_eq_u32 s59, s10\n"
    "s_cbranch_scc1 .Ldin_A2s_skip\n"
    "s_waitcnt lgkmcnt(7)\n"
    "buffer_load_dwordx4 v[236:237], s[40:43], s67 idxen offen offset:0 nt lds\n"
    "buffer_load_dwordx4 v[238:239], s[40:43], s67 idxen offen offset:1024 nt lds\n"
    ".Ldin_A2s_skr:\n"
    "s_mul_i32 s95, s95, s94\n"
    "s_bfe_u32 s94, s60, 0x50019\n"
    "s_ashr_i32 s95, s95, 16\n"
    "s_lshl4_add_u32 s83, s94, s64\n"
    "s_max_i32 s95, s95, -14\n"
    "s_add_u32 s59, s59, 1\n"
    "s_min_i32 s95, s95, 14\n"
    "s_add_u32 s72, s77, s95\n"
    "s_cmp_lt_i32 s60, 0\n"
    "s_cselect_b32 s72, 0, s72\n"
    "s_cmp_eq_u32 s66, s80\n"
    "s_cselect_b64 s[66:67], s[64:65], s[66:67]\n"
    "s_waitcnt lgkmcnt(5)\n"
    "s_branch .Ldin_G3s\n"
    ".Ldin_A2s_poll:\n"
    "s_sleep 1\n"
    "ds_read_b64 v[216:217], v246 offset:128\n"
    "s_waitcnt lgkmcnt(0)\n"
    "v_readlane_b32 s60, v216, s59\n"
    "v_readlane_b32 s61, v217, s59\n"
    "s_cmp_eq_u32 s60, -1\n"
    "s_cbranch_scc1 .Ldin_A2s_poll\n"
    "s_branch .Ldin_A2s_pr\n"
    ".Ldin_A2s_skip:\n"
    "s_add_u32 s8, s8, 6144\n"
    "s_sub_u32 s68, s68, 2048\n"
    "s_mov_b32 s9, 48\n"
    "s_sub_u32 s82, s82, 16\n"
    "s_sub_u32 s66, s66, 16\n"
    "s_sub_u32 s76, s76, s72\n"
    "s_waitcnt lgkmcnt(0)\n"
    "s_branch .Ldin_A2s_skr\n"
    ".p2align 5\n"
    ".Ldin_L0s:\n"
    "s_cmp_lt_i32 s60, 0\n"
    "s_cbranch_scc1 .Ldin_E0s\n"
    "ds_read_b128 v[144:147], v252 offset:8192\n"
    "ds_read_b128 v[148:151], v252 offset:8256\n"
    "ds_read_b128 v[152:155], v252 offset:16384\n"
    "ds_read_b128 v[156:159], v252 offset:16448\n"
    "v_add_u32 v251, s66, v244\n"
    "v_and_b32 v216, v248, v216\n"
    "v_and_b32 v217, v249, v217\n"
    "v_and_b32 v218, v248, v218\n"
    "v_and_b32 v219, v249, v219\n"
    "v_mfma_f32_4x4x4_16b_bf16 v[176:179], v[216:217], v[96:97], v[32:35]\n"
    "v_and_b32 v220, v248, v220\n"
    "v_and_b32 v221, v249, v221\n"
    "v_mfma_f32_4x4x4_16b_bf16 v[180:183], v[218:219], v[98:99], v[36:39]\n"
    "v_and_b32 v222, v248, v222\n"
    "v_and_b32_e64 v223, v249, v223\n"
    "v_mfma_f32_4x4x4_16b_bf16 v[184:187], v[220:221], v[100:101], v[40:43]\n"
    "v_cvt_pk_bf16_f32 v0, v176, v177\n"
    "v_mfma_f32_4x4x4_16b_bf16 v[224:227], v[228:229], v[208:209], v[224:227] cbsz:4 abid:0\n"
    "v_cvt_pk_bf16_f32 v1, v178, v179\n"
    "v_mfma_f32_4x4x4_16b_bf16 v[188:191], v[222:223], v[102:103], v[44:47]\n"
    "v_cvt_pk_bf16_f32 v2, v180, v181\n"
    "v_cvt_pk_bf16_f32 v3, v182, v183\n"
    "v_mfma_f32_4x4x4_16b_bf16 v[176:179], v[216:217], v[104:105], v[48:51]\n"
    "v_cvt_pk_bf16_f32 v4, v184, v185\n"
    "v_cvt_pk_bf16_f32 v5, v186, v187\n"
    "v_mfma_f32_4x4x4_16b_bf16 v[180:183], v[218:219], v[106:107], v[52:55]\n"
    "v_cvt_pk_bf16_f32 v6, v188, v189\n"
    "v_mfma_f32_4x4x4_16b_bf16 v[224:227], v[228:229], v[210:211], v[224:227] cbsz:4 abid:1\n"
    "v_cvt_pk_bf16_f32 v7, v190, v191\n"
    "v_mfma_f32_4x4x4_16b_bf16 v[184:187], v[220:221], v[108:109], v[56:59]\n"
    "v_cvt_pk_bf16_f32 v8, v176, v177\n"
    "v_cvt_pk_bf16_f32 v9, v178, v179\n"
    "v_mfma_f32_4x4x4_16b_bf16 v[188:191], v[222:223], v[110:111], v[60:63]\n"
    "v_cvt_pk_bf16_f32 v10, v180, v181\n"
    "v_cvt_pk_bf16_f32 v11, v182, v183\n"
    "v_mfma_f32_4x4x4_16b_bf16 v[176:179], v[216:217], v[112:113], v[64:67]\n"
    "v_cvt_pk_bf16_f32 v12, v184, v185\n"
    "v_mfma_f32_4x4x4_16b_bf16 v[224:227], v[228:229], v[212:213], v[224:227] cbsz:4 abid:2\n"
    "v_cvt_pk_bf16_f32 v13, v186, v187\n"
    "s_setprio 1\n"
    "s_waitcnt lgkmcnt(0)\n"
    "v_mfma_f32_4x4x4_16b_bf16 v[180:183], v[218:219], v[114:115], v[68:71]\n"
    "v_cvt_pk_bf16_f32 v14, v188, v189\n"
    "v_cvt_pk_bf16_f32 v15, v190, v191\n"
    "v_mfma_f32_4x4x4_16b_bf16 v[184:187], v[220:221], v[116:117], v[72:75]\n"
    "v_cvt_pk_bf16_f32 v16, v176, v177\n"
    "v_cvt_pk_bf16_f32 v17, v178, v179\n"
    "v_mfma_f32_4x4x4_16b_bf16 v[188:191], v[222:223], v[118:119], v[76:79]\n"
    "v_cvt_pk_bf16_f32 v18, v180, v181\n"
    "v_mfma_f32_4x4x4_16b_bf16 v[224:227], v[228:229], v[214:215], v[224:227] cbsz:4 abid:3\n"
    "v_cvt_pk_bf16_f32 v19, v182, v183\n"
    "v_mfma_f32_4x4x4_16b_bf16 v[176:179], v[216:217], v[120:121], v[80:83]\n"
    "v_cvt_pk_bf16_f32 v20, v184, v185\n"
    "v_cvt_pk_bf16_f32 v21, v186, v187\n"
    "v_mfma_f32_4x4x4_16b_bf16 v[180:183], v[218:219], v[122:123], v[84:87]\n"
    "v_cvt_pk_bf16_f32 v22, v188, v189\n"
    "v_cvt_pk_bf16_f32 v23, v190, v191\n"
    "v_mfma_f32_4x4x4_16b_bf16 v[184:187], v[220:221], v[124:125], v[88:91]\n"
    "v_cvt_pk_bf16_f32 v24, v176, v177\n"
    "v_cvt_pk_bf16_f32 v25, v178, v179\n"
    "v_mfma_f32_4x4x4_16b_bf16 v[188:191], v[222:223], v[126:127], v[92:95]\n"
    "v_cvt_pk_bf16_f32 v26, v180, v181\n"
    "v_cvt_pk_bf16_f32 v27, v182, v183\n"
    "v_cvt_pk_bf16_f32 v28, v184, v185\n"
    "v_cvt_pk_bf16_f32 v29, v186, v187\n"
    "v_mfma_f32_16x16x32_bf16 v[176:179], v[0:3], v[192:195], v[144:147]\n"
    "s_add_u32 m0, s68, 2048\n"
    "v_cvt_pk_bf16_f32 v30, v188, v189\n"
    "v_cvt_pk_bf16_f32 v31, v190, v191\n"
    "v_exp_f32 v160, v160 clamp\n"
    "v_mfma_f32_16x16x32_bf16 v[176:179], v[4:7], v[196:199], v[176:179]\n"
    "s_add_u32 s66, s66, 16\n"
    "v_exp_f32 v161, v161 clamp\n"
    "v_exp_f32 v162, v162 clamp\n"
    "v_mfma_f32_16x16x32_bf16 v[180:183], v[8:11], v[192:195], v[148:151]\n"
    "s_add_u32 s76, s76, s73\n"
    "v_exp_f32 v163, v163 clamp\n"
    "v_exp_f32 v164, v164 clamp\n"
    "v_mfma_f32_16x16x32_bf16 v[180:183], v[12:15], v[196:199], v[180:183]\n"
    "s_lshl1_add_u32 s76, s72, s76\n"
    "v_exp_f32 v165, v165 clamp\n"
    "v_exp_f32 v166, v166 clamp\n"
    "v_mfma_f32_16x16x32_bf16 v[184:187], v[16:19], v[192:195], v[152:155]\n"
    "s_mov_b32 s80, s83\n"
    "v_exp_f32 v167, v167 clamp\n"
    "v_exp_f32 v168, v168 clamp\n"
    "v_mfma_f32_16x16x32_bf16 v[184:187], v[20:23], v[196:199], v[184:187]\n"
    "s_add_u32 s62, s62, 256\n"
    "v_exp_f32 v169, v169 clamp\n"
    "v_exp_f32 v170, v170 clamp\n"
    "v_mfma_f32_16x16x32_bf16 v[188:191], v[24:27], v[192:195], v[156:159]\n"
    "v_exp_f32 v171, v171 clamp\n"
    "v_exp_f32 v172, v172 clamp\n"
    "v_mfma_f32_16x16x32_bf16 v[188:191], v[28:31], v[196:199], v[188:191]\n"
    "v_exp_f32 v173, v173 clamp\n"
    "v_exp_f32 v174, v174 clamp\n"
    "v_exp_f32 v175, v175 clamp\n"
    "v_pk_fma_f32 v[160:161], v[160:161], s[70:71], v[128:129]\n"
    "v_pk_fma_f32 v[162:163], v[162:163], s[70:71], v[130:131]\n"
    "v_pk_add_f32 v[230:231], v[160:161], v[162:163]\n"
    "v_pk_mul_f32 v[232:233], v[160:161], v[162:163]\n"
    "v_pk_fma_f32 v[164:165], v[164:165], s[70:71], v[132:133]\n"
    "v_pk_fma_f32 v[230:231], v[230:231], v[164:165], v[232:233]\n"
    "v_pk_mul_f32 v[232:233], v[232:233], v[164:165]\n"
    "v_pk_fma_f32 v[166:167], v[166:167], s[70:71], v[134:135]\n"
    "v_pk_fma_f32 v[230:231], v[230:231], v[166:167], v[232:233]\n"
    "v_pk_mul_f32 v[232:233], v[232:233], v[166:167]\n"
    "v_pk_fma_f32 v[168:169], v[168:169], s[70:71], v[136:137]\n"
    "v_pk_fma_f32 v[230:231], v[230:231], v[168:169], v[232:233]\n"
    "v_pk_mul_f32 v[232:233], v[232:233], v[168:169]\n"
    "v_pk_fma_f32 v[170:171], v[170:171], s[70:71], v[138:139]\n"
    "v_pk_fma_f32 v[230:231], v[230:231], v[170:171], v[232:233]\n"
    "v_pk_mul_f32 v[232:233], v[232:233], v[170:171]\n"
    "v_pk_fma_f32 v[172:173], v[172:173], s[70:71], v[140:141]\n"
    "v_pk_fma_f32 v[230:231], v[230:231], v[172:173], v[232:233]\n"
    "v_pk_mul_f32 v[232:233], v[232:233], v[172:173]\n"
    "v_pk_fma_f32 v[174:175], v[174:175], s[70:71], v[142:143]\n"
    "v_pk_fma_f32 v[230:231], v[230:231], v[174:175], v[232:233]\n"
    "v_pk_mul_f32 v[232:233], v[232:233], v[174:175]\n"
    "v_rcp_f32 v232, v232\n"
    "v_rcp_f32 v233, v233\n"
    "v_fma_f32 v230, v230, v232, v247\n"
    "v_fmac_f32 v230, v231, v233\n"
    "v_mbcnt_lo_u32_b32 v232, -1, 0\n"
    "v_mbcnt_hi_u32_b32 v232, -1, v232\n"
    "v_bfe_u32 v233, v232, 3, 1\n"
    "v_and_b32 v232, 7, v232\n"
    "v_lshl_add_u32 v232, v232, 1, v233\n"
    "v_add_u32 v232, s81, v232\n"
    "ds_read_u8 v232, v232 offset:49664\n"
    "s_add_u32 s81, s81, 16\n"
    "s_waitcnt lgkmcnt(0)\n"
    "buffer_load_ushort v232, v232, s[44:47], s63 idxen\n"
    "s_waitcnt vmcnt(0)\n"
    "v_lshlrev_b32 v232, 16, v232\n"
    "v_mul_f32_e64 v230, v230, v232\n"
    "s_setprio 0\n"
    "ds_read_b64_tr_b16 v[208:209], v240 offset:2048\n"
    "ds_read_b64_tr_b16 v[210:211], v241 offset:2048\n"
    "ds_read_b64_tr_b16 v[212:213], v242 offset:2048\n"
    "ds_read_b64_tr_b16 v[214:215], v243 offset:2048\n"
    "ds_read_u8 v236, v251 offset:0\n"
    "ds_read_u8 v238, v251 offset:1\n"
    "ds_write_b16_d16_hi v245, v230\n"
    "ds_read_b64 v[228:229], v246\n"
    "ds_read_b64 v[216:217], v246 offset:128\n"
    "s_mov_b32 s81, s64\n"
    "s_lshr_b32 s63, s65, 6\n"
    "s_waitcnt vmcnt(2)\n"
    "ds_read_b128 v[192:195], v234 offset:4096\n"
    "ds_read_b128 v[196:199], v235 offset:4096\n"
    "s_waitcnt lgkmcnt(5)\n"
    "buffer_load_dwordx4 v[236:237], s[40:43], s67 idxen offen offset:0 nt lds\n"
    "buffer_load_dwordx4 v[238:239], s[40:43], s67 idxen offen offset:1024 nt lds\n"
    "s_waitcnt lgkmcnt(2)\n"
    "s_bitcmp1_b32 s60, 30\n"
    "s_cbranch_scc0 .Ldin_A1\n"
    ".Ldin_A1s:\n"
    "v_add_u32 v251, s66, v244\n"
    "v_readlane_b32 s60, v216, s59\n"
    "v_readlane_b32 s61, v217, s59\n"
    "v_mfma_f32_4x4x4_16b_bf16 v[224:227], v[228:229], v[200:201], v[224:227] cbsz:4 abid:0\n"
    "v_exp_f32 v176, v176 clamp\n"
    "v_exp_f32 v177, v177 clamp\n"
    "v_mfma_f32_4x4x4_16b_bf16 v[224:227], v[228:229], v[202:203], v[224:227] cbsz:4 abid:1\n"
    "v_exp_f32 v178, v178 clamp\n"
    "v_exp_f32 v179, v179 clamp\n"
    "v_mfma_f32_4x4x4_16b_bf16 v[224:227], v[228:229], v[204:205], v[224:227] cbsz:4 abid:2\n"
    "v_exp_f32 v180, v180 clamp\n"
    "v_exp_f32 v181, v181 clamp\n"
    "v_mfma_f32_4x4x4_16b_bf16 v[224:227], v[228:229], v[206:207], v[224:227] cbsz:4 abid:3\n"
    "s_waitcnt lgkmcnt(0)\n"
    "v_mfma_f32_16x16x32_bf16 v[160:163], v[0:3], v[192:195], v[144:147]\n"
    "s_setprio 1\n"
    "v_exp_f32 v182, v182 clamp\n"
    "v_mfma_f32_16x16x32_bf16 v[160:163], v[4:7], v[196:199], v[160:163]\n"
    "s_cmp_eq_u32 s60, -1\n"
    "v_exp_f32 v183, v183 clamp\n"
    "v_mfma_f32_16x16x32_bf16 v[164:167], v[8:11], v[192:195], v[148:151]\n"
    "s_cbranch_scc1 .Ldin_A1s_poll\n"
    ".Ldin_A1s_pr:\n"
    "v_exp_f32 v184, v184 clamp\n"
    "v_mfma_f32_16x16x32_bf16 v[164:167], v[12:15], v[196:199], v[164:167]\n"
    "s_lshr_b32 s94, s60, 12\n"
    "v_exp_f32 v185, v185 clamp\n"
    "v_mfma_f32_16x16x32_bf16 v[168:171], v[16:19], v[192:195], v[152:155]\n"
    "s_and_b32 s94, s94, 0x1ff0\n"
    "v_exp_f32 v186, v186 clamp\n"
    "v_mfma_f32_16x16x32_bf16 v[168:171], v[20:23], v[196:199], v[168:171]\n"
    "s_mov_b32 m0, s62\n"
    "v_exp_f32 v187, v187 clamp\n"
    "v_mfma_f32_16x16x32_bf16 v[172:175], v[24:27], v[192:195], v[156:159]\n"
    "s_add_u32 s66, s66, 16\n"
    "v_exp_f32 v188, v188 clamp\n"
    "v_mfma_f32_16x16x32_bf16 v[172:175], v[28:31], v[196:199], v[172:175]\n"
    "s_and_b32 s64, s60, 0xffff\n"
    "v_xor_b32 v252, s94, v250\n"
    "v_exp_f32 v189, v189 clamp\n"
    "v_exp_f32 v190, v190 clamp\n"
    "v_exp_f32 v191, v191 clamp\n"
    "v_pk_fma_f32 v[176:177], v[176:177], s[70:71], v[128:129]\n"
    "v_pk_fma_f32 v[178:179], v[178:179], s[70:71], v[130:131]\n"
    "v_pk_add_f32 v[230:231], v[176:177], v[178:179]\n"
    "v_pk_mul_f32 v[232:233], v[176:177], v[178:179]\n"
    "v_pk_add_f32 v[218:219], v[224:225], v[226:227]\n"
    "v_mov_b64 v[224:225], 0\n"
    "v_mov_b64 v[226:227], 0\n"
    "v_pk_fma_f32 v[180:181], v[180:181], s[70:71], v[132:133]\n"
    "v_pk_fma_f32 v[230:231], v[230:231], v[180:181], v[232:233]\n"
    "v_pk_mul_f32 v[232:233], v[232:233], v[180:181]\n"
    "v_pk_fma_f32 v[182:183], v[182:183], s[70:71], v[134:135]\n"
    "v_pk_fma_f32 v[230:231], v[230:231], v[182:183], v[232:233]\n"
    "v_pk_mul_f32 v[232:233], v[232:233], v[182:183]\n"
    "v_pk_fma_f32 v[184:185], v[184:185], s[70:71], v[136:137]\n"
    "v_pk_fma_f32 v[230:231], v[230:231], v[184:185], v[232:233]\n"
    "v_pk_mul_f32 v[232:233], v[232:233], v[184:185]\n"
    "v_pk_fma_f32 v[186:187], v[186:187], s[70:71], v[138:139]\n"
    "v_pk_fma_f32 v[230:231], v[230:231], v[186:187], v[232:233]\n"
    "v_pk_mul_f32 v[232:233], v[232:233], v[186:187]\n"
    "v_pk_fma_f32 v[188:189], v[188:189], s[70:71], v[140:141]\n"
    "v_pk_fma_f32 v[230:231], v[230:231], v[188:189], v[232:233]\n"
    "v_pk_mul_f32 v[232:233], v[232:233], v[188:189]\n"
    "v_pk_fma_f32 v[190:191], v[190:191], s[70:71], v[142:143]\n"
    "v_pk_fma_f32 v[230:231], v[230:231], v[190:191], v[232:233]\n"
    "v_pk_mul_f32 v[232:233], v[232:233], v[190:191]\n"
    "v_rcp_f32 v232, v232\n"
    "v_rcp_f32 v233, v233\n"
    "v_fma_f32 v230, v230, v232, v247\n"
    "v_fmac_f32 v230, v231, v233\n"
    "v_mbcnt_lo_u32_b32 v232, -1, 0\n"
    "v_mbcnt_hi_u32_b32 v232, -1, v232\n"
    "v_bfe_u32 v233, v232, 3, 1\n"
    "v_and_b32 v232, 7, v232\n"
    "v_lshl_add_u32 v232, v232, 1, v233\n"
    "v_add_u32 v232, s81, v232\n"
    "ds_read_u8 v232, v232 offset:49664\n"
    "s_add_u32 s81, s81, 16\n"
    "s_waitcnt lgkmcnt(0)\n"
    "buffer_load_ushort v232, v232, s[44:47], s63 idxen\n"
    "s_waitcnt vmcnt(0)\n"
    "v_lshlrev_b32 v232, 16, v232\n"
    "v_mul_f32 v230, v230, v232\n"
    "v_add_f32 v218, v218, v219\n"
    "s_setprio 0\n"
    "ds_read_b64_tr_b16 v[200:201], v240 offset:4096\n"
    "ds_read_b64_tr_b16 v[202:203], v241 offset:4096\n"
    "ds_read_b64_tr_b16 v[204:205], v242 offset:4096\n"
    "ds_read_b64_tr_b16 v[206:207], v243 offset:4096\n"
    "ds_read_u8 v236, v251 offset:0\n"
    "ds_read_u8 v238, v251 offset:1\n"
    "ds_write_b16_d16_hi v245, v230\n"
    "ds_read_b64 v[228:229], v246\n"
    "ds_write_addtid_b32 v218 offset:49152\n"
    "s_add_u32 m0, s68, 4096\n"
    "s_bfe_u32 s94, s60, 0x30013\n"
    "s_add_u32 s58, s58, s57\n"
    "s_mul_i32 s94, s94, s56\n"
    "s_add_u32 s82, s64, s9\n"
    "s_add_u32 s65, s58, s94\n"
    "s_waitcnt vmcnt(2)\n"
    "ds_read_b128 v[192:195], v234 offset:6144\n"
    "ds_read_b128 v[196:199], v235 offset:6144\n"
    "ds_read_b128 v[216:219], v252 offset:0\n"
    "ds_read_b128 v[220:223], v252 offset:64\n"
    "s_mul_i32 s15, s15, s16\n"
    "s_cmp_lt_u32 s72, 49\n"
    "s_cselect_b32 s15, 0, s15\n"
    "s_add_u32 s14, s14, s15\n"
    "s_mov_b32 s15, 0\n"
    "s_min_u32 s14, s14, s17\n"
    "s_add_u32 s95, s61, s14\n"
    "s_sub_u32 s95, s95, s76\n"
    "s_lshr_b32 s94, s61, 16\n"
    "s_sext_i32_i16 s95, s95\n"
    "s_cmp_eq_u32 s59, s10\n"
    "s_cbranch_scc1 .Ldin_A1s_skip\n"
    "s_waitcnt lgkmcnt(7)\n"
    "buffer_load_dwordx4 v[236:237], s[40:43], s67 idxen offen offset:0 nt lds\n"
    "buffer_load_dwordx4 v[238:239], s[40:43], s67 idxen offen offset:1024 nt lds\n"
    ".Ldin_A1s_skr:\n"
    "s_mul_i32 s95, s95, s94\n"
    "s_bfe_u32 s94, s60, 0x50019\n"
    "s_ashr_i32 s95, s95, 16\n"
    "s_lshl4_add_u32 s83, s94, s64\n"
    "s_max_i32 s95, s95, -14\n"
    "s_add_u32 s59, s59, 1\n"
    "s_min_i32 s95, s95, 14\n"
    "s_add_u32 s72, s77, s95\n"
    "s_cmp_lt_i32 s60, 0\n"
    "s_cselect_b32 s72, 0, s72\n"
    "s_cmp_eq_u32 s66, s80\n"
    "s_cselect_b64 s[66:67], s[64:65], s[66:67]\n"
    "s_waitcnt lgkmcnt(5)\n"
    "s_branch .Ldin_G2s\n"
    ".Ldin_A1s_poll:\n"
    "s_sleep 1\n"
    "ds_read_b64 v[216:217], v246 offset:128\n"
    "s_waitcnt lgkmcnt(0)\n"
    "v_readlane_b32 s60, v216, s59\n"
    "v_readlane_b32 s61, v217, s59\n"
    "s_cmp_eq_u32 s60, -1\n"
    "s_cbranch_scc1 .Ldin_A1s_poll\n"
    "s_branch .Ldin_A1s_pr\n"
    ".Ldin_A1s_skip:\n"
    "s_add_u32 s8, s8, 6144\n"
    "s_sub_u32 s68, s68, 2048\n"
    "s_mov_b32 s9, 48\n"
    "s_sub_u32 s82, s82, 16\n"
    "s_sub_u32 s66, s66, 16\n"
    "s_sub_u32 s76, s76, s72\n"
    "s_waitcnt lgkmcnt(0)\n"
    "s_branch .Ldin_A1s_skr\n"
    ".p2align 5\n"
    ".Ldin_L3s:\n"
    "s_cmp_lt_i32 s60, 0\n"
    "s_cbranch_scc1 .Ldin_E1s\n"
    "ds_read_b128 v[144:147], v252 offset:8192\n"
    "ds_read_b128 v[148:151], v252 offset:8256\n"
    "ds_read_b128 v[152:155], v252 offset:16384\n"
    "ds_read_b128 v[156:159], v252 offset:16448\n"
    "v_add_u32 v251, s66, v244\n"
    "v_and_b32 v216, v248, v216\n"
    "v_and_b32 v217, v249, v217\n"
    "v_and_b32 v218, v248, v218\n"
    "v_and_b32 v219, v249, v219\n"
    "v_mfma_f32_4x4x4_16b_bf16 v[160:163], v[216:217], v[96:97], v[32:35]\n"
    "v_and_b32 v220, v248, v220\n"
    "v_and_b32 v221, v249, v221\n"
    "v_mfma_f32_4x4x4_16b_bf16 v[164:167], v[218:219], v[98:99], v[36:39]\n"
    "v_and_b32 v222, v248, v222\n"
    "v_and_b32_e64 v223, v249, v223\n"
    "v_mfma_f32_4x4x4_16b_bf16 v[168:171], v[220:221], v[100:101], v[40:43]\n"
    "v_cvt_pk_bf16_f32 v0, v160, v161\n"
    "v_mfma_f32_4x4x4_16b_bf16 v[224:227], v[228:229], v[200:201], v[224:227] cbsz:4 abid:0\n"
    "v_cvt_pk_bf16_f32 v1, v162, v163\n"
    "v_mfma_f32_4x4x4_16b_bf16 v[172:175], v[222:223], v[102:103], v[44:47]\n"
    "v_cvt_pk_bf16_f32 v2, v164, v165\n"
    "v_cvt_pk_bf16_f32 v3, v166, v167\n"
    "v_mfma_f32_4x4x4_16b_bf16 v[160:163], v[216:217], v[104:105], v[48:51]\n"
    "v_cvt_pk_bf16_f32 v4, v168, v169\n"
    "v_cvt_pk_bf16_f32 v5, v170, v171\n"
    "v_mfma_f32_4x4x4_16b_bf16 v[164:167], v[218:219], v[106:107], v[52:55]\n"
    "v_cvt_pk_bf16_f32 v6, v172, v173\n"
    "v_mfma_f32_4x4x4_16b_bf16 v[224:227], v[228:229], v[202:203], v[224:227] cbsz:4 abid:1\n"
    "v_cvt_pk_bf16_f32 v7, v174, v175\n"
    "v_mfma_f32_4x4x4_16b_bf16 v[168:171], v[220:221], v[108:109], v[56:59]\n"
    "v_cvt_pk_bf16_f32 v8, v160, v161\n"
    "v_cvt_pk_bf16_f32 v9, v162, v163\n"
    "v_mfma_f32_4x4x4_16b_bf16 v[172:175], v[222:223], v[110:111], v[60:63]\n"
    "v_cvt_pk_bf16_f32 v10, v164, v165\n"
    "v_cvt_pk_bf16_f32 v11, v166, v167\n"
    "v_mfma_f32_4x4x4_16b_bf16 v[160:163], v[216:217], v[112:113], v[64:67]\n"
    "v_cvt_pk_bf16_f32 v12, v168, v169\n"
    "v_mfma_f32_4x4x4_16b_bf16 v[224:227], v[228:229], v[204:205], v[224:227] cbsz:4 abid:2\n"
    "v_cvt_pk_bf16_f32 v13, v170, v171\n"
    "s_setprio 1\n"
    "s_waitcnt lgkmcnt(0)\n"
    "v_mfma_f32_4x4x4_16b_bf16 v[164:167], v[218:219], v[114:115], v[68:71]\n"
    "v_cvt_pk_bf16_f32 v14, v172, v173\n"
    "v_cvt_pk_bf16_f32 v15, v174, v175\n"
    "v_mfma_f32_4x4x4_16b_bf16 v[168:171], v[220:221], v[116:117], v[72:75]\n"
    "v_cvt_pk_bf16_f32 v16, v160, v161\n"
    "v_cvt_pk_bf16_f32 v17, v162, v163\n"
    "v_mfma_f32_4x4x4_16b_bf16 v[172:175], v[222:223], v[118:119], v[76:79]\n"
    "v_cvt_pk_bf16_f32 v18, v164, v165\n"
    "v_mfma_f32_4x4x4_16b_bf16 v[224:227], v[228:229], v[206:207], v[224:227] cbsz:4 abid:3\n"
    "v_cvt_pk_bf16_f32 v19, v166, v167\n"
    "v_mfma_f32_4x4x4_16b_bf16 v[160:163], v[216:217], v[120:121], v[80:83]\n"
    "v_cvt_pk_bf16_f32 v20, v168, v169\n"
    "v_cvt_pk_bf16_f32 v21, v170, v171\n"
    "v_mfma_f32_4x4x4_16b_bf16 v[164:167], v[218:219], v[122:123], v[84:87]\n"
    "v_cvt_pk_bf16_f32 v22, v172, v173\n"
    "v_cvt_pk_bf16_f32 v23, v174, v175\n"
    "v_mfma_f32_4x4x4_16b_bf16 v[168:171], v[220:221], v[124:125], v[88:91]\n"
    "v_cvt_pk_bf16_f32 v24, v160, v161\n"
    "v_cvt_pk_bf16_f32 v25, v162, v163\n"
    "v_mfma_f32_4x4x4_16b_bf16 v[172:175], v[222:223], v[126:127], v[92:95]\n"
    "v_cvt_pk_bf16_f32 v26, v164, v165\n"
    "v_cvt_pk_bf16_f32 v27, v166, v167\n"
    "v_cvt_pk_bf16_f32 v28, v168, v169\n"
    "v_cvt_pk_bf16_f32 v29, v170, v171\n"
    "v_mfma_f32_16x16x32_bf16 v[160:163], v[0:3], v[192:195], v[144:147]\n"
    "s_add_u32 m0, s8, 0\n"
    "v_cvt_pk_bf16_f32 v30, v172, v173\n"
    "v_cvt_pk_bf16_f32 v31, v174, v175\n"
    "v_exp_f32 v176, v176 clamp\n"
    "v_mfma_f32_16x16x32_bf16 v[160:163], v[4:7], v[196:199], v[160:163]\n"
    "s_add_u32 s66, s66, 16\n"
    "v_exp_f32 v177, v177 clamp\n"
    "v_exp_f32 v178, v178 clamp\n"
    "v_mfma_f32_16x16x32_bf16 v[164:167], v[8:11], v[192:195], v[148:151]\n"
    "s_add_u32 s76, s76, s73\n"
    "v_exp_f32 v179, v179 clamp\n"
    "v_exp_f32 v180, v180 clamp\n"
    "v_mfma_f32_16x16x32_bf16 v[164:167], v[12:15], v[196:199], v[164:167]\n"
    "s_lshl1_add_u32 s76, s72, s76\n"
    "v_exp_f32 v181, v181 clamp\n"
    "v_exp_f32 v182, v182 clamp\n"
    "v_mfma_f32_16x16x32_bf16 v[168:171], v[16:19], v[192:195], v[152:155]\n"
    "s_mov_b32 s80, s83\n"
    "v_exp_f32 v183, v183 clamp\n"
    "v_exp_f32 v184, v184 clamp\n"
    "v_mfma_f32_16x16x32_bf16 v[168:171], v[20:23], v[196:199], v[168:171]\n"
    "s_add_u32 s62, s62, 256\n"
    "v_exp_f32 v185, v185 clamp\n"
    "v_exp_f32 v186, v186 clamp\n"
    "v_mfma_f32_16x16x32_bf16 v[172:175], v[24:27], v[192:195], v[156:159]\n"
    "v_exp_f32 v187, v187 clamp\n"
    "v_exp_f32 v188, v188 clamp\n"
    "v_mfma_f32_16x16x32_bf16 v[172:175], v[28:31], v[196:199], v[172:175]\n"
    "v_exp_f32 v189, v189 clamp\n"
    "v_exp_f32 v190, v190 clamp\n"
    "v_exp_f32 v191, v191 clamp\n"
    "s_nop 0\n"
    "v_pk_fma_f32 v[176:177], v[176:177], s[70:71], v[128:129]\n"
    "v_pk_fma_f32 v[178:179], v[178:179], s[70:71], v[130:131]\n"
    "v_pk_add_f32 v[230:231], v[176:177], v[178:179]\n"
    "v_pk_mul_f32 v[232:233], v[176:177], v[178:179]\n"
    "v_pk_fma_f32 v[180:181], v[180:181], s[70:71], v[132:133]\n"
    "v_pk_fma_f32 v[230:231], v[230:231], v[180:181], v[232:233]\n"
    "v_pk_mul_f32 v[232:233], v[232:233], v[180:181]\n"
    "v_pk_fma_f32 v[182:183], v[182:183], s[70:71], v[134:135]\n"
    "v_pk_fma_f32 v[230:231], v[230:231], v[182:183], v[232:233]\n"
    "v_pk_mul_f32 v[232:233], v[232:233], v[182:183]\n"
    "v_pk_fma_f32 v[184:185], v[184:185], s[70:71], v[136:137]\n"
    "v_pk_fma_f32 v[230:231], v[230:231], v[184:185], v[232:233]\n"
    "v_pk_mul_f32 v[232:233], v[232:233], v[184:185]\n"
    "v_pk_fma_f32 v[186:187], v[186:187], s[70:71], v[138:139]\n"
    "v_pk_fma_f32 v[230:231], v[230:231], v[186:187], v[232:233]\n"
    "v_pk_mul_f32 v[232:233], v[232:233], v[186:187]\n"
    "v_pk_fma_f32 v[188:189], v[188:189], s[70:71], v[140:141]\n"
    "v_pk_fma_f32 v[230:231], v[230:231], v[188:189], v[232:233]\n"
    "v_pk_mul_f32 v[232:233], v[232:233], v[188:189]\n"
    "v_pk_fma_f32 v[190:191], v[190:191], s[70:71], v[142:143]\n"
    "v_pk_fma_f32 v[230:231], v[230:231], v[190:191], v[232:233]\n"
    "v_pk_mul_f32 v[232:233], v[232:233], v[190:191]\n"
    "v_rcp_f32 v232, v232\n"
    "v_rcp_f32 v233, v233\n"
    "v_fma_f32 v230, v230, v232, v247\n"
    "v_fmac_f32 v230, v231, v233\n"
    "v_mbcnt_lo_u32_b32 v232, -1, 0\n"
    "v_mbcnt_hi_u32_b32 v232, -1, v232\n"
    "v_bfe_u32 v233, v232, 3, 1\n"
    "v_and_b32 v232, 7, v232\n"
    "v_lshl_add_u32 v232, v232, 1, v233\n"
    "v_add_u32 v232, s81, v232\n"
    "ds_read_u8 v232, v232 offset:49664\n"
    "s_add_u32 s81, s81, 16\n"
    "s_waitcnt lgkmcnt(0)\n"
    "buffer_load_ushort v232, v232, s[44:47], s63 idxen\n"
    "s_waitcnt vmcnt(0)\n"
    "v_lshlrev_b32 v232, 16, v232\n"
    "v_mul_f32_e64 v230, v230, v232\n"
    "s_setprio 0\n"
    "ds_read_b64_tr_b16 v[200:201], v240 offset:0\n"
    "ds_read_b64_tr_b16 v[202:203], v241 offset:0\n"
    "ds_read_b64_tr_b16 v[204:205], v242 offset:0\n"
    "ds_read_b64_tr_b16 v[206:207], v243 offset:0\n"
    "ds_read_u8 v236, v251 offset:0\n"
    "ds_read_u8 v238, v251 offset:1\n"
    "ds_write_b16_d16_hi v245, v230\n"
    "ds_read_b64 v[228:229], v246\n"
    "ds_read_b64 v[216:217], v246 offset:128\n"
    "s_mov_b32 s81, s64\n"
    "s_lshr_b32 s63, s65, 6\n"
    "s_waitcnt vmcnt(2)\n"
    "ds_read_b128 v[192:195], v234 offset:2048\n"
    "ds_read_b128 v[196:199], v235 offset:2048\n"
    "s_waitcnt lgkmcnt(5)\n"
    "buffer_load_dwordx4 v[236:237], s[40:43], s67 idxen offen offset:0 nt lds\n"
    "buffer_load_dwordx4 v[238:239], s[40:43], s67 idxen offen offset:1024 nt lds\n"
    "s_waitcnt lgkmcnt(2)\n"
    "s_bitcmp1_b32 s60, 30\n"
    "s_cbranch_scc0 .Ldin_A0\n"
    "s_branch .Ldin_A0s\n"
    ".Ldin_E0s:\n"
    "s_add_u32 m0, s62, 256\n"
    "v_exp_f32 v160, v160 clamp\n"
    "s_setprio 1\n"
    "v_exp_f32 v161, v161 clamp\n"
    "v_exp_f32 v162, v162 clamp\n"
    "v_exp_f32 v163, v163 clamp\n"
    "v_exp_f32 v164, v164 clamp\n"
    "v_exp_f32 v165, v165 clamp\n"
    "v_exp_f32 v166, v166 clamp\n"
    "v_exp_f32 v167, v167 clamp\n"
    "v_mfma_f32_4x4x4_16b_bf16 v[224:227], v[228:229], v[208:209], v[224:227] cbsz:4 abid:0\n"
    "v_exp_f32 v168, v168 clamp\n"
    "v_exp_f32 v169, v169 clamp\n"
    "v_mfma_f32_4x4x4_16b_bf16 v[224:227], v[228:229], v[210:211], v[224:227] cbsz:4 abid:1\n"
    "v_exp_f32 v170, v170 clamp\n"
    "v_exp_f32 v171, v171 clamp\n"
    "v_mfma_f32_4x4x4_16b_bf16 v[224:227], v[228:229], v[212:213], v[224:227] cbsz:4 abid:2\n"
    "v_exp_f32 v172, v172 clamp\n"
    "v_exp_f32 v173, v173 clamp\n"
    "v_mfma_f32_4x4x4_16b_bf16 v[224:227], v[228:229], v[214:215], v[224:227] cbsz:4 abid:3\n"
    "v_exp_f32 v174, v174 clamp\n"
    "v_exp_f32 v175, v175 clamp\n"
    "s_nop 0\n"
    "v_pk_fma_f32 v[160:161], v[160:161], s[70:71], v[128:129]\n"
    "v_pk_fma_f32 v[162:163], v[162:163], s[70:71], v[130:131]\n"
    "v_pk_add_f32 v[230:231], v[160:161], v[162:163]\n"
    "v_pk_mul_f32 v[232:233], v[160:161], v[162:163]\n"
    "v_pk_fma_f32 v[164:165], v[164:165], s[70:71], v[132:133]\n"
    "v_pk_fma_f32 v[230:231], v[230:231], v[164:165], v[232:233]\n"
    "v_pk_mul_f32 v[232:233], v[232:233], v[164:165]\n"
    "v_pk_fma_f32 v[166:167], v[166:167], s[70:71], v[134:135]\n"
    "v_pk_fma_f32 v[230:231], v[230:231], v[166:167], v[232:233]\n"
    "v_pk_mul_f32 v[232:233], v[232:233], v[166:167]\n"
    "v_pk_fma_f32 v[168:169], v[168:169], s[70:71], v[136:137]\n"
    "v_pk_fma_f32 v[230:231], v[230:231], v[168:169], v[232:233]\n"
    "v_pk_mul_f32 v[232:233], v[232:233], v[168:169]\n"
    "v_pk_fma_f32 v[170:171], v[170:171], s[70:71], v[138:139]\n"
    "v_pk_fma_f32 v[230:231], v[230:231], v[170:171], v[232:233]\n"
    "v_pk_mul_f32 v[232:233], v[232:233], v[170:171]\n"
    "v_pk_fma_f32 v[172:173], v[172:173], s[70:71], v[140:141]\n"
    "v_pk_fma_f32 v[230:231], v[230:231], v[172:173], v[232:233]\n"
    "v_pk_mul_f32 v[232:233], v[232:233], v[172:173]\n"
    "v_pk_fma_f32 v[174:175], v[174:175], s[70:71], v[142:143]\n"
    "v_pk_fma_f32 v[230:231], v[230:231], v[174:175], v[232:233]\n"
    "v_pk_mul_f32 v[232:233], v[232:233], v[174:175]\n"
    "v_rcp_f32 v232, v232\n"
    "v_rcp_f32 v233, v233\n"
    "v_fma_f32 v230, v230, v232, v247\n"
    "v_fmac_f32 v230, v231, v233\n"
    "v_mbcnt_lo_u32_b32 v232, -1, 0\n"
    "v_mbcnt_hi_u32_b32 v232, -1, v232\n"
    "v_bfe_u32 v233, v232, 3, 1\n"
    "v_and_b32 v232, 7, v232\n"
    "v_lshl_add_u32 v232, v232, 1, v233\n"
    "v_add_u32 v232, s81, v232\n"
    "ds_read_u8 v232, v232 offset:49664\n"
    "s_add_u32 s81, s81, 16\n"
    "s_waitcnt lgkmcnt(0)\n"
    "buffer_load_ushort v232, v232, s[44:47], s63 idxen\n"
    "s_waitcnt vmcnt(0)\n"
    "v_lshlrev_b32 v232, 16, v232\n"
    "v_mul_f32 v230, v230, v232\n"
    "ds_write_b16_d16_hi v245, v230\n"
    "ds_read_b64 v[228:229], v246\n"
    "s_branch .Ldin_drain0\n"
    ".Ldin_E1s:\n"
    "s_add_u32 m0, s62, 256\n"
    "v_exp_f32 v176, v176 clamp\n"
    "s_setprio 1\n"
    "v_exp_f32 v177, v177 clamp\n"
    "v_exp_f32 v178, v178 clamp\n"
    "v_exp_f32 v179, v179 clamp\n"
    "v_exp_f32 v180, v180 clamp\n"
    "v_exp_f32 v181, v181 clamp\n"
    "v_exp_f32 v182, v182 clamp\n"
    "v_exp_f32 v183, v183 clamp\n"
    "v_mfma_f32_4x4x4_16b_bf16 v[224:227], v[228:229], v[200:201], v[224:227] cbsz:4 abid:0\n"
    "v_exp_f32 v184, v184 clamp\n"
    "v_exp_f32 v185, v185 clamp\n"
    "v_mfma_f32_4x4x4_16b_bf16 v[224:227], v[228:229], v[202:203], v[224:227] cbsz:4 abid:1\n"
    "v_exp_f32 v186, v186 clamp\n"
    "v_exp_f32 v187, v187 clamp\n"
    "v_mfma_f32_4x4x4_16b_bf16 v[224:227], v[228:229], v[204:205], v[224:227] cbsz:4 abid:2\n"
    "v_exp_f32 v188, v188 clamp\n"
    "v_exp_f32 v189, v189 clamp\n"
    "v_mfma_f32_4x4x4_16b_bf16 v[224:227], v[228:229], v[206:207], v[224:227] cbsz:4 abid:3\n"
    "v_exp_f32 v190, v190 clamp\n"
    "v_exp_f32 v191, v191 clamp\n"
    "v_pk_fma_f32 v[176:177], v[176:177], s[70:71], v[128:129]\n"
    "v_pk_fma_f32 v[178:179], v[178:179], s[70:71], v[130:131]\n"
    "v_pk_add_f32 v[230:231], v[176:177], v[178:179]\n"
    "v_pk_mul_f32 v[232:233], v[176:177], v[178:179]\n"
    "v_pk_fma_f32 v[180:181], v[180:181], s[70:71], v[132:133]\n"
    "v_pk_fma_f32 v[230:231], v[230:231], v[180:181], v[232:233]\n"
    "v_pk_mul_f32 v[232:233], v[232:233], v[180:181]\n"
    "v_pk_fma_f32 v[182:183], v[182:183], s[70:71], v[134:135]\n"
    "v_pk_fma_f32 v[230:231], v[230:231], v[182:183], v[232:233]\n"
    "v_pk_mul_f32 v[232:233], v[232:233], v[182:183]\n"
    "v_pk_fma_f32 v[184:185], v[184:185], s[70:71], v[136:137]\n"
    "v_pk_fma_f32 v[230:231], v[230:231], v[184:185], v[232:233]\n"
    "v_pk_mul_f32 v[232:233], v[232:233], v[184:185]\n"
    "v_pk_fma_f32 v[186:187], v[186:187], s[70:71], v[138:139]\n"
    "v_pk_fma_f32 v[230:231], v[230:231], v[186:187], v[232:233]\n"
    "v_pk_mul_f32 v[232:233], v[232:233], v[186:187]\n"
    "v_pk_fma_f32 v[188:189], v[188:189], s[70:71], v[140:141]\n"
    "v_pk_fma_f32 v[230:231], v[230:231], v[188:189], v[232:233]\n"
    "v_pk_mul_f32 v[232:233], v[232:233], v[188:189]\n"
    "v_pk_fma_f32 v[190:191], v[190:191], s[70:71], v[142:143]\n"
    "v_pk_fma_f32 v[230:231], v[230:231], v[190:191], v[232:233]\n"
    "v_pk_mul_f32 v[232:233], v[232:233], v[190:191]\n"
    "v_rcp_f32 v232, v232\n"
    "v_rcp_f32 v233, v233\n"
    "v_fma_f32 v230, v230, v232, v247\n"
    "v_fmac_f32 v230, v231, v233\n"
    "v_mbcnt_lo_u32_b32 v232, -1, 0\n"
    "v_mbcnt_hi_u32_b32 v232, -1, v232\n"
    "v_bfe_u32 v233, v232, 3, 1\n"
    "v_and_b32 v232, 7, v232\n"
    "v_lshl_add_u32 v232, v232, 1, v233\n"
    "v_add_u32 v232, s81, v232\n"
    "ds_read_u8 v232, v232 offset:49664\n"
    "s_add_u32 s81, s81, 16\n"
    "s_waitcnt lgkmcnt(0)\n"
    "buffer_load_ushort v232, v232, s[44:47], s63 idxen\n"
    "s_waitcnt vmcnt(0)\n"
    "v_lshlrev_b32 v232, 16, v232\n"
    "v_mul_f32 v230, v230, v232\n"
    "ds_write_b16_d16_hi v245, v230\n"
    "ds_read_b64 v[228:229], v246\n"
    "s_branch .Ldin_drain1\n"
    ".Ldin_idle:\n"
    "s_waitcnt vmcnt(0)\n"
    ".Ldin_exit:\n"

      :
      : "{s[0:1]}"(__builtin_amdgcn_kernarg_segment_ptr()), "{s2}"(blockIdx.x), "{v0}"(threadIdx.x), "{s97}"((uint32_t)(uintptr_t)smem)
      : "memory", "vcc", "scc", "v1", "v2", "v3", "v4", "v5", "v6", "v7", "v8", "v9", "v10", "v11", "v12", "v13", "v14", "v15", "v16", "v17", "v18", "v19", "v20", "v21", "v22", "v23", "v24", "v25", "v26", "v27", "v28", "v29", "v30", "v31", "v32", "v33", "v34", "v35", "v36", "v37", "v38", "v39", "v40", "v41", "v42", "v43", "v44", "v45", "v46", "v47", "v48", "v49", "v50", "v51", "v52", "v53", "v54", "v55", "v56", "v57", "v58", "v59", "v60", "v61", "v62", "v63", "v64", "v65", "v66", "v67", "v68", "v69", "v70", "v71", "v72", "v73", "v74", "v75", "v76", "v77", "v78", "v79", "v80", "v81", "v82", "v83", "v84", "v85", "v86", "v87", "v88", "v89", "v90", "v91", "v92", "v93", "v94", "v95", "v96", "v97", "v98", "v99", "v100", "v101", "v102", "v103", "v104", "v105", "v106", "v107", "v108", "v109", "v110", "v111", "v112", "v113", "v114", "v115", "v116", "v117", "v118", "v119", "v120", "v121", "v122", "v123", "v124", "v125", "v126", "v127", "v128", "v129", "v130", "v131", "v132", "v133", "v134", "v135", "v136", "v137", "v138", "v139", "v140", "v141", "v142", "v143", "v144", "v145", "v146", "v147", "v148", "v149", "v150", "v151", "v152", "v153", "v154", "v155", "v156", "v157", "v158", "v159", "v160", "v161", "v162", "v163", "v164", "v165", "v166", "v167", "v168", "v169", "v170", "v171", "v172", "v173", "v174", "v175", "v176", "v177", "v178", "v179", "v180", "v181", "v182", "v183", "v184", "v185", "v186", "v187", "v188", "v189", "v190", "v191", "v192", "v193", "v194", "v195", "v196", "v197", "v198", "v199", "v200", "v201", "v202", "v203", "v204", "v205", "v206", "v207", "v208", "v209", "v210", "v211", "v212", "v213", "v214", "v215", "v216", "v217", "v218", "v219", "v220", "v221", "v222", "v223", "v224", "v225", "v226", "v227", "v228", "v229", "v230", "v231", "v232", "v233", "v234", "v235", "v236", "v237", "v238", "v239", "v240", "v241", "v242", "v243", "v244", "v245", "v246", "v247", "v248", "v249", "v250", "v251", "v252", "v253", "s3", "s4", "s5", "s6", "s7", "s8", "s9", "s10", "s11", "s12", "s13", "s14", "s15", "s16", "s17", "s18", "s19", "s20", "s21", "s22", "s23", "s24", "s25", "s26", "s27", "s28", "s29", "s30", "s31", "s33", "s34", "s35", "s36", "s37", "s38", "s39", "s40", "s41", "s42", "s43", "s44", "s45", "s46", "s47", "s48", "s49", "s50", "s51", "s52", "s53", "s54", "s55", "s56", "s57", "s58", "s59", "s60", "s61", "s62", "s63", "s64", "s65", "s66", "s67", "s68", "s69", "s70", "s71", "s72", "s73", "s74", "s75", "s76", "s77", "s78", "s79", "s80", "s81", "s82", "s83", "s84", "s85", "s86", "s87", "s88", "s89", "s90", "s91", "s92", "s93", "s94", "s95", "s96");
}

__global__ __launch_bounds__(64) void din_fallback_kernel(
    const unsigned short* __restrict__ target, const unsigned short* __restrict__ hist,
    const unsigned short* __restrict__ mask, const unsigned short* __restrict__ w1,
    const unsigned short* __restrict__ b1, const unsigned short* __restrict__ w2,
    const unsigned short* __restrict__ b2, unsigned short* __restrict__ out, int B, int L) {
  __shared__ float s_w[64 * 64];
  __shared__ float s_x[64];
  const int b = blockIdx.x, h = threadIdx.x;
  float bias = bf2f(b1[h]);
  for (int d = 0; d < 64; ++d) {
    const float t = bf2f(target[(size_t)b * 64 + d]);
    const float wc = bf2f(w1[(128 + d) * 64 + h]);
    bias += t * (bf2f(w1[(64 + d) * 64 + h]) - wc);
    s_w[d * 64 + h] = bf2f(w1[d * 64 + h]) + wc + t * bf2f(w1[(192 + d) * 64 + h]);
  }
  const float w2h = bf2f(w2[h]), b2v = bf2f(b2[0]);
  float acc = 0.f;
  for (int l = 0; l < L; ++l) {
    const float m = bf2f(mask[(size_t)b * L + l]);
    if (m == 0.f) continue;
    const float x = bf2f(hist[((size_t)b * L + l) * 64 + h]);
    __syncthreads();
    s_x[h] = x;
    __syncthreads();
    float z = bias;
    for (int d = 0; d < 64; ++d) z += s_x[d] * s_w[d * 64 + h];
    float s = w2h / (1.0f + __expf(-z));
    for (int o = 32; o > 0; o >>= 1) s += __shfl_xor(s, o);
    acc += (s + b2v) * m * x;
  }
  const uint32_t u = __float_as_uint(acc);
  out[(size_t)b * 64 + h] = (unsigned short)((u + 0x7fffu + ((u >> 16) & 1u)) >> 16);
}

extern "C" void kernel_launch(void* const* d_in, const int* in_sizes, int n_in, void* d_out,
                              int out_size, void* d_ws, size_t ws_size, hipStream_t stream) {
  const unsigned short* target = (const unsigned short*)d_in[0];
  const unsigned short* hist = (const unsigned short*)d_in[1];
  const unsigned short* mask = (const unsigned short*)d_in[2];
  const unsigned short* w1 = (const unsigned short*)d_in[3];
  const unsigned short* b1 = (const unsigned short*)d_in[4];
  const unsigned short* w2 = (const unsigned short*)d_in[5];
  const unsigned short* b2 = (const unsigned short*)d_in[6];
  int B = in_sizes[0] / 64;
  int L = in_sizes[2] / B;
  if ((L & 1) || L > 254 || B >= (1 << 24) - 16) {
    din_fallback_kernel<<<B, 64, 0, stream>>>(target, hist, mask, w1, b1, w2, b2, (unsigned short*)d_out, B, L);
    return;
  }
  const uint32_t G = (B + 63) / 64, L2 = 2u * L, lanes = (L2 + 15) / 16, F = 0x20000u;
  const bool down = B % 8 == 0;
  const uint32_t step = down ? 0u - 8u * G : 8u * G;
  din_kernel<<<G, 512, 0, stream>>>(hist, (uint32_t)L, F, mask, (uint32_t)B * L2, F, w1, 32768u, F, (unsigned short*)d_out, (uint32_t)B * 128u, F,
                                    target, (uint32_t)B * 128u, F, b1, w2, b2, L2, step * L2, lanes >= 32 ? 0xffffffffu : (1u << lanes) - 1u,
                                    step * 128u, G >= 224 ? 0xffffffffu : 0u, down ? (uint32_t)B - 8u * G : 0u);
}
```
